```python
import torch

def _softmax(x, axis=-1):
    return torch.softmax(x, dim=axis)
def _gelu(x):
    return torch.nn.functional.gelu(x, approximate="tanh")
def _silu(x):
    return torch.nn.functional.silu(x)
def _sigmoid(x):
    return torch.sigmoid(x)
def _layernorm(x, axis=-1, eps=1e-5):
    m = x.mean(dim=axis, keepdim=True)
    v = x.var(dim=axis, keepdim=True, unbiased=False)
    return (x - m) / torch.sqrt(v + eps)
def _scatter_add(tgt, idx, src, axis=0):
    return tgt.clone().index_add_(axis, idx, src)
def _segment_sum(src, seg, n):
    out = torch.zeros((n,) + src.shape[1:], dtype=src.dtype)
    return out.index_add_(0, seg, src)
def _pad(x, pw):
    flat = tuple(v for pair in reversed(pw) for v in pair)
    return torch.nn.functional.pad(x, flat)

kernel_name = 'standalone__memory_aug_memorizing_tr__prompt'

def setup_inputs():

    _w = (lambda a: (lambda t: t.to(torch.bfloat16) if t.is_floating_point() else t)(torch.as_tensor(a)))
    B, H, S, D = 1, 32, 2048, 192
    M, K = 2048, 2
    return {
        "q": _w((lambda *s: torch.randn(*s, dtype=torch.bfloat16) * 0.8)(B, H, S, D)),
        "k": _w((lambda *s: torch.randn(*s, dtype=torch.bfloat16) * 0.8)(B, H, S, D)),
        "v": _w((lambda *s: torch.randn(*s, dtype=torch.bfloat16) * 0.05)(B, H, S, D)),
        "mem_k": _w((lambda *s: torch.randn(*s, dtype=torch.bfloat16) * 0.8)(B, H, M, D)),
        "mem_v": _w((lambda *s: torch.randn(*s, dtype=torch.bfloat16) * 0.05)(B, H, M, D)),
        "idx": _w(torch.randint(0, M, (B, H, S, K)).to(torch.int64)),
        "gate_bias": _w((lambda *s: torch.randn(*s, dtype=torch.bfloat16) * 0.2)(H)),
    }

def reference(q, k, v, mem_k, mem_v, idx, gate_bias):
    B, H, S, D = q.shape
    K = idx.shape[-1]
    scale = D ** -0.5
    sl = torch.einsum("bhqd,bhkd->bhqk", q, k) * scale
    cm = torch.tril(torch.ones((S, S), dtype=q.dtype))
    sl = torch.where(cm[None, None] > 0, sl, -1e9)
    pl = _softmax(sl, axis=-1)
    ol = torch.einsum("bhqk,bhkd->bhqd", pl, v)
    idx_e = (lambda x, n, axis: torch.repeat_interleave(x, n, dim=axis))(idx.reshape(B, H, S * K)[..., None], D, axis=-1)
    mk = (lambda x, i, axis: torch.take_along_dim(x, i, dim=axis))(mem_k, idx_e, axis=2).reshape(B, H, S, K, D)
    mv = (lambda x, i, axis: torch.take_along_dim(x, i, dim=axis))(mem_v, idx_e, axis=2).reshape(B, H, S, K, D)
    sm = torch.einsum("bhqd,bhqkd->bhqk", q, mk) * scale
    pm = _softmax(sm, axis=-1)
    om = torch.einsum("bhqk,bhqkd->bhqd", pm, mv)
    g = _sigmoid(gate_bias)[None, :, None, None]
    return g * om + (1 - g) * ol
```

```cpp
#include <hip/hip_runtime.h>
#include <hip/hip_bf16.h>
#include <stdint.h>

typedef short bf16x8 __attribute__((ext_vector_type(8)));
typedef short s16x4 __attribute__((ext_vector_type(4)));
typedef float f32x16 __attribute__((ext_vector_type(16)));
typedef unsigned u32x4 __attribute__((ext_vector_type(4)));
typedef unsigned u32x2 __attribute__((ext_vector_type(2)));
constexpr int H = 32, S = 2048, D = 192, M = 2048;
constexpr int KP = 400;

__device__ __forceinline__ unsigned cvtpk(float lo, float hi) {
    unsigned r; asm("v_cvt_pk_bf16_f32 %0, %1, %2" : "=v"(r) : "v"(lo), "v"(hi)); return r;
}
__device__ __forceinline__ float bflo(unsigned u) { return __uint_as_float(u << 16); }
__device__ __forceinline__ float bfhi(unsigned u) { return __uint_as_float(u & 0xffff0000u); }
typedef __attribute__((address_space(3))) const char* lds_cptr;
typedef short v4i16_t __attribute__((ext_vector_type(4)));
__device__ __forceinline__ s16x4 vtr(lds_cptr p) {
    return __builtin_bit_cast(s16x4, __builtin_amdgcn_ds_read_tr16_b64_v4i16((__attribute__((address_space(3))) v4i16_t*)p));
}

__global__ __launch_bounds__(256, 2) void attn_base(const uint16_t* __restrict__ q, const uint16_t* __restrict__ k,
        const uint16_t* __restrict__ v, const uint16_t* __restrict__ mk, const uint16_t* __restrict__ mv,
        const int* __restrict__ idx, const uint16_t* __restrict__ gb, uint16_t* __restrict__ out) {
    __shared__ __attribute__((aligned(16))) char lds[2 * 64 * KP];
    const int tid = threadIdx.x, lane = tid & 63, w = tid >> 6, r32 = lane & 31, hi = lane >> 5;
    const int bid = blockIdx.x;
    const int h = (bid & 7) + 8 * ((bid >> 3) & 3);
    const int qb = 15 - (bid >> 5);
    const int row = qb * 128 + w * 32 + r32;
    const size_t hb = (size_t)h * S * D;
    bf16x8 qf[12];
#pragma unroll
    for (int i = 0; i < 12; ++i) qf[i] = *(const bf16x8*)(q + hb + (size_t)row * D + i * 16 + hi * 8);
    f32x16 o[6];
#pragma unroll
    for (int i = 0; i < 6; ++i) o[i] = f32x16{};
    float m = -1e30f, l = 0.f;
    const float C = 0.07216878364870322f * 1.4426950408889634f;
    const int nt = qb * 2 + 2;
    char* Kl = lds; char* Vl = lds + 64 * KP;
    for (int t = 0; t < nt; ++t) {
        __syncthreads();
#pragma unroll
        for (int i = 0; i < 6; ++i) {
            const int c = tid + 256 * i, r = c / 24, ch = c % 24;
            *(u32x4*)(Kl + r * KP + ch * 16) = *(const u32x4*)(k + hb + (size_t)(t * 64 + r) * D + ch * 8);
            *(u32x4*)(Vl + r * KP + ch * 16) = *(const u32x4*)(v + hb + (size_t)(t * 64 + r) * D + ch * 8);
        }
        __syncthreads();
#pragma unroll
        for (int kb = 0; kb < 2; ++kb) {
            const int key0 = t * 64 + kb * 32;
            if (key0 > qb * 128 + w * 32 + 31) continue;
            f32x16 s = f32x16{};
#pragma unroll
            for (int i = 0; i < 12; ++i) {
                bf16x8 kf = *(const bf16x8*)(Kl + (kb * 32 + r32) * KP + i * 32 + hi * 16);
                s = __builtin_amdgcn_mfma_f32_32x32x16_bf16(kf, qf[i], s, 0, 0, 0);
            }
            float mx = -__builtin_inff();
#pragma unroll
            for (int r = 0; r < 16; ++r) {
                const int key = key0 + (r & 3) + 8 * (r >> 2) + 4 * hi;
                s[r] = key <= row ? s[r] * C : -__builtin_inff();
                mx = fmaxf(mx, s[r]);
            }
            mx = fmaxf(mx, __shfl_xor(mx, 32));
            const float mn = fmaxf(m, mx), al = __builtin_amdgcn_exp2f(m - mn);
            m = mn; float ps = 0.f;
#pragma unroll
            for (int r = 0; r < 16; ++r) { s[r] = __builtin_amdgcn_exp2f(s[r] - mn); ps += s[r]; }
            l = l * al + ps;
#pragma unroll
            for (int i = 0; i < 6; ++i) o[i] *= al;
#pragma unroll
            for (int ks = 0; ks < 2; ++ks) {
                u32x4 pw = {cvtpk(s[8*ks], s[8*ks+1]), cvtpk(s[8*ks+2], s[8*ks+3]), cvtpk(s[8*ks+4], s[8*ks+5]), cvtpk(s[8*ks+6], s[8*ks+7])};
                bf16x8 pf = __builtin_bit_cast(bf16x8, pw);
                const int kr = kb * 32 + ks * 16 + 4 * hi + ((lane & 15) >> 2);
#pragma unroll
                for (int db = 0; db < 6; ++db) {
                    lds_cptr p = (lds_cptr)Vl + kr * KP + db * 64 + ((lane >> 4) & 1) * 32 + (lane & 3) * 8;
                    s16x4 a = vtr(p), b = vtr(p + 8 * KP);
                    bf16x8 vf = {a[0], a[1], a[2], a[3], b[0], b[1], b[2], b[3]};
                    o[db] = __builtin_amdgcn_mfma_f32_32x32x16_bf16(vf, pf, o[db], 0, 0, 0);
                }
            }
        }
    }
    l += __shfl_xor(l, 32);
    const int i0 = idx[((size_t)h * S + row) * 2], i1 = idx[((size_t)h * S + row) * 2 + 1];
    const uint16_t* k0 = mk + (size_t)h * M * D + (size_t)i0 * D; const uint16_t* k1 = mk + (size_t)h * M * D + (size_t)i1 * D;
    const uint16_t* v0 = mv + (size_t)h * M * D + (size_t)i0 * D; const uint16_t* v1 = mv + (size_t)h * M * D + (size_t)i1 * D;
    float s0 = 0.f, s1 = 0.f;
#pragma unroll
    for (int i = 0; i < 12; ++i) {
        u32x4 a = *(const u32x4*)(k0 + i * 16 + hi * 8), b = *(const u32x4*)(k1 + i * 16 + hi * 8), qq = __builtin_bit_cast(u32x4, qf[i]);
#pragma unroll
        for (int e = 0; e < 4; ++e) {
            s0 += bflo(qq[e]) * bflo(a[e]) + bfhi(qq[e]) * bfhi(a[e]);
            s1 += bflo(qq[e]) * bflo(b[e]) + bfhi(qq[e]) * bfhi(b[e]);
        }
    }
    s0 += __shfl_xor(s0, 32); s1 += __shfl_xor(s1, 32);
    s0 *= C; s1 *= C;
    const float mm = fmaxf(s0, s1), e0 = __builtin_amdgcn_exp2f(s0 - mm), e1 = __builtin_amdgcn_exp2f(s1 - mm);
    const float g = 1.f / (1.f + __expf(-bflo(gb[h])));
    const float w0 = g * e0 / (e0 + e1), w1 = g * e1 / (e0 + e1), wl = (1.f - g) / l;
#pragma unroll
    for (int db = 0; db < 6; ++db)
#pragma unroll
        for (int gq = 0; gq < 4; ++gq) {
            const int d = db * 32 + gq * 8 + hi * 4;
            u32x2 a = *(const u32x2*)(v0 + d), b = *(const u32x2*)(v1 + d);
            float r0 = o[db][4*gq+0] * wl + w0 * bflo(a[0]) + w1 * bflo(b[0]);
            float r1 = o[db][4*gq+1] * wl + w0 * bfhi(a[0]) + w1 * bfhi(b[0]);
            float r2 = o[db][4*gq+2] * wl + w0 * bflo(a[1]) + w1 * bflo(b[1]);
            float r3 = o[db][4*gq+3] * wl + w0 * bfhi(a[1]) + w1 * bfhi(b[1]);
            u32x2 ov = {cvtpk(r0, r1), cvtpk(r2, r3)};
            *(u32x2*)(out + hb + (size_t)row * D + d) = ov;
        }
}

extern "C" void kernel_launch(void* const* d_in, const int* in_sizes, int n_in, void* d_out, int out_size,
                              void* d_ws, size_t ws_size, hipStream_t stream) {
    attn_base<<<512, 256, 0, stream>>>((const uint16_t*)d_in[0], (const uint16_t*)d_in[1], (const uint16_t*)d_in[2],
        (const uint16_t*)d_in[3], (const uint16_t*)d_in[4], (const int*)d_in[5], (const uint16_t*)d_in[6], (uint16_t*)d_out);
}
```

```cpp
#include <hip/hip_runtime.h>
#include <hip/hip_bf16.h>
#include <stdint.h>

typedef short bf16x8 __attribute__((ext_vector_type(8)));
typedef short s16x4 __attribute__((ext_vector_type(4)));
typedef float f32x16 __attribute__((ext_vector_type(16)));
typedef unsigned u32x4 __attribute__((ext_vector_type(4)));
typedef unsigned u32x2 __attribute__((ext_vector_type(2)));
constexpr int H = 32, S = 2048, D = 192, M = 2048;
constexpr int KP = 400;

__device__ __forceinline__ unsigned cvtpk(float lo, float hi) {
    unsigned r; asm("v_cvt_pk_bf16_f32 %0, %1, %2" : "=v"(r) : "v"(lo), "v"(hi)); return r;
}
__device__ __forceinline__ float bflo(unsigned u) { return __uint_as_float(u << 16); }
__device__ __forceinline__ float bfhi(unsigned u) { return __uint_as_float(u & 0xffff0000u); }
typedef __attribute__((address_space(3))) const char* lds_cptr;
typedef short v4i16_t __attribute__((ext_vector_type(4)));
__device__ __forceinline__ s16x4 vtr(lds_cptr p) {
    return __builtin_bit_cast(s16x4, __builtin_amdgcn_ds_read_tr16_b64_v4i16((__attribute__((address_space(3))) v4i16_t*)p));
}

__global__ __launch_bounds__(256, 2) void attn_base(const uint16_t* __restrict__ q, const uint16_t* __restrict__ k,
        const uint16_t* __restrict__ v, const uint16_t* __restrict__ mk, const uint16_t* __restrict__ mv,
        const int* __restrict__ idx, const uint16_t* __restrict__ gb, uint16_t* __restrict__ out) {
    __shared__ __attribute__((aligned(16))) char lds[2 * 64 * KP];
    const int tid = threadIdx.x, lane = tid & 63, w = tid >> 6, r32 = lane & 31, hi = lane >> 5;
    const int bid = blockIdx.x;
    const int h = (bid & 7) + 8 * ((bid >> 3) & 3);
    const int qb = 15 - (bid >> 5);
    const int row = qb * 128 + w * 32 + r32;
    const size_t hb = (size_t)h * S * D;
    bf16x8 qf[12];
#pragma unroll
    for (int i = 0; i < 12; ++i) qf[i] = *(const bf16x8*)(q + hb + (size_t)row * D + i * 16 + hi * 8);
    f32x16 o[6];
#pragma unroll
    for (int i = 0; i < 6; ++i) o[i] = f32x16{};
    float m = -1e30f, l = 0.f;
    const float C = 0.07216878364870322f * 1.4426950408889634f;
    const int nt = qb * 2 + 2;
    char* Kl = lds; char* Vl = lds + 64 * KP;
    for (int t = 0; t < nt; ++t) {
        __syncthreads();
#pragma unroll
        for (int i = 0; i < 6; ++i) {
            const int c = tid + 256 * i, r = c / 24, ch = c % 24;
            *(u32x4*)(Kl + r * KP + ch * 16) = *(const u32x4*)(k + hb + (size_t)(t * 64 + r) * D + ch * 8);
            *(u32x4*)(Vl + r * KP + ch * 16) = *(const u32x4*)(v + hb + (size_t)(t * 64 + r) * D + ch * 8);
        }
        __syncthreads();
#pragma unroll
        for (int kb = 0; kb < 2; ++kb) {
            const int key0 = t * 64 + kb * 32;
            if (key0 > qb * 128 + w * 32 + 31) continue;
            f32x16 s = f32x16{};
#pragma unroll
            for (int i = 0; i < 12; ++i) {
                bf16x8 kf = *(const bf16x8*)(Kl + (kb * 32 + r32) * KP + i * 32 + hi * 16);
                s = __builtin_amdgcn_mfma_f32_32x32x16_bf16(kf, qf[i], s, 0, 0, 0);
            }
            float mx = -__builtin_inff();
#pragma unroll
            for (int r = 0; r < 16; ++r) {
                const int key = key0 + (r & 3) + 8 * (r >> 2) + 4 * hi;
                s[r] = key <= row ? s[r] * C : -__builtin_inff();
                mx = fmaxf(mx, s[r]);
            }
            mx = fmaxf(mx, __shfl_xor(mx, 32));
            const float mn = fmaxf(m, mx), al = __builtin_amdgcn_exp2f(m - mn);
            m = mn; float ps = 0.f;
#pragma unroll
            for (int r = 0; r < 16; ++r) { s[r] = __builtin_amdgcn_exp2f(s[r] - mn); ps += s[r]; }
            l = l * al + ps;
#pragma unroll
            for (int i = 0; i < 6; ++i) o[i] *= al;
#pragma unroll
            for (int ks = 0; ks < 2; ++ks) {
                u32x4 pw = {cvtpk(s[8*ks], s[8*ks+1]), cvtpk(s[8*ks+2], s[8*ks+3]), cvtpk(s[8*ks+4], s[8*ks+5]), cvtpk(s[8*ks+6], s[8*ks+7])};
                bf16x8 pf = __builtin_bit_cast(bf16x8, pw);
                const int kr = kb * 32 + ks * 16 + 4 * hi + ((lane & 15) >> 2);
#pragma unroll
                for (int db = 0; db < 6; ++db) {
                    lds_cptr p = (lds_cptr)Vl + kr * KP + db * 64 + ((lane >> 4) & 1) * 32 + (lane & 3) * 8;
                    s16x4 a = vtr(p), b = vtr(p + 8 * KP);
                    bf16x8 vf = {a[0], a[1], a[2], a[3], b[0], b[1], b[2], b[3]};
                    o[db] = __builtin_amdgcn_mfma_f32_32x32x16_bf16(vf, pf, o[db], 0, 0, 0);
                }
            }
        }
    }
    l += __shfl_xor(l, 32);
    const int i0 = idx[((size_t)h * S + row) * 2], i1 = idx[((size_t)h * S + row) * 2 + 1];
    const uint16_t* k0 = mk + (size_t)h * M * D + (size_t)i0 * D; const uint16_t* k1 = mk + (size_t)h * M * D + (size_t)i1 * D;
    const uint16_t* v0 = mv + (size_t)h * M * D + (size_t)i0 * D; const uint16_t* v1 = mv + (size_t)h * M * D + (size_t)i1 * D;
    float s0 = 0.f, s1 = 0.f;
#pragma unroll
    for (int i = 0; i < 12; ++i) {
        u32x4 a = *(const u32x4*)(k0 + i * 16 + hi * 8), b = *(const u32x4*)(k1 + i * 16 + hi * 8), qq = __builtin_bit_cast(u32x4, qf[i]);
#pragma unroll
        for (int e = 0; e < 4; ++e) {
            s0 += bflo(qq[e]) * bflo(a[e]) + bfhi(qq[e]) * bfhi(a[e]);
            s1 += bflo(qq[e]) * bflo(b[e]) + bfhi(qq[e]) * bfhi(b[e]);
        }
    }
    s0 += __shfl_xor(s0, 32); s1 += __shfl_xor(s1, 32);
    s0 *= C; s1 *= C;
    const float mm = fmaxf(s0, s1), e0 = __builtin_amdgcn_exp2f(s0 - mm), e1 = __builtin_amdgcn_exp2f(s1 - mm);
    const float g = 1.f / (1.f + __expf(-bflo(gb[h])));
    const float w0 = g * e0 / (e0 + e1), w1 = g * e1 / (e0 + e1), wl = (1.f - g) / l;
#pragma unroll
    for (int db = 0; db < 6; ++db)
#pragma unroll
        for (int gq = 0; gq < 4; ++gq) {
            const int d = db * 32 + gq * 8 + hi * 4;
            u32x2 a = *(const u32x2*)(v0 + d), b = *(const u32x2*)(v1 + d);
            float r0 = o[db][4*gq+0] * wl + w0 * bflo(a[0]) + w1 * bflo(b[0]);
            float r1 = o[db][4*gq+1] * wl + w0 * bfhi(a[0]) + w1 * bfhi(b[0]);
            float r2 = o[db][4*gq+2] * wl + w0 * bflo(a[1]) + w1 * bflo(b[1]);
            float r3 = o[db][4*gq+3] * wl + w0 * bfhi(a[1]) + w1 * bfhi(b[1]);
            u32x2 ov = {cvtpk(r0, r1), cvtpk(r2, r3)};
            *(u32x2*)(out + hb + (size_t)row * D + d) = ov;
        }
}


__device__ __forceinline__ unsigned long long rt() { unsigned long long t; asm volatile("s_memrealtime %0\n\ts_waitcnt lgkmcnt(0)" : "=s"(t) :: "memory"); return t; }
__device__ __forceinline__ unsigned long long ct() { unsigned long long t; asm volatile("s_memtime %0\n\ts_waitcnt lgkmcnt(0)" : "=s"(t) :: "memory"); return t; }
typedef float f32x4 __attribute__((ext_vector_type(4)));
__global__ void spin_k(const unsigned long long* ws, int n, int mode) {
    unsigned long long t0 = rt();
    unsigned long long lo = ~0ull, hi = 0, cyc = 0, rts = 0;
    for (int i = 0; i < n; ++i) { lo = min(lo, ws[4*i]); hi = max(hi, ws[4*i+1]); cyc += ws[4*i+2]; rts += ws[4*i+1] - ws[4*i]; }
    unsigned long long d = mode == 0 ? (hi - lo) * 20 : (cyc * 1000 / rts);
    if (d > 200000) d = 200000;
    while (rt() - t0 < d) __builtin_amdgcn_s_sleep(1);
}

__global__ __launch_bounds__(256,1) void diag_k(const uint16_t* q, const uint16_t* k, const uint16_t* v, const uint16_t* mk, const uint16_t* mv, unsigned long long* ws) {
    const bf16x8* p = (const bf16x8*)q + (blockIdx.x*256+threadIdx.x)*6;
    bf16x8 x0=p[0],x1=p[1],x2=p[2],x3=p[3],y0=p[4],y1=p[5];
    f32x16 a0={},a1={},a2={},a3={};
    asm volatile("" : "+v"(x0),"+v"(x1),"+v"(x2),"+v"(x3),"+v"(y0),"+v"(y1));
    __syncthreads();
    unsigned long long t0 = rt(), c0 = ct();
    for (int i = 0; i < 195; ++i) { a0=__builtin_amdgcn_mfma_f32_32x32x16_bf16(x0,y0,a0,0,0,0);a1=__builtin_amdgcn_mfma_f32_32x32x16_bf16(x1,y1,a1,0,0,0);a2=__builtin_amdgcn_mfma_f32_32x32x16_bf16(x2,y0,a2,0,0,0);a3=__builtin_amdgcn_mfma_f32_32x32x16_bf16(x3,y1,a3,0,0,0); a0=__builtin_amdgcn_mfma_f32_32x32x16_bf16(x0,y0,a0,0,0,0);a1=__builtin_amdgcn_mfma_f32_32x32x16_bf16(x1,y1,a1,0,0,0);a2=__builtin_amdgcn_mfma_f32_32x32x16_bf16(x2,y0,a2,0,0,0);a3=__builtin_amdgcn_mfma_f32_32x32x16_bf16(x3,y1,a3,0,0,0); }
    float s = a0[0]+a1[1]+a2[2]+a3[3];
    asm volatile("" :: "v"(s));
    unsigned long long t1 = rt(), c1 = ct();
    if (threadIdx.x == 0) { ws[4*blockIdx.x] = t0; ws[4*blockIdx.x+1] = t1; ws[4*blockIdx.x+2] = c1-c0; }
}
extern "C" void kernel_launch(void* const* d_in, const int* in_sizes, int n_in, void* d_out, int out_size,
                              void* d_ws, size_t ws_size, hipStream_t stream) {
    diag_k<<<256,256,0,stream>>>((const uint16_t*)d_in[0], (const uint16_t*)d_in[1], (const uint16_t*)d_in[2], (const uint16_t*)d_in[3], (const uint16_t*)d_in[4], (unsigned long long*)d_ws);
    spin_k<<<1,64,0,stream>>>((const unsigned long long*)d_ws, 256, 1);
    attn_base<<<512, 256, 0, stream>>>((const uint16_t*)d_in[0], (const uint16_t*)d_in[1], (const uint16_t*)d_in[2],
        (const uint16_t*)d_in[3], (const uint16_t*)d_in[4], (const int*)d_in[5], (const uint16_t*)d_in[6], (uint16_t*)d_out);
}
```

```cpp
#include <hip/hip_runtime.h>

__global__ void __launch_bounds__(256, 1) attn_mem_kernel(const void* q, const void* k, const void* v, const void* mk,
                                                          const void* mv, const void* idx, const void* gb, void* out, void* ws);

extern "C" void kernel_launch(void* const* d_in, const int* in_sizes, int n_in, void* d_out, int out_size,
                              void* d_ws, size_t ws_size, hipStream_t stream) {
    attn_mem_kernel<<<256, 256, 0, stream>>>(d_in[0], d_in[1], d_in[2], d_in[3], d_in[4], d_in[5], d_in[6], d_out, d_ws);
}

#pragma clang diagnostic ignored "-Winline-asm"
#pragma clang diagnostic ignored "-Woverlength-strings"
#define C0m "v_and_b32 v0, 63, v0\n"
#define E1 "s_waitcnt lgkmcnt(0)\n"
#define E8X "s_mov_b32 s92, 737280\n"
#define A0N "s_add_u32 m0, s89, 126976\n"
#define C0s "v_bfe_u32 v27, s22, 0, 5\n"
#define C0t "v_lshl_add_u32 v27, v27, 2, v1\n"
#define C4F "buffer_load_dwordx4 v223, s[24:27], s92 offen offset:0 nt lds\n"
#define j2 "s_addk_i32 m0, 288\n"
#define D2G "s_mov_b32 s55, 0x20000\n"
#define C4G "buffer_load_dwordx4 v223, s[24:27], s92 offen offset:768 nt lds\n"
#define B78 "buffer_load_dwordx4 v223, s[24:27], s92 offen offset:1536 nt lds\n"
#define D4J "buffer_load_dwordx4 v223, s[24:27], s92 offen offset:2304 nt lds\n"
#define D4K "s_addk_i32 m0, 15904\n"
#define C4H "buffer_load_dwordx4 v223, s[24:27], s92 offen offset:128 nt lds\n"
#define C06 "v_and_b32 v15, 3, v0\n"
#define C07 "v_cmp_eq_u32 vcc, 0, v15\n"
#define C08 "v_mov_b32 v19, 0x3f803f80\n"
#define C09 "s_mov_b32 s97, 0x3dd53b95\n"
#define p8 "v_cndmask_b32 v20, 0, v19, vcc\n"
#define B79 "buffer_load_dwordx4 v223, s[24:27], s92 offen offset:896 nt lds\n"
#define C1D "v_and_b32 v24, 2, v0\n"
#define C1E "v_cmp_eq_u32 vcc, 2, v15\n"
#define C1H "v_cmp_ne_u32_e64 s[4:5], 0, v24\n"
#define C1J "v_cndmask_b32_e64 v227, v25, v26, s[4:5]\n"
#define C4J "buffer_load_dwordx4 v223, s[24:27], s92 offen offset:1664 nt lds\n"
#define C1K "v_and_b32 v9, 15, v0\n"
#define C1L "v_cmp_eq_u32 vcc, 3, v15\n"
#define C1P "v_bfe_u32 v24, v0, 2, 1\n"
#define C1Q "v_and_b32 v25, 1, v0\n"
#define C1R "v_lshl_add_u32 v17, v1, 4, v9\n"
#define C1S "v_lshlrev_b32 v18, 2, v10\n"
#define A5I "buffer_load_dwordx4 v223, s[24:27], s92 offen offset:2432 nt lds\n"
#define C1T "v_bfe_u32 v14, v0, 3, 2\n"
#define C1U "v_lshl_add_u32 v24, v24, 1, v25\n"
#define C1V "v_sub_u32 v17, v17, v18\n"
#define C1W "v_accvgpr_write_b32 a253, v20\n"
#define C1X "v_accvgpr_write_b32 a254, v20\n"
#define C1Y "v_accvgpr_write_b32 a255, v20\n"
#define C1Z "v_mul_u32_u24 v14, 1056, v14\n"
#define C1b "v_bfe_u32 v12, v0, 2, 1\n"
#define C1c "v_mul_u32_u24 v16, 144, v9\n"
#define C1d "v_mad_u32_u24 v224, v8, s0, v228\n"
#define C1e "buffer_load_dword v226, v27, s[52:55], 0 offen\n"
#define C1f "s_add_u32 m0, s96, 0\n"
#define C1g "v_lshl_add_u32 v8, v1, 5, v2\n"
#define C1h "v_lshl_add_u32 v14, v12, 7, v14\n"
#define C1i "v_lshl_add_u32 v16, v10, 3, v16\n"
#define C1j "v_lshlrev_b32 v8, 4, v8\n"
#define C1k "v_lshl_add_u32 v14, v15, 3, v14\n"
#define C1l "v_add_u32 v220, v13, v16\n"
#define C1m "v_mul_u32_u24 v16, 144, v2\n"
#define C1o "s_mov_b32 s38, 0xC0000\n"
#define C1p "s_mov_b32 s39, 0x20000\n"
#define A5J "buffer_load_dword v232, s[48:51], s92 offen lds\n"
#define C1q "s_addk_i32 m0, 256\n"
#define C1s "s_mov_b32 s47, 0x20000\n"
#define C1u "v_add_u32 v217, 63616, v14\n"
#define C1w "v_cmp_le_i32_e64 s[56:57], 0, v17\n"
#define C1x "v_cmp_le_i32_e64 s[58:59], 1, v17\n"
#define C11 "v_cmp_le_i32_e64 s[74:75], 33, v17\n"
#define C12 "v_cmp_le_i32_e64 s[76:77], 34, v17\n"
#define C14 "v_cmp_le_i32_e64 s[84:85], 50, v17\n"
#define D23 "s_add_u32 m0, s88, 256\n"
#define C16 "v_cmp_le_i32_e64 s[86:87], 51, v17\n"
#define C17 "v_mov_b32 v209, 0xff800000\n"
#define C19 "v_mov_b32 v198, 0\n"
#define B7f "s_mov_b32 s93, 0x4e800000\n"
#define K0 "buffer_load_dwordx4 v222, s[28:31], s90 offen offset:0 lds\n"
#define T0 "s_addk_i32 m0, 4096\n"
#define A0 "s_nop 0\n"
#define J0 "buffer_load_dwordx4 v222, s[28:31], s90 offen offset:128 lds\n"
#define N0 "buffer_load_dwordx4 v222, s[28:31], s90 offen offset:256 lds\n"
#define P3 "s_waitcnt vmcnt(11)\n"
#define B7g "ds_read_b128 v[96:99], v218 offset:33792\n"
#define B7h "s_add_u32 m0, s89, 109824\n"
#define B7i "ds_read_b128 v[100:103], v218 offset:33856\n"
#define B7j "ds_read_b128 v[104:107], v218 offset:34304\n"
#define D61 "buffer_load_dwordx4 v223, s[24:27], s92 offen offset:256 nt lds\n"
#define B7k "ds_read_b128 v[108:111], v218 offset:34368\n"
#define D62 "buffer_load_dwordx4 v223, s[24:27], s92 offen offset:1024 nt lds\n"
#define c0 "s_waitcnt lgkmcnt(3)\n"
#define F3c "v_lshlrev_b32 v128, 16, v96\n"
#define G3F "v_and_b32 v129, 0xffff0000, v96\n"
#define G3G "v_lshlrev_b32 v130, 16, v97\n"
#define G3H "v_and_b32 v131, 0xffff0000, v97\n"
#define G3I "v_lshlrev_b32 v132, 16, v98\n"
#define G3J "v_and_b32 v133, 0xffff0000, v98\n"
#define G3K "v_lshlrev_b32 v134, 16, v99\n"
#define E2N "v_and_b32 v135, 0xffff0000, v99\n"
#define D63 "buffer_load_dwordx4 v223, s[24:27], s92 offen offset:1792 nt lds\n"
#define H03 "v_pk_mul_f32 v[134:135], v[134:135], v[230:231]\n"
#define B1w "v_cvt_pk_bf16_f32 v128, v128, v129\n"
#define B5g "v_cvt_pk_bf16_f32 v130, v130, v131\n"
#define B5f "v_cvt_pk_bf16_f32 v132, v132, v133\n"
#define A1N "v_cvt_pk_bf16_f32 v134, v134, v135\n"
#define C2E "v_accvgpr_write_b32 a96, v128\n"
#define C2F "v_accvgpr_write_b32 a97, v130\n"
#define A5K "buffer_load_dwordx4 v223, s[24:27], s92 offen offset:2560 nt lds\n"
#define G3O "v_lshlrev_b32 v134, 16, v103\n"
#define G3P "v_and_b32 v135, 0xffff0000, v103\n"
#define C2I "ds_read_b128 v[96:99], v218 offset:50688\n"
#define Q0 "s_addk_i32 s90, 12288\n"
#define F6C "s_add_u32 m0, s88, 12672\n"
#define C0 "s_addk_i32 m0, 4352\n"
#define E1V "v_lshlrev_b32 v136, 16, v104\n"
#define E1W "v_and_b32 v137, 0xffff0000, v104\n"
#define G3Q "v_lshlrev_b32 v138, 16, v105\n"
#define G3R "v_and_b32 v139, 0xffff0000, v105\n"
#define F33 "v_lshlrev_b32 v140, 16, v106\n"
#define F34 "v_and_b32 v141, 0xffff0000, v106\n"
#define C2L "v_lshlrev_b32 v142, 16, v107\n"
#define C2M "v_and_b32 v143, 0xffff0000, v107\n"
#define u0 "s_addk_i32 m0, 3968\n"
#define B3h "v_cvt_pk_bf16_f32 v136, v136, v137\n"
#define B3g "v_cvt_pk_bf16_f32 v138, v138, v139\n"
#define B9o "v_cvt_pk_bf16_f32 v140, v140, v141\n"
#define B9n "v_cvt_pk_bf16_f32 v142, v142, v143\n"
#define C2P "ds_read_b128 v[100:103], v218 offset:50752\n"
#define C2Q "v_accvgpr_write_b32 a120, v136\n"
#define C2R "v_accvgpr_write_b32 a121, v138\n"
#define C2S "v_accvgpr_write_b32 a122, v140\n"
#define C2T "v_accvgpr_write_b32 a123, v142\n"
#define D6y "v_lshlrev_b32 v136, 16, v108\n"
#define E11 "v_and_b32 v137, 0xffff0000, v108\n"
#define E10 "v_lshlrev_b32 v138, 16, v109\n"
#define D4I "v_and_b32 v139, 0xffff0000, v109\n"
#define E2R "v_lshlrev_b32 v140, 16, v110\n"
#define E2S "v_and_b32 v141, 0xffff0000, v110\n"
#define F3W "v_lshlrev_b32 v142, 16, v111\n"
#define F3X "v_and_b32 v143, 0xffff0000, v111\n"
#define C2V "ds_read_b128 v[104:107], v218 offset:51200\n"
#define T1 "s_waitcnt lgkmcnt(1)\n"
#define C2X "v_accvgpr_write_b32 a108, v128\n"
#define C2Z "v_accvgpr_write_b32 a111, v134\n"
#define C2a "ds_read_b128 v[108:111], v218 offset:51264\n"
#define C2c "v_accvgpr_write_b32 a131, v142\n"
#define A1I "s_waitcnt vmcnt(12)\n"
#define B8J "ds_read_b128 v[232:235], v229 offset:256\n"
#define B8L "ds_read_b128 v[236:239], v229 offset:384\n"
#define w0 "s_waitcnt lgkmcnt(2)\n"
#define C2e "v_accvgpr_write_b32 a134, v140\n"
#define C2f "v_accvgpr_write_b32 a135, v142\n"
#define C2g "v_accvgpr_write_b32 a133, v138\n"
#define C2h "v_accvgpr_write_b32 a132, v136\n"
#define A3D "v_mad_u32_u24 v232, v232, s0, v228\n"
#define A3E "v_mad_u32_u24 v233, v233, s0, v228\n"
#define A4b "v_mad_u32_u24 v234, v234, s0, v228\n"
#define A4c "v_mad_u32_u24 v235, v235, s0, v228\n"
#define A8i "v_mad_u32_u24 v236, v236, s0, v228\n"
#define A8j "v_mad_u32_u24 v237, v237, s0, v228\n"
#define A4f "v_mad_u32_u24 v238, v238, s0, v228\n"
#define A4g "v_mad_u32_u24 v239, v239, s0, v228\n"
#define A0M "s_waitcnt vmcnt(8)\n"
#define x0 "s_barrier\n"
#define A3t "ds_read_b128 a[192:195], v214 offset:0\n"
#define A3u "ds_read_b128 a[196:199], v214 offset:64\n"
#define A3v "ds_read_b128 a[200:203], v214 offset:4224\n"
#define A3w "ds_read_b128 a[208:211], v214 offset:8448\n"
#define A3x "ds_read_b128 a[212:215], v214 offset:8512\n"
#define C2j "ds_read_b128 a[216:219], v214 offset:512\n"
#define A3y "ds_read_b128 a[204:207], v214 offset:4288\n"
#define C2k "ds_read_b128 a[220:223], v214 offset:576\n"
#define C2m "ds_read_b128 a[224:227], v214 offset:4736\n"
#define C2n "s_mov_b32 s91, 294912\n"
#define B0 "s_addk_i32 m0, 4224\n"
#define j0 "buffer_load_dwordx4 v222, s[32:35], s91 offen offset:0 lds\n"
#define A7X "s_waitcnt vmcnt(7)\n"
#define D4A "ds_read_b128 v[96:99], v218 offset:16896\n"
#define D4B "ds_read_b128 v[100:103], v218 offset:16960\n"
#define C2o "s_mov_b32 s95, 307200\n"
#define D4C "ds_read_b128 v[104:107], v218 offset:17408\n"
#define D4D "ds_read_b128 v[108:111], v218 offset:17472\n"
#define r0 "buffer_load_dwordx4 v222, s[32:35], s95 offen offset:0 lds\n"
#define g0 "buffer_load_dwordx4 v222, s[32:35], s91 offen offset:128 lds\n"
#define D7N "v_mfma_f32_16x16x32_bf16 v[160:163], a[192:195], a[96:99], 0\n"
#define a0 "v_mfma_f32_16x16x32_bf16 v[160:163], a[196:199], a[100:103], v[160:163]\n"
#define k0 "buffer_load_dwordx4 v222, s[32:35], s95 offen offset:128 lds\n"
#define U0 "v_mfma_f32_16x16x32_bf16 v[160:163], a[200:203], a[104:107], v[160:163]\n"
#define d0 "v_mfma_f32_16x16x32_bf16 v[160:163], a[204:207], a[108:111], v[160:163]\n"
#define D6w "v_mul_f32 v140, s97, v140\n"
#define D6x "v_mul_f32 v141, s97, v141\n"
#define W0 "v_mfma_f32_16x16x32_bf16 v[160:163], a[208:211], a[112:115], v[160:163]\n"
#define E2P "v_mul_f32 v142, s97, v142\n"
#define E2Q "v_mul_f32 v143, s97, v143\n"
#define C2q "v_accvgpr_write_b32 a116, v128\n"
#define C2r "v_accvgpr_write_b32 a117, v130\n"
#define C2s "v_accvgpr_write_b32 a118, v132\n"
#define C2t "v_accvgpr_write_b32 a119, v134\n"
#define c3 "v_mfma_f32_16x16x32_bf16 v[160:163], a[212:215], a[116:119], v[160:163]\n"
#define C2u "v_mfma_f32_16x16x32_bf16 v[164:167], a[192:195], a[120:123], 0\n"
#define b0 "v_mfma_f32_16x16x32_bf16 v[164:167], a[196:199], a[124:127], v[164:167]\n"
#define B5h "v_max3_f32 v230, v160, v161, v162\n"
#define C2w "v_accvgpr_write_b32 a138, v140\n"
#define q0 "v_mfma_f32_16x16x32_bf16 v[164:167], a[200:203], a[128:131], v[164:167]\n"
#define B5i "v_max_f32 v230, v230, v163\n"
#define C2x "v_accvgpr_write_b32 a139, v142\n"
#define J3 "v_mov_b32 v231, v230\n"
#define D7P "v_mul_f32 v138, s97, v138\n"
#define D7O "v_mul_f32 v139, s97, v139\n"
#define E1P "v_mul_f32 v136, s97, v136\n"
#define E1Q "v_mul_f32 v137, s97, v137\n"
#define m0 "v_mfma_f32_16x16x32_bf16 v[164:167], a[204:207], a[132:135], v[164:167]\n"
#define K6 "v_permlane32_swap_b32 v230, v231\n"
#define h3 "v_max_f32 v230, v230, v231\n"
#define C2z "v_accvgpr_write_b32 a136, v136\n"
#define C20 "v_accvgpr_write_b32 a137, v138\n"
#define Y0 "v_mfma_f32_16x16x32_bf16 v[164:167], a[208:211], a[136:139], v[164:167]\n"
#define A0O "v_permlane16_swap_b32 v230, v231\n"
#define i0 "v_mfma_f32_16x16x32_bf16 v[164:167], a[212:215], a[140:143], v[164:167]\n"
#define C23 "v_sub_f32 v176, 0, v230\n"
#define C24 "v_sub_f32 v177, 0, v230\n"
#define C25 "v_sub_f32 v178, 0, v230\n"
#define C26 "v_sub_f32 v179, 0, v230\n"
#define C27 "v_add_f32 v162, v162, v178\n"
#define C28 "v_add_f32 v163, v163, v179\n"
#define B1x "v_mfma_f32_16x16x32_bf16 v[168:171], a[216:219], a[96:99], v[176:179]\n"
#define B5l "v_max3_f32 v230, v164, v165, v166\n"
#define B5m "v_add_f32 v160, v160, v176\n"
#define B1y "v_mfma_f32_16x16x32_bf16 v[168:171], a[220:223], a[100:103], v[168:171]\n"
#define B5n "v_max_f32 v230, v230, v167\n"
#define C29 "v_add_f32 v161, v161, v177\n"
#define B1z "v_mfma_f32_16x16x32_bf16 v[168:171], a[224:227], a[104:107], v[168:171]\n"
#define B10 "v_mfma_f32_16x16x32_bf16 v[168:171], a[228:231], a[108:111], v[168:171]\n"
#define C3A "v_lshlrev_b32 v210, 16, v210\n"
#define B11 "v_mfma_f32_16x16x32_bf16 v[168:171], a[232:235], a[112:115], v[168:171]\n"
#define C3B "v_mul_f32 v210, 0xbfb8aa3b, v210\n"
#define B12 "v_mfma_f32_16x16x32_bf16 v[168:171], a[236:239], a[116:119], v[168:171]\n"
#define F1 "s_nop 1\n"
#define C3C "v_sub_f32 v181, 0, v230\n"
#define C3D "v_sub_f32 v182, 0, v230\n"
#define C3E "v_sub_f32 v180, 0, v230\n"
#define C3F "v_sub_f32 v183, 0, v230\n"
#define o0 "buffer_load_dwordx4 v222, s[32:35], s91 offen offset:256 lds\n"
#define B13 "v_mfma_f32_16x16x32_bf16 v[172:175], a[236:239], a[140:143], v[180:183]\n"
#define C3G "v_add_f32 v165, v165, v181\n"
#define C3H "v_add_f32 v166, v166, v182\n"
#define B14 "v_mfma_f32_16x16x32_bf16 v[172:175], a[232:235], a[136:139], v[172:175]\n"
#define E9h "ds_read_b128 a[212:215], v214 offset:12736\n"
#define E9i "ds_read_b128 a[208:211], v214 offset:12672\n"
#define B5o "ds_read_b128 a[204:207], v214 offset:16960\n"
#define B15 "v_mfma_f32_16x16x32_bf16 v[172:175], a[228:231], a[132:135], v[172:175]\n"
#define B5p "ds_read_b128 a[200:203], v214 offset:16896\n"
#define C3J "v_add_f32 v167, v167, v183\n"
#define B16 "v_mfma_f32_16x16x32_bf16 v[172:175], a[224:227], a[128:131], v[172:175]\n"
#define B5q "ds_read_b128 a[196:199], v214 offset:21184\n"
#define B5r "v_add_f32 v164, v164, v180\n"
#define B17 "v_mfma_f32_16x16x32_bf16 v[172:175], a[220:223], a[124:127], v[172:175]\n"
#define B5s "ds_read_b128 a[192:195], v214 offset:21120\n"
#define B18 "v_mfma_f32_16x16x32_bf16 v[172:175], a[216:219], a[120:123], v[172:175]\n"
#define l0 "buffer_load_dwordx4 v222, s[32:35], s95 offen offset:256 lds\n"
#define f0 "v_exp_f32 v162, v162\n"
#define X0 "v_exp_f32 v163, v163\n"
#define p0 "v_exp_f32 v160, v160\n"
#define h0 "v_exp_f32 v161, v161\n"
#define o4 "v_cvt_pk_bf16_f32 v96, v160, v161\n"
#define p4 "v_cvt_pk_bf16_f32 v97, v162, v163\n"
#define E8Y "s_cbranch_scc1 Lb0_0\n"
#define t0 "v_mfma_f32_16x16x32_bf16 v[160:163], a[212:215], a[116:119], v[176:179]\n"
#define C3K "ds_read_b128 a[216:219], v214 offset:21632\n"
#define C3L "ds_read_b128 a[220:223], v214 offset:21696\n"
#define C3M "ds_read_b128 a[224:227], v214 offset:17408\n"
#define C3N "ds_read_b128 a[228:231], v214 offset:17472\n"
#define I0 "v_exp_f32 v165, v165\n"
#define M0 "s_waitcnt lgkmcnt(4)\n"
#define F0 "v_exp_f32 v166, v166\n"
#define s0 "v_mfma_f32_16x16x32_bf16 v[160:163], a[192:195], a[96:99], v[160:163]\n"
#define G0 "v_exp_f32 v167, v167\n"
#define P0 "v_exp_f32 v164, v164\n"
#define s3 "v_cvt_pk_bf16_f32 v104, v164, v165\n"
#define n3 "v_cvt_pk_bf16_f32 v105, v166, v167\n"
#define v0 "v_mfma_f32_16x16x32_bf16 v[164:167], a[192:195], a[120:123], v[180:183]\n"
#define C3O "ds_read_b128 a[232:235], v214 offset:13184\n"
#define C3P "ds_read_b128 a[236:239], v214 offset:13248\n"
#define C1 "s_addk_i32 s91, 24576\n"
#define B1 "v_exp_f32 v168, v168\n"
#define Z0 "v_exp_f32 v169, v169\n"
#define O0 "v_exp_f32 v170, v170\n"
#define E0 "v_exp_f32 v171, v171\n"
#define M4 "v_cvt_pk_bf16_f32 v98, v168, v169\n"
#define H4 "v_cvt_pk_bf16_f32 v99, v170, v171\n"
#define y0 "s_addk_i32 s95, 24576\n"
#define H0 "v_exp_f32 v172, v172\n"
#define S0 "v_exp_f32 v173, v173\n"
#define D7Q "s_waitcnt vmcnt(10) lgkmcnt(0)\n"
#define V0 "v_exp_f32 v174, v174\n"
#define e0 "v_exp_f32 v175, v175\n"
#define A0n "ds_read_b128 a[212:215], v214 offset:25408\n"
#define a4 "v_cvt_pk_bf16_f32 v106, v172, v173\n"
#define b4 "v_cvt_pk_bf16_f32 v107, v174, v175\n"
#define V1 "v_mfma_f32_16x16x32_bf16 v[192:195], a[240:243], v[96:99], v[196:199]\n"
#define c2 "v_mfma_f32_16x16x32_bf16 v[192:195], a[244:247], v[104:107], v[192:195]\n"
#define A0o "ds_read_b128 a[208:211], v214 offset:25344\n"
#define A5O "ds_read_b128 a[204:207], v214 offset:29632\n"
#define D2H "ds_read_b128 a[200:203], v214 offset:29568\n"
#define A29 "ds_read_b128 a[196:199], v214 offset:33856\n"
#define T8 "ds_read_b128 a[192:195], v214 offset:33792\n"
#define A3z "ds_read_b64_tr_b16 a[224:225], v216 offset:38048\n"
#define q4 "v_cvt_pk_bf16_f32 v100, v160, v161\n"
#define d5 "v_cvt_pk_bf16_f32 v101, v162, v163\n"
#define R0 "s_waitcnt lgkmcnt(5)\n"
#define A2a "ds_read_b64_tr_b16 a[216:217], v216 offset:38016\n"
#define Z8 "ds_read_b64_tr_b16 a[218:219], v216 offset:38528\n"
#define L0 "s_waitcnt lgkmcnt(6)\n"
#define A9 "ds_read_b64_tr_b16 a[220:221], v216 offset:42240\n"
#define A4W "ds_read_b64_tr_b16 a[222:223], v216 offset:42752\n"
#define j4 "v_cvt_pk_bf16_f32 v102, v168, v169\n"
#define q6 "v_cvt_pk_bf16_f32 v103, v170, v171\n"
#define K4 "v_cvt_pk_bf16_f32 v108, v164, v165\n"
#define Y3 "v_cvt_pk_bf16_f32 v109, v166, v167\n"
#define q8 "ds_read_b64_tr_b16 a[226:227], v216 offset:38560\n"
#define R5 "v_cvt_pk_bf16_f32 v110, v172, v173\n"
#define W5 "v_cvt_pk_bf16_f32 v111, v174, v175\n"
#define C3R "v_exp_f32 v210, v210\n"
#define m9 "ds_read_b64_tr_b16 a[228:229], v216 offset:42272\n"
#define C3S "v_add_f32 v210, 1.0, v210\n"
#define n9 "ds_read_b64_tr_b16 a[230:231], v216 offset:42784\n"
#define C3T "v_rcp_f32 v210, v210\n"
#define m1 "v_mfma_f32_16x16x32_bf16 v[192:195], a[240:243], v[100:103], v[192:195]\n"
#define H7 "ds_read_b64_tr_b16 a[232:233], v216 offset:38080\n"
#define C3U "v_readfirstlane_b32 s94, v210\n"
#define Z2 "v_mfma_f32_16x16x32_bf16 v[192:195], a[244:247], v[108:111], v[192:195]\n"
#define J7 "ds_read_b64_tr_b16 a[234:235], v216 offset:38592\n"
#define a8 "ds_read_b64_tr_b16 a[236:237], v216 offset:42304\n"
#define b8 "ds_read_b64_tr_b16 a[238:239], v216 offset:42816\n"
#define A80 "ds_read_b128 a[192:195], v214 offset:34304\n"
#define j7 "v_max_f32 v208, v192, v193\n"
#define L3 "v_cmp_nge_f32 vcc, s93, v208\n"
#define A7a "ds_read_b128 a[196:199], v214 offset:34368\n"
#define A3A "ds_read_b128 a[200:203], v214 offset:30080\n"
#define B4C "ds_read_b128 a[204:207], v214 offset:30144\n"
#define C3V "ds_read_b128 a[208:211], v214 offset:25856\n"
#define u6 "s_waitcnt lgkmcnt(9)\n"
#define C3W "v_mfma_f32_16x16x32_bf16 v[0:3], a[216:219], v[96:99], 0\n"
#define A7b "ds_read_b128 a[212:215], v214 offset:25920\n"
#define D2 "v_mfma_f32_16x16x32_bf16 v[0:3], a[220:223], v[100:103], v[0:3]\n"
#define C3X "v_mfma_f32_16x16x32_bf16 v[48:51], a[220:223], v[108:111], 0\n"
#define A7r "ds_read_b64_tr_b16 a[220:221], v216 offset:42336\n"
#define A7s "ds_read_b64_tr_b16 a[222:223], v216 offset:42848\n"
#define i2 "v_mfma_f32_16x16x32_bf16 v[48:51], a[216:219], v[104:107], v[48:51]\n"
#define C3Y "v_mfma_f32_16x16x32_bf16 v[4:7], a[224:227], v[96:99], 0\n"
#define A7p "ds_read_b64_tr_b16 a[216:217], v216 offset:38112\n"
#define A7q "ds_read_b64_tr_b16 a[218:219], v216 offset:38624\n"
#define U2 "v_mfma_f32_16x16x32_bf16 v[4:7], a[228:231], v[100:103], v[4:7]\n"
#define C3Z "v_mfma_f32_16x16x32_bf16 v[52:55], a[228:231], v[108:111], 0\n"
#define D4R "ds_read_b64_tr_b16 a[228:229], v216 offset:50688\n"
#define D4S "ds_read_b64_tr_b16 a[230:231], v216 offset:51200\n"
#define j1 "v_mfma_f32_16x16x32_bf16 v[52:55], a[224:227], v[104:107], v[52:55]\n"
#define A1 "s_waitcnt lgkmcnt(7)\n"
#define C3a "v_mfma_f32_16x16x32_bf16 v[8:11], a[232:235], v[96:99], 0\n"
#define E2X "ds_read_b64_tr_b16 a[224:225], v216 offset:46464\n"
#define E2Y "ds_read_b64_tr_b16 a[226:227], v216 offset:46976\n"
#define f2 "v_mfma_f32_16x16x32_bf16 v[8:11], a[236:239], v[100:103], v[8:11]\n"
#define C3b "v_mfma_f32_16x16x32_bf16 v[56:59], a[236:239], v[108:111], 0\n"
#define A7t "ds_read_b64_tr_b16 a[236:237], v216 offset:50720\n"
#define A7u "ds_read_b64_tr_b16 a[238:239], v216 offset:51232\n"
#define P2 "v_mfma_f32_16x16x32_bf16 v[56:59], a[232:235], v[104:107], v[56:59]\n"
#define g5 "s_add_u32 m0, s88, 0\n"
#define G1 "v_mfma_f32_16x16x32_bf16 v[168:171], a[192:195], a[96:99], v[176:179]\n"
#define A5x "ds_read_b64_tr_b16 a[232:233], v216 offset:46496\n"
#define A5y "ds_read_b64_tr_b16 a[234:235], v216 offset:47008\n"
#define K1 "v_mfma_f32_16x16x32_bf16 v[168:171], a[196:199], a[100:103], v[168:171]\n"
#define z0 "v_mfma_f32_16x16x32_bf16 v[168:171], a[200:203], a[104:107], v[168:171]\n"
#define m5 "v_cvt_pk_bf16_f32 v128, v160, v161\n"
#define n5 "v_cvt_pk_bf16_f32 v129, v162, v163\n"
#define I1 "v_mfma_f32_16x16x32_bf16 v[168:171], a[204:207], a[108:111], v[168:171]\n"
#define H1 "v_mfma_f32_16x16x32_bf16 v[168:171], a[208:211], a[112:115], v[168:171]\n"
#define S2 "v_mfma_f32_16x16x32_bf16 v[168:171], a[212:215], a[116:119], v[168:171]\n"
#define A0q "s_waitcnt vmcnt(13)\n"
#define U1 "v_mfma_f32_16x16x32_bf16 v[172:175], a[212:215], a[140:143], v[180:183]\n"
#define u2 "v_mfma_f32_16x16x32_bf16 v[172:175], a[208:211], a[136:139], v[172:175]\n"
#define A3e "ds_read_b128 a[212:215], v214 offset:54976\n"
#define A3f "ds_read_b128 a[208:211], v214 offset:54912\n"
#define R1 "v_mfma_f32_16x16x32_bf16 v[172:175], a[204:207], a[132:135], v[172:175]\n"
#define O1 "v_mfma_f32_16x16x32_bf16 v[172:175], a[200:203], a[128:131], v[172:175]\n"
#define A8H "ds_read_b128 a[204:207], v214 offset:59200\n"
#define A8I "ds_read_b128 a[200:203], v214 offset:59136\n"
#define c1 "v_mfma_f32_16x16x32_bf16 v[172:175], a[196:199], a[124:127], v[172:175]\n"
#define K2 "v_mfma_f32_16x16x32_bf16 v[172:175], a[192:195], a[120:123], v[172:175]\n"
#define A09 "ds_read_b128 a[196:199], v215 offset:64\n"
#define A1A "ds_read_b128 a[192:195], v215 offset:0\n"
#define C3d "v_mfma_f32_16x16x32_bf16 v[12:15], a[216:219], v[96:99], 0\n"
#define v2 "v_mfma_f32_16x16x32_bf16 v[12:15], a[220:223], v[100:103], v[12:15]\n"
#define C3e "v_mfma_f32_16x16x32_bf16 v[60:63], a[220:223], v[108:111], 0\n"
#define a7 "ds_read_b64_tr_b16 a[220:221], v216 offset:50752\n"
#define E4 "v_cvt_pk_bf16_f32 v130, v168, v169\n"
#define i3 "v_mfma_f32_16x16x32_bf16 v[60:63], a[216:219], v[104:107], v[60:63]\n"
#define C3f "v_mfma_f32_16x16x32_bf16 v[16:19], a[224:227], v[96:99], 0\n"
#define e4 "ds_read_b64_tr_b16 a[216:217], v216 offset:46528\n"
#define u3 "v_cvt_pk_bf16_f32 v136, v164, v165\n"
#define S3 "v_mfma_f32_16x16x32_bf16 v[16:19], a[228:231], v[100:103], v[16:19]\n"
#define C3g "v_mfma_f32_16x16x32_bf16 v[64:67], a[228:231], v[108:111], 0\n"
#define f4 "ds_read_b64_tr_b16 a[218:219], v216 offset:47040\n"
#define U5 "v_cvt_pk_bf16_f32 v131, v170, v171\n"
#define U3 "v_mfma_f32_16x16x32_bf16 v[64:67], a[224:227], v[104:107], v[64:67]\n"
#define Q2 "v_mfma_f32_16x16x32_bf16 v[196:199], a[240:243], v[128:131], v[192:195]\n"
#define A1e "ds_read_b64_tr_b16 a[222:223], v216 offset:51264\n"
#define A7v "ds_read_b64_tr_b16 a[224:225], v216 offset:46560\n"
#define C3i "v_mfma_f32_16x16x32_bf16 v[20:23], a[232:235], v[96:99], 0\n"
#define H2 "v_mfma_f32_16x16x32_bf16 v[20:23], a[236:239], v[100:103], v[20:23]\n"
#define p9 "ds_read_b64_tr_b16 a[226:227], v216 offset:47072\n"
#define r3 "v_cvt_pk_bf16_f32 v137, v166, v167\n"
#define C3k "v_mfma_f32_16x16x32_bf16 v[68:71], a[236:239], v[108:111], 0\n"
#define m2 "v_mfma_f32_16x16x32_bf16 v[68:71], a[232:235], v[104:107], v[68:71]\n"
#define A3B "ds_read_b64_tr_b16 a[228:229], v216 offset:50784\n"
#define A3C "ds_read_b64_tr_b16 a[230:231], v216 offset:51296\n"
#define B9 "ds_read_b64_tr_b16 a[232:233], v217 offset:4224\n"
#define C9 "ds_read_b64_tr_b16 a[234:235], v217 offset:4736\n"
#define B5 "v_cvt_pk_bf16_f32 v138, v172, v173\n"
#define U6 "v_cvt_pk_bf16_f32 v139, v174, v175\n"
#define A1f "ds_read_b64_tr_b16 a[236:237], v217 offset:8448\n"
#define A1g "ds_read_b64_tr_b16 a[238:239], v217 offset:8960\n"
#define B4D "ds_read_b128 a[192:195], v215 offset:512\n"
#define B2U "ds_read_b128 a[196:199], v215 offset:576\n"
#define A2R "ds_read_b128 a[200:203], v214 offset:59648\n"
#define B6E "ds_read_b128 a[204:207], v214 offset:59712\n"
#define a3 "v_mfma_f32_16x16x32_bf16 v[196:199], a[244:247], v[136:139], v[196:199]\n"
#define C5E "ds_read_b128 a[208:211], v214 offset:55424\n"
#define A4X "ds_read_b128 a[212:215], v214 offset:55488\n"
#define C3m "v_mfma_f32_16x16x32_bf16 v[24:27], a[216:219], v[96:99], 0\n"
#define s2 "v_mfma_f32_16x16x32_bf16 v[24:27], a[220:223], v[100:103], v[24:27]\n"
#define C3n "v_mfma_f32_16x16x32_bf16 v[72:75], a[220:223], v[108:111], 0\n"
#define A4Z "ds_read_b64_tr_b16 a[220:221], v217 offset:8480\n"
#define A4a "ds_read_b64_tr_b16 a[222:223], v217 offset:8992\n"
#define E2 "v_mfma_f32_16x16x32_bf16 v[72:75], a[216:219], v[104:107], v[72:75]\n"
#define C3o "v_mfma_f32_16x16x32_bf16 v[28:31], a[224:227], v[96:99], 0\n"
#define A54 "ds_read_b64_tr_b16 a[216:217], v217 offset:4256\n"
#define A55 "ds_read_b64_tr_b16 a[218:219], v217 offset:4768\n"
#define w1 "v_mfma_f32_16x16x32_bf16 v[28:31], a[228:231], v[100:103], v[28:31]\n"
#define C3p "v_mfma_f32_16x16x32_bf16 v[76:79], a[228:231], v[108:111], 0\n"
#define B4E "ds_read_b64_tr_b16 a[228:229], v217 offset:8512\n"
#define B4F "ds_read_b64_tr_b16 a[230:231], v217 offset:9024\n"
#define b1 "v_mfma_f32_16x16x32_bf16 v[76:79], a[224:227], v[104:107], v[76:79]\n"
#define C3q "v_mfma_f32_16x16x32_bf16 v[32:35], a[232:235], v[96:99], 0\n"
#define D8G "ds_read_b64_tr_b16 a[224:225], v217 offset:4288\n"
#define D8I "ds_read_b64_tr_b16 a[226:227], v217 offset:4800\n"
#define q2 "v_mfma_f32_16x16x32_bf16 v[32:35], a[236:239], v[100:103], v[32:35]\n"
#define C3r "v_mfma_f32_16x16x32_bf16 v[80:83], a[236:239], v[108:111], 0\n"
#define A58 "ds_read_b64_tr_b16 a[236:237], v217 offset:8544\n"
#define A59 "ds_read_b64_tr_b16 a[238:239], v217 offset:9056\n"
#define W1 "v_mfma_f32_16x16x32_bf16 v[80:83], a[232:235], v[104:107], v[80:83]\n"
#define A56 "ds_read_b64_tr_b16 a[232:233], v217 offset:4320\n"
#define A57 "ds_read_b64_tr_b16 a[234:235], v217 offset:4832\n"
#define k4 "v_cvt_pk_bf16_f32 v132, v160, v161\n"
#define V6 "v_cvt_pk_bf16_f32 v133, v162, v163\n"
#define S4 "v_cvt_pk_bf16_f32 v140, v164, v165\n"
#define B4 "v_cvt_pk_bf16_f32 v141, v166, v167\n"
#define x4 "s_nop 2\n"
#define S7 "s_waitcnt vmcnt(14)\n"
#define B4h "ds_read_b128 a[212:215], v215 offset:12736\n"
#define B4i "ds_read_b128 a[208:211], v215 offset:12672\n"
#define B82 "ds_read_b128 a[204:207], v215 offset:16960\n"
#define B83 "ds_read_b128 a[200:203], v215 offset:16896\n"
#define A6s "ds_read_b128 a[196:199], v215 offset:21184\n"
#define A6t "ds_read_b128 a[192:195], v215 offset:21120\n"
#define C3v "v_mfma_f32_16x16x32_bf16 v[36:39], a[216:219], v[96:99], 0\n"
#define g2 "v_mfma_f32_16x16x32_bf16 v[36:39], a[220:223], v[100:103], v[36:39]\n"
#define C3w "v_mfma_f32_16x16x32_bf16 v[84:87], a[220:223], v[108:111], 0\n"
#define O9 "ds_read_b64_tr_b16 a[220:221], v217 offset:29568\n"
#define O4 "v_cvt_pk_bf16_f32 v134, v168, v169\n"
#define f3 "v_mfma_f32_16x16x32_bf16 v[84:87], a[216:219], v[104:107], v[84:87]\n"
#define C3y "v_mfma_f32_16x16x32_bf16 v[40:43], a[224:227], v[96:99], 0\n"
#define A3Z "ds_read_b64_tr_b16 a[216:217], v217 offset:25344\n"
#define A3a "ds_read_b64_tr_b16 a[218:219], v217 offset:25856\n"
#define m3 "v_mfma_f32_16x16x32_bf16 v[40:43], a[228:231], v[100:103], v[40:43]\n"
#define C3z "v_mfma_f32_16x16x32_bf16 v[88:91], a[228:231], v[108:111], 0\n"
#define P9 "ds_read_b64_tr_b16 a[222:223], v217 offset:30080\n"
#define h4 "v_cvt_pk_bf16_f32 v135, v170, v171\n"
#define g3 "v_mfma_f32_16x16x32_bf16 v[88:91], a[224:227], v[104:107], v[88:91]\n"
#define r2 "v_mfma_f32_16x16x32_bf16 v[196:199], a[240:243], v[132:135], v[196:199]\n"
#define A70 "ds_read_b64_tr_b16 a[224:225], v217 offset:25376\n"
#define A71 "ds_read_b64_tr_b16 a[226:227], v217 offset:25888\n"
#define C30 "v_mfma_f32_16x16x32_bf16 v[44:47], a[232:235], v[96:99], 0\n"
#define F2 "v_mfma_f32_16x16x32_bf16 v[44:47], a[236:239], v[100:103], v[44:47]\n"
#define A2u "ds_read_b64_tr_b16 a[228:229], v217 offset:29600\n"
#define S5 "v_cvt_pk_bf16_f32 v142, v172, v173\n"
#define C31 "v_mfma_f32_16x16x32_bf16 v[92:95], a[236:239], v[108:111], 0\n"
#define C3 "v_mfma_f32_16x16x32_bf16 v[92:95], a[232:235], v[104:107], v[92:95]\n"
#define A2v "ds_read_b64_tr_b16 a[230:231], v217 offset:30112\n"
#define A0s "ds_read_b64_tr_b16 a[232:233], v217 offset:25408\n"
#define A0t "ds_read_b64_tr_b16 a[234:235], v217 offset:25920\n"
#define w4 "v_cvt_pk_bf16_f32 v143, v174, v175\n"
#define i8 "ds_read_b64_tr_b16 a[236:237], v217 offset:29632\n"
#define j8 "ds_read_b64_tr_b16 a[238:239], v217 offset:30144\n"
#define w2 "v_mfma_f32_16x16x32_bf16 v[196:199], a[244:247], v[140:143], v[196:199]\n"
#define A0u "ds_read_b128 a[192:195], v215 offset:21632\n"
#define v6 "ds_read_b128 a[196:199], v215 offset:21696\n"
#define A73 "v_max_f32 v208, v196, v197\n"
#define A0v "ds_read_b128 a[200:203], v215 offset:17408\n"
#define E2J "ds_read_b128 a[204:207], v215 offset:17472\n"
#define F3g "ds_read_b128 a[208:211], v215 offset:13184\n"
#define O2 "v_mfma_f32_16x16x32_bf16 v[0:3], a[216:219], v[128:131], v[0:3]\n"
#define A2E "ds_read_b128 a[212:215], v215 offset:13248\n"
#define D3 "v_mfma_f32_16x16x32_bf16 v[0:3], a[220:223], v[132:135], v[0:3]\n"
#define a2 "v_mfma_f32_16x16x32_bf16 v[48:51], a[220:223], v[140:143], v[48:51]\n"
#define B2t "ds_read_b64_tr_b16 a[220:221], v217 offset:29664\n"
#define B2u "ds_read_b64_tr_b16 a[222:223], v217 offset:30176\n"
#define N2 "v_mfma_f32_16x16x32_bf16 v[48:51], a[216:219], v[136:139], v[48:51]\n"
#define X1 "v_mfma_f32_16x16x32_bf16 v[4:7], a[224:227], v[128:131], v[4:7]\n"
#define B1Y "ds_read_b64_tr_b16 a[216:217], v217 offset:25440\n"
#define B1Z "ds_read_b64_tr_b16 a[218:219], v217 offset:25952\n"
#define I2 "v_mfma_f32_16x16x32_bf16 v[4:7], a[228:231], v[132:135], v[4:7]\n"
#define G2 "v_mfma_f32_16x16x32_bf16 v[52:55], a[228:231], v[140:143], v[52:55]\n"
#define B9y "ds_read_b64_tr_b16 a[228:229], v217 offset:38016\n"
#define B9z "ds_read_b64_tr_b16 a[230:231], v217 offset:38528\n"
#define Y1 "v_mfma_f32_16x16x32_bf16 v[52:55], a[224:227], v[136:139], v[52:55]\n"
#define A2 "v_mfma_f32_16x16x32_bf16 v[8:11], a[232:235], v[128:131], v[8:11]\n"
#define G1i "ds_read_b64_tr_b16 a[224:225], v217 offset:33792\n"
#define G1j "ds_read_b64_tr_b16 a[226:227], v217 offset:34304\n"
#define B3 "v_mfma_f32_16x16x32_bf16 v[8:11], a[236:239], v[132:135], v[8:11]\n"
#define d1 "v_mfma_f32_16x16x32_bf16 v[56:59], a[236:239], v[140:143], v[56:59]\n"
#define B44 "ds_read_b64_tr_b16 a[236:237], v217 offset:38048\n"
#define B45 "ds_read_b64_tr_b16 a[238:239], v217 offset:38560\n"
#define e1 "v_mfma_f32_16x16x32_bf16 v[56:59], a[232:235], v[136:139], v[56:59]\n"
#define g1 "s_waitcnt lgkmcnt(8)\n"
#define B42 "ds_read_b64_tr_b16 a[232:233], v217 offset:33824\n"
#define B43 "ds_read_b64_tr_b16 a[234:235], v217 offset:34336\n"
#define A2g "s_waitcnt vmcnt(16)\n"
#define A6J "ds_read_b128 a[212:215], v215 offset:42304\n"
#define A6K "ds_read_b128 a[208:211], v215 offset:42240\n"
#define B2Y "ds_read_b128 a[204:207], v215 offset:46528\n"
#define B2Z "ds_read_b128 a[200:203], v215 offset:46464\n"
#define J4 "v_mfma_f32_16x16x32_bf16 v[12:15], a[216:219], v[128:131], v[12:15]\n"
#define r9 "ds_read_b128 a[196:199], v215 offset:50752\n"
#define x2 "v_mfma_f32_16x16x32_bf16 v[12:15], a[220:223], v[132:135], v[12:15]\n"
#define y2 "v_mfma_f32_16x16x32_bf16 v[60:63], a[220:223], v[140:143], v[60:63]\n"
#define s9 "ds_read_b128 a[192:195], v215 offset:50688\n"
#define l2 "v_mfma_f32_16x16x32_bf16 v[60:63], a[216:219], v[136:139], v[60:63]\n"
#define d2 "v_mfma_f32_16x16x32_bf16 v[16:19], a[224:227], v[128:131], v[16:19]\n"
#define d8 "ds_read_b64_tr_b16 a[216:217], v217 offset:33856\n"
#define e2 "v_mfma_f32_16x16x32_bf16 v[16:19], a[228:231], v[132:135], v[16:19]\n"
#define G3 "v_mfma_f32_16x16x32_bf16 v[64:67], a[228:231], v[140:143], v[64:67]\n"
#define h5 "ds_read_b64_tr_b16 a[218:219], v217 offset:34368\n"
#define H3 "v_mfma_f32_16x16x32_bf16 v[64:67], a[224:227], v[136:139], v[64:67]\n"
#define w6 "ds_read_b64_tr_b16 a[220:221], v217 offset:38080\n"
#define x6 "ds_read_b64_tr_b16 a[222:223], v217 offset:38592\n"
#define b2 "v_mfma_f32_16x16x32_bf16 v[20:23], a[232:235], v[128:131], v[20:23]\n"
#define n1 "v_mfma_f32_16x16x32_bf16 v[20:23], a[236:239], v[132:135], v[20:23]\n"
#define A3G "ds_read_b64_tr_b16 a[224:225], v217 offset:33888\n"
#define A3H "ds_read_b64_tr_b16 a[226:227], v217 offset:34400\n"
#define o1 "v_mfma_f32_16x16x32_bf16 v[68:71], a[236:239], v[140:143], v[68:71]\n"
#define n2 "v_mfma_f32_16x16x32_bf16 v[68:71], a[232:235], v[136:139], v[68:71]\n"
#define B0l "ds_read_b64_tr_b16 a[228:229], v217 offset:38112\n"
#define B0m "ds_read_b64_tr_b16 a[230:231], v217 offset:38624\n"
#define A2F "ds_read_b64_tr_b16 a[232:233], v217 offset:54912\n"
#define A2H "ds_read_b64_tr_b16 a[234:235], v217 offset:55424\n"
#define A1S "ds_read_b64_tr_b16 a[236:237], v217 offset:59136\n"
#define A1T "ds_read_b64_tr_b16 a[238:239], v217 offset:59648\n"
#define B5A "ds_read_b128 a[192:195], v215 offset:51200\n"
#define A1U "ds_read_b128 a[196:199], v215 offset:51264\n"
#define A4h "ds_read_b128 a[200:203], v215 offset:46976\n"
#define D9G "ds_read_b128 a[204:207], v215 offset:47040\n"
#define D9O "ds_read_b128 a[208:211], v215 offset:42752\n"
#define A4i "ds_read_b128 a[212:215], v215 offset:42816\n"
#define Q3 "v_mfma_f32_16x16x32_bf16 v[24:27], a[216:219], v[128:131], v[24:27]\n"
#define b3 "v_mfma_f32_16x16x32_bf16 v[24:27], a[220:223], v[132:135], v[24:27]\n"
#define B2 "v_mfma_f32_16x16x32_bf16 v[72:75], a[220:223], v[140:143], v[72:75]\n"
#define B48 "ds_read_b64_tr_b16 a[220:221], v217 offset:59168\n"
#define B49 "ds_read_b64_tr_b16 a[222:223], v217 offset:59680\n"
#define C2 "v_mfma_f32_16x16x32_bf16 v[72:75], a[216:219], v[136:139], v[72:75]\n"
#define l1 "v_mfma_f32_16x16x32_bf16 v[28:31], a[224:227], v[128:131], v[28:31]\n"
#define B46 "ds_read_b64_tr_b16 a[216:217], v217 offset:54944\n"
#define B47 "ds_read_b64_tr_b16 a[218:219], v217 offset:55456\n"
#define k1 "v_mfma_f32_16x16x32_bf16 v[28:31], a[228:231], v[132:135], v[28:31]\n"
#define x1 "v_mfma_f32_16x16x32_bf16 v[76:79], a[228:231], v[140:143], v[76:79]\n"
#define D66 "ds_read_b64_tr_b16 a[228:229], v217 offset:59200\n"
#define D67 "ds_read_b64_tr_b16 a[230:231], v217 offset:59712\n"
#define s1 "v_mfma_f32_16x16x32_bf16 v[76:79], a[224:227], v[136:139], v[76:79]\n"
#define M3 "v_mfma_f32_16x16x32_bf16 v[32:35], a[232:235], v[128:131], v[32:35]\n"
#define E9n "ds_read_b64_tr_b16 a[224:225], v217 offset:54976\n"
#define E9o "ds_read_b64_tr_b16 a[226:227], v217 offset:55488\n"
#define o2 "v_mfma_f32_16x16x32_bf16 v[32:35], a[236:239], v[132:135], v[32:35]\n"
#define f1 "v_mfma_f32_16x16x32_bf16 v[80:83], a[236:239], v[140:143], v[80:83]\n"
#define B3J "ds_read_b64_tr_b16 a[236:237], v217 offset:59232\n"
#define B3K "ds_read_b64_tr_b16 a[238:239], v217 offset:59744\n"
#define W2 "v_mfma_f32_16x16x32_bf16 v[80:83], a[232:235], v[136:139], v[80:83]\n"
#define B5C "ds_read_b64_tr_b16 a[232:233], v217 offset:55008\n"
#define B5D "ds_read_b64_tr_b16 a[234:235], v217 offset:55520\n"
#define A8k "ds_read_b128 a[212:215], v214 offset:64\n"
#define A8l "ds_read_b128 a[208:211], v214 offset:0\n"
#define A2k "ds_read_b128 a[196:199], v214 offset:8512\n"
#define A2l "ds_read_b128 a[192:195], v214 offset:8448\n"
#define v3 "v_mfma_f32_16x16x32_bf16 v[36:39], a[216:219], v[128:131], v[36:39]\n"
#define E3 "v_mfma_f32_16x16x32_bf16 v[36:39], a[220:223], v[132:135], v[36:39]\n"
#define Z3 "v_mfma_f32_16x16x32_bf16 v[84:87], a[220:223], v[140:143], v[84:87]\n"
#define u9 "ds_read_b64_tr_b16 a[220:221], v216 offset:16896\n"
#define W3 "v_mfma_f32_16x16x32_bf16 v[84:87], a[216:219], v[136:139], v[84:87]\n"
#define d3 "v_mfma_f32_16x16x32_bf16 v[40:43], a[224:227], v[128:131], v[40:43]\n"
#define A0c "ds_read_b64_tr_b16 a[216:217], v216 offset:12672\n"
#define A0d "ds_read_b64_tr_b16 a[218:219], v216 offset:13184\n"
#define T4 "v_mfma_f32_16x16x32_bf16 v[40:43], a[228:231], v[132:135], v[40:43]\n"
#define F4 "v_mfma_f32_16x16x32_bf16 v[88:91], a[228:231], v[140:143], v[88:91]\n"
#define A5W "ds_read_b64_tr_b16 a[222:223], v216 offset:17408\n"
#define t3 "v_mfma_f32_16x16x32_bf16 v[88:91], a[224:227], v[136:139], v[88:91]\n"
#define B69 "ds_read_b64_tr_b16 a[224:225], v216 offset:12704\n"
#define B6J "ds_read_b64_tr_b16 a[226:227], v216 offset:13216\n"
#define z2 "v_mfma_f32_16x16x32_bf16 v[44:47], a[232:235], v[128:131], v[44:47]\n"
#define p2 "v_mfma_f32_16x16x32_bf16 v[44:47], a[236:239], v[132:135], v[44:47]\n"
#define A74 "ds_read_b64_tr_b16 a[228:229], v216 offset:16928\n"
#define A0e "ds_read_b64_tr_b16 a[230:231], v216 offset:17440\n"
#define y1 "v_mfma_f32_16x16x32_bf16 v[92:95], a[236:239], v[140:143], v[92:95]\n"
#define V3 "v_mfma_f32_16x16x32_bf16 v[92:95], a[232:235], v[136:139], v[92:95]\n"
#define A1l "ds_read_b64_tr_b16 a[232:233], v216 offset:12736\n"
#define B0p "ds_read_b64_tr_b16 a[234:235], v216 offset:13248\n"
#define C4A "s_mov_b32 s90, 0\n"
#define A2z "ds_read_b64_tr_b16 a[236:237], v216 offset:16960\n"
#define r8 "ds_read_b64_tr_b16 a[238:239], v216 offset:17472\n"
#define s8 "ds_read_b128 a[192:195], v214 offset:8960\n"
#define A4C "ds_read_b128 a[196:199], v214 offset:9024\n"
#define A4D "ds_read_b128 a[200:203], v214 offset:4736\n"
#define B3M "ds_read_b128 a[204:207], v214 offset:4800\n"
#define B3L "ds_read_b128 a[208:211], v214 offset:512\n"
#define T2 "v_mfma_f32_16x16x32_bf16 v[0:3], a[216:219], v[96:99], v[0:3]\n"
#define t8 "ds_read_b128 a[212:215], v214 offset:576\n"
#define h2 "v_mfma_f32_16x16x32_bf16 v[48:51], a[220:223], v[108:111], v[48:51]\n"
#define B21 "ds_read_b64_tr_b16 a[220:221], v216 offset:16992\n"
#define B22 "ds_read_b64_tr_b16 a[222:223], v216 offset:17504\n"
#define X2 "v_mfma_f32_16x16x32_bf16 v[4:7], a[224:227], v[96:99], v[4:7]\n"
#define B2z "ds_read_b64_tr_b16 a[216:217], v216 offset:12768\n"
#define B20 "ds_read_b64_tr_b16 a[218:219], v216 offset:13280\n"
#define k2 "v_mfma_f32_16x16x32_bf16 v[52:55], a[228:231], v[108:111], v[52:55]\n"
#define B4k "ds_read_b64_tr_b16 a[228:229], v216 offset:25344\n"
#define B4l "ds_read_b64_tr_b16 a[230:231], v216 offset:25856\n"
#define K3 "v_mfma_f32_16x16x32_bf16 v[8:11], a[232:235], v[96:99], v[8:11]\n"
#define D5s "ds_read_b64_tr_b16 a[224:225], v216 offset:21120\n"
#define D5t "ds_read_b64_tr_b16 a[226:227], v216 offset:21632\n"
#define t1 "v_mfma_f32_16x16x32_bf16 v[56:59], a[236:239], v[108:111], v[56:59]\n"
#define B25 "ds_read_b64_tr_b16 a[236:237], v216 offset:25376\n"
#define B26 "ds_read_b64_tr_b16 a[238:239], v216 offset:25888\n"
#define B23 "ds_read_b64_tr_b16 a[232:233], v216 offset:21152\n"
#define B24 "ds_read_b64_tr_b16 a[234:235], v216 offset:21664\n"
#define d7 "s_waitcnt vmcnt(15)\n"
#define A2V "ds_read_b128 a[212:215], v214 offset:29632\n"
#define A2W "ds_read_b128 a[208:211], v214 offset:29568\n"
#define B0q "ds_read_b128 a[204:207], v214 offset:33856\n"
#define E1x "ds_read_b128 a[200:203], v214 offset:33792\n"
#define A7h "ds_read_b128 a[196:199], v214 offset:38080\n"
#define N4 "v_mfma_f32_16x16x32_bf16 v[12:15], a[216:219], v[96:99], v[12:15]\n"
#define w9 "ds_read_b128 a[192:195], v214 offset:38016\n"
#define R3 "v_mfma_f32_16x16x32_bf16 v[60:63], a[220:223], v[108:111], v[60:63]\n"
#define y4 "ds_read_b64_tr_b16 a[216:217], v216 offset:21184\n"
#define L2 "v_mfma_f32_16x16x32_bf16 v[16:19], a[224:227], v[96:99], v[16:19]\n"
#define z4 "ds_read_b64_tr_b16 a[218:219], v216 offset:21696\n"
#define A5 "ds_read_b64_tr_b16 a[220:221], v216 offset:25408\n"
#define T3 "v_mfma_f32_16x16x32_bf16 v[64:67], a[228:231], v[108:111], v[64:67]\n"
#define D8 "ds_read_b64_tr_b16 a[222:223], v216 offset:25920\n"
#define A3 "v_mfma_f32_16x16x32_bf16 v[20:23], a[232:235], v[96:99], v[20:23]\n"
#define B95 "ds_read_b64_tr_b16 a[224:225], v216 offset:21216\n"
#define A3g "ds_read_b64_tr_b16 a[226:227], v216 offset:21728\n"
#define M2 "v_mfma_f32_16x16x32_bf16 v[68:71], a[236:239], v[108:111], v[68:71]\n"
#define A0w "ds_read_b64_tr_b16 a[228:229], v216 offset:25440\n"
#define B0T "ds_read_b64_tr_b16 a[230:231], v216 offset:25952\n"
#define A0S "ds_read_b64_tr_b16 a[232:233], v216 offset:42240\n"
#define A4s "ds_read_b64_tr_b16 a[234:235], v216 offset:42752\n"
#define A3n "ds_read_b64_tr_b16 a[236:237], v216 offset:46464\n"
#define R9 "ds_read_b64_tr_b16 a[238:239], v216 offset:46976\n"
#define A5X "ds_read_b128 a[192:195], v214 offset:38528\n"
#define B1B "ds_read_b128 a[196:199], v214 offset:38592\n"
#define B4c "ds_read_b128 a[200:203], v214 offset:34304\n"
#define B8t "ds_read_b128 a[204:207], v214 offset:34368\n"
#define B8v "ds_read_b128 a[208:211], v214 offset:30080\n"
#define i1 "v_mfma_f32_16x16x32_bf16 v[24:27], a[216:219], v[96:99], v[24:27]\n"
#define e7 "ds_read_b128 a[212:215], v214 offset:30144\n"
#define z1 "v_mfma_f32_16x16x32_bf16 v[72:75], a[220:223], v[108:111], v[72:75]\n"
#define A9x "ds_read_b64_tr_b16 a[220:221], v216 offset:46496\n"
#define A9y "ds_read_b64_tr_b16 a[222:223], v216 offset:47008\n"
#define Z1 "v_mfma_f32_16x16x32_bf16 v[28:31], a[224:227], v[96:99], v[28:31]\n"
#define A9v "ds_read_b64_tr_b16 a[216:217], v216 offset:42272\n"
#define A9w "ds_read_b64_tr_b16 a[218:219], v216 offset:42784\n"
#define a1 "v_mfma_f32_16x16x32_bf16 v[76:79], a[228:231], v[108:111], v[76:79]\n"
#define B04 "ds_read_b64_tr_b16 a[228:229], v216 offset:46528\n"
#define B05 "ds_read_b64_tr_b16 a[230:231], v216 offset:47040\n"
#define R2 "v_mfma_f32_16x16x32_bf16 v[32:35], a[232:235], v[96:99], v[32:35]\n"
#define D4z "ds_read_b64_tr_b16 a[224:225], v216 offset:42304\n"
#define D40 "ds_read_b64_tr_b16 a[226:227], v216 offset:42816\n"
#define h1 "v_mfma_f32_16x16x32_bf16 v[80:83], a[236:239], v[108:111], v[80:83]\n"
#define B1c "ds_read_b64_tr_b16 a[236:237], v216 offset:46560\n"
#define B1d "ds_read_b64_tr_b16 a[238:239], v216 offset:47072\n"
#define A8m "ds_read_b64_tr_b16 a[232:233], v216 offset:42336\n"
#define A8n "ds_read_b64_tr_b16 a[234:235], v216 offset:42848\n"
#define B8w "ds_read_b128 a[212:215], v214 offset:50752\n"
#define B8x "ds_read_b128 a[208:211], v214 offset:50688\n"
#define B7B "ds_read_b128 a[204:207], v214 offset:54976\n"
#define B7C "ds_read_b128 a[200:203], v214 offset:54912\n"
#define j3 "v_mfma_f32_16x16x32_bf16 v[36:39], a[216:219], v[96:99], v[36:39]\n"
#define A0T "ds_read_b128 a[196:199], v214 offset:59200\n"
#define I3 "v_mfma_f32_16x16x32_bf16 v[84:87], a[220:223], v[108:111], v[84:87]\n"
#define A0U "ds_read_b128 a[192:195], v214 offset:59136\n"
#define o3 "v_mfma_f32_16x16x32_bf16 v[40:43], a[224:227], v[96:99], v[40:43]\n"
#define y9 "ds_read_b64_tr_b16 a[216:217], v217 offset:0\n"
#define X3 "v_mfma_f32_16x16x32_bf16 v[88:91], a[228:231], v[108:111], v[88:91]\n"
#define A7i "ds_read_b64_tr_b16 a[218:219], v217 offset:512\n"
#define C4K "s_add_u32 m0, s88, 114304\n"
#define u8 "ds_read_b64_tr_b16 a[220:221], v217 offset:4224\n"
#define S9 "ds_read_b64_tr_b16 a[222:223], v217 offset:4736\n"
#define e3 "v_mfma_f32_16x16x32_bf16 v[44:47], a[232:235], v[96:99], v[44:47]\n"
#define B6Z "ds_read_b64_tr_b16 a[224:225], v217 offset:32\n"
#define A2c "ds_read_b64_tr_b16 a[226:227], v217 offset:544\n"
#define t2 "v_mfma_f32_16x16x32_bf16 v[92:95], a[236:239], v[108:111], v[92:95]\n"
#define A0x "ds_read_b64_tr_b16 a[228:229], v217 offset:4256\n"
#define A0y "ds_read_b64_tr_b16 a[230:231], v217 offset:4768\n"
#define i5 "ds_read_b64_tr_b16 a[232:233], v217 offset:64\n"
#define X8 "ds_read_b64_tr_b16 a[234:235], v217 offset:576\n"
#define Y8 "ds_read_b64_tr_b16 a[236:237], v217 offset:4288\n"
#define j5 "ds_read_b64_tr_b16 a[238:239], v217 offset:4800\n"
#define A25 "ds_read_b128 a[192:195], v214 offset:59648\n"
#define f7 "ds_read_b128 a[196:199], v214 offset:59712\n"
#define A8K "ds_read_b128 a[200:203], v214 offset:55424\n"
#define D9J "ds_read_b128 a[204:207], v214 offset:55488\n"
#define D20 "ds_read_b128 a[208:211], v214 offset:51200\n"
#define A4t "ds_read_b128 a[212:215], v214 offset:51264\n"
#define B1i "ds_read_b64_tr_b16 a[220:221], v217 offset:4320\n"
#define B1j "ds_read_b64_tr_b16 a[222:223], v217 offset:4832\n"
#define B0F "ds_read_b64_tr_b16 a[216:217], v217 offset:96\n"
#define B0G "ds_read_b64_tr_b16 a[218:219], v217 offset:608\n"
#define B9q "ds_read_b64_tr_b16 a[228:229], v217 offset:12672\n"
#define B9r "ds_read_b64_tr_b16 a[230:231], v217 offset:13184\n"
#define D60 "ds_read_b64_tr_b16 a[224:225], v217 offset:8448\n"
#define D6z "ds_read_b64_tr_b16 a[226:227], v217 offset:8960\n"
#define B1l "ds_read_b64_tr_b16 a[236:237], v217 offset:12704\n"
#define B1m "ds_read_b64_tr_b16 a[238:239], v217 offset:13216\n"
#define A8d "ds_read_b64_tr_b16 a[232:233], v217 offset:8480\n"
#define A8e "ds_read_b64_tr_b16 a[234:235], v217 offset:8992\n"
#define A1r "ds_read_b128 a[212:215], v215 offset:16960\n"
#define A1s "ds_read_b128 a[208:211], v215 offset:16896\n"
#define A2X "ds_read_b128 a[204:207], v215 offset:21184\n"
#define B6b "ds_read_b128 a[200:203], v215 offset:21120\n"
#define A9D "ds_read_b128 a[196:199], v215 offset:25408\n"
#define K7 "s_add_u32 m0, s88, 128\n"
#define z9 "ds_read_b128 a[192:195], v215 offset:25344\n"
#define k5 "ds_read_b64_tr_b16 a[216:217], v217 offset:8512\n"
#define l5 "ds_read_b64_tr_b16 a[218:219], v217 offset:9024\n"
#define W6 "ds_read_b64_tr_b16 a[220:221], v217 offset:12736\n"
#define A0A "ds_read_b64_tr_b16 a[222:223], v217 offset:13248\n"
#define B0t "ds_read_b64_tr_b16 a[224:225], v217 offset:8544\n"
#define B9Q "ds_read_b64_tr_b16 a[226:227], v217 offset:9056\n"
#define E4H "ds_read_b64_tr_b16 a[228:229], v217 offset:12768\n"
#define D5Q "ds_read_b64_tr_b16 a[230:231], v217 offset:13280\n"
#define B1C "ds_read_b64_tr_b16 a[232:233], v217 offset:29568\n"
#define B4n "ds_read_b64_tr_b16 a[234:235], v217 offset:30080\n"
#define B2F "ds_read_b64_tr_b16 a[236:237], v217 offset:33792\n"
#define D7R "ds_read_b128 a[144:147], v218 offset:33792\n"
#define D7S "ds_read_b128 a[148:151], v218 offset:33856\n"
#define D7T "ds_read_b128 a[168:171], v218 offset:34304\n"
#define D7U "ds_read_b128 a[172:175], v218 offset:34368\n"
#define A2f "ds_read_b128 a[192:195], v215 offset:25856\n"
#define A8W "ds_read_b64_tr_b16 a[238:239], v217 offset:34304\n"
#define E1L "ds_read_b128 a[196:199], v215 offset:25920\n"
#define D68 "ds_read_b128 a[200:203], v215 offset:21632\n"
#define B5G "ds_read_b128 a[204:207], v215 offset:21696\n"
#define F36 "ds_read_b128 a[208:211], v215 offset:17408\n"
#define A2K "ds_read_b128 a[212:215], v215 offset:17472\n"
#define v8 "ds_read_b64_tr_b16 a[220:221], v217 offset:33824\n"
#define A76 "ds_read_b64_tr_b16 a[216:217], v217 offset:29600\n"
#define A77 "ds_read_b64_tr_b16 a[218:219], v217 offset:30112\n"
#define w8 "ds_read_b64_tr_b16 a[222:223], v217 offset:34336\n"
#define E8Z "ds_read_b64_tr_b16 a[226:227], v217 offset:30144\n"
#define E4E "ds_read_b64_tr_b16 a[228:229], v217 offset:33856\n"
#define B2g "ds_read_b64_tr_b16 a[230:231], v217 offset:34368\n"
#define B35 "ds_read_b64_tr_b16 a[224:225], v217 offset:29632\n"
#define A26 "ds_read_b64_tr_b16 a[232:233], v217 offset:29664\n"
#define A27 "ds_read_b64_tr_b16 a[234:235], v217 offset:30176\n"
#define A4L "ds_read_b64_tr_b16 a[236:237], v217 offset:33888\n"
#define F4C "s_waitcnt vmcnt(10) lgkmcnt(6)\n"
#define D7V "ds_read_b128 a[152:155], v218 offset:50688\n"
#define D7W "ds_read_b128 a[156:159], v218 offset:50752\n"
#define D7X "ds_read_b128 a[176:179], v218 offset:51200\n"
#define D7Y "ds_read_b128 a[180:183], v218 offset:51264\n"
#define A4M "ds_read_b64_tr_b16 a[238:239], v217 offset:34400\n"
#define A5d "s_add_u32 m0, s89, 126720\n"
#define F4D "s_add_u32 m0, s88, 34048\n"
#define p1 "s_waitcnt vmcnt(10)\n"
#define A4k "ds_read_b128 a[212:215], v215 offset:38080\n"
#define B7O "ds_read_b128 a[208:211], v215 offset:38016\n"
#define C4M "s_mov_b32 s91, 0\n"
#define C0I "ds_read_b128 a[204:207], v215 offset:42304\n"
#define A6d "ds_read_b128 a[200:203], v215 offset:42240\n"
#define A78 "ds_read_b128 a[196:199], v215 offset:46528\n"
#define A79 "ds_read_b128 a[192:195], v215 offset:46464\n"
#define E8 "ds_read_b64_tr_b16 a[216:217], v217 offset:50688\n"
#define F8 "ds_read_b64_tr_b16 a[218:219], v217 offset:51200\n"
#define G8 "ds_read_b64_tr_b16 a[220:221], v217 offset:54912\n"
#define H8 "ds_read_b64_tr_b16 a[222:223], v217 offset:55424\n"
#define A8E "ds_read_b64_tr_b16 a[224:225], v217 offset:50720\n"
#define A8F "ds_read_b64_tr_b16 a[226:227], v217 offset:51232\n"
#define A4N "ds_read_b64_tr_b16 a[228:229], v217 offset:54944\n"
#define C4N "s_mov_b32 s95, 12288\n"
#define A4O "ds_read_b64_tr_b16 a[230:231], v217 offset:55456\n"
#define B8E "s_waitcnt lgkmcnt(13)\n"
#define E2V "s_waitcnt lgkmcnt(12)\n"
#define A1u "ds_read_b64_tr_b16 a[232:233], v217 offset:50752\n"
#define A1v "ds_read_b64_tr_b16 a[234:235], v217 offset:51264\n"
#define A1w "ds_read_b64_tr_b16 a[236:237], v217 offset:54976\n"
#define A1x "ds_read_b64_tr_b16 a[238:239], v217 offset:55488\n"
#define B2H "s_waitcnt lgkmcnt(10)\n"
#define A9G "ds_read_b64_tr_b16 a[220:221], v217 offset:55008\n"
#define A9H "ds_read_b64_tr_b16 a[216:217], v217 offset:50784\n"
#define A9I "ds_read_b64_tr_b16 a[218:219], v217 offset:51296\n"
#define A9J "ds_read_b64_tr_b16 a[222:223], v217 offset:55520\n"
#define D7Z "ds_read_b128 a[164:167], v218 offset:33856\n"
#define D7a "ds_read_b128 a[160:163], v218 offset:33792\n"
#define D7b "ds_read_b128 a[184:187], v218 offset:34304\n"
#define D7c "ds_read_b128 a[188:191], v218 offset:34368\n"
#define U4 "v_mfma_f32_16x16x32_bf16 v[168:171], a[212:215], a[164:167], v[168:171]\n"
#define P1 "v_mfma_f32_16x16x32_bf16 v[168:171], a[208:211], a[160:163], v[168:171]\n"
#define N1 "v_mfma_f32_16x16x32_bf16 v[168:171], a[204:207], a[156:159], v[168:171]\n"
#define x8 "s_add_u32 m0, s89, 143872\n"
#define q1 "v_mfma_f32_16x16x32_bf16 v[168:171], a[200:203], a[152:155], v[168:171]\n"
#define S1 "v_mfma_f32_16x16x32_bf16 v[168:171], a[196:199], a[148:151], v[168:171]\n"
#define w3 "v_mfma_f32_16x16x32_bf16 v[168:171], a[192:195], a[144:147], v[168:171]\n"
#define A5e "buffer_load_dwordx4 v232, s[36:39], 0 offen offset:0 lds\n"
#define D7d "v_mfma_f32_16x16x32_bf16 v[172:175], a[192:195], a[168:171], 0\n"
#define D0 "s_addk_i32 m0, 1056\n"
#define B2h "ds_read_b128 a[192:195], v215 offset:46976\n"
#define L1 "v_mfma_f32_16x16x32_bf16 v[172:175], a[196:199], a[172:175], v[172:175]\n"
#define B2i "ds_read_b128 a[196:199], v215 offset:47040\n"
#define D7e "v_max3_f32 v230, v168, v169, v170\n"
#define D7f "v_max_f32 v230, v230, v171\n"
#define D1 "v_mfma_f32_16x16x32_bf16 v[172:175], a[200:203], a[176:179], v[172:175]\n"
#define A5f "buffer_load_dwordx4 v233, s[36:39], 0 offen offset:0 lds\n"
#define J1 "v_mfma_f32_16x16x32_bf16 v[172:175], a[204:207], a[180:183], v[172:175]\n"
#define A9K "ds_read_b128 a[200:203], v215 offset:42752\n"
#define C4R "v_sub_f32 v184, 0, v230\n"
#define C4S "v_sub_f32 v185, 0, v230\n"
#define C4T "v_sub_f32 v186, 0, v230\n"
#define C4U "v_sub_f32 v187, 0, v230\n"
#define J2 "v_mfma_f32_16x16x32_bf16 v[172:175], a[208:211], a[184:187], v[172:175]\n"
#define A9L "ds_read_b128 a[204:207], v215 offset:42816\n"
#define D7g "v_add_f32 v169, v169, v185\n"
#define V2 "v_mfma_f32_16x16x32_bf16 v[172:175], a[212:215], a[188:191], v[172:175]\n"
#define A5g "buffer_load_dwordx4 v234, s[36:39], 0 offen offset:0 lds\n"
#define B2J "ds_read_b128 a[208:211], v215 offset:38528\n"
#define D7h "v_add_f32 v170, v170, v186\n"
#define D7i "v_add_f32 v171, v171, v187\n"
#define D7j "v_max3_f32 v230, v172, v173, v174\n"
#define D7k "v_max_f32 v230, v230, v175\n"
#define B2K "ds_read_b128 a[212:215], v215 offset:38592\n"
#define A5h "buffer_load_dwordx4 v235, s[36:39], 0 offen offset:0 lds\n"
#define C4V "v_lshlrev_b32 v231, 3, v228\n"
#define L7 "s_addk_i32 m0, -20192\n"
#define D4L "ds_read_b64_tr_b16 a[224:225], v217 offset:59136\n"
#define C4W "v_sub_f32 v189, 0, v230\n"
#define C4X "v_sub_f32 v190, 0, v230\n"
#define C4Y "v_sub_f32 v191, 0, v230\n"
#define u1 "v_mfma_f32_16x16x32_bf16 v[160:163], a[192:195], a[96:99], v[176:179]\n"
#define D4M "ds_read_b64_tr_b16 a[226:227], v217 offset:59648\n"
#define Y2 "v_mul_f32 v171, s97, v171\n"
#define A5i "buffer_load_dwordx4 v232, s[36:39], 0 offen offset:128 lds\n"
#define B0b "ds_read_b64_tr_b16 a[228:229], v216 offset:0\n"
#define C4Z "v_sub_f32 v188, 0, v230\n"
#define C4a "v_add_f32 v172, v172, v188\n"
#define D7l "v_add_f32 v173, v173, v189\n"
#define B0c "ds_read_b64_tr_b16 a[230:231], v216 offset:512\n"
#define D7m "v_add_f32 v174, v174, v190\n"
#define A5j "buffer_load_dwordx4 v233, s[36:39], 0 offen offset:128 lds\n"
#define p3 "v_mfma_f32_16x16x32_bf16 v[164:167], a[212:215], a[140:143], v[180:183]\n"
#define B0d "ds_read_b64_tr_b16 a[232:233], v217 offset:59168\n"
#define D7n "v_add_f32 v175, v175, v191\n"
#define N7 "v_mul_f32 v169, s97, v169\n"
#define k3 "v_mul_f32 v170, s97, v170\n"
#define B0e "ds_read_b64_tr_b16 a[234:235], v217 offset:59680\n"
#define Q1 "v_mul_f32 v172, s97, v172\n"
#define A5k "buffer_load_dwordx4 v234, s[36:39], 0 offen offset:128 lds\n"
#define A9M "ds_read_b64_tr_b16 a[236:237], v216 offset:32\n"
#define P4 "v_mul_f32 v173, s97, v173\n"
#define i4 "v_mfma_f32_16x16x32_bf16 v[164:167], a[192:195], a[120:123], v[164:167]\n"
#define A9N "ds_read_b64_tr_b16 a[238:239], v216 offset:544\n"
#define q3 "v_mul_f32 v174, s97, v174\n"
#define A5l "buffer_load_dwordx4 v235, s[36:39], 0 offen offset:128 lds\n"
#define M1 "v_mul_f32 v175, s97, v175\n"
#define C4b "v_add_f32 v168, v168, v184\n"
#define Y5 "v_mul_f32 v168, s97, v168\n"
#define A0X "global_load_dword v226, v231, s[2:3]\n"
#define A01 "v_cvt_pk_bf16_f32 v144, v168, v169\n"
#define C4c "v_cndmask_b32_e64 v168, v209, v184, s[64:65]\n"
#define C4d "v_cndmask_b32_e64 v169, v209, v185, s[66:67]\n"
#define B0f "v_cvt_pk_bf16_f32 v145, v170, v171\n"
#define C4e "v_cndmask_b32_e64 v170, v209, v186, s[68:69]\n"
#define C4f "v_cndmask_b32_e64 v171, v209, v187, s[70:71]\n"
#define F4E "s_add_u32 m0, s88, 84736\n"
#define F4z "v_cvt_pk_bf16_f32 v130, v160, v161\n"
#define F40 "v_cvt_pk_bf16_f32 v131, v162, v163\n"
#define D3Q "v_cvt_pk_bf16_f32 v152, v172, v173\n"
#define D3S "v_cvt_pk_bf16_f32 v153, v174, v175\n"
#define x3 "v_mfma_f32_16x16x32_bf16 v[172:175], a[212:215], a[188:191], v[188:191]\n"
#define A4Q "ds_read_b128 a[212:215], v214 offset:4288\n"
#define A4R "ds_read_b128 a[208:211], v214 offset:4224\n"
#define G4 "v_mfma_f32_16x16x32_bf16 v[172:175], a[192:195], a[168:171], v[172:175]\n"
#define A9O "ds_read_b128 a[204:207], v214 offset:8512\n"
#define C92 "v_cvt_pk_bf16_f32 v138, v164, v165\n"
#define C9x "v_cvt_pk_bf16_f32 v139, v166, v167\n"
#define A9P "ds_read_b128 a[200:203], v214 offset:8448\n"
#define A2L "ds_read_b128 a[196:199], v214 offset:12736\n"
#define Y6 "v_cvt_pk_bf16_f32 v146, v168, v169\n"
#define A0E "v_cvt_pk_bf16_f32 v147, v170, v171\n"
#define A2M "ds_read_b128 a[192:195], v214 offset:12672\n"
#define I8 "ds_read_b64_tr_b16 a[216:217], v217 offset:59200\n"
#define z6 "v_mfma_f32_16x16x32_bf16 v[196:199], a[248:251], v[144:147], v[196:199]\n"
#define A7R "v_cvt_pk_bf16_f32 v154, v172, v173\n"
#define A7S "v_cvt_pk_bf16_f32 v155, v174, v175\n"
#define J8 "ds_read_b64_tr_b16 a[218:219], v217 offset:59712\n"
#define y8 "ds_read_b64_tr_b16 a[220:221], v216 offset:64\n"
#define A0Y "v_mfma_f32_16x16x32_bf16 v[196:199], a[252:255], v[152:155], v[196:199]\n"
#define z8 "ds_read_b64_tr_b16 a[222:223], v216 offset:576\n"
#define A2N "ds_read_b64_tr_b16 a[224:225], v217 offset:59232\n"
#define A2O "ds_read_b64_tr_b16 a[226:227], v217 offset:59744\n"
#define A4S "ds_read_b64_tr_b16 a[228:229], v216 offset:96\n"
#define A4T "ds_read_b64_tr_b16 a[230:231], v216 offset:608\n"
#define D7o "v_cndmask_b32_e64 v168, v209, v184, s[72:73]\n"
#define A1c "ds_read_b64_tr_b16 a[232:233], v216 offset:16896\n"
#define D7p "v_cndmask_b32_e64 v169, v209, v185, s[74:75]\n"
#define D7q "v_cndmask_b32_e64 v170, v209, v186, s[76:77]\n"
#define D7r "v_cndmask_b32_e64 v171, v209, v187, s[78:79]\n"
#define A1d "ds_read_b64_tr_b16 a[234:235], v216 offset:17408\n"
#define A03 "ds_read_b64_tr_b16 a[236:237], v216 offset:21120\n"
#define A04 "ds_read_b64_tr_b16 a[238:239], v216 offset:21632\n"
#define B5P "ds_read_b64_tr_b16 a[216:217], v216 offset:16928\n"
#define B5Q "ds_read_b64_tr_b16 a[218:219], v216 offset:17440\n"
#define A9Q "ds_read_b64_tr_b16 a[220:221], v216 offset:21152\n"
#define A9R "ds_read_b64_tr_b16 a[222:223], v216 offset:21664\n"
#define z3 "v_mfma_f32_16x16x32_bf16 v[172:175], a[192:195], a[168:171], v[188:191]\n"
#define B39 "ds_read_b128 a[192:195], v214 offset:13184\n"
#define A2P "ds_read_b128 a[200:203], v214 offset:8960\n"
#define A28 "ds_read_b128 a[196:199], v214 offset:13248\n"
#define D64 "ds_read_b128 a[204:207], v214 offset:9024\n"
#define E1F "ds_read_b128 a[208:211], v214 offset:4736\n"
#define A4U "ds_read_b128 a[212:215], v214 offset:4800\n"
#define D4N "ds_read_b64_tr_b16 a[224:225], v216 offset:16960\n"
#define D4O "ds_read_b64_tr_b16 a[226:227], v216 offset:17472\n"
#define B3E "ds_read_b64_tr_b16 a[228:229], v216 offset:21184\n"
#define B3F "ds_read_b64_tr_b16 a[230:231], v216 offset:21696\n"
#define E7 "ds_read_b128 v[248:251], v218 offset:50688\n"
#define F7 "ds_read_b128 v[252:255], v218 offset:51200\n"
#define M7 "s_cmp_eq_u32 0, 1\n"
#define A9S "ds_read_b64_tr_b16 a[232:233], v216 offset:16992\n"
#define A06 "v_cvt_pk_bf16_f32 v148, v168, v169\n"
#define A9T "v_cvt_pk_bf16_f32 v149, v170, v171\n"
#define C4l "v_cndmask_b32_e64 v168, v209, v184, s[80:81]\n"
#define C4m "v_cndmask_b32_e64 v169, v209, v185, s[82:83]\n"
#define C4n "v_cndmask_b32_e64 v170, v209, v186, s[84:85]\n"
#define C4o "v_cndmask_b32_e64 v171, v209, v187, s[86:87]\n"
#define A9U "ds_read_b64_tr_b16 a[234:235], v216 offset:17504\n"
#define C4p "v_mfma_f32_16x16x32_bf16 v[200:203], v[248:251], a[144:147], 0\n"
#define Z5 "ds_read_b128 v[248:251], v218 offset:50752\n"
#define C4q "v_mfma_f32_16x16x32_bf16 v[204:207], v[252:255], a[144:147], 0\n"
#define a5 "ds_read_b128 v[252:255], v218 offset:51264\n"
#define B2P "ds_read_b64_tr_b16 a[236:237], v216 offset:21216\n"
#define D4P "v_cvt_pk_bf16_f32 v134, v160, v161\n"
#define B2Q "ds_read_b64_tr_b16 a[238:239], v216 offset:21728\n"
#define D4Q "v_cvt_pk_bf16_f32 v135, v162, v163\n"
#define B6B "v_cvt_pk_bf16_f32 v156, v172, v173\n"
#define B6D "v_cvt_pk_bf16_f32 v157, v174, v175\n"
#define C4r "v_mfma_f32_16x16x32_bf16 v[200:203], v[248:251], a[148:151], v[200:203]\n"
#define m4 "ds_read_b128 v[248:251], v218 offset:33792\n"
#define C4s "v_mfma_f32_16x16x32_bf16 v[204:207], v[252:255], a[148:151], v[204:207]\n"
#define n4 "ds_read_b128 v[252:255], v218 offset:34304\n"
#define B93 "v_cvt_pk_bf16_f32 v142, v164, v165\n"
#define B94 "v_cvt_pk_bf16_f32 v143, v166, v167\n"
#define C4t "v_mfma_f32_16x16x32_bf16 v[200:203], v[248:251], a[152:155], v[200:203]\n"
#define C4 "ds_read_b128 v[248:251], v218 offset:33856\n"
#define C4u "v_mfma_f32_16x16x32_bf16 v[204:207], v[252:255], a[152:155], v[204:207]\n"
#define D4 "ds_read_b128 v[252:255], v218 offset:34368\n"
#define T9 "v_cvt_pk_bf16_f32 v150, v168, v169\n"
#define U9 "v_cvt_pk_bf16_f32 v151, v170, v171\n"
#define D7s "v_cndmask_b32_e64 v168, v209, v188, s[56:57]\n"
#define Z6 "v_mfma_f32_16x16x32_bf16 v[196:199], a[248:251], v[148:151], v[196:199]\n"
#define O8 "v_cvt_pk_bf16_f32 v158, v172, v173\n"
#define P8 "v_cvt_pk_bf16_f32 v159, v174, v175\n"
#define C4w "v_mfma_f32_16x16x32_bf16 v[200:203], v[248:251], a[156:159], v[200:203]\n"
#define D7t "v_cndmask_b32_e64 v169, v209, v189, s[58:59]\n"
#define A4V "v_mfma_f32_16x16x32_bf16 v[196:199], a[252:255], v[156:159], v[196:199]\n"
#define C4x "v_mfma_f32_16x16x32_bf16 v[204:207], v[252:255], a[156:159], v[204:207]\n"
#define O7 "s_add_u32 m0, s89, 143616\n"
#define D7u "v_cndmask_b32_e64 v170, v209, v190, s[60:61]\n"
#define D7v "v_cndmask_b32_e64 v171, v209, v191, s[62:63]\n"
#define A5p "buffer_load_dwordx4 v232, s[36:39], 0 offen offset:256 lds\n"
#define B0h "v_max3_f32 v208, v196, v197, v198\n"
#define A9V "v_max_f32 v208, v208, v199\n"
#define C4y "v_mfma_f32_16x16x32_bf16 v[168:171], a[212:215], a[188:191], v[168:171]\n"
#define A5q "buffer_load_dwordx4 v233, s[36:39], 0 offen offset:256 lds\n"
#define C4z "v_mfma_f32_16x16x32_bf16 v[168:171], a[208:211], a[184:187], v[168:171]\n"
#define C40 "v_mfma_f32_16x16x32_bf16 v[168:171], a[204:207], a[180:183], v[168:171]\n"
#define C41 "v_mfma_f32_16x16x32_bf16 v[168:171], a[200:203], a[176:179], v[168:171]\n"
#define C42 "v_mfma_f32_16x16x32_bf16 v[168:171], a[196:199], a[172:175], v[168:171]\n"
#define C43 "v_mfma_f32_16x16x32_bf16 v[168:171], a[192:195], a[168:171], v[168:171]\n"
#define A5r "buffer_load_dwordx4 v234, s[36:39], 0 offen offset:256 lds\n"
#define E2W "s_cbranch_scc1 Lb0_1\n"
#define E8b "v_mfma_f32_16x16x32_bf16 a[0:3], a[216:219], v[144:147], 0\n"
#define A5s "buffer_load_dwordx4 v235, s[36:39], 0 offen offset:256 lds\n"
#define a6 "v_mfma_f32_16x16x32_bf16 a[0:3], a[220:223], v[148:151], a[0:3]\n"
#define A9W "s_addk_i32 m0, -19808\n"
#define E8c "v_mfma_f32_16x16x32_bf16 a[48:51], a[220:223], v[156:159], 0\n"
#define D5 "v_mfma_f32_16x16x32_bf16 a[48:51], a[216:219], v[152:155], a[48:51]\n"
#define A5t "buffer_load_dwordx4 v236, s[36:39], 0 offen offset:0 lds\n"
#define C44 "v_cndmask_b32_e64 v164, v209, v188, s[64:65]\n"
#define E8d "v_mfma_f32_16x16x32_bf16 a[4:7], a[224:227], v[144:147], 0\n"
#define A5u "buffer_load_dwordx4 v237, s[36:39], 0 offen offset:0 lds\n"
#define V9 "v_mfma_f32_16x16x32_bf16 a[4:7], a[228:231], v[148:151], a[4:7]\n"
#define C45 "v_cndmask_b32_e64 v165, v209, v189, s[66:67]\n"
#define E8e "v_mfma_f32_16x16x32_bf16 a[52:55], a[228:231], v[156:159], 0\n"
#define E5 "v_mfma_f32_16x16x32_bf16 a[52:55], a[224:227], v[152:155], a[52:55]\n"
#define C46 "v_cndmask_b32_e64 v166, v209, v190, s[68:69]\n"
#define A5v "buffer_load_dwordx4 v238, s[36:39], 0 offen offset:0 lds\n"
#define C48 "v_cndmask_b32_e64 v167, v209, v191, s[70:71]\n"
#define E8f "v_mfma_f32_16x16x32_bf16 a[8:11], a[232:235], v[144:147], 0\n"
#define b6 "v_mfma_f32_16x16x32_bf16 a[8:11], a[236:239], v[148:151], a[8:11]\n"
#define A5w "buffer_load_dwordx4 v239, s[36:39], 0 offen offset:0 lds\n"
#define E8g "v_mfma_f32_16x16x32_bf16 a[56:59], a[236:239], v[156:159], 0\n"
#define F5 "v_mfma_f32_16x16x32_bf16 a[56:59], a[232:235], v[152:155], a[56:59]\n"
#define D7w "v_mfma_f32_16x16x32_bf16 v[172:175], a[212:215], a[116:119], v[176:179]\n"
#define D7x "v_mfma_f32_16x16x32_bf16 v[172:175], a[208:211], a[112:115], v[172:175]\n"
#define D7y "v_mfma_f32_16x16x32_bf16 v[172:175], a[204:207], a[108:111], v[172:175]\n"
#define D7z "v_mfma_f32_16x16x32_bf16 v[172:175], a[200:203], a[104:107], v[172:175]\n"
#define D70 "v_mfma_f32_16x16x32_bf16 v[172:175], a[196:199], a[100:103], v[172:175]\n"
#define D71 "v_mfma_f32_16x16x32_bf16 v[172:175], a[192:195], a[96:99], v[172:175]\n"
#define E8h "v_cvt_pk_bf16_f32 v120, v168, v169\n"
#define D72 "v_mfma_f32_16x16x32_bf16 v[160:163], a[192:195], a[120:123], v[180:183]\n"
#define A9X "v_mfma_f32_16x16x32_bf16 v[160:163], a[196:199], a[124:127], v[160:163]\n"
#define E8i "v_cvt_pk_bf16_f32 v121, v170, v171\n"
#define A9Y "v_mfma_f32_16x16x32_bf16 v[160:163], a[200:203], a[128:131], v[160:163]\n"
#define A9Z "v_mfma_f32_16x16x32_bf16 v[160:163], a[204:207], a[132:135], v[160:163]\n"
#define A9a "v_mfma_f32_16x16x32_bf16 v[160:163], a[208:211], a[136:139], v[160:163]\n"
#define E8j "v_cvt_pk_bf16_f32 v99, v174, v175\n"
#define A9b "v_mfma_f32_16x16x32_bf16 v[160:163], a[212:215], a[140:143], v[160:163]\n"
#define A5z "v_mfma_f32_16x16x32_bf16 v[164:167], a[212:215], a[188:191], v[164:167]\n"
#define A50 "v_mfma_f32_16x16x32_bf16 v[164:167], a[208:211], a[184:187], v[164:167]\n"
#define A51 "v_mfma_f32_16x16x32_bf16 v[164:167], a[204:207], a[180:183], v[164:167]\n"
#define A52 "v_mfma_f32_16x16x32_bf16 v[164:167], a[200:203], a[176:179], v[164:167]\n"
#define A53 "v_mfma_f32_16x16x32_bf16 v[164:167], a[196:199], a[172:175], v[164:167]\n"
#define C49 "v_mfma_f32_16x16x32_bf16 v[164:167], a[192:195], a[168:171], v[164:167]\n"
#define E8k "v_cvt_pk_bf16_f32 v98, v172, v173\n"
#define D73 "v_mul_f32 v164, s97, v164\n"
#define D74 "v_mul_f32 v165, s97, v165\n"
#define D75 "v_mul_f32 v166, s97, v166\n"
#define E8l "v_cvt_pk_bf16_f32 v106, v160, v161\n"
#define E8m "v_cvt_pk_bf16_f32 v107, v162, v163\n"
#define D76 "v_mul_f32 v167, s97, v167\n"
#define D77 "v_cndmask_b32_e64 v160, v209, v188, s[72:73]\n"
#define E8n "v_mfma_f32_16x16x32_bf16 a[12:15], a[216:219], v[144:147], 0\n"
#define W9 "v_mfma_f32_16x16x32_bf16 a[12:15], a[220:223], v[148:151], a[12:15]\n"
#define E8o "v_mfma_f32_16x16x32_bf16 a[60:63], a[220:223], v[156:159], 0\n"
#define G5 "v_mfma_f32_16x16x32_bf16 a[60:63], a[216:219], v[152:155], a[60:63]\n"
#define E2Z "v_cvt_pk_bf16_f32 v122, v164, v165\n"
#define E2a "v_cvt_pk_bf16_f32 v123, v166, v167\n"
#define D78 "v_cndmask_b32_e64 v161, v209, v189, s[74:75]\n"
#define D79 "v_cndmask_b32_e64 v162, v209, v190, s[76:77]\n"
#define c8 "v_mfma_f32_16x16x32_bf16 v[192:195], a[252:255], v[120:123], v[192:195]\n"
#define D8A "v_cndmask_b32_e64 v163, v209, v191, s[78:79]\n"
#define E8p "v_mfma_f32_16x16x32_bf16 a[16:19], a[224:227], v[144:147], 0\n"
#define c6 "v_mfma_f32_16x16x32_bf16 a[16:19], a[228:231], v[148:151], a[16:19]\n"
#define E8q "v_mfma_f32_16x16x32_bf16 a[64:67], a[228:231], v[156:159], 0\n"
#define H5 "v_mfma_f32_16x16x32_bf16 a[64:67], a[224:227], v[152:155], a[64:67]\n"
#define E8r "v_mfma_f32_16x16x32_bf16 a[20:23], a[232:235], v[144:147], 0\n"
#define d6 "v_mfma_f32_16x16x32_bf16 a[20:23], a[236:239], v[148:151], a[20:23]\n"
#define E8s "v_mfma_f32_16x16x32_bf16 a[68:71], a[236:239], v[156:159], 0\n"
#define h7 "v_mfma_f32_16x16x32_bf16 a[68:71], a[232:235], v[152:155], a[68:71]\n"
#define D8B "v_mfma_f32_16x16x32_bf16 v[168:171], a[212:215], a[116:119], v[176:179]\n"
#define C5A "v_mfma_f32_16x16x32_bf16 v[168:171], a[192:195], a[96:99], v[168:171]\n"
#define D8C "v_mfma_f32_16x16x32_bf16 v[172:175], a[192:195], a[120:123], v[180:183]\n"
#define D8D "v_mfma_f32_16x16x32_bf16 v[172:175], a[212:215], a[140:143], v[172:175]\n"
#define C5B "v_mfma_f32_16x16x32_bf16 v[160:163], a[212:215], a[188:191], v[160:163]\n"
#define E2b "v_cvt_pk_bf16_f32 v100, v168, v169\n"
#define C5C "v_mfma_f32_16x16x32_bf16 v[160:163], a[208:211], a[184:187], v[160:163]\n"
#define C5D "v_mfma_f32_16x16x32_bf16 v[160:163], a[204:207], a[180:183], v[160:163]\n"
#define C5F "v_mfma_f32_16x16x32_bf16 v[160:163], a[200:203], a[176:179], v[160:163]\n"
#define C5G "v_mfma_f32_16x16x32_bf16 v[160:163], a[196:199], a[172:175], v[160:163]\n"
#define C5H "v_mfma_f32_16x16x32_bf16 v[160:163], a[192:195], a[168:171], v[160:163]\n"
#define E8t "v_cvt_pk_bf16_f32 v109, v174, v175\n"
#define E8u "v_mfma_f32_16x16x32_bf16 a[24:27], a[216:219], v[144:147], 0\n"
#define A4Y "v_mfma_f32_16x16x32_bf16 a[24:27], a[220:223], v[148:151], a[24:27]\n"
#define E8v "v_mfma_f32_16x16x32_bf16 a[72:75], a[220:223], v[156:159], 0\n"
#define E2c "v_cvt_pk_bf16_f32 v101, v170, v171\n"
#define C5I "v_cndmask_b32_e64 v174, v209, v190, s[84:85]\n"
#define A1h "v_mfma_f32_16x16x32_bf16 a[72:75], a[216:219], v[152:155], a[72:75]\n"
#define C5J "v_cndmask_b32_e64 v175, v209, v191, s[86:87]\n"
#define D8E "v_mul_f32 v160, s97, v160\n"
#define E8w "v_mfma_f32_16x16x32_bf16 a[28:31], a[224:227], v[144:147], 0\n"
#define E8x "v_cvt_pk_bf16_f32 v108, v172, v173\n"
#define A0G "v_mfma_f32_16x16x32_bf16 a[28:31], a[228:231], v[148:151], a[28:31]\n"
#define E8y "v_mfma_f32_16x16x32_bf16 a[76:79], a[228:231], v[156:159], 0\n"
#define C5K "v_cndmask_b32_e64 v172, v209, v188, s[80:81]\n"
#define K8 "v_mfma_f32_16x16x32_bf16 a[76:79], a[224:227], v[152:155], a[76:79]\n"
#define C5L "v_cndmask_b32_e64 v173, v209, v189, s[82:83]\n"
#define D8F "v_mul_f32 v161, s97, v161\n"
#define D8H "v_mul_f32 v162, s97, v162\n"
#define C5M "v_mfma_f32_16x16x32_bf16 v[200:203], v[248:251], a[160:163], v[200:203]\n"
#define C5N "v_mfma_f32_16x16x32_bf16 v[204:207], v[252:255], a[160:163], v[204:207]\n"
#define D8J "v_mul_f32 v163, s97, v163\n"
#define E8z "v_mfma_f32_16x16x32_bf16 a[32:35], a[232:235], v[144:147], 0\n"
#define e6 "v_mfma_f32_16x16x32_bf16 a[32:35], a[236:239], v[148:151], a[32:35]\n"
#define E80 "v_cvt_pk_bf16_f32 v124, v160, v161\n"
#define E81 "v_mfma_f32_16x16x32_bf16 a[80:83], a[236:239], v[156:159], 0\n"
#define A0a "v_mfma_f32_16x16x32_bf16 a[80:83], a[232:235], v[152:155], a[80:83]\n"
#define E82 "v_cvt_pk_bf16_f32 v125, v162, v163\n"
#define D8K "v_mfma_f32_16x16x32_bf16 v[164:167], a[212:215], a[116:119], v[176:179]\n"
#define D8L "v_mfma_f32_16x16x32_bf16 v[164:167], a[208:211], a[112:115], v[164:167]\n"
#define D8M "v_mfma_f32_16x16x32_bf16 v[164:167], a[204:207], a[108:111], v[164:167]\n"
#define D8N "v_mfma_f32_16x16x32_bf16 v[164:167], a[200:203], a[104:107], v[164:167]\n"
#define D8O "v_mfma_f32_16x16x32_bf16 v[164:167], a[196:199], a[100:103], v[164:167]\n"
#define D8P "v_mfma_f32_16x16x32_bf16 v[164:167], a[192:195], a[96:99], v[164:167]\n"
#define C5O "v_mfma_f32_16x16x32_bf16 v[200:203], v[248:251], a[164:167], v[200:203]\n"
#define C5P "v_mfma_f32_16x16x32_bf16 v[204:207], v[252:255], a[164:167], v[204:207]\n"
#define D8Q "v_mfma_f32_16x16x32_bf16 v[168:171], a[192:195], a[120:123], v[180:183]\n"
#define X9 "v_cndmask_b32_e64 v200, v200, v201, s[4:5]\n"
#define A9c "v_mfma_f32_16x16x32_bf16 v[168:171], a[196:199], a[124:127], v[168:171]\n"
#define A9d "v_mfma_f32_16x16x32_bf16 v[168:171], a[200:203], a[128:131], v[168:171]\n"
#define A9e "v_mfma_f32_16x16x32_bf16 v[168:171], a[204:207], a[132:135], v[168:171]\n"
#define A9f "v_mfma_f32_16x16x32_bf16 v[168:171], a[208:211], a[136:139], v[168:171]\n"
#define E83 "v_cvt_pk_bf16_f32 v102, v164, v165\n"
#define E84 "v_cvt_pk_bf16_f32 v103, v166, v167\n"
#define A9g "v_mfma_f32_16x16x32_bf16 v[168:171], a[212:215], a[140:143], v[168:171]\n"
#define Y9 "v_cndmask_b32_e64 v204, v204, v205, s[4:5]\n"
#define A1i "v_cndmask_b32_e64 v202, v202, v203, s[4:5]\n"
#define A1j "v_cndmask_b32_e64 v206, v206, v207, s[4:5]\n"
#define A2S "v_cndmask_b32_e64 v200, v200, v204, s[6:7]\n"
#define A2T "v_cndmask_b32_e64 v202, v202, v206, s[6:7]\n"
#define D9 "v_max_f32 v201, v200, v202\n"
#define B5E "v_sub_f32 v200, v200, v201\n"
#define A3d "v_sub_f32 v202, v202, v201\n"
#define B2V "v_mul_f32 v200, s97, v200\n"
#define B2W "v_mul_f32 v202, s97, v202\n"
#define E85 "v_cvt_pk_bf16_f32 v110, v168, v169\n"
#define E86 "v_cvt_pk_bf16_f32 v111, v170, v171\n"
#define L6 "v_exp_f32 v200, v200\n"
#define M6 "v_exp_f32 v202, v202\n"
#define C5Q "v_mfma_f32_16x16x32_bf16 v[204:207], v[252:255], a[168:171], 0\n"
#define A14 "v_add_f32 v201, v200, v202\n"
#define A15 "v_rcp_f32 v201, v201\n"
#define A7M "v_mul_f32 v201, s94, v201\n"
#define A7N "v_mul_f32 v200, v200, v201\n"
#define A3K "v_mul_f32 v202, v202, v201\n"
#define A3M "v_cvt_pk_bf16_f32 v200, v200, v200\n"
#define N6 "v_cvt_pk_bf16_f32 v202, v202, v202\n"
#define C5T "v_and_b32 v244, v200, v227\n"
#define C5U "v_and_b32 v245, v202, v227\n"
#define C5V "v_mfma_f32_16x16x32_bf16 v[200:203], v[248:251], a[168:171], 0\n"
#define C5W "v_cndmask_b32_e64 v184, 0, v244, s[8:9]\n"
#define E87 "v_mfma_f32_16x16x32_bf16 a[36:39], a[216:219], v[144:147], 0\n"
#define f6 "v_mfma_f32_16x16x32_bf16 a[36:39], a[220:223], v[148:151], a[36:39]\n"
#define K9 "v_cvt_pk_bf16_f32 v126, v172, v173\n"
#define L9 "v_cvt_pk_bf16_f32 v127, v174, v175\n"
#define E88 "v_mfma_f32_16x16x32_bf16 a[84:87], a[220:223], v[156:159], 0\n"
#define P7 "s_add_u32 m0, s89, 143744\n"
#define C5X "v_cndmask_b32_e64 v185, 0, v245, s[8:9]\n"
#define C5Y "v_cndmask_b32_e64 v186, 0, v244, s[10:11]\n"
#define I5 "v_mfma_f32_16x16x32_bf16 a[84:87], a[216:219], v[152:155], a[84:87]\n"
#define A6A "buffer_load_dwordx4 v236, s[36:39], 0 offen offset:128 lds\n"
#define C5Z "v_cndmask_b32_e64 v187, 0, v245, s[10:11]\n"
#define C5a "v_mfma_f32_16x16x32_bf16 v[200:203], v[248:251], a[172:175], v[200:203]\n"
#define A6C "buffer_load_dwordx4 v237, s[36:39], 0 offen offset:128 lds\n"
#define C5b "v_mfma_f32_16x16x32_bf16 v[204:207], v[252:255], a[172:175], v[204:207]\n"
#define L8 "v_mfma_f32_16x16x32_bf16 v[192:195], a[252:255], v[124:127], v[192:195]\n"
#define A6D "buffer_load_dwordx4 v238, s[36:39], 0 offen offset:128 lds\n"
#define E89 "v_mfma_f32_16x16x32_bf16 a[40:43], a[224:227], v[144:147], 0\n"
#define Z9 "v_mfma_f32_16x16x32_bf16 a[40:43], a[228:231], v[148:151], a[40:43]\n"
#define E9A "v_mfma_f32_16x16x32_bf16 a[88:91], a[228:231], v[156:159], 0\n"
#define J5 "v_mfma_f32_16x16x32_bf16 a[88:91], a[224:227], v[152:155], a[88:91]\n"
#define A6E "buffer_load_dwordx4 v239, s[36:39], 0 offen offset:128 lds\n"
#define E9B "v_max3_f32 v208, v192, v193, v195\n"
#define A6F "buffer_load_dwordx4 v236, s[36:39], 0 offen offset:256 lds\n"
#define E9C "v_mfma_f32_16x16x32_bf16 a[44:47], a[232:235], v[144:147], 0\n"
#define C5c "v_accvgpr_read_b32 v144, a0\n"
#define g6 "v_mfma_f32_16x16x32_bf16 a[44:47], a[236:239], v[148:151], a[44:47]\n"
#define C5d "v_accvgpr_read_b32 v145, a1\n"
#define C5e "v_accvgpr_read_b32 v146, a2\n"
#define E9D "v_mfma_f32_16x16x32_bf16 a[92:95], a[236:239], v[156:159], 0\n"
#define K5 "v_mfma_f32_16x16x32_bf16 a[92:95], a[232:235], v[152:155], a[92:95]\n"
#define C5f "v_accvgpr_read_b32 v147, a3\n"
#define A6G "buffer_load_dwordx4 v237, s[36:39], 0 offen offset:256 lds\n"
#define A6H "buffer_load_dwordx4 v238, s[36:39], 0 offen offset:256 lds\n"
#define A6I "buffer_load_dwordx4 v239, s[36:39], 0 offen offset:256 lds\n"
#define F4F "s_add_u32 m0, s88, 46464\n"
#define L5 "v_mfma_f32_16x16x32_bf16 a[48:51], a[216:219], v[120:123], a[48:51]\n"
#define h6 "v_mfma_f32_16x16x32_bf16 a[48:51], a[220:223], v[124:127], a[48:51]\n"
#define M5 "v_mfma_f32_16x16x32_bf16 a[52:55], a[224:227], v[120:123], a[52:55]\n"
#define i6 "v_mfma_f32_16x16x32_bf16 a[52:55], a[228:231], v[124:127], a[52:55]\n"
#define Q7 "v_mfma_f32_16x16x32_bf16 a[56:59], a[232:235], v[120:123], a[56:59]\n"
#define j6 "v_mfma_f32_16x16x32_bf16 a[56:59], a[236:239], v[124:127], a[56:59]\n"
#define C5g "v_accvgpr_read_b32 v152, a48\n"
#define C5h "v_accvgpr_read_b32 v153, a49\n"
#define C5i "v_accvgpr_read_b32 v154, a50\n"
#define C5j "v_accvgpr_read_b32 v155, a51\n"
#define N5 "v_mfma_f32_16x16x32_bf16 a[60:63], a[216:219], v[120:123], a[60:63]\n"
#define A2U "v_mfma_f32_16x16x32_bf16 a[60:63], a[220:223], v[124:127], a[60:63]\n"
#define O5 "v_mfma_f32_16x16x32_bf16 a[64:67], a[224:227], v[120:123], a[64:67]\n"
#define k6 "v_mfma_f32_16x16x32_bf16 a[64:67], a[228:231], v[124:127], a[64:67]\n"
#define a9 "v_mfma_f32_16x16x32_bf16 a[68:71], a[232:235], v[120:123], a[68:71]\n"
#define l6 "v_mfma_f32_16x16x32_bf16 a[68:71], a[236:239], v[124:127], a[68:71]\n"
#define A4j "v_mfma_f32_16x16x32_bf16 a[72:75], a[216:219], v[120:123], a[72:75]\n"
#define A0b "v_mfma_f32_16x16x32_bf16 a[72:75], a[220:223], v[124:127], a[72:75]\n"
#define R7 "v_mfma_f32_16x16x32_bf16 a[76:79], a[224:227], v[120:123], a[76:79]\n"
#define A0H "v_mfma_f32_16x16x32_bf16 a[76:79], a[228:231], v[124:127], a[76:79]\n"
#define A3I "v_mfma_f32_16x16x32_bf16 a[80:83], a[232:235], v[120:123], a[80:83]\n"
#define b9 "v_mfma_f32_16x16x32_bf16 a[80:83], a[236:239], v[124:127], a[80:83]\n"
#define C5n "v_mfma_f32_16x16x32_bf16 v[200:203], v[248:251], a[176:179], v[200:203]\n"
#define C5o "v_mfma_f32_16x16x32_bf16 v[204:207], v[252:255], a[176:179], v[204:207]\n"
#define c9 "v_mfma_f32_16x16x32_bf16 a[84:87], a[216:219], v[120:123], a[84:87]\n"
#define A07 "v_mfma_f32_16x16x32_bf16 a[84:87], a[220:223], v[124:127], a[84:87]\n"
#define Q5 "v_mfma_f32_16x16x32_bf16 a[88:91], a[224:227], v[120:123], a[88:91]\n"
#define m6 "v_mfma_f32_16x16x32_bf16 a[88:91], a[228:231], v[124:127], a[88:91]\n"
#define C5q "v_mfma_f32_16x16x32_bf16 v[200:203], v[248:251], a[180:183], v[200:203]\n"
#define C5r "v_mfma_f32_16x16x32_bf16 v[204:207], v[252:255], a[180:183], v[204:207]\n"
#define A1k "v_mfma_f32_16x16x32_bf16 a[92:95], a[232:235], v[120:123], a[92:95]\n"
#define n6 "v_mfma_f32_16x16x32_bf16 a[92:95], a[236:239], v[124:127], a[92:95]\n"
#define C5s "v_mfma_f32_16x16x32_bf16 v[200:203], v[248:251], a[184:187], v[200:203]\n"
#define C5t "v_mfma_f32_16x16x32_bf16 v[204:207], v[252:255], a[184:187], v[204:207]\n"
#define A6M "buffer_load_dwordx4 v232, s[40:43], 0 offen offset:0 lds\n"
#define A6N "buffer_load_dwordx4 v233, s[40:43], 0 offen offset:0 lds\n"
#define C5u "v_mfma_f32_16x16x32_bf16 v[200:203], v[248:251], a[188:191], v[200:203]\n"
#define C5v "v_mfma_f32_16x16x32_bf16 v[204:207], v[252:255], a[188:191], v[204:207]\n"
#define A6O "buffer_load_dwordx4 v234, s[40:43], 0 offen offset:0 lds\n"
#define A6P "buffer_load_dwordx4 v235, s[40:43], 0 offen offset:0 lds\n"
#define B4K "s_addk_i32 m0, -20064\n"
#define A6Q "buffer_load_dwordx4 v236, s[40:43], 0 offen offset:0 lds\n"
#define D8R "s_waitcnt vmcnt(21)\n"
#define A6R "buffer_load_dwordx4 v237, s[40:43], 0 offen offset:0 lds\n"
#define A6S "buffer_load_dwordx4 v238, s[40:43], 0 offen offset:0 lds\n"
#define A6T "buffer_load_dwordx4 v239, s[40:43], 0 offen offset:0 lds\n"
#define E2e "s_add_u32 m0, s88, 118528\n"
#define B4M "s_add_u32 m0, s88, 4352\n"
#define B6h "s_waitcnt vmcnt(20)\n"
#define C5z "v_and_b32 v246, v200, v227\n"
#define C50 "v_and_b32 v247, v202, v227\n"
#define A0f "s_addk_i32 m0, -8320\n"
#define A9l "s_addk_i32 m0, 12800\n"
#define E2f "ds_read_b64_tr_b16 a[144:145], v219 offset:50688\n"
#define E2g "ds_read_b64_tr_b16 a[146:147], v219 offset:51200\n"
#define C51 "v_cndmask_b32_e64 v188, 0, v246, s[8:9]\n"
#define C52 "v_cndmask_b32_e64 v189, 0, v247, s[8:9]\n"
#define C53 "v_cndmask_b32_e64 v190, 0, v246, s[10:11]\n"
#define E2i "ds_read_b64_tr_b16 a[148:149], v219 offset:50720\n"
#define E2j "ds_read_b64_tr_b16 a[150:151], v219 offset:51232\n"
#define E2k "ds_read_b64_tr_b16 a[152:153], v219 offset:50752\n"
#define C54 "v_cndmask_b32_e64 v191, 0, v247, s[10:11]\n"
#define A6V "v_mfma_f32_16x16x32_bf16 v[112:115], a[144:147], v[184:187], 0\n"
#define E2l "ds_read_b64_tr_b16 a[154:155], v219 offset:51264\n"
#define E2m "ds_read_b64_tr_b16 a[156:157], v219 offset:50784\n"
#define E2n "ds_read_b64_tr_b16 a[158:159], v219 offset:51296\n"
#define A6W "v_mfma_f32_16x16x32_bf16 v[116:119], a[148:151], v[184:187], 0\n"
#define E9E "v_rcp_f32 v210, v194\n"
#define A6X "v_fma_f32 v210, -s94, v210, v210\n"
#define C7 "v_fmac_f32 v112, v144, v210\n"
#define D7 "v_fmac_f32 v113, v145, v210\n"
#define o6 "v_fmac_f32 v114, v146, v210\n"
#define p6 "v_fmac_f32 v115, v147, v210\n"
#define T7 "v_cvt_pk_bf16_f32 v148, v112, v113\n"
#define U7 "v_cvt_pk_bf16_f32 v149, v114, v115\n"
#define A6Y "v_mfma_f32_16x16x32_bf16 v[112:115], a[152:155], v[184:187], 0\n"
#define A3N "ds_write_b64 v220, v[148:149] offset:50688\n"
#define C56 "v_accvgpr_read_b32 v144, a4\n"
#define C57 "v_accvgpr_read_b32 v145, a5\n"
#define E2o "ds_read_b64_tr_b16 a[168:169], v219 offset:33792\n"
#define C58 "v_accvgpr_read_b32 v146, a6\n"
#define C59 "v_accvgpr_read_b32 v147, a7\n"
#define V5 "v_fmac_f32 v116, v144, v210\n"
#define o8 "v_fmac_f32 v117, v145, v210\n"
#define E2p "ds_read_b64_tr_b16 a[170:171], v219 offset:34304\n"
#define E2q "ds_read_b64_tr_b16 a[172:173], v219 offset:33824\n"
#define y7 "v_fmac_f32 v118, v146, v210\n"
#define Z7 "v_fmac_f32 v119, v147, v210\n"
#define M9 "v_cvt_pk_bf16_f32 v150, v116, v117\n"
#define o5 "v_cvt_pk_bf16_f32 v151, v118, v119\n"
#define A6a "v_mfma_f32_16x16x32_bf16 v[116:119], a[156:159], v[184:187], 0\n"
#define A3O "ds_write_b64 v220, v[150:151] offset:50720\n"
#define E2r "ds_read_b64_tr_b16 a[174:175], v219 offset:34336\n"
#define E0C "v_rcp_f32 v211, v195\n"
#define E2s "ds_read_b64_tr_b16 a[176:177], v219 offset:33856\n"
#define A6b "v_mfma_f32_16x16x32_bf16 v[120:123], a[168:171], v[188:191], 0\n"
#define C6A "v_accvgpr_read_b32 v144, a8\n"
#define C6B "v_accvgpr_read_b32 v145, a9\n"
#define E2t "ds_read_b64_tr_b16 a[178:179], v219 offset:34368\n"
#define A6c "v_mfma_f32_16x16x32_bf16 v[124:127], a[172:175], v[188:191], 0\n"
#define C6D "v_accvgpr_read_b32 v146, a10\n"
#define C6E "v_accvgpr_read_b32 v147, a11\n"
#define A3Q "ds_write_b64 v220, v[148:149] offset:50752\n"
#define C6F "v_accvgpr_read_b32 v144, a12\n"
#define C6G "v_accvgpr_read_b32 v145, a13\n"
#define C6H "v_accvgpr_read_b32 v146, a14\n"
#define C6I "v_accvgpr_read_b32 v147, a15\n"
#define E2u "ds_read_b64_tr_b16 a[180:181], v219 offset:33888\n"
#define E2v "ds_read_b64_tr_b16 a[182:183], v219 offset:34400\n"
#define A6e "v_fma_f32 v211, -s94, v211, v211\n"
#define V7 "v_fmac_f32 v120, v152, v211\n"
#define W7 "v_fmac_f32 v121, v153, v211\n"
#define A3R "ds_write_b64 v220, v[150:151] offset:50784\n"
#define e8 "v_fmac_f32 v122, v154, v211\n"
#define f8 "v_fmac_f32 v123, v155, v211\n"
#define X7 "v_cvt_pk_bf16_f32 v156, v120, v121\n"
#define Y7 "v_cvt_pk_bf16_f32 v157, v122, v123\n"
#define A6f "v_mfma_f32_16x16x32_bf16 v[120:123], a[176:179], v[188:191], 0\n"
#define E2w "ds_read_b128 a[0:3], v221 offset:50688\n"
#define E2x "ds_read_b128 a[4:7], v221 offset:51840\n"
#define A3S "ds_write_b64 v220, v[156:157] offset:33792\n"
#define C6J "v_accvgpr_read_b32 v152, a52\n"
#define C6K "v_accvgpr_read_b32 v153, a53\n"
#define C6L "v_accvgpr_read_b32 v154, a54\n"
#define C6M "v_accvgpr_read_b32 v155, a55\n"
#define M8 "v_fmac_f32 v124, v152, v211\n"
#define N8 "v_fmac_f32 v125, v153, v211\n"
#define G9 "v_fmac_f32 v126, v154, v211\n"
#define H9 "v_fmac_f32 v127, v155, v211\n"
#define Q8 "v_cvt_pk_bf16_f32 v158, v124, v125\n"
#define R8 "v_cvt_pk_bf16_f32 v159, v126, v127\n"
#define C6N "v_accvgpr_read_b32 v152, a56\n"
#define A3T "ds_write_b64 v220, v[158:159] offset:33824\n"
#define A6g "v_mfma_f32_16x16x32_bf16 v[124:127], a[180:183], v[188:191], 0\n"
#define C6O "buffer_store_dwordx4 a[0:3], v224, s[44:47], s92 offen offset:0 nt\n"
#define C6P "buffer_store_dwordx4 a[4:7], v224, s[44:47], s92 offen offset:3072 nt\n"
#define C6R "v_accvgpr_read_b32 v153, a57\n"
#define C6S "v_accvgpr_read_b32 v154, a58\n"
#define C6T "v_accvgpr_read_b32 v155, a59\n"
#define E9F "ds_read_b64_tr_b16 a[228:229], v216 offset:4224\n"
#define E9G "ds_read_b64_tr_b16 a[230:231], v216 offset:4736\n"
#define B0x "s_waitcnt vmcnt(18)\n"
#define A3V "ds_write_b64 v220, v[156:157] offset:33856\n"
#define C6U "v_accvgpr_read_b32 v152, a60\n"
#define C6V "v_accvgpr_read_b32 v153, a61\n"
#define C6W "v_accvgpr_read_b32 v154, a62\n"
#define C6X "v_accvgpr_read_b32 v155, a63\n"
#define E9H "ds_read_b64_tr_b16 a[236:237], v216 offset:4256\n"
#define E9I "ds_read_b64_tr_b16 a[238:239], v216 offset:4768\n"
#define A3W "ds_write_b64 v220, v[158:159] offset:33888\n"
#define E2y "ds_read_b128 a[48:51], v221 offset:33792\n"
#define E2z "ds_read_b128 a[52:55], v221 offset:34944\n"
#define B4P "ds_read_b128 a[204:207], v214 offset:64\n"
#define B4Q "ds_read_b128 a[200:203], v214 offset:0\n"
#define e9 "s_addk_i32 s92, 24576\n"
#define D4U "ds_read_b64_tr_b16 a[220:221], v216 offset:4288\n"
#define D4V "ds_read_b64_tr_b16 a[222:223], v216 offset:4800\n"
#define C6Z "buffer_store_dwordx4 a[48:51], v224, s[44:47], s92 offen offset:0 nt\n"
#define D4W "ds_read_b64_tr_b16 a[228:229], v216 offset:4320\n"
#define D4X "ds_read_b64_tr_b16 a[230:231], v216 offset:4832\n"
#define C6a "buffer_store_dwordx4 a[52:55], v224, s[44:47], s92 offen offset:3072 nt\n"
#define D4Y "ds_read_b128 a[200:203], v214 offset:512\n"
#define D4Z "ds_read_b128 a[204:207], v214 offset:576\n"
#define E0D "ds_read_b128 a[208:211], v214 offset:8960\n"
#define E0E "ds_read_b128 a[212:215], v214 offset:9024\n"
#define A6j "buffer_load_dwordx4 v232, s[40:43], 0 offen offset:128 lds\n"
#define B01 "s_waitcnt vmcnt(19)\n"
#define A6k "buffer_load_dwordx4 v233, s[40:43], 0 offen offset:128 lds\n"
#define A6m "buffer_load_dwordx4 v234, s[40:43], 0 offen offset:128 lds\n"
#define A6n "buffer_load_dwordx4 v235, s[40:43], 0 offen offset:128 lds\n"
#define A6o "buffer_load_dwordx4 v236, s[40:43], 0 offen offset:128 lds\n"
#define A6p "buffer_load_dwordx4 v237, s[40:43], 0 offen offset:128 lds\n"
#define A6q "buffer_load_dwordx4 v238, s[40:43], 0 offen offset:128 lds\n"
#define A6r "buffer_load_dwordx4 v239, s[40:43], 0 offen offset:128 lds\n"
#define C6f "v_accvgpr_read_b32 v144, a16\n"
#define C6g "v_accvgpr_read_b32 v145, a17\n"
#define C6h "v_accvgpr_read_b32 v146, a18\n"
#define C6i "v_accvgpr_read_b32 v147, a19\n"
#define C6j "v_accvgpr_read_b32 v152, a64\n"
#define C6k "v_accvgpr_read_b32 v153, a65\n"
#define C6l "v_accvgpr_read_b32 v154, a66\n"
#define C6m "v_accvgpr_read_b32 v155, a67\n"
#define D4a "s_add_u32 m0, s88, 4224\n"
#define A9r "s_addk_i32 m0, 12672\n"
#define D4b "ds_read_b64_tr_b16 a[148:149], v219 offset:50784\n"
#define D4c "ds_read_b64_tr_b16 a[150:151], v219 offset:51296\n"
#define D4d "ds_read_b64_tr_b16 a[160:161], v219 offset:50688\n"
#define D4e "ds_read_b64_tr_b16 a[162:163], v219 offset:51200\n"
#define D4f "ds_read_b64_tr_b16 a[164:165], v219 offset:50720\n"
#define D4g "ds_read_b64_tr_b16 a[166:167], v219 offset:51232\n"
#define D4h "ds_read_b64_tr_b16 a[144:145], v219 offset:50752\n"
#define D4i "ds_read_b64_tr_b16 a[146:147], v219 offset:51264\n"
#define A6u "v_mfma_f32_16x16x32_bf16 v[112:115], a[160:163], v[184:187], 0\n"
#define A6w "v_mfma_f32_16x16x32_bf16 v[116:119], a[164:167], v[184:187], 0\n"
#define D4j "ds_read_b64_tr_b16 a[184:185], v219 offset:33792\n"
#define D4k "ds_read_b64_tr_b16 a[186:187], v219 offset:34304\n"
#define D4l "ds_read_b64_tr_b16 a[188:189], v219 offset:33824\n"
#define D4m "ds_read_b64_tr_b16 a[190:191], v219 offset:34336\n"
#define D4n "ds_read_b64_tr_b16 a[168:169], v219 offset:33856\n"
#define D4o "ds_read_b64_tr_b16 a[170:171], v219 offset:34368\n"
#define D4p "ds_read_b64_tr_b16 a[172:173], v219 offset:33888\n"
#define D4q "ds_read_b64_tr_b16 a[174:175], v219 offset:34400\n"
#define C6o "v_accvgpr_read_b32 v144, a20\n"
#define C6p "v_accvgpr_read_b32 v145, a21\n"
#define C6q "v_accvgpr_read_b32 v146, a22\n"
#define C6r "v_accvgpr_read_b32 v147, a23\n"
#define A6x "v_mfma_f32_16x16x32_bf16 v[120:123], a[184:187], v[188:191], 0\n"
#define C6s "v_accvgpr_read_b32 v144, a24\n"
#define C6t "v_accvgpr_read_b32 v145, a25\n"
#define C6u "v_accvgpr_read_b32 v146, a26\n"
#define C6v "v_accvgpr_read_b32 v147, a27\n"
#define A6y "v_mfma_f32_16x16x32_bf16 v[124:127], a[188:191], v[188:191], 0\n"
#define C6w "v_accvgpr_read_b32 v144, a28\n"
#define C6x "v_accvgpr_read_b32 v145, a29\n"
#define C6y "v_accvgpr_read_b32 v146, a30\n"
#define C6z "v_accvgpr_read_b32 v147, a31\n"
#define D4r "ds_read_b128 a[16:19], v221 offset:50688\n"
#define D4s "ds_read_b128 a[20:23], v221 offset:51840\n"
#define C61 "v_accvgpr_read_b32 v152, a68\n"
#define C62 "v_accvgpr_read_b32 v153, a69\n"
#define A4p "s_addk_i32 s92, -24576\n"
#define C63 "v_accvgpr_read_b32 v154, a70\n"
#define C64 "v_accvgpr_read_b32 v155, a71\n"
#define C65 "buffer_store_dwordx4 a[16:19], v224, s[44:47], s92 offen offset:128 nt\n"
#define C66 "buffer_store_dwordx4 a[20:23], v224, s[44:47], s92 offen offset:3200 nt\n"
#define E20 "s_cbranch_scc1 Lb0_2\n"
#define C67 "v_accvgpr_read_b32 v152, a72\n"
#define C68 "v_accvgpr_read_b32 v153, a73\n"
#define C69 "v_accvgpr_read_b32 v154, a74\n"
#define C7A "v_accvgpr_read_b32 v155, a75\n"
#define C7B "v_accvgpr_read_b32 v152, a76\n"
#define C7C "v_accvgpr_read_b32 v153, a77\n"
#define C7D "v_accvgpr_read_b32 v154, a78\n"
#define C7E "v_accvgpr_read_b32 v155, a79\n"
#define D4t "ds_read_b128 a[64:67], v221 offset:33792\n"
#define D4u "ds_read_b128 a[68:71], v221 offset:34944\n"
#define C7F "v_accvgpr_read_b32 v144, a32\n"
#define C7G "v_accvgpr_read_b32 v145, a33\n"
#define C7H "buffer_store_dwordx4 a[64:67], v224, s[44:47], s92 offen offset:128 nt\n"
#define C7I "buffer_store_dwordx4 a[68:71], v224, s[44:47], s92 offen offset:3200 nt\n"
#define D4w "ds_read_b128 a[192:195], v214 offset:512\n"
#define D4x "ds_read_b128 a[196:199], v214 offset:576\n"
#define A60 "buffer_load_dwordx4 v232, s[40:43], 0 offen offset:256 lds\n"
#define A61 "buffer_load_dwordx4 v233, s[40:43], 0 offen offset:256 lds\n"
#define A62 "buffer_load_dwordx4 v234, s[40:43], 0 offen offset:256 lds\n"
#define A63 "buffer_load_dwordx4 v235, s[40:43], 0 offen offset:256 lds\n"
#define A64 "buffer_load_dwordx4 v236, s[40:43], 0 offen offset:256 lds\n"
#define A65 "buffer_load_dwordx4 v237, s[40:43], 0 offen offset:256 lds\n"
#define A66 "buffer_load_dwordx4 v238, s[40:43], 0 offen offset:256 lds\n"
#define A67 "buffer_load_dwordx4 v239, s[40:43], 0 offen offset:256 lds\n"
#define A68 "ds_read_b128 v[232:235], v229 offset:0\n"
#define E0F "s_waitcnt vmcnt(24)\n"
#define C7N "v_accvgpr_read_b32 v146, a34\n"
#define C7O "v_accvgpr_read_b32 v147, a35\n"
#define C7P "v_accvgpr_read_b32 v152, a80\n"
#define A69 "ds_read_b128 v[236:239], v229 offset:128\n"
#define C7Q "v_accvgpr_read_b32 v153, a81\n"
#define D42 "ds_read_b64_tr_b16 a[166:167], v219 offset:51296\n"
#define D43 "ds_read_b64_tr_b16 a[152:153], v219 offset:50688\n"
#define D44 "ds_read_b64_tr_b16 a[154:155], v219 offset:51200\n"
#define D45 "ds_read_b64_tr_b16 a[156:157], v219 offset:50720\n"
#define D46 "ds_read_b64_tr_b16 a[158:159], v219 offset:51232\n"
#define D47 "ds_read_b64_tr_b16 a[160:161], v219 offset:50752\n"
#define D48 "ds_read_b64_tr_b16 a[162:163], v219 offset:51264\n"
#define D49 "ds_read_b64_tr_b16 a[164:165], v219 offset:50784\n"
#define O6 "s_waitcnt vmcnt(9)\n"
#define D5B "ds_read_b64_tr_b16 a[176:177], v219 offset:33792\n"
#define D5C "ds_read_b64_tr_b16 a[178:179], v219 offset:34304\n"
#define D5D "ds_read_b64_tr_b16 a[180:181], v219 offset:33824\n"
#define D5E "ds_read_b64_tr_b16 a[182:183], v219 offset:34336\n"
#define D5F "ds_read_b64_tr_b16 a[184:185], v219 offset:33856\n"
#define D5G "ds_read_b64_tr_b16 a[186:187], v219 offset:34368\n"
#define D5H "ds_read_b64_tr_b16 a[188:189], v219 offset:33888\n"
#define D5I "ds_read_b64_tr_b16 a[190:191], v219 offset:34400\n"
#define C7S "v_accvgpr_read_b32 v144, a36\n"
#define C7T "v_accvgpr_read_b32 v145, a37\n"
#define C7U "v_accvgpr_read_b32 v146, a38\n"
#define C7V "v_accvgpr_read_b32 v147, a39\n"
#define E9J "s_mov_b32 s90, 417792\n"
#define C7W "v_accvgpr_read_b32 v144, a40\n"
#define C7X "v_accvgpr_read_b32 v145, a41\n"
#define C7Y "v_accvgpr_read_b32 v146, a42\n"
#define C7Z "v_accvgpr_read_b32 v147, a43\n"
#define C7a "v_accvgpr_read_b32 v144, a44\n"
#define C7b "v_accvgpr_read_b32 v145, a45\n"
#define C7c "v_accvgpr_read_b32 v146, a46\n"
#define C7d "v_accvgpr_read_b32 v147, a47\n"
#define C7e "v_accvgpr_read_b32 v154, a82\n"
#define C7f "v_accvgpr_read_b32 v155, a83\n"
#define D5J "ds_read_b128 a[32:35], v221 offset:50688\n"
#define D5K "ds_read_b128 a[36:39], v221 offset:51840\n"
#define C7g "v_accvgpr_read_b32 v152, a84\n"
#define C7h "v_accvgpr_read_b32 v153, a85\n"
#define C7i "v_accvgpr_read_b32 v154, a86\n"
#define C7j "v_accvgpr_read_b32 v155, a87\n"
#define C7k "buffer_store_dwordx4 a[32:35], v224, s[44:47], s92 offen offset:256 nt\n"
#define C7l "buffer_store_dwordx4 a[36:39], v224, s[44:47], s92 offen offset:3328 nt\n"
#define B2r "s_addk_i32 m0, 8448\n"
#define C7m "v_accvgpr_read_b32 v152, a88\n"
#define C7n "v_accvgpr_read_b32 v153, a89\n"
#define C7o "v_accvgpr_read_b32 v154, a90\n"
#define C7p "v_accvgpr_read_b32 v155, a91\n"
#define C7q "v_accvgpr_read_b32 v152, a92\n"
#define C7r "v_accvgpr_read_b32 v153, a93\n"
#define C7s "v_accvgpr_read_b32 v154, a94\n"
#define C7t "v_accvgpr_read_b32 v155, a95\n"
#define D5L "ds_read_b128 a[80:83], v221 offset:33792\n"
#define D5M "ds_read_b128 a[84:87], v221 offset:34944\n"
#define C7u "buffer_store_dwordx4 a[80:83], v224, s[44:47], s92 offen offset:256 nt\n"
#define C7w "buffer_store_dwordx4 a[84:87], v224, s[44:47], s92 offen offset:3328 nt\n"
#define F4G "s_add_u32 m0, s88, 101504\n"
#define A90 "s_waitcnt vmcnt(22)\n"
#define E9K "s_mov_b32 s91, 417792\n"
#define E9L "s_mov_b32 s95, 430080\n"
#define C7z "v_mfma_f32_16x16x32_bf16 v[200:203], v[248:251], a[96:99], 0\n"
#define C70 "v_mfma_f32_16x16x32_bf16 v[204:207], v[252:255], a[96:99], 0\n"
#define F4I "ds_read_b64_tr_b16 a[228:229], v217 offset:42240\n"
#define F4J "ds_read_b64_tr_b16 a[230:231], v217 offset:42752\n"
#define F4K "ds_read_b64_tr_b16 a[236:237], v217 offset:42272\n"
#define F4L "ds_read_b64_tr_b16 a[238:239], v217 offset:42784\n"
#define C73 "v_mfma_f32_16x16x32_bf16 v[200:203], v[248:251], a[100:103], v[200:203]\n"
#define C74 "v_mfma_f32_16x16x32_bf16 v[204:207], v[252:255], a[100:103], v[204:207]\n"
#define D5O "ds_read_b128 a[212:215], v215 offset:46528\n"
#define D5P "ds_read_b128 a[208:211], v215 offset:46464\n"
#define E22 "ds_read_b128 a[204:207], v215 offset:38080\n"
#define E23 "ds_read_b128 a[200:203], v215 offset:38016\n"
#define C75 "v_mfma_f32_16x16x32_bf16 v[200:203], v[248:251], a[104:107], v[200:203]\n"
#define C76 "v_mfma_f32_16x16x32_bf16 v[204:207], v[252:255], a[104:107], v[204:207]\n"
#define F4M "ds_read_b64_tr_b16 a[220:221], v217 offset:42304\n"
#define F4N "ds_read_b64_tr_b16 a[222:223], v217 offset:42816\n"
#define F4O "ds_read_b64_tr_b16 a[228:229], v217 offset:42336\n"
#define F4P "ds_read_b64_tr_b16 a[230:231], v217 offset:42848\n"
#define C78 "v_mfma_f32_16x16x32_bf16 v[200:203], v[248:251], a[108:111], v[200:203]\n"
#define C79 "v_mfma_f32_16x16x32_bf16 v[204:207], v[252:255], a[108:111], v[204:207]\n"
#define E9M "ds_read_b128 a[200:203], v215 offset:38528\n"
#define E9N "ds_read_b128 a[204:207], v215 offset:38592\n"
#define E0J "ds_read_b128 a[208:211], v215 offset:46976\n"
#define E0K "ds_read_b128 a[212:215], v215 offset:47040\n"
#define E0L "s_add_u32 m0, s88, 88960\n"
#define C8C "v_mfma_f32_16x16x32_bf16 v[200:203], v[248:251], a[112:115], v[200:203]\n"
#define C8D "v_mfma_f32_16x16x32_bf16 v[204:207], v[252:255], a[112:115], v[204:207]\n"
#define C8E "v_mfma_f32_16x16x32_bf16 v[200:203], v[248:251], a[116:119], v[200:203]\n"
#define C8F "v_mfma_f32_16x16x32_bf16 v[204:207], v[252:255], a[116:119], v[204:207]\n"
#define C8H "v_mfma_f32_16x16x32_bf16 v[204:207], v[252:255], a[120:123], 0\n"
#define C8I "v_and_b32 v240, v200, v227\n"
#define C8J "v_and_b32 v241, v202, v227\n"
#define C8L "v_mfma_f32_16x16x32_bf16 v[200:203], v[248:251], a[120:123], 0\n"
#define C8M "v_mfma_f32_16x16x32_bf16 v[204:207], v[252:255], a[124:127], v[204:207]\n"
#define C8N "v_mfma_f32_16x16x32_bf16 v[200:203], v[248:251], a[124:127], v[200:203]\n"
#define E9Q "s_add_u32 m0, s88, 42496\n"
#define E0M "ds_read_b128 a[196:199], v215 offset:38080\n"
#define E0N "ds_read_b128 a[192:195], v215 offset:38016\n"
#define E9R "ds_read_b128 a[192:195], v215 offset:38528\n"
#define E9S "ds_read_b128 a[196:199], v215 offset:38592\n"
#define C8S "v_mfma_f32_16x16x32_bf16 v[200:203], v[248:251], a[128:131], v[200:203]\n"
#define C8T "v_mfma_f32_16x16x32_bf16 v[204:207], v[252:255], a[128:131], v[204:207]\n"
#define E24 "s_cbranch_scc1 Lb0_3\n"
#define C8U "v_mfma_f32_16x16x32_bf16 v[200:203], v[248:251], a[132:135], v[200:203]\n"
#define C8V "v_mfma_f32_16x16x32_bf16 v[204:207], v[252:255], a[132:135], v[204:207]\n"
#define C8X "v_mfma_f32_16x16x32_bf16 v[200:203], v[248:251], a[136:139], v[200:203]\n"
#define C8Y "v_mfma_f32_16x16x32_bf16 v[204:207], v[252:255], a[136:139], v[204:207]\n"
#define A1D "s_addk_i32 m0, -16896\n"
#define C8Z "v_mfma_f32_16x16x32_bf16 v[200:203], v[248:251], a[140:143], v[200:203]\n"
#define C8a "v_mfma_f32_16x16x32_bf16 v[204:207], v[252:255], a[140:143], v[204:207]\n"
#define A1E "s_addk_i32 m0, 14784\n"
#define C8b "v_and_b32 v242, v200, v227\n"
#define C8c "v_and_b32 v243, v202, v227\n"
#define C8d "v_cndmask_b32_e64 v248, 0, v242, s[8:9]\n"
#define C8e "v_cndmask_b32_e64 v249, 0, v243, s[8:9]\n"
#define C8f "v_cndmask_b32_e64 v250, 0, v242, s[10:11]\n"
#define C8g "v_cndmask_b32_e64 v251, 0, v243, s[10:11]\n"
#define D5S "ds_read_b64_tr_b16 a[48:49], v219 offset:33792\n"
#define D5T "ds_read_b64_tr_b16 a[50:51], v219 offset:34304\n"
#define D5U "ds_read_b64_tr_b16 a[52:53], v219 offset:33824\n"
#define D5V "ds_read_b64_tr_b16 a[54:55], v219 offset:34336\n"
#define D5W "ds_read_b64_tr_b16 a[56:57], v219 offset:33856\n"
#define D5X "ds_read_b64_tr_b16 a[58:59], v219 offset:34368\n"
#define D5Y "ds_read_b64_tr_b16 a[60:61], v219 offset:33888\n"
#define D5Z "ds_read_b64_tr_b16 a[62:63], v219 offset:34400\n"
#define D5a "ds_read_b64_tr_b16 a[0:1], v219 offset:50688\n"
#define D5b "ds_read_b64_tr_b16 a[2:3], v219 offset:51200\n"
#define D5c "ds_read_b64_tr_b16 a[4:5], v219 offset:50720\n"
#define D5d "ds_read_b64_tr_b16 a[6:7], v219 offset:51232\n"
#define D5e "ds_read_b64_tr_b16 a[8:9], v219 offset:50752\n"
#define D5f "ds_read_b64_tr_b16 a[10:11], v219 offset:51264\n"
#define D5g "ds_read_b64_tr_b16 a[12:13], v219 offset:50784\n"
#define D5h "ds_read_b64_tr_b16 a[14:15], v219 offset:51296\n"
#define C8h "v_mfma_f32_16x16x32_bf16 v[112:115], a[48:51], v[248:251], 0\n"
#define C8i "v_mfma_f32_16x16x32_bf16 v[116:119], a[52:55], v[248:251], 0\n"
#define C8j "v_mfma_f32_16x16x32_bf16 v[120:123], a[56:59], v[248:251], 0\n"
#define C8k "v_mfma_f32_16x16x32_bf16 v[124:127], a[60:63], v[248:251], 0\n"
#define D5i "s_add_u32 m0, s88, 38272\n"
#define A1G "s_waitcnt vmcnt(17)\n"
#define D5k "ds_read_b64_tr_b16 a[64:65], v219 offset:33792\n"
#define D5l "ds_read_b64_tr_b16 a[66:67], v219 offset:34304\n"
#define D5m "ds_read_b64_tr_b16 a[68:69], v219 offset:33824\n"
#define D5n "ds_read_b64_tr_b16 a[70:71], v219 offset:34336\n"
#define D5o "ds_read_b64_tr_b16 a[72:73], v219 offset:33856\n"
#define D5p "ds_read_b64_tr_b16 a[74:75], v219 offset:34368\n"
#define D5q "ds_read_b64_tr_b16 a[76:77], v219 offset:33888\n"
#define D5r "ds_read_b64_tr_b16 a[78:79], v219 offset:34400\n"
#define D5u "ds_read_b64_tr_b16 a[20:21], v219 offset:50720\n"
#define D5v "ds_read_b64_tr_b16 a[22:23], v219 offset:51232\n"
#define D5w "ds_read_b64_tr_b16 a[24:25], v219 offset:50752\n"
#define D5x "ds_read_b64_tr_b16 a[26:27], v219 offset:51264\n"
#define D5y "ds_read_b64_tr_b16 a[28:29], v219 offset:50784\n"
#define D5z "ds_read_b64_tr_b16 a[30:31], v219 offset:51296\n"
#define D50 "ds_read_b64_tr_b16 a[16:17], v219 offset:50688\n"
#define D51 "ds_read_b64_tr_b16 a[18:19], v219 offset:51200\n"
#define C8n "v_mfma_f32_16x16x32_bf16 v[144:147], a[64:67], v[248:251], 0\n"
#define C8o "v_mfma_f32_16x16x32_bf16 v[148:151], a[68:71], v[248:251], 0\n"
#define C8p "v_mfma_f32_16x16x32_bf16 v[152:155], a[72:75], v[248:251], 0\n"
#define C8q "v_mfma_f32_16x16x32_bf16 v[156:159], a[76:79], v[248:251], 0\n"
#define E26 "s_add_u32 m0, s88, 12928\n"
#define D52 "ds_read_b64_tr_b16 a[80:81], v219 offset:33792\n"
#define D53 "ds_read_b64_tr_b16 a[82:83], v219 offset:34304\n"
#define D54 "ds_read_b64_tr_b16 a[84:85], v219 offset:33824\n"
#define D55 "ds_read_b64_tr_b16 a[86:87], v219 offset:34336\n"
#define D56 "ds_read_b64_tr_b16 a[88:89], v219 offset:33856\n"
#define D57 "ds_read_b64_tr_b16 a[90:91], v219 offset:34368\n"
#define D58 "ds_read_b64_tr_b16 a[92:93], v219 offset:33888\n"
#define D59 "ds_read_b64_tr_b16 a[94:95], v219 offset:34400\n"
#define D6A "ds_read_b64_tr_b16 a[32:33], v219 offset:50688\n"
#define D6B "ds_read_b64_tr_b16 a[34:35], v219 offset:51200\n"
#define D6C "ds_read_b64_tr_b16 a[36:37], v219 offset:50720\n"
#define D6D "ds_read_b64_tr_b16 a[38:39], v219 offset:51232\n"
#define D6E "ds_read_b64_tr_b16 a[40:41], v219 offset:50752\n"
#define D6F "ds_read_b64_tr_b16 a[42:43], v219 offset:51264\n"
#define D6G "ds_read_b64_tr_b16 a[44:45], v219 offset:50784\n"
#define D6H "ds_read_b64_tr_b16 a[46:47], v219 offset:51296\n"
#define C8u "v_mfma_f32_16x16x32_bf16 v[184:187], a[80:83], v[248:251], 0\n"
#define C8v "v_mfma_f32_16x16x32_bf16 v[188:191], a[84:87], v[248:251], 0\n"
#define C8w "v_mfma_f32_16x16x32_bf16 v[200:203], a[88:91], v[248:251], 0\n"
#define C8x "v_mfma_f32_16x16x32_bf16 v[204:207], a[92:95], v[248:251], 0\n"
#define F4R "ds_read_b128 a[144:147], v215 offset:38528\n"
#define F4S "ds_read_b128 a[148:151], v215 offset:38592\n"
#define D8T "ds_read_b128 a[152:155], v215 offset:46976\n"
#define D8U "ds_read_b128 a[156:159], v215 offset:47040\n"
#define E0O "ds_read_b128 a[160:163], v215 offset:42752\n"
#define E0P "ds_read_b128 a[164:167], v215 offset:42816\n"
#define E0Q "ds_read_b64_tr_b16 a[168:169], v217 offset:50784\n"
#define E0R "ds_read_b64_tr_b16 a[170:171], v217 offset:51296\n"
#define E0S "ds_read_b64_tr_b16 a[172:173], v217 offset:55008\n"
#define E0T "ds_read_b64_tr_b16 a[174:175], v217 offset:55520\n"
#define P6 "v_mfma_f32_16x16x32_bf16 v[168:171], a[144:147], a[96:99], v[176:179]\n"
#define l4 "v_mfma_f32_16x16x32_bf16 v[168:171], a[148:151], a[100:103], v[168:171]\n"
#define E0U "ds_read_b64_tr_b16 a[176:177], v217 offset:59136\n"
#define E0V "ds_read_b64_tr_b16 a[178:179], v217 offset:59648\n"
#define r4 "v_mfma_f32_16x16x32_bf16 v[168:171], a[152:155], a[104:107], v[168:171]\n"
#define s4 "v_mfma_f32_16x16x32_bf16 v[168:171], a[156:159], a[108:111], v[168:171]\n"
#define E27 "ds_read_b64_tr_b16 a[180:181], v216 offset:4224\n"
#define E28 "ds_read_b64_tr_b16 a[182:183], v216 offset:4736\n"
#define e5 "v_mfma_f32_16x16x32_bf16 v[168:171], a[160:163], a[112:115], v[168:171]\n"
#define g9 "v_mfma_f32_16x16x32_bf16 v[168:171], a[164:167], a[116:119], v[168:171]\n"
#define A11 "v_mfma_f32_16x16x32_bf16 v[172:175], a[164:167], a[140:143], v[180:183]\n"
#define E0W "ds_read_b64_tr_b16 a[184:185], v217 offset:59168\n"
#define E0X "ds_read_b64_tr_b16 a[186:187], v217 offset:59680\n"
#define A2h "v_mfma_f32_16x16x32_bf16 v[172:175], a[160:163], a[136:139], v[172:175]\n"
#define g8 "v_mfma_f32_16x16x32_bf16 v[172:175], a[156:159], a[132:135], v[172:175]\n"
#define E29 "ds_read_b64_tr_b16 a[188:189], v216 offset:4256\n"
#define E3A "ds_read_b64_tr_b16 a[190:191], v216 offset:4768\n"
#define f5 "v_mfma_f32_16x16x32_bf16 v[172:175], a[152:155], a[128:131], v[172:175]\n"
#define k7 "v_mfma_f32_16x16x32_bf16 v[172:175], a[148:151], a[124:127], v[172:175]\n"
#define h8 "v_mfma_f32_16x16x32_bf16 v[172:175], a[144:147], a[120:123], v[172:175]\n"
#define D7F "v_mfma_f32_16x16x32_bf16 v[12:15], a[168:171], v[96:99], v[12:15]\n"
#define A4v "v_mfma_f32_16x16x32_bf16 v[12:15], a[172:175], v[100:103], v[12:15]\n"
#define A4w "v_mfma_f32_16x16x32_bf16 v[60:63], a[172:175], v[108:111], v[60:63]\n"
#define B6n "v_mfma_f32_16x16x32_bf16 v[60:63], a[168:171], v[104:107], v[60:63]\n"
#define A4x "v_mfma_f32_16x16x32_bf16 v[16:19], a[176:179], v[96:99], v[16:19]\n"
#define H1B "v_mfma_f32_16x16x32_bf16 v[16:19], a[180:183], v[100:103], v[16:19]\n"
#define B9W "v_mfma_f32_16x16x32_bf16 v[64:67], a[180:183], v[108:111], v[64:67]\n"
#define B9X "v_mfma_f32_16x16x32_bf16 v[64:67], a[176:179], v[104:107], v[64:67]\n"
#define A4y "v_mfma_f32_16x16x32_bf16 v[20:23], a[184:187], v[96:99], v[20:23]\n"
#define B6o "v_mfma_f32_16x16x32_bf16 v[20:23], a[188:191], v[100:103], v[20:23]\n"
#define A4z "v_mfma_f32_16x16x32_bf16 v[68:71], a[188:191], v[108:111], v[68:71]\n"
#define A40 "v_mfma_f32_16x16x32_bf16 v[68:71], a[184:187], v[104:107], v[68:71]\n"
#define D8V "ds_read_b128 a[144:147], v214 offset:13184\n"
#define D8W "ds_read_b128 a[148:151], v214 offset:13248\n"
#define E3B "ds_read_b128 a[152:155], v214 offset:512\n"
#define E3C "ds_read_b128 a[156:159], v214 offset:576\n"
#define E3D "ds_read_b128 a[160:163], v214 offset:8960\n"
#define E3E "ds_read_b128 a[164:167], v214 offset:9024\n"
#define D6I "ds_read_b64_tr_b16 a[168:169], v216 offset:16928\n"
#define D6J "ds_read_b64_tr_b16 a[170:171], v216 offset:17440\n"
#define D6K "ds_read_b64_tr_b16 a[172:173], v216 offset:21152\n"
#define D6L "ds_read_b64_tr_b16 a[174:175], v216 offset:21664\n"
#define D6M "ds_read_b64_tr_b16 a[176:177], v216 offset:16960\n"
#define D6N "ds_read_b64_tr_b16 a[178:179], v216 offset:17472\n"
#define D6O "ds_read_b64_tr_b16 a[180:181], v216 offset:21184\n"
#define D6P "ds_read_b64_tr_b16 a[182:183], v216 offset:21696\n"
#define D6Q "ds_read_b64_tr_b16 a[184:185], v216 offset:16992\n"
#define D6R "ds_read_b64_tr_b16 a[186:187], v216 offset:17504\n"
#define D6S "ds_read_b64_tr_b16 a[188:189], v216 offset:21216\n"
#define D6T "ds_read_b64_tr_b16 a[190:191], v216 offset:21728\n"
#define A41 "v_mfma_f32_16x16x32_bf16 v[36:39], a[168:171], v[96:99], v[36:39]\n"
#define B6r "v_mfma_f32_16x16x32_bf16 v[36:39], a[172:175], v[100:103], v[36:39]\n"
#define B6s "v_mfma_f32_16x16x32_bf16 v[84:87], a[172:175], v[108:111], v[84:87]\n"
#define B6t "v_mfma_f32_16x16x32_bf16 v[84:87], a[168:171], v[104:107], v[84:87]\n"
#define B6u "v_mfma_f32_16x16x32_bf16 v[40:43], a[176:179], v[96:99], v[40:43]\n"
#define B6v "v_mfma_f32_16x16x32_bf16 v[40:43], a[180:183], v[100:103], v[40:43]\n"
#define B6w "v_mfma_f32_16x16x32_bf16 v[88:91], a[180:183], v[108:111], v[88:91]\n"
#define A42 "v_mfma_f32_16x16x32_bf16 v[88:91], a[176:179], v[104:107], v[88:91]\n"
#define A43 "v_mfma_f32_16x16x32_bf16 v[44:47], a[184:187], v[96:99], v[44:47]\n"
#define A44 "v_mfma_f32_16x16x32_bf16 v[44:47], a[188:191], v[100:103], v[44:47]\n"
#define A45 "v_mfma_f32_16x16x32_bf16 v[92:95], a[188:191], v[108:111], v[92:95]\n"
#define A46 "v_mfma_f32_16x16x32_bf16 v[92:95], a[184:187], v[104:107], v[92:95]\n"
#define E3F "ds_read_b128 a[144:147], v214 offset:34304\n"
#define E3G "ds_read_b128 a[148:151], v214 offset:34368\n"
#define E3H "ds_read_b128 a[152:155], v214 offset:30080\n"
#define E3I "ds_read_b128 a[156:159], v214 offset:30144\n"
#define E3J "ds_read_b128 a[160:163], v214 offset:25856\n"
#define E3K "ds_read_b128 a[164:167], v214 offset:25920\n"
#define E3L "ds_read_b64_tr_b16 a[168:169], v216 offset:38112\n"
#define E3M "ds_read_b64_tr_b16 a[170:171], v216 offset:38624\n"
#define E3N "ds_read_b64_tr_b16 a[172:173], v216 offset:42336\n"
#define E3O "ds_read_b64_tr_b16 a[174:175], v216 offset:42848\n"
#define E3P "ds_read_b64_tr_b16 a[176:177], v216 offset:46464\n"
#define E3Q "ds_read_b64_tr_b16 a[178:179], v216 offset:46976\n"
#define E9T "ds_read_b64_tr_b16 a[180:181], v216 offset:50688\n"
#define E9U "ds_read_b64_tr_b16 a[182:183], v216 offset:51200\n"
#define E3R "ds_read_b64_tr_b16 a[184:185], v216 offset:46496\n"
#define E3S "ds_read_b64_tr_b16 a[186:187], v216 offset:47008\n"
#define E9V "ds_read_b64_tr_b16 a[188:189], v216 offset:50720\n"
#define E9W "ds_read_b64_tr_b16 a[190:191], v216 offset:51232\n"
#define B1F "v_mfma_f32_16x16x32_bf16 v[12:15], a[168:171], v[128:131], v[12:15]\n"
#define B1G "v_mfma_f32_16x16x32_bf16 v[12:15], a[172:175], v[132:135], v[12:15]\n"
#define B1H "v_mfma_f32_16x16x32_bf16 v[60:63], a[172:175], v[140:143], v[60:63]\n"
#define B1I "v_mfma_f32_16x16x32_bf16 v[60:63], a[168:171], v[136:139], v[60:63]\n"
#define B1J "v_mfma_f32_16x16x32_bf16 v[16:19], a[176:179], v[128:131], v[16:19]\n"
#define B1K "v_mfma_f32_16x16x32_bf16 v[16:19], a[180:183], v[132:135], v[16:19]\n"
#define B1L "v_mfma_f32_16x16x32_bf16 v[64:67], a[180:183], v[140:143], v[64:67]\n"
#define B1M "v_mfma_f32_16x16x32_bf16 v[64:67], a[176:179], v[136:139], v[64:67]\n"
#define B1N "v_mfma_f32_16x16x32_bf16 v[20:23], a[184:187], v[128:131], v[20:23]\n"
#define B1O "v_mfma_f32_16x16x32_bf16 v[20:23], a[188:191], v[132:135], v[20:23]\n"
#define B1P "v_mfma_f32_16x16x32_bf16 v[68:71], a[188:191], v[140:143], v[68:71]\n"
#define B1Q "v_mfma_f32_16x16x32_bf16 v[68:71], a[184:187], v[136:139], v[68:71]\n"
#define E3T "ds_read_b128 a[144:147], v215 offset:512\n"
#define E3U "ds_read_b128 a[148:151], v215 offset:576\n"
#define E0Y "ds_read_b128 a[152:155], v214 offset:59648\n"
#define E0Z "ds_read_b128 a[156:159], v214 offset:59712\n"
#define E3V "ds_read_b128 a[160:163], v214 offset:55424\n"
#define E3W "ds_read_b128 a[164:167], v214 offset:55488\n"
#define E0a "ds_read_b64_tr_b16 a[168:169], v217 offset:4256\n"
#define E0b "ds_read_b64_tr_b16 a[170:171], v217 offset:4768\n"
#define E0c "ds_read_b64_tr_b16 a[172:173], v217 offset:8480\n"
#define E0d "ds_read_b64_tr_b16 a[174:175], v217 offset:8992\n"
#define E0e "ds_read_b64_tr_b16 a[176:177], v217 offset:4288\n"
#define E0f "ds_read_b64_tr_b16 a[178:179], v217 offset:4800\n"
#define E0g "ds_read_b64_tr_b16 a[180:181], v217 offset:8512\n"
#define E0h "ds_read_b64_tr_b16 a[182:183], v217 offset:9024\n"
#define E0i "ds_read_b64_tr_b16 a[184:185], v217 offset:4320\n"
#define E0j "ds_read_b64_tr_b16 a[186:187], v217 offset:4832\n"
#define E0k "ds_read_b64_tr_b16 a[188:189], v217 offset:8544\n"
#define E0l "ds_read_b64_tr_b16 a[190:191], v217 offset:9056\n"
#define A8L "v_mfma_f32_16x16x32_bf16 v[36:39], a[168:171], v[128:131], v[36:39]\n"
#define A8M "v_mfma_f32_16x16x32_bf16 v[36:39], a[172:175], v[132:135], v[36:39]\n"
#define A7D "v_mfma_f32_16x16x32_bf16 v[84:87], a[172:175], v[140:143], v[84:87]\n"
#define A7E "v_mfma_f32_16x16x32_bf16 v[84:87], a[168:171], v[136:139], v[84:87]\n"
#define A8N "v_mfma_f32_16x16x32_bf16 v[40:43], a[176:179], v[128:131], v[40:43]\n"
#define D7E "v_mfma_f32_16x16x32_bf16 v[40:43], a[180:183], v[132:135], v[40:43]\n"
#define D22 "v_mfma_f32_16x16x32_bf16 v[88:91], a[180:183], v[140:143], v[88:91]\n"
#define A7F "v_mfma_f32_16x16x32_bf16 v[88:91], a[176:179], v[136:139], v[88:91]\n"
#define A8O "v_mfma_f32_16x16x32_bf16 v[44:47], a[184:187], v[128:131], v[44:47]\n"
#define A8P "v_mfma_f32_16x16x32_bf16 v[44:47], a[188:191], v[132:135], v[44:47]\n"
#define A7G "v_mfma_f32_16x16x32_bf16 v[92:95], a[188:191], v[140:143], v[92:95]\n"
#define A7H "v_mfma_f32_16x16x32_bf16 v[92:95], a[184:187], v[136:139], v[92:95]\n"
#define E3X "ds_read_b128 a[144:147], v215 offset:21632\n"
#define E3Y "ds_read_b128 a[148:151], v215 offset:21696\n"
#define E0m "ds_read_b128 a[152:155], v215 offset:17408\n"
#define E0n "ds_read_b128 a[156:159], v215 offset:17472\n"
#define E0o "ds_read_b128 a[160:163], v215 offset:13184\n"
#define E0p "ds_read_b128 a[164:167], v215 offset:13248\n"
#define E0q "ds_read_b64_tr_b16 a[168:169], v217 offset:25440\n"
#define E0r "ds_read_b64_tr_b16 a[170:171], v217 offset:25952\n"
#define E0s "ds_read_b64_tr_b16 a[172:173], v217 offset:29664\n"
#define E0t "ds_read_b64_tr_b16 a[174:175], v217 offset:30176\n"
#define E0u "ds_read_b64_tr_b16 a[176:177], v217 offset:33792\n"
#define E0v "ds_read_b64_tr_b16 a[178:179], v217 offset:34304\n"
#define F4T "ds_read_b64_tr_b16 a[180:181], v217 offset:46464\n"
#define F4U "ds_read_b64_tr_b16 a[182:183], v217 offset:46976\n"
#define E0w "ds_read_b64_tr_b16 a[184:185], v217 offset:33824\n"
#define E0x "ds_read_b64_tr_b16 a[186:187], v217 offset:34336\n"
#define F4V "ds_read_b64_tr_b16 a[188:189], v217 offset:46496\n"
#define F4W "ds_read_b64_tr_b16 a[190:191], v217 offset:47008\n"
#define F4X "ds_read_b64_tr_b16 a[220:221], v217 offset:46528\n"
#define F4Y "ds_read_b64_tr_b16 a[222:223], v217 offset:47040\n"
#define F4Z "ds_read_b64_tr_b16 a[228:229], v217 offset:46560\n"
#define F4a "ds_read_b64_tr_b16 a[230:231], v217 offset:47072\n"
#define D8Y "ds_read_b128 a[144:147], v215 offset:51200\n"
#define D8Z "ds_read_b128 a[148:151], v215 offset:51264\n"
#define E3Z "ds_read_b128 a[152:155], v215 offset:42752\n"
#define E3a "ds_read_b128 a[156:159], v215 offset:42816\n"
#define E3b "ds_read_b128 a[160:163], v215 offset:38528\n"
#define E3c "ds_read_b128 a[164:167], v215 offset:38592\n"
#define D8a "ds_read_b64_tr_b16 a[168:169], v217 offset:54944\n"
#define D8b "ds_read_b64_tr_b16 a[170:171], v217 offset:55456\n"
#define D8c "ds_read_b64_tr_b16 a[172:173], v217 offset:59168\n"
#define D8d "ds_read_b64_tr_b16 a[174:175], v217 offset:59680\n"
#define D8e "ds_read_b64_tr_b16 a[176:177], v217 offset:54976\n"
#define D8f "ds_read_b64_tr_b16 a[178:179], v217 offset:55488\n"
#define D8g "ds_read_b64_tr_b16 a[180:181], v217 offset:59200\n"
#define D8h "ds_read_b64_tr_b16 a[182:183], v217 offset:59712\n"
#define D8i "ds_read_b64_tr_b16 a[184:185], v217 offset:55008\n"
#define D8j "ds_read_b64_tr_b16 a[186:187], v217 offset:55520\n"
#define D8k "ds_read_b64_tr_b16 a[188:189], v217 offset:59232\n"
#define D8l "ds_read_b64_tr_b16 a[190:191], v217 offset:59744\n"
#define F4b "ds_read_b128 a[144:147], v214 offset:512\n"
#define F4c "ds_read_b128 a[148:151], v214 offset:576\n"
#define E3d "ds_read_b128 a[152:155], v214 offset:8960\n"
#define E3e "ds_read_b128 a[156:159], v214 offset:9024\n"
#define E3f "ds_read_b128 a[160:163], v214 offset:4736\n"
#define E3g "ds_read_b128 a[164:167], v214 offset:4800\n"
#define C82 "ds_read_b64_tr_b16 a[168:169], v216 offset:12768\n"
#define C83 "ds_read_b64_tr_b16 a[170:171], v216 offset:13280\n"
#define C84 "ds_read_b64_tr_b16 a[172:173], v216 offset:16992\n"
#define C85 "ds_read_b64_tr_b16 a[174:175], v216 offset:17504\n"
#define C86 "ds_read_b64_tr_b16 a[176:177], v216 offset:21120\n"
#define C87 "ds_read_b64_tr_b16 a[178:179], v216 offset:21632\n"
#define C88 "ds_read_b64_tr_b16 a[180:181], v216 offset:25344\n"
#define C89 "ds_read_b64_tr_b16 a[182:183], v216 offset:25856\n"
#define C9A "ds_read_b64_tr_b16 a[184:185], v216 offset:21152\n"
#define C9B "ds_read_b64_tr_b16 a[186:187], v216 offset:21664\n"
#define C9C "ds_read_b64_tr_b16 a[188:189], v216 offset:25376\n"
#define C9D "ds_read_b64_tr_b16 a[190:191], v216 offset:25888\n"
#define E9X "s_cbranch_scc1 Lb0_4\n"
#define D6U "ds_read_b128 a[144:147], v214 offset:38528\n"
#define D6V "ds_read_b128 a[148:151], v214 offset:38592\n"
#define D6W "ds_read_b128 a[152:155], v214 offset:34304\n"
#define D6X "ds_read_b128 a[156:159], v214 offset:34368\n"
#define D6Y "ds_read_b128 a[160:163], v214 offset:30080\n"
#define D6Z "ds_read_b128 a[164:167], v214 offset:30144\n"
#define B9E "ds_read_b64_tr_b16 a[168:169], v216 offset:42272\n"
#define B9F "ds_read_b64_tr_b16 a[170:171], v216 offset:42784\n"
#define B9G "ds_read_b64_tr_b16 a[172:173], v216 offset:46496\n"
#define B9H "ds_read_b64_tr_b16 a[174:175], v216 offset:47008\n"
#define B9I "ds_read_b64_tr_b16 a[176:177], v216 offset:42304\n"
#define B9J "ds_read_b64_tr_b16 a[178:179], v216 offset:42816\n"
#define B9K "ds_read_b64_tr_b16 a[180:181], v216 offset:46528\n"
#define B9L "ds_read_b64_tr_b16 a[182:183], v216 offset:47040\n"
#define B9M "ds_read_b64_tr_b16 a[184:185], v216 offset:42336\n"
#define B9N "ds_read_b64_tr_b16 a[186:187], v216 offset:42848\n"
#define B9O "ds_read_b64_tr_b16 a[188:189], v216 offset:46560\n"
#define B9P "ds_read_b64_tr_b16 a[190:191], v216 offset:47072\n"
#define E3h "ds_read_b128 a[144:147], v214 offset:59648\n"
#define E3i "ds_read_b128 a[148:151], v214 offset:59712\n"
#define E3j "ds_read_b128 a[152:155], v214 offset:55424\n"
#define E3k "ds_read_b128 a[156:159], v214 offset:55488\n"
#define E0y "ds_read_b128 a[160:163], v214 offset:51200\n"
#define E0z "ds_read_b128 a[164:167], v214 offset:51264\n"
#define E00 "ds_read_b64_tr_b16 a[168:169], v217 offset:96\n"
#define E01 "ds_read_b64_tr_b16 a[170:171], v217 offset:608\n"
#define E02 "ds_read_b64_tr_b16 a[172:173], v217 offset:4320\n"
#define E03 "ds_read_b64_tr_b16 a[174:175], v217 offset:4832\n"
#define E04 "ds_read_b64_tr_b16 a[176:177], v217 offset:8448\n"
#define E05 "ds_read_b64_tr_b16 a[178:179], v217 offset:8960\n"
#define E9Y "ds_read_b64_tr_b16 a[180:181], v217 offset:12672\n"
#define E9Z "ds_read_b64_tr_b16 a[182:183], v217 offset:13184\n"
#define E06 "ds_read_b64_tr_b16 a[184:185], v217 offset:8480\n"
#define E07 "ds_read_b64_tr_b16 a[186:187], v217 offset:8992\n"
#define E9a "ds_read_b64_tr_b16 a[188:189], v217 offset:12704\n"
#define E9b "ds_read_b64_tr_b16 a[190:191], v217 offset:13216\n"
#define E08 "ds_read_b128 a[144:147], v215 offset:25856\n"
#define E09 "ds_read_b128 a[148:151], v215 offset:25920\n"
#define E3l "ds_read_b128 a[152:155], v215 offset:21632\n"
#define E3m "ds_read_b128 a[156:159], v215 offset:21696\n"
#define E3n "ds_read_b128 a[160:163], v215 offset:17408\n"
#define E3o "ds_read_b128 a[164:167], v215 offset:17472\n"
#define D8m "ds_read_b64_tr_b16 a[168:169], v217 offset:29600\n"
#define D8n "ds_read_b64_tr_b16 a[170:171], v217 offset:30112\n"
#define D8o "ds_read_b64_tr_b16 a[172:173], v217 offset:33824\n"
#define D8p "ds_read_b64_tr_b16 a[174:175], v217 offset:34336\n"
#define D8q "ds_read_b64_tr_b16 a[176:177], v217 offset:29632\n"
#define D8r "ds_read_b64_tr_b16 a[178:179], v217 offset:30144\n"
#define D8s "ds_read_b64_tr_b16 a[180:181], v217 offset:33856\n"
#define D8t "ds_read_b64_tr_b16 a[182:183], v217 offset:34368\n"
#define D8u "ds_read_b64_tr_b16 a[184:185], v217 offset:29664\n"
#define D8v "ds_read_b64_tr_b16 a[186:187], v217 offset:30176\n"
#define D8w "ds_read_b64_tr_b16 a[188:189], v217 offset:33888\n"
#define D8x "ds_read_b64_tr_b16 a[190:191], v217 offset:34400\n"
#define C9H "v_cndmask_b32_e64 v160, v209, v176, s[56:57]\n"
#define C9I "v_cndmask_b32_e64 v161, v209, v177, s[58:59]\n"
#define C9J "v_cndmask_b32_e64 v162, v209, v178, s[60:61]\n"
#define C9K "v_cndmask_b32_e64 v163, v209, v179, s[62:63]\n"
#define F4d "ds_read_b128 a[196:199], v215 offset:42304\n"
#define F4e "ds_read_b128 a[192:195], v215 offset:42240\n"
#define D8y "s_addk_i32 m0, 4480\n"
#define C9L "v_cndmask_b32_e64 v168, v209, v176, s[64:65]\n"
#define C9M "v_cndmask_b32_e64 v169, v209, v177, s[66:67]\n"
#define F4f "ds_read_b128 a[160:163], v215 offset:46976\n"
#define F4g "ds_read_b128 a[164:167], v215 offset:47040\n"
#define C9N "v_cndmask_b32_e64 v170, v209, v178, s[68:69]\n"
#define C9O "v_cndmask_b32_e64 v171, v209, v179, s[70:71]\n"
#define A96 "v_mfma_f32_16x16x32_bf16 v[168:171], a[144:147], a[96:99], v[168:171]\n"
#define C9P "v_cndmask_b32_e64 v160, v209, v176, s[72:73]\n"
#define C9Q "v_cndmask_b32_e64 v161, v209, v177, s[74:75]\n"
#define C9R "v_cndmask_b32_e64 v162, v209, v178, s[76:77]\n"
#define C9S "v_cndmask_b32_e64 v163, v209, v179, s[78:79]\n"
#define C9U "v_cndmask_b32_e64 v168, v209, v176, s[80:81]\n"
#define C9V "v_cndmask_b32_e64 v169, v209, v177, s[82:83]\n"
#define C9W "v_cndmask_b32_e64 v170, v209, v178, s[84:85]\n"
#define C9X "v_cndmask_b32_e64 v171, v209, v179, s[86:87]\n"
#define C9Y "v_cndmask_b32_e64 v160, v209, v180, s[56:57]\n"
#define C9Z "v_cndmask_b32_e64 v161, v209, v181, s[58:59]\n"
#define C9a "v_cndmask_b32_e64 v162, v209, v182, s[60:61]\n"
#define C9b "v_cndmask_b32_e64 v163, v209, v183, s[62:63]\n"
#define C9c "v_cndmask_b32_e64 v166, v209, v182, s[68:69]\n"
#define C9d "v_cndmask_b32_e64 v167, v209, v183, s[70:71]\n"
#define C9e "v_cndmask_b32_e64 v164, v209, v180, s[64:65]\n"
#define C9f "v_cndmask_b32_e64 v165, v209, v181, s[66:67]\n"
#define C9g "v_cndmask_b32_e64 v168, v209, v180, s[72:73]\n"
#define C9h "v_cndmask_b32_e64 v169, v209, v181, s[74:75]\n"
#define C9i "v_cndmask_b32_e64 v170, v209, v182, s[76:77]\n"
#define C9j "v_cndmask_b32_e64 v171, v209, v183, s[78:79]\n"
#define C9k "v_cndmask_b32_e64 v174, v209, v182, s[84:85]\n"
#define C9l "v_cndmask_b32_e64 v175, v209, v183, s[86:87]\n"
#define C9n "v_cndmask_b32_e64 v172, v209, v180, s[80:81]\n"
#define C9o "v_cndmask_b32_e64 v173, v209, v181, s[82:83]\n"
#define C9p "v_mfma_f32_16x16x32_bf16 v[164:167], a[164:167], a[140:143], v[164:167]\n"
#define C9q "v_mfma_f32_16x16x32_bf16 v[164:167], a[160:163], a[136:139], v[164:167]\n"
#define C9r "v_mfma_f32_16x16x32_bf16 v[164:167], a[156:159], a[132:135], v[164:167]\n"
#define C9t "v_mfma_f32_16x16x32_bf16 v[164:167], a[148:151], a[124:127], v[164:167]\n"
#define C9u "v_mfma_f32_16x16x32_bf16 v[164:167], a[144:147], a[120:123], v[164:167]\n"
#define C9v "v_rcp_f32 v210, v192\n"
#define B9U "v_cvt_pk_bf16_f32 v136, v160, v161\n"
#define A47 "s_waitcnt vmcnt(0)\n"
#define B9V "v_cvt_pk_bf16_f32 v137, v162, v163\n"
#define C9z "v_mfma_f32_16x16x32_bf16 v[0:3], a[0:3], v[176:179], v[0:3]\n"
#define C90 "v_mul_f32 v6, v6, v210\n"
#define A8S "ds_write_b64 v220, v[128:129] offset:50688\n"
#define C94 "v_mul_f32 v8, v8, v210\n"
#define C95 "v_mfma_f32_16x16x32_bf16 v[4:7], a[4:7], v[176:179], v[4:7]\n"
#define C96 "v_mul_f32 v9, v9, v210\n"
#define C97 "v_mfma_f32_16x16x32_bf16 v[196:199], a[244:247], v[136:139], v[192:195]\n"
#define C98 "v_mul_f32 v10, v10, v210\n"
#define C99 "v_mul_f32 v11, v11, v210\n"
#define A8T "ds_write_b64 v220, v[130:131] offset:50720\n"
#define D0B "v_mfma_f32_16x16x32_bf16 v[8:11], a[8:11], v[176:179], v[8:11]\n"
#define D0C "v_mul_f32 v12, v12, v210\n"
#define D0D "v_mul_f32 v13, v13, v210\n"
#define A8U "ds_write_b64 v220, v[132:133] offset:50752\n"
#define D0G "v_mul_f32 v14, v14, v210\n"
#define D0H "v_mul_f32 v15, v15, v210\n"
#define D0I "v_mfma_f32_16x16x32_bf16 v[12:15], a[12:15], v[176:179], v[12:15]\n"
#define D0J "v_mul_f32 v17, v17, v210\n"
#define D0K "v_mul_f32 v18, v18, v210\n"
#define D0L "v_cvt_pk_bf16_f32 v134, v12, v13\n"
#define D0M "v_cvt_pk_bf16_f32 v135, v14, v15\n"
#define A8V "ds_write_b64 v220, v[134:135] offset:50784\n"
#define D6a "ds_read_b128 v[0:3], v221 offset:50688\n"
#define D6b "ds_read_b128 v[4:7], v221 offset:51840\n"
#define D0N "v_mfma_f32_16x16x32_bf16 v[168:171], a[192:195], a[120:123], v[168:171]\n"
#define D0O "v_mul_f32 v19, v19, v210\n"
#define D0P "v_mul_f32 v20, v20, v210\n"
#define D0Q "v_mfma_f32_16x16x32_bf16 v[172:175], a[164:167], a[140:143], v[172:175]\n"
#define D0R "v_mul_f32 v16, v16, v210\n"
#define D0S "v_mul_f32 v21, v21, v210\n"
#define D0T "v_mul_f32 v22, v22, v210\n"
#define D0U "buffer_store_dwordx4 v[0:3], v224, s[44:47], s92 offen offset:0 nt\n"
#define D0V "v_mfma_f32_16x16x32_bf16 v[16:19], a[16:19], v[176:179], v[16:19]\n"
#define D0W "v_mul_f32 v23, v23, v210\n"
#define D0Y "buffer_store_dwordx4 v[4:7], v224, s[44:47], s92 offen offset:3072 nt\n"
#define B9Y "ds_write_b64 v220, v[128:129] offset:33792\n"
#define D0Z "v_mfma_f32_16x16x32_bf16 v[20:23], a[20:23], v[176:179], v[20:23]\n"
#define D0a "v_cvt_pk_bf16_f32 v130, v20, v21\n"
#define D0b "v_cvt_pk_bf16_f32 v131, v22, v23\n"
#define B9Z "ds_write_b64 v220, v[130:131] offset:33824\n"
#define D0f "v_mul_f32 v26, v26, v210\n"
#define D0g "v_mul_f32 v27, v27, v210\n"
#define D0h "v_mfma_f32_16x16x32_bf16 v[24:27], a[24:27], v[176:179], v[24:27]\n"
#define B9a "v_cvt_pk_bf16_f32 v140, v168, v169\n"
#define B9b "v_cvt_pk_bf16_f32 v141, v170, v171\n"
#define B9c "ds_write_b64 v220, v[132:133] offset:33856\n"
#define D0k "v_mul_f32 v28, v28, v210\n"
#define D0l "v_mul_f32 v29, v29, v210\n"
#define B9d "ds_write_b64 v220, v[134:135] offset:33888\n"
#define D6c "ds_read_b128 v[16:19], v221 offset:33792\n"
#define D6d "ds_read_b128 v[20:23], v221 offset:34944\n"
#define D0r "v_mfma_f32_16x16x32_bf16 v[32:35], a[32:35], v[176:179], v[32:35]\n"
#define D0u "v_mul_f32 v38, v38, v210\n"
#define D0v "v_mul_f32 v39, v39, v210\n"
#define D0w "v_mfma_f32_16x16x32_bf16 v[36:39], a[36:39], v[176:179], v[36:39]\n"
#define D0x "buffer_store_dwordx4 v[16:19], v224, s[44:47], s92 offen offset:128 nt\n"
#define D0y "v_cvt_pk_bf16_f32 v130, v36, v37\n"
#define D0z "buffer_store_dwordx4 v[20:23], v224, s[44:47], s92 offen offset:3200 nt\n"
#define D00 "v_cvt_pk_bf16_f32 v131, v38, v39\n"
#define D01 "v_mfma_f32_16x16x32_bf16 v[48:51], a[48:51], v[136:139], v[48:51]\n"
#define D02 "v_mfma_f32_16x16x32_bf16 v[48:51], a[52:55], v[140:143], v[48:51]\n"
#define D03 "v_mul_f32 v40, v40, v210\n"
#define D06 "v_mfma_f32_16x16x32_bf16 v[52:55], a[60:63], v[140:143], v[52:55]\n"
#define D07 "v_mfma_f32_16x16x32_bf16 v[40:43], a[40:43], v[176:179], v[40:43]\n"
#define D08 "v_rcp_f32 v211, v197\n"
#define D09 "v_mfma_f32_16x16x32_bf16 v[56:59], a[64:67], v[136:139], v[56:59]\n"
#define D1A "v_mfma_f32_16x16x32_bf16 v[56:59], a[68:71], v[140:143], v[56:59]\n"
#define B9e "s_waitcnt lgkmcnt(11)\n"
#define D1E "v_fmac_f32 v112, v48, v211\n"
#define D1F "v_fmac_f32 v113, v49, v211\n"
#define D1G "v_mfma_f32_16x16x32_bf16 v[60:63], a[72:75], v[136:139], v[60:63]\n"
#define D1J "v_mfma_f32_16x16x32_bf16 v[64:67], a[80:83], v[136:139], v[64:67]\n"
#define D1K "v_fmac_f32 v114, v50, v211\n"
#define D1L "v_fmac_f32 v115, v51, v211\n"
#define D1M "v_mfma_f32_16x16x32_bf16 v[64:67], a[84:87], v[140:143], v[64:67]\n"
#define D1N "v_fmac_f32 v126, v62, v211\n"
#define D6e "ds_read_b128 v[32:35], v221 offset:50688\n"
#define D6f "ds_read_b128 v[36:39], v221 offset:51840\n"
#define D1P "v_mfma_f32_16x16x32_bf16 v[68:71], a[88:91], v[136:139], v[68:71]\n"
#define D1Q "v_fmac_f32 v127, v63, v211\n"
#define D1R "v_fmac_f32 v116, v52, v211\n"
#define D1S "v_fmac_f32 v117, v53, v211\n"
#define A8X "ds_write_b64 v220, v[104:105] offset:33792\n"
#define D1X "v_fmac_f32 v124, v60, v211\n"
#define A8Y "ds_write_b64 v220, v[106:107] offset:33824\n"
#define A8Z "ds_write_b64 v220, v[108:109] offset:33856\n"
#define D1Z "v_fmac_f32 v125, v61, v211\n"
#define A8a "ds_write_b64 v220, v[110:111] offset:33888\n"
#define B9f "ds_write_b64 v220, v[104:105] offset:50688\n"
#define D6g "ds_read_b128 v[52:55], v221 offset:34944\n"
#define D1f "v_fmac_f32 v186, v82, v211\n"
#define D6h "ds_read_b128 v[48:51], v221 offset:33792\n"
#define B9g "ds_write_b64 v220, v[106:107] offset:50720\n"
#define B9h "ds_write_b64 v220, v[108:109] offset:50752\n"
#define B9i "ds_write_b64 v220, v[110:111] offset:50784\n"
#define D6i "ds_read_b128 v[64:67], v221 offset:50688\n"
#define D1j "v_cvt_pk_bf16_f32 v106, v188, v189\n"
#define D6j "ds_read_b128 v[68:71], v221 offset:51840\n"
#define D6k "ds_read_b128 v[80:83], v221 offset:33792\n"
#define D6l "ds_read_b128 v[84:87], v221 offset:34944\n"
#define B0K "s_nop 15\n"
#define A97 "s_mov_b32 s13, 1152\n"
#define E1z "s_mov_b32 s14, 1\n"
#define H01 "s_mov_b32 s1, 1.0\n"
#define B1R "s_mov_b32 s13, 1216\n"
#define H07 "s_add_u32 vcc_lo, s98, Lrare1_0-Lrare0_0\n"
#define A8b "s_mov_b32 s13, 1088\n"
#define F37 "s_mov_b32 s14, 0\n"
#define A7I "s_mov_b32 s13, 1024\n"
#define A48 "s_mov_b32 s13, 960\n"
#define B4x "s_mov_b32 s13, 896\n"
#define D1q "s_mov_b32 s12, 0\n"
#define F4h "s_mov_b32 s13, 1920\n"
#define A3i "s_mov_b32 s13, 0\n"
#define F38 "s_mov_b32 s1, 0x3dd53b95\n"
#define H1A "s_add_u32 vcc_lo, s98, Lrare2_1-Lrare0_0\n"
#define A49 "s_mov_b32 s13, 64\n"
#define E1A "s_add_u32 vcc_lo, s98, Lrare3_1-Lrare0_0\n"
#define D1r "s_mov_b32 s12, 24576\n"
#define E9c "s_mov_b32 s13, 1856\n"
#define H02 "s_add_u32 vcc_lo, s98, Lrare3_0-Lrare0_0\n"
#define D1s "s_mov_b32 s12, 49152\n"
#define E3q "s_mov_b32 s13, 1792\n"
#define D1t "s_mov_b32 s12, 73728\n"
#define E1B "s_mov_b32 s13, 1728\n"
#define D1u "s_mov_b32 s12, 98304\n"
#define D8z "s_mov_b32 s13, 1664\n"
#define D1v "s_mov_b32 s12, 122880\n"
#define D6n "s_mov_b32 s13, 1600\n"
#define D1w "s_mov_b32 s12, 147456\n"
#define D1x "s_mov_b32 s13, 1536\n"
#define D1y "s_mov_b32 s12, 172032\n"
#define B9m "s_mov_b32 s13, 1472\n"
#define D1z "s_mov_b32 s12, 196608\n"
#define B63 "s_mov_b32 s13, 1408\n"
#define D10 "s_mov_b32 s12, 221184\n"
#define B4y "s_mov_b32 s13, 1344\n"
#define D11 "s_mov_b32 s12, 245760\n"
#define B3C "s_mov_b32 s13, 1280\n"
#define D12 "s_mov_b32 s12, 270336\n"
#define B3D "s_mov_b32 s13, 832\n"
#define D6o "s_mov_b32 s12, 442368\n"
#define B1T "s_mov_b32 s13, 768\n"
#define D6p "s_mov_b32 s12, 466944\n"
#define A98 "s_mov_b32 s13, 704\n"
#define D80 "s_mov_b32 s12, 491520\n"
#define A8c "s_mov_b32 s13, 640\n"
#define D81 "s_mov_b32 s12, 516096\n"
#define A7J "s_mov_b32 s13, 576\n"
#define E1C "s_mov_b32 s12, 540672\n"
#define A5A "s_mov_b32 s13, 512\n"
#define E1D "s_mov_b32 s12, 565248\n"
#define B5X "s_mov_b32 s13, 448\n"
#define E3r "s_mov_b32 s12, 589824\n"
#define B3Z "s_mov_b32 s13, 384\n"
#define E3s "s_mov_b32 s12, 614400\n"
#define B1k "s_mov_b32 s13, 320\n"
#define E9d "s_mov_b32 s12, 638976\n"
#define B0H "s_mov_b32 s13, 256\n"
#define E9e "s_mov_b32 s12, 663552\n"
#define A8o "s_mov_b32 s13, 192\n"
#define F4i "s_mov_b32 s12, 688128\n"
#define A7O "s_mov_b32 s13, 128\n"
#define F4j "s_mov_b32 s12, 712704\n"
#define E9f "s_mov_b32 s92, 688128\n"
#define F4k "s_sleep 10\n"
#define D6q "v_and_b32 v28, 1, v1\n"
#define D6r "v_lshl_add_u32 v28, v28, 6, v0\n"
#define D6s "v_lshlrev_b32 v225, 7, v28\n"
#define D6t "s_cmp_lt_u32 s23, 2\n"
#define D6u "s_cselect_b64 s[52:53], s[28:29], s[32:33]\n"
#define D6v "s_mov_b32 s54, 0xC0000\n"
#define E9j "s_cbranch_scc1 Lb1_0\n"
#define E9k "s_mov_b32 s92, 49152\n"
#define E9l "s_add_u32 m0, s88, 25344\n"
#define E1E "v_mfma_f32_16x16x32_bf16 v[168:171], a[212:215], a[164:167], 0\n"
#define D84 "s_add_u32 m0, s88, 76032\n"
#define t4 "v_mfma_f32_16x16x32_bf16 v[168:171], a[192:195], a[144:147], v[184:187]\n"
#define X4 "buffer_load_dword v226, v225, s[52:55], s92 offen\n"
#define O3 "v_mfma_f32_16x16x32_bf16 v[168:171], a[212:215], a[164:167], v[184:187]\n"
#define F4l "s_waitcnt vmcnt(6) lgkmcnt(4)\n"
#define E3u "s_cbranch_scc1 Lb1_1\n"
#define A5F "v_cvt_pk_bf16_f32 v112, v168, v169\n"
#define A5G "v_cvt_pk_bf16_f32 v113, v170, v171\n"
#define C0Z "v_cvt_pk_bf16_f32 v120, v172, v173\n"
#define C0a "v_cvt_pk_bf16_f32 v121, v174, v175\n"
#define D7C "v_cvt_pk_bf16_f32 v98, v160, v161\n"
#define D7D "v_cvt_pk_bf16_f32 v99, v162, v163\n"
#define D3H "v_cvt_pk_bf16_f32 v106, v164, v165\n"
#define D3I "v_cvt_pk_bf16_f32 v107, v166, v167\n"
#define E3v "s_add_u32 m0, s88, 105600\n"
#define k8 "v_cvt_pk_bf16_f32 v114, v168, v169\n"
#define A2o "v_cvt_pk_bf16_f32 v115, v170, v171\n"
#define A0J "v_mfma_f32_16x16x32_bf16 v[192:195], a[248:251], v[112:115], v[192:195]\n"
#define C0P "v_cvt_pk_bf16_f32 v122, v172, v173\n"
#define A7Q "v_cvt_pk_bf16_f32 v123, v174, v175\n"
#define A3p "v_cvt_pk_bf16_f32 v116, v168, v169\n"
#define B5M "v_cvt_pk_bf16_f32 v117, v170, v171\n"
#define B3O "v_cvt_pk_bf16_f32 v124, v172, v173\n"
#define B3P "v_cvt_pk_bf16_f32 v125, v174, v175\n"
#define D3E "v_cvt_pk_bf16_f32 v102, v160, v161\n"
#define D3F "v_cvt_pk_bf16_f32 v103, v162, v163\n"
#define D7I "v_cvt_pk_bf16_f32 v110, v164, v165\n"
#define D7J "v_cvt_pk_bf16_f32 v111, v166, v167\n"
#define l8 "v_cvt_pk_bf16_f32 v118, v168, v169\n"
#define r5 "v_cvt_pk_bf16_f32 v119, v170, v171\n"
#define s5 "v_mfma_f32_16x16x32_bf16 v[192:195], a[248:251], v[116:119], v[192:195]\n"
#define E1G "v_cndmask_b32_e64 v168, v209, v184, s[56:57]\n"
#define E1H "v_cndmask_b32_e64 v169, v209, v185, s[58:59]\n"
#define E1I "v_cndmask_b32_e64 v170, v209, v186, s[60:61]\n"
#define E1J "v_cndmask_b32_e64 v171, v209, v187, s[62:63]\n"
#define B1X "v_max3_f32 v208, v192, v193, v194\n"
#define B0D "v_max_f32 v208, v208, v195\n"
#define l7 "v_mfma_f32_16x16x32_bf16 a[0:3], a[216:219], v[112:115], a[0:3]\n"
#define t5 "v_mfma_f32_16x16x32_bf16 a[0:3], a[220:223], v[116:119], a[0:3]\n"
#define m7 "v_mfma_f32_16x16x32_bf16 a[4:7], a[224:227], v[112:115], a[4:7]\n"
#define u5 "v_mfma_f32_16x16x32_bf16 a[4:7], a[228:231], v[116:119], a[4:7]\n"
#define n7 "v_mfma_f32_16x16x32_bf16 a[8:11], a[232:235], v[112:115], a[8:11]\n"
#define v5 "v_mfma_f32_16x16x32_bf16 a[8:11], a[236:239], v[116:119], a[8:11]\n"
#define B1a "v_mfma_f32_16x16x32_bf16 a[12:15], a[216:219], v[112:115], a[12:15]\n"
#define A16 "v_mfma_f32_16x16x32_bf16 a[12:15], a[220:223], v[116:119], a[12:15]\n"
#define A1J "v_mfma_f32_16x16x32_bf16 a[16:19], a[224:227], v[112:115], a[16:19]\n"
#define w5 "v_mfma_f32_16x16x32_bf16 a[16:19], a[228:231], v[116:119], a[16:19]\n"
#define o7 "v_mfma_f32_16x16x32_bf16 a[20:23], a[232:235], v[112:115], a[20:23]\n"
#define D65 "s_add_u32 m0, s88, 29568\n"
#define x5 "v_mfma_f32_16x16x32_bf16 a[20:23], a[236:239], v[116:119], a[20:23]\n"
#define B0I "v_mfma_f32_16x16x32_bf16 a[24:27], a[216:219], v[112:115], a[24:27]\n"
#define A7T "v_mfma_f32_16x16x32_bf16 a[24:27], a[220:223], v[116:119], a[24:27]\n"
#define p7 "v_mfma_f32_16x16x32_bf16 a[28:31], a[224:227], v[112:115], a[28:31]\n"
#define N9 "v_mfma_f32_16x16x32_bf16 a[28:31], a[228:231], v[116:119], a[28:31]\n"
#define q7 "v_mfma_f32_16x16x32_bf16 a[32:35], a[232:235], v[112:115], a[32:35]\n"
#define y5 "v_mfma_f32_16x16x32_bf16 a[32:35], a[236:239], v[116:119], a[32:35]\n"
#define A3m "v_mfma_f32_16x16x32_bf16 a[36:39], a[216:219], v[112:115], a[36:39]\n"
#define A0K "v_mfma_f32_16x16x32_bf16 a[36:39], a[220:223], v[116:119], a[36:39]\n"
#define A2j "v_mfma_f32_16x16x32_bf16 a[40:43], a[224:227], v[112:115], a[40:43]\n"
#define m8 "v_mfma_f32_16x16x32_bf16 a[40:43], a[228:231], v[116:119], a[40:43]\n"
#define r7 "v_mfma_f32_16x16x32_bf16 a[44:47], a[232:235], v[112:115], a[44:47]\n"
#define z5 "v_mfma_f32_16x16x32_bf16 a[44:47], a[236:239], v[116:119], a[44:47]\n"
#define A1L "v_mfma_f32_16x16x32_bf16 a[0:3], a[216:219], v[144:147], a[0:3]\n"
#define A6 "v_mfma_f32_16x16x32_bf16 a[48:51], a[220:223], v[156:159], a[48:51]\n"
#define s7 "v_mfma_f32_16x16x32_bf16 a[4:7], a[224:227], v[144:147], a[4:7]\n"
#define n8 "v_mfma_f32_16x16x32_bf16 a[52:55], a[228:231], v[156:159], a[52:55]\n"
#define t7 "v_mfma_f32_16x16x32_bf16 a[8:11], a[232:235], v[144:147], a[8:11]\n"
#define B6 "v_mfma_f32_16x16x32_bf16 a[56:59], a[236:239], v[156:159], a[56:59]\n"
#define A2m "v_mfma_f32_16x16x32_bf16 a[12:15], a[216:219], v[144:147], a[12:15]\n"
#define D2d "s_waitcnt vmcnt(6)\n"
#define C6 "v_mfma_f32_16x16x32_bf16 a[60:63], a[220:223], v[156:159], a[60:63]\n"
#define A17 "v_mfma_f32_16x16x32_bf16 a[16:19], a[224:227], v[144:147], a[16:19]\n"
#define D6 "v_mfma_f32_16x16x32_bf16 a[64:67], a[228:231], v[156:159], a[64:67]\n"
#define u7 "v_mfma_f32_16x16x32_bf16 a[20:23], a[232:235], v[144:147], a[20:23]\n"
#define E6 "v_mfma_f32_16x16x32_bf16 a[68:71], a[236:239], v[156:159], a[68:71]\n"
#define A1M "v_mfma_f32_16x16x32_bf16 a[24:27], a[216:219], v[144:147], a[24:27]\n"
#define A0L "v_mfma_f32_16x16x32_bf16 a[72:75], a[220:223], v[156:159], a[72:75]\n"
#define v7 "v_mfma_f32_16x16x32_bf16 a[28:31], a[224:227], v[144:147], a[28:31]\n"
#define F6 "v_mfma_f32_16x16x32_bf16 a[76:79], a[228:231], v[156:159], a[76:79]\n"
#define w7 "v_mfma_f32_16x16x32_bf16 a[32:35], a[232:235], v[144:147], a[32:35]\n"
#define G6 "v_mfma_f32_16x16x32_bf16 a[80:83], a[236:239], v[156:159], a[80:83]\n"
#define A2n "v_mfma_f32_16x16x32_bf16 a[36:39], a[216:219], v[144:147], a[36:39]\n"
#define H6 "v_mfma_f32_16x16x32_bf16 a[84:87], a[220:223], v[156:159], a[84:87]\n"
#define B1e "v_mfma_f32_16x16x32_bf16 a[40:43], a[224:227], v[144:147], a[40:43]\n"
#define I6 "v_mfma_f32_16x16x32_bf16 a[88:91], a[228:231], v[156:159], a[88:91]\n"
#define x7 "v_mfma_f32_16x16x32_bf16 a[44:47], a[232:235], v[144:147], a[44:47]\n"
#define J6 "v_mfma_f32_16x16x32_bf16 a[92:95], a[236:239], v[156:159], a[92:95]\n"
#define F4p "s_waitcnt vmcnt(10) lgkmcnt(5)\n"
#define E3z "s_cbranch_scc1 Lb1_2\n"
#define F4r "s_add_u32 m0, s88, 63616\n"
#define E31 "s_waitcnt vmcnt(23)\n"
#define E32 "s_cbranch_scc1 Lb1_3\n"
#define D89 "ds_read_b128 a[144:147], v214 offset:8960\n"
#define D9A "ds_read_b128 a[148:151], v214 offset:9024\n"
#define D9B "ds_read_b128 a[152:155], v214 offset:4736\n"
#define D9C "ds_read_b128 a[156:159], v214 offset:4800\n"
#define D9D "ds_read_b128 a[160:163], v214 offset:512\n"
#define D9E "ds_read_b128 a[164:167], v214 offset:576\n"
#define E9p "ds_read_b128 a[144:147], v215 offset:46976\n"
#define E9q "ds_read_b128 a[148:151], v215 offset:47040\n"
#define F4v "s_cbranch_scc1 Lb1_4\n"
#define E33 "ds_read_b64_tr_b16 a[180:181], v217 offset:38016\n"
#define E34 "ds_read_b64_tr_b16 a[182:183], v217 offset:38528\n"
#define E35 "ds_read_b64_tr_b16 a[188:189], v217 offset:38048\n"
#define E36 "ds_read_b64_tr_b16 a[190:191], v217 offset:38560\n"
#define E9r "ds_read_b64_tr_b16 a[220:221], v217 offset:50752\n"
#define E9s "ds_read_b64_tr_b16 a[222:223], v217 offset:51264\n"
#define E9t "ds_read_b64_tr_b16 a[216:217], v217 offset:46528\n"
#define E9u "ds_read_b64_tr_b16 a[218:219], v217 offset:47040\n"
#define E9v "ds_read_b64_tr_b16 a[224:225], v217 offset:46560\n"
#define E9w "ds_read_b64_tr_b16 a[226:227], v217 offset:47072\n"
#define E9x "ds_read_b64_tr_b16 a[228:229], v217 offset:50784\n"
#define E9y "ds_read_b64_tr_b16 a[230:231], v217 offset:51296\n"
#define F4w "s_branch Lb1_4\n"
#define H09 "s_add_u32 vcc_lo, s98, Lrare2_0-Lrare0_0\n"
#define E9z "s_mov_b32 s92, 638976\n"
#define E90 "s_cbranch_scc1 Lb2_0\n"
#define E91 "s_mov_b32 s92, 98304\n"
#define F4y "s_add_u32 m0, s88, 71808\n"
#define E4A "s_cbranch_scc1 Lb2_1\n"
#define E4F "s_cbranch_scc1 Lb2_2\n"
#define F42 "s_add_u32 m0, s88, 93184\n"
#define E4G "s_cbranch_scc1 Lb2_3\n"
#define E94 "s_add_u32 m0, s88, 50816\n"
#define E1T "ds_read_b128 a[212:215], v214 offset:59200\n"
#define E1U "ds_read_b128 a[208:211], v214 offset:59136\n"
#define E4J "ds_read_b128 a[204:207], v214 offset:50752\n"
#define E4K "ds_read_b128 a[200:203], v214 offset:50688\n"
#define F43 "ds_read_b64_tr_b16 a[220:221], v216 offset:54976\n"
#define F44 "ds_read_b64_tr_b16 a[222:223], v216 offset:55488\n"
#define F45 "ds_read_b64_tr_b16 a[228:229], v216 offset:55008\n"
#define F46 "ds_read_b64_tr_b16 a[230:231], v216 offset:55520\n"
#define E95 "ds_read_b128 a[200:203], v214 offset:51200\n"
#define E96 "ds_read_b128 a[204:207], v214 offset:51264\n"
#define E4L "ds_read_b128 a[208:211], v214 offset:59648\n"
#define E4M "ds_read_b128 a[212:215], v214 offset:59712\n"
#define E97 "ds_read_b128 a[196:199], v214 offset:50752\n"
#define E98 "ds_read_b128 a[192:195], v214 offset:50688\n"
#define F49 "ds_read_b64_tr_b16 a[180:181], v217 offset:16896\n"
#define F5A "ds_read_b64_tr_b16 a[182:183], v217 offset:17408\n"
#define F5B "ds_read_b64_tr_b16 a[188:189], v217 offset:16928\n"
#define F5C "ds_read_b64_tr_b16 a[190:191], v217 offset:17440\n"
#define E4N "ds_read_b128 a[212:215], v215 offset:21184\n"
#define E4O "ds_read_b128 a[208:211], v215 offset:21120\n"
#define D9H "ds_read_b128 a[204:207], v215 offset:12736\n"
#define D9I "ds_read_b128 a[200:203], v215 offset:12672\n"
#define F5D "ds_read_b64_tr_b16 a[220:221], v217 offset:16960\n"
#define F5E "ds_read_b64_tr_b16 a[222:223], v217 offset:17472\n"
#define F5F "ds_read_b64_tr_b16 a[228:229], v217 offset:16992\n"
#define F5G "ds_read_b64_tr_b16 a[230:231], v217 offset:17504\n"
#define E4P "ds_read_b128 a[152:155], v215 offset:13184\n"
#define E4Q "ds_read_b128 a[156:159], v215 offset:13248\n"
#define F5H "ds_read_b128 a[160:163], v215 offset:21632\n"
#define F5I "ds_read_b128 a[164:167], v215 offset:21696\n"
#define F5J "ds_read_b64_tr_b16 a[180:181], v216 offset:0\n"
#define F5K "ds_read_b64_tr_b16 a[182:183], v216 offset:512\n"
#define F5M "ds_read_b64_tr_b16 a[188:189], v216 offset:32\n"
#define F5N "ds_read_b64_tr_b16 a[190:191], v216 offset:544\n"
#define F5O "s_cbranch_scc1 Lb2_4\n"
#define E99 "ds_read_b128 a[196:199], v215 offset:12736\n"
#define F0A "ds_read_b128 a[192:195], v215 offset:12672\n"
#define F5P "s_branch Lb2_4\n"
#define F0B "s_mov_b32 s92, 589824\n"
#define F0C "s_cbranch_scc1 Lb3_0\n"
#define F0D "s_mov_b32 s92, 147456\n"
#define F5Q "s_add_u32 m0, s88, 67840\n"
#define F5R "s_add_u32 m0, s88, 105728\n"
#define E1X "s_add_u32 m0, s88, 29696\n"
#define E1Y "v_mfma_f32_16x16x32_bf16 a[0:3], a[216:219], v[112:115], 0\n"
#define E1Z "v_mfma_f32_16x16x32_bf16 a[48:51], a[220:223], v[124:127], 0\n"
#define E1a "v_mfma_f32_16x16x32_bf16 a[4:7], a[224:227], v[112:115], 0\n"
#define E1b "v_mfma_f32_16x16x32_bf16 a[52:55], a[228:231], v[124:127], 0\n"
#define E1c "v_mfma_f32_16x16x32_bf16 a[8:11], a[232:235], v[112:115], 0\n"
#define E1d "v_mfma_f32_16x16x32_bf16 a[56:59], a[236:239], v[124:127], 0\n"
#define E1e "v_mfma_f32_16x16x32_bf16 a[12:15], a[216:219], v[112:115], 0\n"
#define E1f "v_mfma_f32_16x16x32_bf16 a[60:63], a[220:223], v[124:127], 0\n"
#define E1g "v_mfma_f32_16x16x32_bf16 a[16:19], a[224:227], v[112:115], 0\n"
#define E1h "v_mfma_f32_16x16x32_bf16 a[64:67], a[228:231], v[124:127], 0\n"
#define E1i "v_mfma_f32_16x16x32_bf16 a[20:23], a[232:235], v[112:115], 0\n"
#define E1j "v_mfma_f32_16x16x32_bf16 a[68:71], a[236:239], v[124:127], 0\n"
#define E1k "v_mfma_f32_16x16x32_bf16 a[24:27], a[216:219], v[112:115], 0\n"
#define E1l "v_mfma_f32_16x16x32_bf16 a[72:75], a[220:223], v[124:127], 0\n"
#define E1m "v_mfma_f32_16x16x32_bf16 a[28:31], a[224:227], v[112:115], 0\n"
#define E1n "v_mfma_f32_16x16x32_bf16 a[76:79], a[228:231], v[124:127], 0\n"
#define E1o "v_mfma_f32_16x16x32_bf16 a[32:35], a[232:235], v[112:115], 0\n"
#define E4T "s_cbranch_scc1 Lb3_1\n"
#define E1p "v_mfma_f32_16x16x32_bf16 a[80:83], a[236:239], v[124:127], 0\n"
#define E1q "v_mfma_f32_16x16x32_bf16 a[36:39], a[216:219], v[112:115], 0\n"
#define E1r "v_mfma_f32_16x16x32_bf16 a[84:87], a[220:223], v[124:127], 0\n"
#define E1s "v_mfma_f32_16x16x32_bf16 a[40:43], a[224:227], v[112:115], 0\n"
#define E1t "v_mfma_f32_16x16x32_bf16 a[88:91], a[228:231], v[124:127], 0\n"
#define E1u "v_mfma_f32_16x16x32_bf16 a[44:47], a[232:235], v[112:115], 0\n"
#define E1v "v_mfma_f32_16x16x32_bf16 a[92:95], a[236:239], v[124:127], 0\n"
#define E4U "s_add_u32 m0, s88, 59392\n"
#define E4W "s_cbranch_scc1 Lb3_2\n"
#define F0I "v_cvt_pk_bf16_f32 v152, v168, v169\n"
#define F0J "v_cvt_pk_bf16_f32 v153, v170, v171\n"
#define F0K "v_cvt_pk_bf16_f32 v130, v172, v173\n"
#define F0L "v_cvt_pk_bf16_f32 v131, v174, v175\n"
#define F0M "v_cvt_pk_bf16_f32 v138, v160, v161\n"
#define F0N "v_cvt_pk_bf16_f32 v139, v162, v163\n"
#define D9M "v_cvt_pk_bf16_f32 v154, v164, v165\n"
#define D9N "v_cvt_pk_bf16_f32 v155, v166, v167\n"
#define E4X "v_cvt_pk_bf16_f32 v132, v168, v169\n"
#define F0O "v_cvt_pk_bf16_f32 v140, v172, v173\n"
#define F0P "v_cvt_pk_bf16_f32 v141, v174, v175\n"
#define E4Y "v_cvt_pk_bf16_f32 v133, v170, v171\n"
#define F0Q "v_cvt_pk_bf16_f32 v156, v160, v161\n"
#define F0R "v_cvt_pk_bf16_f32 v157, v162, v163\n"
#define F0S "v_cvt_pk_bf16_f32 v134, v164, v165\n"
#define F0T "v_cvt_pk_bf16_f32 v135, v166, v167\n"
#define F0U "v_cvt_pk_bf16_f32 v142, v168, v169\n"
#define F0V "v_cvt_pk_bf16_f32 v143, v170, v171\n"
#define F0W "v_max3_f32 v208, v196, v197, v199\n"
#define F0X "v_rcp_f32 v211, v199\n"
#define E1w "v_rcp_f32 v210, v198\n"
#define F0Y "s_mov_b32 s90, 393216\n"
#define F0Z "s_mov_b32 s91, 393216\n"
#define F0a "s_mov_b32 s95, 405504\n"
#define E4Z "s_cbranch_scc1 Lb3_3\n"
#define F5U "s_waitcnt vmcnt(11) lgkmcnt(6)\n"
#define E1y "s_add_u32 m0, s88, 8704\n"
#define F0b "s_waitcnt vmcnt(10) lgkmcnt(4)\n"
#define F5W "s_cbranch_scc1 Lb3_4\n"
#define F5X "ds_read_b64_tr_b16 a[48:49], v216 offset:25344\n"
#define F5Y "ds_read_b64_tr_b16 a[50:51], v216 offset:25856\n"
#define F5Z "ds_read_b64_tr_b16 a[52:53], v216 offset:29568\n"
#define F5a "ds_read_b64_tr_b16 a[54:55], v216 offset:30080\n"
#define F5b "ds_read_b64_tr_b16 a[60:61], v216 offset:29600\n"
#define F5c "ds_read_b64_tr_b16 a[62:63], v216 offset:30112\n"
#define F5d "ds_read_b64_tr_b16 a[56:57], v216 offset:25376\n"
#define F5e "ds_read_b64_tr_b16 a[58:59], v216 offset:25888\n"
#define F5f "ds_read_b64_tr_b16 a[64:65], v216 offset:25408\n"
#define F5g "ds_read_b64_tr_b16 a[66:67], v216 offset:25920\n"
#define F5h "ds_read_b64_tr_b16 a[68:69], v216 offset:29632\n"
#define F5i "ds_read_b64_tr_b16 a[70:71], v216 offset:30144\n"
#define F5j "ds_read_b64_tr_b16 a[72:73], v216 offset:25440\n"
#define F5k "ds_read_b64_tr_b16 a[74:75], v216 offset:25952\n"
#define F5l "ds_read_b64_tr_b16 a[76:77], v216 offset:29664\n"
#define F5m "ds_read_b64_tr_b16 a[78:79], v216 offset:30176\n"
#define F5n "ds_read_b64_tr_b16 a[80:81], v216 offset:33792\n"
#define F5o "ds_read_b64_tr_b16 a[82:83], v216 offset:34304\n"
#define F5p "ds_read_b64_tr_b16 a[84:85], v216 offset:38016\n"
#define F5q "ds_read_b64_tr_b16 a[86:87], v216 offset:38528\n"
#define F5r "ds_read_b64_tr_b16 a[92:93], v216 offset:38048\n"
#define F5s "ds_read_b64_tr_b16 a[94:95], v216 offset:38560\n"
#define F5t "ds_read_b64_tr_b16 a[88:89], v216 offset:33824\n"
#define F5u "ds_read_b64_tr_b16 a[90:91], v216 offset:34336\n"
#define F5v "ds_read_b64_tr_b16 a[220:221], v216 offset:38080\n"
#define F5w "ds_read_b64_tr_b16 a[222:223], v216 offset:38592\n"
#define F5x "ds_read_b64_tr_b16 a[216:217], v216 offset:33856\n"
#define F5y "ds_read_b64_tr_b16 a[218:219], v216 offset:34368\n"
#define F5z "ds_read_b64_tr_b16 a[224:225], v216 offset:33888\n"
#define F50 "ds_read_b64_tr_b16 a[226:227], v216 offset:34400\n"
#define F51 "ds_read_b64_tr_b16 a[228:229], v216 offset:38112\n"
#define F52 "ds_read_b64_tr_b16 a[230:231], v216 offset:38624\n"
#define F53 "s_branch Lb3_4\n"
#define E4b "s_mov_b32 s92, 294912\n"
#define F0c "s_mov_b32 s92, 540672\n"
#define F0d "s_cbranch_scc1 Lb4_0\n"
#define F54 "s_mov_b32 s92, 196608\n"
#define F55 "s_add_u32 m0, s88, 50688\n"
#define F0e "s_add_u32 m0, s88, 101376\n"
#define D9S "v_mbcnt_lo_u32_b32 v223, -1, 0\n"
#define D9T "v_mbcnt_hi_u32_b32 v223, -1, v223\n"
#define D9U "v_lshl_add_u32 v223, v223, 2, s96\n"
#define E4c "ds_read_b32 v223, v223 offset:0\n"
#define D9V "v_mul_u32_u24 v223, s0, v223\n"
#define E4d "buffer_load_dword v226, v223, s[40:43], 0 offen offset:0\n"
#define E4f "s_cbranch_scc1 Lb4_1\n"
#define E4h "buffer_load_dword v226, v223, s[40:43], 0 offen offset:128\n"
#define D9X "buffer_load_dword v226, v223, s[40:43], 0 offen offset:256\n"
#define F0h "s_mov_b32 s92, 245760\n"
#define E4i "s_cbranch_scc1 Lb4_2\n"
#define F0i "s_waitcnt vmcnt(5)\n"
#define F6F "ds_read_b64_tr_b16 a[220:221], v216 offset:0\n"
#define F6G "ds_read_b64_tr_b16 a[222:223], v216 offset:512\n"
#define F6H "ds_read_b64_tr_b16 a[228:229], v216 offset:32\n"
#define F6I "ds_read_b64_tr_b16 a[230:231], v216 offset:544\n"
#define F6J "ds_read_b64_tr_b16 a[236:237], v216 offset:64\n"
#define F6K "ds_read_b64_tr_b16 a[238:239], v216 offset:576\n"
#define F6L "ds_read_b64_tr_b16 a[220:221], v216 offset:96\n"
#define F6M "ds_read_b64_tr_b16 a[222:223], v216 offset:608\n"
#define F6N "ds_read_b64_tr_b16 a[224:225], v216 offset:4224\n"
#define F6O "ds_read_b64_tr_b16 a[226:227], v216 offset:4736\n"
#define F6P "ds_read_b64_tr_b16 a[228:229], v216 offset:8448\n"
#define F6Q "ds_read_b64_tr_b16 a[230:231], v216 offset:8960\n"
#define F6R "ds_read_b64_tr_b16 a[232:233], v216 offset:4256\n"
#define F6T "ds_read_b64_tr_b16 a[234:235], v216 offset:4768\n"
#define F6U "ds_read_b64_tr_b16 a[236:237], v216 offset:8480\n"
#define F6V "ds_read_b64_tr_b16 a[238:239], v216 offset:8992\n"
#define F6W "ds_read_b64_tr_b16 a[216:217], v216 offset:4288\n"
#define F6X "ds_read_b64_tr_b16 a[218:219], v216 offset:4800\n"
#define F6Y "ds_read_b64_tr_b16 a[220:221], v216 offset:8512\n"
#define F6Z "ds_read_b64_tr_b16 a[222:223], v216 offset:9024\n"
#define F6a "ds_read_b64_tr_b16 a[224:225], v216 offset:4320\n"
#define F6b "ds_read_b64_tr_b16 a[226:227], v216 offset:4832\n"
#define F6c "ds_read_b64_tr_b16 a[228:229], v216 offset:8544\n"
#define F6d "ds_read_b64_tr_b16 a[230:231], v216 offset:9056\n"
#define F6e "ds_read_b64_tr_b16 a[232:233], v216 offset:25344\n"
#define F6f "ds_read_b64_tr_b16 a[234:235], v216 offset:25856\n"
#define F0j "ds_read_b64_tr_b16 a[236:237], v216 offset:29568\n"
#define F0k "ds_read_b64_tr_b16 a[238:239], v216 offset:30080\n"
#define F6g "ds_read_b64_tr_b16 a[216:217], v216 offset:25376\n"
#define F6h "ds_read_b64_tr_b16 a[218:219], v216 offset:25888\n"
#define F6i "ds_read_b64_tr_b16 a[220:221], v216 offset:29600\n"
#define F6j "ds_read_b64_tr_b16 a[222:223], v216 offset:30112\n"
#define F6k "ds_read_b128 a[192:195], v214 offset:21632\n"
#define F6l "ds_read_b128 a[196:199], v214 offset:21696\n"
#define F6m "ds_read_b128 a[200:203], v214 offset:17408\n"
#define F6n "ds_read_b128 a[204:207], v214 offset:17472\n"
#define F6o "ds_read_b128 a[208:211], v214 offset:13184\n"
#define F6p "ds_read_b128 a[212:215], v214 offset:13248\n"
#define F6q "ds_read_b64_tr_b16 a[228:229], v216 offset:29632\n"
#define F6r "ds_read_b64_tr_b16 a[230:231], v216 offset:30144\n"
#define F6s "ds_read_b64_tr_b16 a[224:225], v216 offset:25408\n"
#define F6t "ds_read_b64_tr_b16 a[226:227], v216 offset:25920\n"
#define F6u "ds_read_b64_tr_b16 a[232:233], v216 offset:25440\n"
#define F6v "ds_read_b64_tr_b16 a[234:235], v216 offset:25952\n"
#define F6w "ds_read_b64_tr_b16 a[236:237], v216 offset:29664\n"
#define F6x "ds_read_b64_tr_b16 a[238:239], v216 offset:30176\n"
#define E4k "s_add_u32 m0, s89, 110080\n"
#define F6y "ds_read_b128 a[212:215], v214 offset:33856\n"
#define F6z "ds_read_b128 a[208:211], v214 offset:33792\n"
#define F60 "ds_read_b128 a[204:207], v214 offset:38080\n"
#define F61 "ds_read_b128 a[200:203], v214 offset:38016\n"
#define F62 "ds_read_b128 a[196:199], v214 offset:42304\n"
#define F63 "ds_read_b128 a[192:195], v214 offset:42240\n"
#define F64 "ds_read_b64_tr_b16 a[216:217], v216 offset:46464\n"
#define F65 "ds_read_b64_tr_b16 a[218:219], v216 offset:46976\n"
#define F66 "ds_read_b64_tr_b16 a[220:221], v216 offset:50688\n"
#define F67 "ds_read_b64_tr_b16 a[222:223], v216 offset:51200\n"
#define F68 "ds_read_b64_tr_b16 a[224:225], v216 offset:46496\n"
#define F69 "ds_read_b64_tr_b16 a[226:227], v216 offset:47008\n"
#define F7A "ds_read_b64_tr_b16 a[228:229], v216 offset:50720\n"
#define F7B "ds_read_b64_tr_b16 a[230:231], v216 offset:51232\n"
#define F7C "ds_read_b64_tr_b16 a[232:233], v216 offset:46528\n"
#define F7D "ds_read_b64_tr_b16 a[234:235], v216 offset:47040\n"
#define F7E "ds_read_b64_tr_b16 a[236:237], v216 offset:50752\n"
#define F7F "ds_read_b64_tr_b16 a[238:239], v216 offset:51264\n"
#define F7G "ds_read_b128 a[212:215], v214 offset:34368\n"
#define F7H "ds_read_b128 a[208:211], v214 offset:34304\n"
#define F7I "ds_read_b128 a[204:207], v214 offset:38592\n"
#define F7J "ds_read_b128 a[200:203], v214 offset:38528\n"
#define F7K "ds_read_b128 a[196:199], v214 offset:42816\n"
#define F7L "ds_read_b128 a[192:195], v214 offset:42752\n"
#define F7M "ds_read_b64_tr_b16 a[216:217], v216 offset:46560\n"
#define F7N "ds_read_b64_tr_b16 a[218:219], v216 offset:47072\n"
#define F7O "ds_read_b64_tr_b16 a[220:221], v216 offset:50784\n"
#define F7P "ds_read_b64_tr_b16 a[222:223], v216 offset:51296\n"
#define F0l "ds_read_b64_tr_b16 a[224:225], v216 offset:54912\n"
#define F0m "ds_read_b64_tr_b16 a[226:227], v216 offset:55424\n"
#define F0n "ds_read_b64_tr_b16 a[228:229], v216 offset:59136\n"
#define F0o "ds_read_b64_tr_b16 a[230:231], v216 offset:59648\n"
#define F0p "ds_read_b64_tr_b16 a[232:233], v216 offset:54944\n"
#define F0q "ds_read_b64_tr_b16 a[234:235], v216 offset:55456\n"
#define F0r "ds_read_b64_tr_b16 a[236:237], v216 offset:59168\n"
#define F0s "ds_read_b64_tr_b16 a[238:239], v216 offset:59680\n"
#define F0t "ds_read_b128 a[212:215], v215 offset:64\n"
#define F0u "ds_read_b128 a[208:211], v215 offset:0\n"
#define F0v "ds_read_b128 a[204:207], v215 offset:4288\n"
#define F0w "ds_read_b128 a[200:203], v215 offset:4224\n"
#define E4l "ds_read_b128 a[196:199], v215 offset:8512\n"
#define E4m "ds_read_b128 a[192:195], v215 offset:8448\n"
#define E4n "ds_read_b64_tr_b16 a[216:217], v216 offset:54976\n"
#define E4o "ds_read_b64_tr_b16 a[218:219], v216 offset:55488\n"
#define F0x "ds_read_b64_tr_b16 a[220:221], v216 offset:59200\n"
#define F0y "ds_read_b64_tr_b16 a[222:223], v216 offset:59712\n"
#define E4p "ds_read_b64_tr_b16 a[224:225], v216 offset:55008\n"
#define E4q "ds_read_b64_tr_b16 a[226:227], v216 offset:55520\n"
#define F0z "ds_read_b64_tr_b16 a[228:229], v216 offset:59232\n"
#define F00 "ds_read_b64_tr_b16 a[230:231], v216 offset:59744\n"
#define E4r "ds_read_b64_tr_b16 a[232:233], v217 offset:12672\n"
#define E4s "ds_read_b64_tr_b16 a[234:235], v217 offset:13184\n"
#define E4t "ds_read_b64_tr_b16 a[236:237], v217 offset:16896\n"
#define E4u "ds_read_b64_tr_b16 a[238:239], v217 offset:17408\n"
#define E4v "ds_read_b64_tr_b16 a[144:145], v219 offset:16896\n"
#define E4w "ds_read_b64_tr_b16 a[146:147], v219 offset:17408\n"
#define E4x "ds_read_b64_tr_b16 a[148:149], v219 offset:16928\n"
#define F01 "ds_read_b128 a[212:215], v215 offset:576\n"
#define F02 "ds_read_b128 a[208:211], v215 offset:512\n"
#define F03 "ds_read_b128 a[204:207], v215 offset:4800\n"
#define F04 "ds_read_b128 a[200:203], v215 offset:4736\n"
#define F05 "ds_read_b128 a[196:199], v215 offset:9024\n"
#define F06 "ds_read_b128 a[192:195], v215 offset:8960\n"
#define F07 "ds_read_b64_tr_b16 a[220:221], v217 offset:16928\n"
#define F08 "ds_read_b64_tr_b16 a[216:217], v217 offset:12704\n"
#define F09 "ds_read_b64_tr_b16 a[218:219], v217 offset:13216\n"
#define F1A "ds_read_b64_tr_b16 a[222:223], v217 offset:17440\n"
#define F1B "ds_read_b64_tr_b16 a[228:229], v217 offset:16960\n"
#define F1C "ds_read_b64_tr_b16 a[224:225], v217 offset:12736\n"
#define F1D "ds_read_b64_tr_b16 a[226:227], v217 offset:13248\n"
#define F1E "ds_read_b64_tr_b16 a[230:231], v217 offset:17472\n"
#define F1F "ds_read_b64_tr_b16 a[232:233], v217 offset:12768\n"
#define F1G "ds_read_b64_tr_b16 a[234:235], v217 offset:13280\n"
#define F1H "ds_read_b64_tr_b16 a[236:237], v217 offset:16992\n"
#define F1I "ds_read_b64_tr_b16 a[238:239], v217 offset:17504\n"
#define E4y "ds_read_b64_tr_b16 a[150:151], v219 offset:17440\n"
#define E4z "ds_read_b64_tr_b16 a[152:153], v219 offset:16960\n"
#define E40 "ds_read_b64_tr_b16 a[154:155], v219 offset:17472\n"
#define E41 "ds_read_b64_tr_b16 a[156:157], v219 offset:16992\n"
#define F7Q "ds_read_b128 a[204:207], v215 offset:25408\n"
#define F7R "ds_read_b128 a[200:203], v215 offset:25344\n"
#define E42 "ds_read_b64_tr_b16 a[216:217], v216 offset:4224\n"
#define E43 "ds_read_b64_tr_b16 a[218:219], v216 offset:4736\n"
#define E44 "ds_read_b64_tr_b16 a[220:221], v216 offset:8448\n"
#define E45 "ds_read_b64_tr_b16 a[222:223], v216 offset:8960\n"
#define E46 "ds_read_b64_tr_b16 a[224:225], v216 offset:4256\n"
#define E47 "ds_read_b64_tr_b16 a[226:227], v216 offset:4768\n"
#define E48 "ds_read_b64_tr_b16 a[228:229], v216 offset:8480\n"
#define E49 "ds_read_b64_tr_b16 a[230:231], v216 offset:8992\n"
#define E5A "ds_read_b64_tr_b16 a[158:159], v219 offset:17504\n"
#define E5B "ds_read_b64_tr_b16 a[236:237], v216 offset:8512\n"
#define E5C "ds_read_b64_tr_b16 a[232:233], v216 offset:4288\n"
#define E5D "ds_read_b64_tr_b16 a[234:235], v216 offset:4800\n"
#define E5E "ds_read_b64_tr_b16 a[238:239], v216 offset:9024\n"
#define E5F "ds_read_b64_tr_b16 a[168:169], v219 offset:0\n"
#define E5G "ds_read_b64_tr_b16 a[170:171], v219 offset:512\n"
#define F7S "ds_read_b128 a[200:203], v215 offset:25856\n"
#define F7T "ds_read_b128 a[204:207], v215 offset:25920\n"
#define F7U "ds_read_b128 a[208:211], v215 offset:21632\n"
#define F7V "ds_read_b128 a[212:215], v215 offset:21696\n"
#define E5H "ds_read_b64_tr_b16 a[216:217], v216 offset:4320\n"
#define E5I "ds_read_b64_tr_b16 a[218:219], v216 offset:4832\n"
#define E5J "ds_read_b64_tr_b16 a[220:221], v216 offset:8544\n"
#define E5K "ds_read_b64_tr_b16 a[222:223], v216 offset:9056\n"
#define E5L "ds_read_b64_tr_b16 a[224:225], v216 offset:12672\n"
#define E5M "ds_read_b64_tr_b16 a[226:227], v216 offset:13184\n"
#define E5N "ds_read_b64_tr_b16 a[228:229], v216 offset:16896\n"
#define E5O "ds_read_b64_tr_b16 a[230:231], v216 offset:17408\n"
#define E5P "ds_read_b64_tr_b16 a[172:173], v219 offset:32\n"
#define E5Q "ds_read_b64_tr_b16 a[174:175], v219 offset:544\n"
#define E5R "ds_read_b64_tr_b16 a[232:233], v216 offset:12704\n"
#define E5S "ds_read_b64_tr_b16 a[234:235], v216 offset:13216\n"
#define E5T "ds_read_b64_tr_b16 a[236:237], v216 offset:16928\n"
#define E5U "ds_read_b64_tr_b16 a[238:239], v216 offset:17440\n"
#define E5V "ds_read_b64_tr_b16 a[176:177], v219 offset:64\n"
#define E5W "ds_read_b64_tr_b16 a[178:179], v219 offset:576\n"
#define E5X "ds_read_b64_tr_b16 a[180:181], v219 offset:96\n"
#define E5Y "ds_read_b64_tr_b16 a[182:183], v219 offset:608\n"
#define E5Z "ds_read_b128 a[212:215], v214 offset:21184\n"
#define E5a "ds_read_b128 a[208:211], v214 offset:21120\n"
#define E5b "ds_read_b128 a[204:207], v214 offset:25408\n"
#define E5c "ds_read_b128 a[200:203], v214 offset:25344\n"
#define E5d "ds_read_b128 a[196:199], v214 offset:29632\n"
#define E5e "ds_read_b128 a[192:195], v214 offset:29568\n"
#define E5f "ds_read_b64_tr_b16 a[216:217], v216 offset:12736\n"
#define E5g "ds_read_b64_tr_b16 a[218:219], v216 offset:13248\n"
#define E5h "ds_read_b64_tr_b16 a[220:221], v216 offset:16960\n"
#define E5i "ds_read_b64_tr_b16 a[222:223], v216 offset:17472\n"
#define E5j "ds_read_b64_tr_b16 a[224:225], v216 offset:12768\n"
#define E5k "ds_read_b64_tr_b16 a[226:227], v216 offset:13280\n"
#define E5l "ds_read_b64_tr_b16 a[228:229], v216 offset:16992\n"
#define E5m "ds_read_b64_tr_b16 a[230:231], v216 offset:17504\n"
#define E5n "ds_read_b64_tr_b16 a[232:233], v216 offset:33792\n"
#define E5o "ds_read_b64_tr_b16 a[234:235], v216 offset:34304\n"
#define F7W "ds_read_b64_tr_b16 a[236:237], v216 offset:38016\n"
#define F7X "ds_read_b64_tr_b16 a[238:239], v216 offset:38528\n"
#define E5p "ds_read_b128 a[192:195], v214 offset:30080\n"
#define E5q "ds_read_b128 a[196:199], v214 offset:30144\n"
#define E5r "ds_read_b128 a[200:203], v214 offset:25856\n"
#define E5s "ds_read_b128 a[204:207], v214 offset:25920\n"
#define E5t "ds_read_b128 a[208:211], v214 offset:21632\n"
#define E5u "ds_read_b128 a[212:215], v214 offset:21696\n"
#define E5v "ds_read_b64_tr_b16 a[216:217], v216 offset:33824\n"
#define E5w "ds_read_b64_tr_b16 a[218:219], v216 offset:34336\n"
#define F7Y "ds_read_b64_tr_b16 a[220:221], v216 offset:38048\n"
#define F7Z "ds_read_b64_tr_b16 a[222:223], v216 offset:38560\n"
#define E5x "ds_read_b64_tr_b16 a[224:225], v216 offset:33856\n"
#define E5y "ds_read_b64_tr_b16 a[226:227], v216 offset:34368\n"
#define F7a "ds_read_b64_tr_b16 a[228:229], v216 offset:38080\n"
#define F7b "ds_read_b64_tr_b16 a[230:231], v216 offset:38592\n"
#define E5z "ds_read_b64_tr_b16 a[232:233], v216 offset:33888\n"
#define E50 "ds_read_b64_tr_b16 a[234:235], v216 offset:34400\n"
#define F7c "ds_read_b64_tr_b16 a[236:237], v216 offset:38112\n"
#define F7d "ds_read_b64_tr_b16 a[238:239], v216 offset:38624\n"
#define E51 "ds_write_b64 v220, v[148:149] offset:16896\n"
#define F7e "ds_read_b128 a[212:215], v214 offset:42304\n"
#define F7f "ds_read_b128 a[208:211], v214 offset:42240\n"
#define F7g "ds_read_b128 a[204:207], v214 offset:46528\n"
#define E52 "ds_write_b64 v220, v[150:151] offset:16928\n"
#define F7h "ds_read_b128 a[200:203], v214 offset:46464\n"
#define F7i "ds_read_b64_tr_b16 a[216:217], v216 offset:54912\n"
#define F7j "ds_read_b64_tr_b16 a[218:219], v216 offset:55424\n"
#define E53 "ds_read_b64_tr_b16 a[220:221], v216 offset:59136\n"
#define E54 "ds_read_b64_tr_b16 a[222:223], v216 offset:59648\n"
#define F7k "ds_read_b64_tr_b16 a[224:225], v216 offset:54944\n"
#define F7l "ds_read_b64_tr_b16 a[226:227], v216 offset:55456\n"
#define E55 "ds_read_b64_tr_b16 a[228:229], v216 offset:59168\n"
#define E56 "ds_read_b64_tr_b16 a[230:231], v216 offset:59680\n"
#define F7m "ds_read_b64_tr_b16 a[232:233], v216 offset:54976\n"
#define F7n "ds_read_b64_tr_b16 a[234:235], v216 offset:55488\n"
#define E57 "ds_read_b64_tr_b16 a[236:237], v216 offset:59200\n"
#define E58 "ds_read_b64_tr_b16 a[238:239], v216 offset:59712\n"
#define F7o "ds_read_b128 a[192:195], v214 offset:51200\n"
#define E59 "ds_write_b64 v220, v[148:149] offset:16960\n"
#define F7p "ds_read_b128 a[196:199], v214 offset:51264\n"
#define F7q "ds_read_b128 a[200:203], v214 offset:46976\n"
#define E6A "ds_write_b64 v220, v[150:151] offset:16992\n"
#define E6B "ds_read_b128 a[0:3], v221 offset:16896\n"
#define E6C "ds_read_b128 a[4:7], v221 offset:18048\n"
#define E6D "ds_write_b64 v220, v[156:157] offset:0\n"
#define F7r "ds_read_b128 a[204:207], v214 offset:47040\n"
#define F7s "ds_read_b128 a[208:211], v214 offset:42752\n"
#define F7t "ds_read_b128 a[212:215], v214 offset:42816\n"
#define F7u "ds_read_b64_tr_b16 a[216:217], v216 offset:55008\n"
#define F7v "ds_read_b64_tr_b16 a[218:219], v216 offset:55520\n"
#define E6E "ds_read_b64_tr_b16 a[220:221], v216 offset:59232\n"
#define E6F "ds_read_b64_tr_b16 a[222:223], v216 offset:59744\n"
#define E6G "ds_read_b64_tr_b16 a[224:225], v217 offset:0\n"
#define E6H "ds_write_b64 v220, v[158:159] offset:32\n"
#define E6I "ds_read_b64_tr_b16 a[226:227], v217 offset:512\n"
#define E6J "ds_read_b64_tr_b16 a[228:229], v217 offset:4224\n"
#define E6K "ds_read_b64_tr_b16 a[230:231], v217 offset:4736\n"
#define E6L "ds_read_b64_tr_b16 a[232:233], v217 offset:32\n"
#define E6M "ds_read_b64_tr_b16 a[234:235], v217 offset:544\n"
#define E6N "ds_read_b64_tr_b16 a[236:237], v217 offset:4256\n"
#define E6O "ds_read_b64_tr_b16 a[238:239], v217 offset:4768\n"
#define E6P "ds_read_b128 a[212:215], v215 offset:8512\n"
#define E6Q "ds_read_b128 a[208:211], v215 offset:8448\n"
#define E6R "ds_read_b128 a[196:199], v215 offset:16960\n"
#define E6S "ds_read_b128 a[192:195], v215 offset:16896\n"
#define E6T "ds_read_b64_tr_b16 a[216:217], v217 offset:64\n"
#define E6U "ds_write_b64 v220, v[156:157] offset:64\n"
#define E6V "ds_read_b64_tr_b16 a[218:219], v217 offset:576\n"
#define E6W "ds_read_b64_tr_b16 a[220:221], v217 offset:4288\n"
#define E6X "ds_write_b64 v220, v[158:159] offset:96\n"
#define E6Y "ds_read_b128 a[48:51], v221 offset:0\n"
#define E6Z "ds_read_b128 a[52:55], v221 offset:1152\n"
#define E6a "ds_read_b64_tr_b16 a[222:223], v217 offset:4800\n"
#define E6b "ds_read_b64_tr_b16 a[224:225], v217 offset:96\n"
#define E6c "ds_read_b64_tr_b16 a[226:227], v217 offset:608\n"
#define E6d "ds_read_b64_tr_b16 a[228:229], v217 offset:4320\n"
#define E6e "ds_read_b64_tr_b16 a[230:231], v217 offset:4832\n"
#define D9Y "ds_read_b64_tr_b16 a[232:233], v217 offset:21120\n"
#define D9Z "ds_read_b64_tr_b16 a[234:235], v217 offset:21632\n"
#define D9a "ds_read_b64_tr_b16 a[236:237], v217 offset:25344\n"
#define D9b "ds_read_b64_tr_b16 a[238:239], v217 offset:25856\n"
#define F7w "ds_read_b128 a[192:195], v215 offset:17408\n"
#define F7x "ds_read_b128 a[196:199], v215 offset:17472\n"
#define F7y "ds_read_b128 a[200:203], v215 offset:13184\n"
#define F7z "ds_read_b128 a[204:207], v215 offset:13248\n"
#define F70 "ds_read_b128 a[208:211], v215 offset:8960\n"
#define F71 "ds_read_b128 a[212:215], v215 offset:9024\n"
#define F1J "ds_read_b64_tr_b16 a[216:217], v217 offset:21152\n"
#define F1K "ds_read_b64_tr_b16 a[218:219], v217 offset:21664\n"
#define F1L "ds_read_b64_tr_b16 a[220:221], v217 offset:25376\n"
#define F1M "ds_read_b64_tr_b16 a[222:223], v217 offset:25888\n"
#define F1N "ds_read_b64_tr_b16 a[228:229], v217 offset:25408\n"
#define F1O "ds_read_b64_tr_b16 a[230:231], v217 offset:25920\n"
#define E6f "s_cbranch_scc1 Lb4_3\n"
#define F1P "ds_read_b64_tr_b16 a[232:233], v217 offset:21216\n"
#define F1Q "ds_read_b64_tr_b16 a[234:235], v217 offset:21728\n"
#define E16 "s_add_u32 m0, s88, 109952\n"
#define F1R "ds_read_b64_tr_b16 a[224:225], v217 offset:21184\n"
#define F1S "ds_read_b64_tr_b16 a[226:227], v217 offset:21696\n"
#define F1T "ds_read_b64_tr_b16 a[236:237], v217 offset:25440\n"
#define F1U "ds_read_b64_tr_b16 a[238:239], v217 offset:25952\n"
#define F1V "s_add_u32 m0, s88, 55040\n"
#define F72 "s_addk_i32 m0, -21120\n"
#define F1W "ds_read_b64_tr_b16 a[220:221], v217 offset:59136\n"
#define F1X "ds_read_b64_tr_b16 a[216:217], v217 offset:54912\n"
#define F1Y "ds_read_b64_tr_b16 a[218:219], v217 offset:55424\n"
#define F1Z "ds_read_b64_tr_b16 a[222:223], v217 offset:59648\n"
#define F1a "ds_read_b64_tr_b16 a[224:225], v217 offset:54944\n"
#define F1b "ds_read_b64_tr_b16 a[226:227], v217 offset:55456\n"
#define F1c "ds_read_b64_tr_b16 a[228:229], v217 offset:59168\n"
#define F1d "ds_read_b64_tr_b16 a[230:231], v217 offset:59680\n"
#define F1e "ds_read_b64_tr_b16 a[232:233], v217 offset:54976\n"
#define F1f "ds_read_b64_tr_b16 a[234:235], v217 offset:55488\n"
#define F1g "ds_read_b64_tr_b16 a[236:237], v217 offset:59200\n"
#define F1h "ds_read_b64_tr_b16 a[238:239], v217 offset:59712\n"
#define F73 "ds_read_b64_tr_b16 a[220:221], v217 offset:59232\n"
#define F74 "ds_read_b64_tr_b16 a[222:223], v217 offset:59744\n"
#define F75 "ds_read_b64_tr_b16 a[216:217], v217 offset:55008\n"
#define F76 "ds_read_b64_tr_b16 a[218:219], v217 offset:55520\n"
#define F1i "ds_read_b128 a[212:215], v215 offset:29632\n"
#define F1j "ds_read_b128 a[208:211], v215 offset:29568\n"
#define F1k "ds_read_b128 a[204:207], v215 offset:33856\n"
#define F1l "ds_read_b128 a[200:203], v215 offset:33792\n"
#define E6g "s_add_u32 m0, s88, 109824\n"
#define F1m "ds_read_b64_tr_b16 a[216:217], v217 offset:42240\n"
#define F1n "ds_read_b64_tr_b16 a[218:219], v217 offset:42752\n"
#define E17 "ds_read_b64_tr_b16 a[220:221], v217 offset:21120\n"
#define E18 "ds_read_b64_tr_b16 a[222:223], v217 offset:21632\n"
#define F1o "ds_read_b64_tr_b16 a[224:225], v217 offset:42272\n"
#define F1p "ds_read_b64_tr_b16 a[226:227], v217 offset:42784\n"
#define E19 "ds_read_b64_tr_b16 a[228:229], v217 offset:21152\n"
#define E2A "ds_read_b64_tr_b16 a[230:231], v217 offset:21664\n"
#define F1q "ds_read_b64_tr_b16 a[232:233], v217 offset:42304\n"
#define F1r "ds_read_b64_tr_b16 a[234:235], v217 offset:42816\n"
#define E2B "ds_read_b64_tr_b16 a[236:237], v217 offset:21184\n"
#define E2C "ds_read_b64_tr_b16 a[238:239], v217 offset:21696\n"
#define F77 "ds_read_b128 a[200:203], v215 offset:34304\n"
#define F78 "ds_read_b128 a[204:207], v215 offset:34368\n"
#define F79 "ds_read_b128 a[208:211], v215 offset:30080\n"
#define F8A "ds_read_b128 a[212:215], v215 offset:30144\n"
#define F8B "s_add_u32 m0, s88, 54912\n"
#define F8C "ds_read_b64_tr_b16 a[216:217], v217 offset:42336\n"
#define F8D "ds_read_b64_tr_b16 a[218:219], v217 offset:42848\n"
#define E6h "ds_read_b64_tr_b16 a[220:221], v217 offset:21216\n"
#define E6i "ds_read_b64_tr_b16 a[222:223], v217 offset:21728\n"
#define E6j "ds_read_b64_tr_b16 a[224:225], v217 offset:25344\n"
#define E6k "ds_read_b64_tr_b16 a[226:227], v217 offset:25856\n"
#define E6l "ds_read_b64_tr_b16 a[232:233], v217 offset:25376\n"
#define E6m "ds_read_b64_tr_b16 a[234:235], v217 offset:25888\n"
#define E2D "ds_read_b64_tr_b16 a[216:217], v217 offset:25408\n"
#define E2E "ds_read_b64_tr_b16 a[218:219], v217 offset:25920\n"
#define E2F "ds_read_b64_tr_b16 a[224:225], v217 offset:25440\n"
#define E2G "ds_read_b64_tr_b16 a[226:227], v217 offset:25952\n"
#define E6n "ds_read_b128 a[204:207], v215 offset:50752\n"
#define E6o "ds_read_b128 a[200:203], v215 offset:50688\n"
#define E6p "ds_read_b128 a[196:199], v215 offset:54976\n"
#define E6q "ds_read_b128 a[192:195], v215 offset:54912\n"
#define F8E "ds_read_b64_tr_b16 a[232:233], v217 offset:59136\n"
#define F8F "ds_read_b64_tr_b16 a[234:235], v217 offset:59648\n"
#define E2H "ds_read_b64_tr_b16 a[236:237], v216 offset:54912\n"
#define E2I "ds_read_b64_tr_b16 a[238:239], v216 offset:55424\n"
#define F1s "ds_read_b128 a[192:195], v215 offset:55424\n"
#define F1t "ds_read_b128 a[196:199], v215 offset:55488\n"
#define F1u "ds_read_b128 a[200:203], v215 offset:51200\n"
#define F1v "ds_read_b128 a[204:207], v215 offset:51264\n"
#define E6r "ds_read_b64_tr_b16 a[220:221], v216 offset:54944\n"
#define E6s "ds_read_b64_tr_b16 a[222:223], v216 offset:55456\n"
#define E6t "ds_read_b64_tr_b16 a[228:229], v216 offset:54976\n"
#define E6u "ds_read_b64_tr_b16 a[230:231], v216 offset:55488\n"
#define E6v "ds_read_b64_tr_b16 a[236:237], v216 offset:55008\n"
#define E6w "ds_read_b64_tr_b16 a[238:239], v216 offset:55520\n"
#define F1w "ds_read_b128 a[204:207], v215 offset:64\n"
#define F1x "ds_read_b128 a[200:203], v215 offset:0\n"
#define F1y "ds_read_b128 a[196:199], v215 offset:4288\n"
#define F1z "ds_read_b128 a[192:195], v215 offset:4224\n"
#define F10 "ds_read_b64_tr_b16 a[220:221], v217 offset:12672\n"
#define F11 "ds_read_b64_tr_b16 a[216:217], v217 offset:8448\n"
#define F12 "ds_read_b64_tr_b16 a[218:219], v217 offset:8960\n"
#define F13 "ds_read_b64_tr_b16 a[222:223], v217 offset:13184\n"
#define F14 "ds_read_b64_tr_b16 a[224:225], v217 offset:8480\n"
#define F15 "ds_read_b64_tr_b16 a[226:227], v217 offset:8992\n"
#define F16 "ds_read_b64_tr_b16 a[228:229], v217 offset:12704\n"
#define F17 "ds_read_b64_tr_b16 a[230:231], v217 offset:13216\n"
#define F18 "ds_read_b64_tr_b16 a[232:233], v217 offset:8512\n"
#define F19 "ds_read_b64_tr_b16 a[234:235], v217 offset:9024\n"
#define D9c "s_add_u32 m0, s89, 126848\n"
#define F2A "ds_read_b64_tr_b16 a[236:237], v217 offset:12736\n"
#define F2B "ds_read_b64_tr_b16 a[238:239], v217 offset:13248\n"
#define F2C "ds_read_b128 a[192:195], v215 offset:4736\n"
#define F2D "ds_read_b128 a[196:199], v215 offset:4800\n"
#define F2E "ds_read_b128 a[200:203], v215 offset:512\n"
#define F2F "ds_read_b128 a[204:207], v215 offset:576\n"
#define F2G "ds_read_b64_tr_b16 a[216:217], v217 offset:8544\n"
#define F2H "ds_read_b64_tr_b16 a[218:219], v217 offset:9056\n"
#define F2I "ds_read_b64_tr_b16 a[220:221], v217 offset:12768\n"
#define F2J "ds_read_b64_tr_b16 a[222:223], v217 offset:13280\n"
#define F2K "ds_read_b64_tr_b16 a[224:225], v217 offset:16896\n"
#define F2L "ds_read_b64_tr_b16 a[226:227], v217 offset:17408\n"
#define F2M "ds_read_b64_tr_b16 a[232:233], v217 offset:16928\n"
#define F2N "ds_read_b64_tr_b16 a[234:235], v217 offset:17440\n"
#define F2O "ds_read_b64_tr_b16 a[216:217], v217 offset:16960\n"
#define F2P "ds_read_b64_tr_b16 a[218:219], v217 offset:17472\n"
#define F2Q "ds_read_b64_tr_b16 a[224:225], v217 offset:16992\n"
#define F2R "ds_read_b64_tr_b16 a[226:227], v217 offset:17504\n"
#define F8G "s_add_u32 m0, s88, 101632\n"
#define F2S "ds_read_b128 a[212:215], v215 offset:4288\n"
#define F2T "ds_read_b128 a[208:211], v215 offset:4224\n"
#define F8H "ds_read_b64_tr_b16 a[220:221], v217 offset:64\n"
#define F8I "ds_read_b64_tr_b16 a[222:223], v217 offset:576\n"
#define F8J "ds_read_b64_tr_b16 a[228:229], v217 offset:96\n"
#define F8K "ds_read_b64_tr_b16 a[230:231], v217 offset:608\n"
#define F2U "s_mov_b32 s92, 344064\n"
#define F2V "s_mov_b32 s92, 491520\n"
#define F2W "s_cbranch_scc1 Lb5_0\n"
#define F8L "ds_read_b32 v223, v223 offset:256\n"
#define E6y "s_cbranch_scc1 Lb5_1\n"
#define E60 "s_cbranch_scc1 Lb5_2\n"
#define E61 "s_waitcnt vmcnt(4)\n"
#define F8Q "ds_read_b64_tr_b16 a[232:233], v216 offset:0\n"
#define F8R "ds_read_b64_tr_b16 a[234:235], v216 offset:512\n"
#define F8S "ds_read_b64_tr_b16 a[236:237], v216 offset:4224\n"
#define F8T "ds_read_b64_tr_b16 a[238:239], v216 offset:4736\n"
#define F8U "ds_read_b64_tr_b16 a[216:217], v216 offset:32\n"
#define F8V "ds_read_b64_tr_b16 a[218:219], v216 offset:544\n"
#define F8W "ds_read_b64_tr_b16 a[220:221], v216 offset:4256\n"
#define F8X "ds_read_b64_tr_b16 a[222:223], v216 offset:4768\n"
#define F8Y "ds_read_b64_tr_b16 a[228:229], v216 offset:4288\n"
#define F8Z "ds_read_b64_tr_b16 a[230:231], v216 offset:4800\n"
#define F8a "ds_read_b64_tr_b16 a[224:225], v216 offset:64\n"
#define F8b "ds_read_b64_tr_b16 a[226:227], v216 offset:576\n"
#define F8c "ds_read_b64_tr_b16 a[232:233], v216 offset:96\n"
#define F8d "ds_read_b64_tr_b16 a[234:235], v216 offset:608\n"
#define F8e "ds_read_b64_tr_b16 a[236:237], v216 offset:4320\n"
#define F8f "ds_read_b64_tr_b16 a[238:239], v216 offset:4832\n"
#define F8g "ds_read_b128 a[204:207], v214 offset:12736\n"
#define F8h "ds_read_b128 a[200:203], v214 offset:12672\n"
#define F8i "ds_read_b128 a[196:199], v214 offset:16960\n"
#define F8j "ds_read_b128 a[192:195], v214 offset:16896\n"
#define F8k "ds_read_b64_tr_b16 a[216:217], v216 offset:21120\n"
#define F8l "ds_read_b64_tr_b16 a[218:219], v216 offset:21632\n"
#define F8m "ds_read_b64_tr_b16 a[220:221], v216 offset:25344\n"
#define F8n "ds_read_b64_tr_b16 a[222:223], v216 offset:25856\n"
#define F8o "ds_read_b64_tr_b16 a[224:225], v216 offset:21152\n"
#define F8p "ds_read_b64_tr_b16 a[226:227], v216 offset:21664\n"
#define F8q "ds_read_b64_tr_b16 a[228:229], v216 offset:25376\n"
#define F8r "ds_read_b64_tr_b16 a[230:231], v216 offset:25888\n"
#define F8s "ds_read_b64_tr_b16 a[232:233], v216 offset:21184\n"
#define F8t "ds_read_b64_tr_b16 a[234:235], v216 offset:21696\n"
#define F8u "ds_read_b64_tr_b16 a[236:237], v216 offset:25408\n"
#define F8v "ds_read_b64_tr_b16 a[238:239], v216 offset:25920\n"
#define F8w "ds_read_b128 a[204:207], v214 offset:13248\n"
#define F8x "ds_read_b128 a[200:203], v214 offset:13184\n"
#define F8y "ds_read_b128 a[196:199], v214 offset:17472\n"
#define F8z "ds_read_b128 a[192:195], v214 offset:17408\n"
#define F80 "ds_read_b64_tr_b16 a[216:217], v216 offset:21216\n"
#define F81 "ds_read_b64_tr_b16 a[218:219], v216 offset:21728\n"
#define F82 "ds_read_b64_tr_b16 a[220:221], v216 offset:25440\n"
#define F83 "ds_read_b64_tr_b16 a[222:223], v216 offset:25952\n"
#define F2Z "ds_read_b64_tr_b16 a[228:229], v216 offset:33792\n"
#define F2a "ds_read_b64_tr_b16 a[224:225], v216 offset:29568\n"
#define F2b "ds_read_b64_tr_b16 a[226:227], v216 offset:30080\n"
#define F2c "ds_read_b64_tr_b16 a[230:231], v216 offset:34304\n"
#define F2d "ds_read_b64_tr_b16 a[236:237], v216 offset:33824\n"
#define F2e "ds_read_b64_tr_b16 a[232:233], v216 offset:29600\n"
#define F2f "ds_read_b64_tr_b16 a[234:235], v216 offset:30112\n"
#define F2g "ds_read_b64_tr_b16 a[238:239], v216 offset:34336\n"
#define F2h "ds_read_b128 a[212:215], v214 offset:38080\n"
#define F2i "ds_read_b128 a[208:211], v214 offset:38016\n"
#define F2j "ds_read_b128 a[204:207], v214 offset:42304\n"
#define F2k "ds_read_b128 a[200:203], v214 offset:42240\n"
#define F2l "ds_read_b128 a[196:199], v214 offset:46528\n"
#define F2m "ds_read_b128 a[192:195], v214 offset:46464\n"
#define F2n "ds_read_b64_tr_b16 a[216:217], v216 offset:29632\n"
#define F2o "ds_read_b64_tr_b16 a[218:219], v216 offset:30144\n"
#define F2p "ds_read_b64_tr_b16 a[220:221], v216 offset:33856\n"
#define F2q "ds_read_b64_tr_b16 a[222:223], v216 offset:34368\n"
#define F2r "ds_read_b64_tr_b16 a[224:225], v216 offset:29664\n"
#define F2s "ds_read_b64_tr_b16 a[226:227], v216 offset:30176\n"
#define E62 "s_cbranch_scc1 Lb5_3\n"
#define F2t "ds_read_b64_tr_b16 a[228:229], v216 offset:33888\n"
#define F2u "ds_read_b64_tr_b16 a[230:231], v216 offset:34400\n"
#define E63 "ds_read_b64_tr_b16 a[232:233], v216 offset:50688\n"
#define E64 "ds_read_b64_tr_b16 a[234:235], v216 offset:51200\n"
#define F2v "ds_read_b128 a[212:215], v214 offset:38592\n"
#define F2w "ds_read_b128 a[208:211], v214 offset:38528\n"
#define F2x "ds_read_b128 a[204:207], v214 offset:42816\n"
#define F2y "ds_read_b128 a[200:203], v214 offset:42752\n"
#define F2z "ds_read_b128 a[196:199], v214 offset:47040\n"
#define F20 "ds_read_b128 a[192:195], v214 offset:46976\n"
#define F21 "ds_read_b64_tr_b16 a[216:217], v216 offset:50720\n"
#define F22 "ds_read_b64_tr_b16 a[218:219], v216 offset:51232\n"
#define F23 "ds_read_b64_tr_b16 a[224:225], v216 offset:50752\n"
#define F24 "ds_read_b64_tr_b16 a[226:227], v216 offset:51264\n"
#define F25 "ds_read_b64_tr_b16 a[232:233], v216 offset:50784\n"
#define F26 "ds_read_b64_tr_b16 a[234:235], v216 offset:51296\n"
#define F84 "s_add_u32 m0, s88, 29824\n"
#define F85 "ds_read_b64_tr_b16 a[228:229], v217 offset:21120\n"
#define F86 "ds_read_b64_tr_b16 a[230:231], v217 offset:21632\n"
#define F87 "ds_read_b64_tr_b16 a[236:237], v217 offset:21152\n"
#define F88 "ds_read_b64_tr_b16 a[238:239], v217 offset:21664\n"
#define F89 "ds_read_b128 a[212:215], v215 offset:25408\n"
#define F9A "ds_read_b128 a[208:211], v215 offset:25344\n"
#define E65 "ds_read_b128 a[196:199], v214 offset:4288\n"
#define E66 "ds_read_b128 a[192:195], v214 offset:4224\n"
#define F9B "ds_read_b64_tr_b16 a[220:221], v217 offset:21184\n"
#define F9C "ds_read_b64_tr_b16 a[222:223], v217 offset:21696\n"
#define F9D "ds_read_b64_tr_b16 a[228:229], v217 offset:21216\n"
#define F9E "ds_read_b64_tr_b16 a[230:231], v217 offset:21728\n"
#define E67 "ds_read_b64_tr_b16 a[232:233], v216 offset:8448\n"
#define E68 "ds_read_b64_tr_b16 a[234:235], v216 offset:8960\n"
#define E69 "ds_read_b64_tr_b16 a[236:237], v216 offset:12672\n"
#define E7A "ds_read_b128 a[192:195], v214 offset:4736\n"
#define E7B "ds_read_b64_tr_b16 a[238:239], v216 offset:13184\n"
#define E7C "ds_read_b128 a[196:199], v214 offset:4800\n"
#define F9F "ds_read_b128 a[208:211], v215 offset:25856\n"
#define F9G "ds_read_b128 a[212:215], v215 offset:25920\n"
#define E7D "ds_read_b64_tr_b16 a[216:217], v216 offset:8480\n"
#define E7E "ds_read_b64_tr_b16 a[218:219], v216 offset:8992\n"
#define E7F "ds_read_b64_tr_b16 a[220:221], v216 offset:12704\n"
#define E7G "ds_read_b64_tr_b16 a[222:223], v216 offset:13216\n"
#define E7H "ds_read_b64_tr_b16 a[224:225], v216 offset:8512\n"
#define E7I "ds_read_b64_tr_b16 a[226:227], v216 offset:9024\n"
#define E7J "ds_read_b64_tr_b16 a[228:229], v216 offset:12736\n"
#define E7K "ds_read_b64_tr_b16 a[230:231], v216 offset:13248\n"
#define E7L "ds_read_b64_tr_b16 a[232:233], v216 offset:8544\n"
#define E7M "ds_read_b64_tr_b16 a[236:237], v216 offset:12768\n"
#define E7N "ds_read_b64_tr_b16 a[238:239], v216 offset:13280\n"
#define E7O "ds_read_b64_tr_b16 a[234:235], v216 offset:9056\n"
#define E7P "ds_read_b128 a[212:215], v214 offset:16960\n"
#define E7Q "ds_read_b128 a[208:211], v214 offset:16896\n"
#define E7R "ds_read_b128 a[204:207], v214 offset:21184\n"
#define E7S "ds_read_b128 a[200:203], v214 offset:21120\n"
#define E7T "ds_read_b128 a[196:199], v214 offset:25408\n"
#define E7U "ds_read_b128 a[192:195], v214 offset:25344\n"
#define E7V "ds_read_b64_tr_b16 a[216:217], v216 offset:29568\n"
#define E7W "ds_read_b64_tr_b16 a[218:219], v216 offset:30080\n"
#define E7X "ds_read_b64_tr_b16 a[220:221], v216 offset:33792\n"
#define E7Y "ds_read_b64_tr_b16 a[222:223], v216 offset:34304\n"
#define E7Z "ds_read_b64_tr_b16 a[224:225], v216 offset:29600\n"
#define E7a "ds_read_b64_tr_b16 a[226:227], v216 offset:30112\n"
#define E7b "ds_read_b64_tr_b16 a[228:229], v216 offset:33824\n"
#define E7c "ds_read_b64_tr_b16 a[230:231], v216 offset:34336\n"
#define E7d "ds_read_b64_tr_b16 a[232:233], v216 offset:29632\n"
#define E7e "ds_read_b64_tr_b16 a[234:235], v216 offset:30144\n"
#define E7f "ds_read_b64_tr_b16 a[236:237], v216 offset:33856\n"
#define E7g "ds_read_b64_tr_b16 a[238:239], v216 offset:34368\n"
#define E7h "ds_read_b128 a[192:195], v214 offset:25856\n"
#define E7i "ds_read_b128 a[196:199], v214 offset:25920\n"
#define E7j "ds_read_b128 a[200:203], v214 offset:21632\n"
#define E7k "ds_read_b128 a[204:207], v214 offset:21696\n"
#define E7l "ds_read_b128 a[208:211], v214 offset:17408\n"
#define E7m "ds_read_b128 a[212:215], v214 offset:17472\n"
#define E7n "ds_read_b64_tr_b16 a[216:217], v216 offset:29664\n"
#define E7o "ds_read_b64_tr_b16 a[218:219], v216 offset:30176\n"
#define E7p "ds_read_b64_tr_b16 a[220:221], v216 offset:33888\n"
#define E7q "ds_read_b64_tr_b16 a[222:223], v216 offset:34400\n"
#define F9H "ds_read_b64_tr_b16 a[224:225], v216 offset:38016\n"
#define F9I "ds_read_b64_tr_b16 a[226:227], v216 offset:38528\n"
#define F9J "ds_read_b64_tr_b16 a[228:229], v216 offset:42240\n"
#define F9K "ds_read_b64_tr_b16 a[230:231], v216 offset:42752\n"
#define F9L "ds_read_b64_tr_b16 a[232:233], v216 offset:38048\n"
#define F9M "ds_read_b64_tr_b16 a[234:235], v216 offset:38560\n"
#define F9N "ds_read_b64_tr_b16 a[236:237], v216 offset:42272\n"
#define F9O "ds_read_b64_tr_b16 a[238:239], v216 offset:42784\n"
#define F9P "ds_read_b128 a[212:215], v214 offset:46528\n"
#define F9Q "ds_read_b128 a[208:211], v214 offset:46464\n"
#define F9R "ds_read_b128 a[196:199], v214 offset:54976\n"
#define F9S "ds_read_b128 a[192:195], v214 offset:54912\n"
#define F9T "ds_read_b64_tr_b16 a[216:217], v216 offset:38080\n"
#define F9U "ds_read_b64_tr_b16 a[218:219], v216 offset:38592\n"
#define F9V "ds_read_b64_tr_b16 a[220:221], v216 offset:42304\n"
#define F9W "ds_read_b64_tr_b16 a[222:223], v216 offset:42816\n"
#define F9X "ds_read_b64_tr_b16 a[224:225], v216 offset:38112\n"
#define F9Y "ds_read_b64_tr_b16 a[226:227], v216 offset:38624\n"
#define F9Z "ds_read_b64_tr_b16 a[228:229], v216 offset:42336\n"
#define F9a "ds_read_b64_tr_b16 a[230:231], v216 offset:42848\n"
#define F9b "ds_read_b64_tr_b16 a[232:233], v216 offset:59136\n"
#define F9c "ds_read_b64_tr_b16 a[234:235], v216 offset:59648\n"
#define F9d "ds_read_b64_tr_b16 a[236:237], v217 offset:0\n"
#define F9e "ds_read_b64_tr_b16 a[238:239], v217 offset:512\n"
#define F9f "ds_read_b128 a[192:195], v214 offset:55424\n"
#define F9g "ds_read_b128 a[196:199], v214 offset:55488\n"
#define F9h "ds_read_b128 a[208:211], v214 offset:46976\n"
#define F9i "ds_read_b128 a[212:215], v214 offset:47040\n"
#define F9j "ds_read_b64_tr_b16 a[216:217], v216 offset:59168\n"
#define F9k "ds_read_b64_tr_b16 a[218:219], v216 offset:59680\n"
#define F9l "ds_read_b64_tr_b16 a[220:221], v217 offset:32\n"
#define F9m "ds_read_b64_tr_b16 a[222:223], v217 offset:544\n"
#define F9n "ds_read_b64_tr_b16 a[224:225], v216 offset:59200\n"
#define F9o "ds_read_b64_tr_b16 a[226:227], v216 offset:59712\n"
#define F9p "ds_read_b64_tr_b16 a[228:229], v217 offset:64\n"
#define F9q "ds_read_b64_tr_b16 a[230:231], v217 offset:576\n"
#define F9r "ds_read_b64_tr_b16 a[232:233], v216 offset:59232\n"
#define F9s "ds_read_b64_tr_b16 a[234:235], v216 offset:59744\n"
#define F9t "ds_read_b64_tr_b16 a[236:237], v217 offset:96\n"
#define F9u "ds_read_b64_tr_b16 a[238:239], v217 offset:608\n"
#define F9v "ds_read_b128 a[204:207], v215 offset:8512\n"
#define F9w "ds_read_b128 a[200:203], v215 offset:8448\n"
#define F9x "ds_read_b64_tr_b16 a[216:217], v217 offset:16896\n"
#define F9y "ds_read_b64_tr_b16 a[218:219], v217 offset:17408\n"
#define F9z "ds_read_b64_tr_b16 a[224:225], v217 offset:16928\n"
#define F90 "ds_read_b64_tr_b16 a[226:227], v217 offset:17440\n"
#define F91 "ds_read_b64_tr_b16 a[232:233], v217 offset:16960\n"
#define F92 "ds_read_b64_tr_b16 a[234:235], v217 offset:17472\n"
#define F93 "ds_read_b128 a[192:195], v215 offset:13184\n"
#define F94 "ds_read_b128 a[196:199], v215 offset:13248\n"
#define F95 "ds_read_b128 a[200:203], v215 offset:8960\n"
#define F96 "ds_read_b128 a[204:207], v215 offset:9024\n"
#define F97 "ds_read_b128 a[208:211], v215 offset:4736\n"
#define F98 "ds_read_b128 a[212:215], v215 offset:4800\n"
#define F99 "ds_read_b64_tr_b16 a[216:217], v217 offset:16992\n"
#define G0A "ds_read_b64_tr_b16 a[218:219], v217 offset:17504\n"
#define G0B "s_add_u32 m0, s88, 38016\n"
#define G0C "s_add_u32 m0, s88, 84480\n"
#define G0D "ds_read_b128 a[144:147], v215 offset:17408\n"
#define G0E "ds_read_b128 a[148:151], v215 offset:17472\n"
#define G0F "ds_read_b128 a[160:163], v215 offset:8960\n"
#define G0G "ds_read_b128 a[164:167], v215 offset:9024\n"
#define F27 "ds_read_b64_tr_b16 a[168:169], v217 offset:21152\n"
#define F28 "ds_read_b64_tr_b16 a[170:171], v217 offset:21664\n"
#define F29 "ds_read_b64_tr_b16 a[172:173], v217 offset:25376\n"
#define F3A "ds_read_b64_tr_b16 a[174:175], v217 offset:25888\n"
#define F3B "ds_read_b64_tr_b16 a[176:177], v217 offset:21184\n"
#define F3C "ds_read_b64_tr_b16 a[178:179], v217 offset:21696\n"
#define F3D "ds_read_b64_tr_b16 a[180:181], v217 offset:25408\n"
#define F3E "ds_read_b64_tr_b16 a[182:183], v217 offset:25920\n"
#define F3F "ds_read_b64_tr_b16 a[184:185], v217 offset:21216\n"
#define F3G "ds_read_b64_tr_b16 a[186:187], v217 offset:21728\n"
#define F3H "ds_read_b64_tr_b16 a[188:189], v217 offset:25440\n"
#define F3I "ds_read_b64_tr_b16 a[190:191], v217 offset:25952\n"
#define E7r "s_waitcnt lgkmcnt(15)\n"
#define G0H "ds_read_b128 v[112:115], v218 offset:33792\n"
#define G0I "ds_read_b128 v[116:119], v218 offset:33856\n"
#define G0J "ds_read_b128 v[120:123], v218 offset:34304\n"
#define G0K "ds_read_b128 v[124:127], v218 offset:34368\n"
#define D9d "v_lshlrev_b32 v144, 16, v112\n"
#define D9e "v_and_b32 v145, 0xffff0000, v112\n"
#define D9f "v_lshlrev_b32 v146, 16, v113\n"
#define D9g "v_and_b32 v147, 0xffff0000, v113\n"
#define D9h "v_lshlrev_b32 v148, 16, v114\n"
#define D9i "v_and_b32 v149, 0xffff0000, v114\n"
#define D9j "v_lshlrev_b32 v150, 16, v115\n"
#define D9k "v_and_b32 v151, 0xffff0000, v115\n"
#define F3K "v_pk_mul_f32 v[150:151], v[150:151], v[230:231]\n"
#define E8P "v_cvt_pk_bf16_f32 v144, v144, v145\n"
#define G29 "v_cvt_pk_bf16_f32 v146, v146, v147\n"
#define G3A "v_cvt_pk_bf16_f32 v148, v148, v149\n"
#define B3a "v_cvt_pk_bf16_f32 v150, v150, v151\n"
#define G0L "v_accvgpr_write_b32 a144, v144\n"
#define G0M "v_accvgpr_write_b32 a145, v146\n"
#define G0N "v_accvgpr_write_b32 a146, v148\n"
#define G0O "v_accvgpr_write_b32 a147, v150\n"
#define D9l "v_lshlrev_b32 v144, 16, v116\n"
#define D9m "v_and_b32 v145, 0xffff0000, v116\n"
#define D9n "v_lshlrev_b32 v146, 16, v117\n"
#define D9o "v_and_b32 v147, 0xffff0000, v117\n"
#define D9p "v_lshlrev_b32 v148, 16, v118\n"
#define D9q "v_and_b32 v149, 0xffff0000, v118\n"
#define D9r "v_lshlrev_b32 v150, 16, v119\n"
#define D9s "v_and_b32 v151, 0xffff0000, v119\n"
#define G0P "ds_read_b128 v[112:115], v218 offset:50688\n"
#define G0Q "v_accvgpr_write_b32 a148, v144\n"
#define G0R "v_accvgpr_write_b32 a149, v146\n"
#define G0S "v_accvgpr_write_b32 a150, v148\n"
#define G0T "v_accvgpr_write_b32 a151, v150\n"
#define D9t "v_lshlrev_b32 v152, 16, v120\n"
#define D9u "v_and_b32 v153, 0xffff0000, v120\n"
#define D9v "v_lshlrev_b32 v154, 16, v121\n"
#define D9w "v_and_b32 v155, 0xffff0000, v121\n"
#define D9x "v_lshlrev_b32 v156, 16, v122\n"
#define D9y "v_and_b32 v157, 0xffff0000, v122\n"
#define D9z "v_lshlrev_b32 v158, 16, v123\n"
#define D90 "v_and_b32 v159, 0xffff0000, v123\n"
#define D3V "v_pk_mul_f32 v[152:153], v[152:153], v[230:231]\n"
#define D3W "v_pk_mul_f32 v[154:155], v[154:155], v[230:231]\n"
#define B3b "v_cvt_pk_bf16_f32 v152, v152, v153\n"
#define E8R "v_cvt_pk_bf16_f32 v154, v154, v155\n"
#define E8Q "v_cvt_pk_bf16_f32 v156, v156, v157\n"
#define B3c "v_cvt_pk_bf16_f32 v158, v158, v159\n"
#define G0U "ds_read_b128 v[116:119], v218 offset:50752\n"
#define G0V "v_accvgpr_write_b32 a168, v152\n"
#define G0W "v_accvgpr_write_b32 a169, v154\n"
#define G0X "v_accvgpr_write_b32 a170, v156\n"
#define G0Y "v_accvgpr_write_b32 a171, v158\n"
#define G0Z "v_accvgpr_write_b32 a152, v144\n"
#define G0a "v_accvgpr_write_b32 a153, v146\n"
#define G0b "v_accvgpr_write_b32 a154, v148\n"
#define G0c "v_accvgpr_write_b32 a155, v150\n"
#define D91 "v_lshlrev_b32 v152, 16, v124\n"
#define D92 "v_and_b32 v153, 0xffff0000, v124\n"
#define D93 "v_lshlrev_b32 v154, 16, v125\n"
#define D94 "v_and_b32 v155, 0xffff0000, v125\n"
#define D95 "v_lshlrev_b32 v156, 16, v126\n"
#define D96 "v_and_b32 v157, 0xffff0000, v126\n"
#define D97 "v_lshlrev_b32 v158, 16, v127\n"
#define D98 "v_and_b32 v159, 0xffff0000, v127\n"
#define G0d "ds_read_b128 v[120:123], v218 offset:51200\n"
#define G0e "v_accvgpr_write_b32 a172, v152\n"
#define G0f "v_accvgpr_write_b32 a173, v154\n"
#define G0g "v_accvgpr_write_b32 a174, v156\n"
#define G0h "v_accvgpr_write_b32 a175, v158\n"
#define G0i "v_accvgpr_write_b32 a156, v144\n"
#define G0j "v_accvgpr_write_b32 a157, v146\n"
#define G0k "v_accvgpr_write_b32 a158, v148\n"
#define G0l "v_accvgpr_write_b32 a159, v150\n"
#define G0m "ds_read_b128 v[124:127], v218 offset:51264\n"
#define G0n "v_accvgpr_write_b32 a176, v152\n"
#define G0o "v_accvgpr_write_b32 a177, v154\n"
#define G0p "v_accvgpr_write_b32 a178, v156\n"
#define G0q "v_accvgpr_write_b32 a179, v158\n"
#define G0r "v_accvgpr_write_b32 a183, v158\n"
#define G0s "v_accvgpr_write_b32 a181, v154\n"
#define G0t "v_accvgpr_write_b32 a182, v156\n"
#define G0u "v_accvgpr_write_b32 a180, v152\n"
#define G0v "s_mov_b32 s92, 442368\n"
#define G0w "ds_read_b128 v[112:115], v218 offset:16896\n"
#define G0x "ds_read_b128 v[116:119], v218 offset:16960\n"
#define G0y "ds_read_b128 v[120:123], v218 offset:17408\n"
#define G0z "ds_read_b128 v[124:127], v218 offset:17472\n"
#define G00 "v_accvgpr_write_b32 a160, v144\n"
#define G01 "v_accvgpr_write_b32 a161, v146\n"
#define G02 "v_accvgpr_write_b32 a162, v148\n"
#define G03 "v_accvgpr_write_b32 a163, v150\n"
#define D7L "v_mfma_f32_16x16x32_bf16 v[160:163], a[192:195], a[144:147], v[160:163]\n"
#define D3Y "v_mfma_f32_16x16x32_bf16 v[160:163], a[196:199], a[148:151], v[160:163]\n"
#define D3Z "v_mfma_f32_16x16x32_bf16 v[160:163], a[200:203], a[152:155], v[160:163]\n"
#define D3a "v_mfma_f32_16x16x32_bf16 v[160:163], a[204:207], a[156:159], v[160:163]\n"
#define D3b "v_mfma_f32_16x16x32_bf16 v[160:163], a[208:211], a[160:163], v[160:163]\n"
#define E7s "v_mul_f32 v158, s97, v158\n"
#define E7t "v_mul_f32 v159, s97, v159\n"
#define G04 "v_accvgpr_write_b32 a167, v150\n"
#define G05 "v_accvgpr_write_b32 a164, v144\n"
#define G06 "v_accvgpr_write_b32 a165, v146\n"
#define G07 "v_accvgpr_write_b32 a166, v148\n"
#define E2M "v_mfma_f32_16x16x32_bf16 v[160:163], a[212:215], a[164:167], v[160:163]\n"
#define G08 "v_mfma_f32_16x16x32_bf16 v[164:167], a[192:195], a[168:171], 0\n"
#define E7u "v_mul_f32 v154, s97, v154\n"
#define E7v "v_mul_f32 v155, s97, v155\n"
#define E7w "v_mul_f32 v156, s97, v156\n"
#define E7x "v_mul_f32 v157, s97, v157\n"
#define E7y "v_mul_f32 v152, s97, v152\n"
#define E7z "v_mul_f32 v153, s97, v153\n"
#define G09 "v_accvgpr_write_b32 a187, v158\n"
#define G1A "v_accvgpr_write_b32 a185, v154\n"
#define G1B "v_accvgpr_write_b32 a186, v156\n"
#define G1C "v_accvgpr_write_b32 a184, v152\n"
#define G1D "v_accvgpr_write_b32 a188, v152\n"
#define G1E "v_accvgpr_write_b32 a189, v154\n"
#define G1F "v_accvgpr_write_b32 a190, v156\n"
#define G1G "v_accvgpr_write_b32 a191, v158\n"
#define G1H "v_add_f32 v163, v163, v187\n"
#define G1I "v_mfma_f32_16x16x32_bf16 v[168:171], a[216:219], a[144:147], v[168:171]\n"
#define E70 "v_add_f32 v160, v160, v184\n"
#define E71 "v_mfma_f32_16x16x32_bf16 v[168:171], a[220:223], a[148:151], v[168:171]\n"
#define E72 "v_mfma_f32_16x16x32_bf16 v[168:171], a[224:227], a[152:155], v[168:171]\n"
#define G1J "v_add_f32 v161, v161, v185\n"
#define E73 "v_mfma_f32_16x16x32_bf16 v[168:171], a[228:231], a[156:159], v[168:171]\n"
#define G1K "v_add_f32 v162, v162, v186\n"
#define E74 "v_mfma_f32_16x16x32_bf16 v[168:171], a[232:235], a[160:163], v[168:171]\n"
#define E75 "v_mfma_f32_16x16x32_bf16 v[168:171], a[236:239], a[164:167], v[168:171]\n"
#define E76 "v_mfma_f32_16x16x32_bf16 v[172:175], a[236:239], a[188:191], v[188:191]\n"
#define F3L "s_cbranch_scc1 Lb6_0\n"
#define E77 "v_mfma_f32_16x16x32_bf16 v[172:175], a[232:235], a[184:187], v[172:175]\n"
#define G1L "v_add_f32 v165, v165, v189\n"
#define E78 "v_mfma_f32_16x16x32_bf16 v[172:175], a[228:231], a[180:183], v[172:175]\n"
#define G1M "v_add_f32 v166, v166, v190\n"
#define E79 "v_mfma_f32_16x16x32_bf16 v[172:175], a[224:227], a[176:179], v[172:175]\n"
#define G1N "v_add_f32 v167, v167, v191\n"
#define E8A "v_mfma_f32_16x16x32_bf16 v[172:175], a[220:223], a[172:175], v[172:175]\n"
#define E8B "v_add_f32 v164, v164, v188\n"
#define E8C "v_mfma_f32_16x16x32_bf16 v[172:175], a[216:219], a[168:171], v[172:175]\n"
#define E8D "v_cvt_pk_bf16_f32 v112, v160, v161\n"
#define E8E "v_cvt_pk_bf16_f32 v113, v162, v163\n"
#define G1O "v_cndmask_b32_e64 v160, v209, v184, s[72:73]\n"
#define G1P "v_cndmask_b32_e64 v161, v209, v185, s[74:75]\n"
#define G1Q "v_cndmask_b32_e64 v162, v209, v186, s[76:77]\n"
#define G1R "v_cndmask_b32_e64 v163, v209, v187, s[78:79]\n"
#define F3M "v_cvt_pk_bf16_f32 v121, v166, v167\n"
#define F3N "v_cvt_pk_bf16_f32 v120, v164, v165\n"
#define D99 "v_mfma_f32_16x16x32_bf16 v[164:167], a[192:195], a[168:171], v[188:191]\n"
#define F3O "v_mfma_f32_16x16x32_bf16 v[192:195], a[248:251], v[112:115], v[196:199]\n"
#define F3P "v_cvt_pk_bf16_f32 v116, v160, v161\n"
#define F3Q "v_cvt_pk_bf16_f32 v117, v162, v163\n"
#define G1S "v_cndmask_b32_e64 v160, v209, v188, s[56:57]\n"
#define G1T "v_cndmask_b32_e64 v161, v209, v189, s[58:59]\n"
#define G1U "v_cndmask_b32_e64 v162, v209, v190, s[60:61]\n"
#define G1V "v_cndmask_b32_e64 v163, v209, v191, s[62:63]\n"
#define F3R "v_cvt_pk_bf16_f32 v125, v166, v167\n"
#define F3S "v_cvt_pk_bf16_f32 v124, v164, v165\n"
#define G1W "v_cndmask_b32_e64 v168, v209, v188, s[72:73]\n"
#define G1X "v_cndmask_b32_e64 v169, v209, v189, s[74:75]\n"
#define G1Y "v_cndmask_b32_e64 v170, v209, v190, s[76:77]\n"
#define G1Z "v_cndmask_b32_e64 v171, v209, v191, s[78:79]\n"
#define E8K "v_mul_f32 v128, s97, v128\n"
#define F3Y "v_mul_f32 v129, s97, v129\n"
#define F3b "v_mul_f32 v130, s97, v130\n"
#define E2O "v_mul_f32 v131, s97, v131\n"
#define E8G "v_mul_f32 v132, s97, v132\n"
#define E8H "v_mul_f32 v133, s97, v133\n"
#define F3d "v_mul_f32 v134, s97, v134\n"
#define F3e "v_mul_f32 v135, s97, v135\n"
#define D7M "s_waitcnt lgkmcnt(14)\n"
#define F3T "v_max_f32 v208, v194, v195\n"
#define F3U "v_cvt_pk_bf16_f32 v152, v160, v161\n"
#define F3V "v_cvt_pk_bf16_f32 v153, v162, v163\n"
#define G1a "v_mfma_f32_16x16x32_bf16 v[196:199], a[252:255], v[152:155], v[192:195]\n"
#define F3Z "v_cvt_pk_bf16_f32 v156, v168, v169\n"
#define F3a "v_cvt_pk_bf16_f32 v157, v170, v171\n"
#define G1c "v_cmp_nge_f32 vcc, s93, v199\n"
#define G1h "v_mfma_f32_16x16x32_bf16 v[160:163], a[212:215], a[116:119], 0\n"
#define G1k "ds_read_b64_tr_b16 a[228:229], v217 offset:50688\n"
#define G1l "ds_read_b64_tr_b16 a[230:231], v217 offset:51200\n"
#define G1m "ds_read_b64_tr_b16 a[224:225], v217 offset:46464\n"
#define G1n "ds_read_b64_tr_b16 a[226:227], v217 offset:46976\n"
#define G1o "ds_read_b64_tr_b16 a[232:233], v217 offset:46496\n"
#define G1p "ds_read_b64_tr_b16 a[234:235], v217 offset:47008\n"
#define G1q "ds_read_b64_tr_b16 a[236:237], v217 offset:50720\n"
#define G1r "ds_read_b64_tr_b16 a[238:239], v217 offset:51232\n"
#define G1s "ds_read_b128 a[212:215], v215 offset:54976\n"
#define G1t "ds_read_b128 a[208:211], v215 offset:54912\n"
#define G1u "ds_read_b128 a[204:207], v215 offset:59200\n"
#define G1v "ds_read_b128 a[200:203], v215 offset:59136\n"
#define G1w "ds_read_b128 a[200:203], v215 offset:59648\n"
#define G1x "ds_read_b128 a[204:207], v215 offset:59712\n"
#define G1y "ds_read_b128 a[208:211], v215 offset:55424\n"
#define G1z "ds_read_b128 a[212:215], v215 offset:55488\n"
#define E8L "s_addk_i32 m0, 13600\n"
#define E8M "s_cbranch_scc1 Lb6_1\n"
#define G10 "s_addk_i32 m0, 13984\n"
#define G11 "ds_read_b64_tr_b16 a[236:237], v217 offset:46464\n"
#define G12 "ds_read_b64_tr_b16 a[238:239], v217 offset:46976\n"
#define G13 "ds_read_b64_tr_b16 a[220:221], v217 offset:46496\n"
#define G14 "ds_read_b64_tr_b16 a[222:223], v217 offset:47008\n"
#define G15 "ds_read_b64_tr_b16 a[228:229], v217 offset:46528\n"
#define G16 "ds_read_b64_tr_b16 a[230:231], v217 offset:47040\n"
#define G17 "ds_read_b64_tr_b16 a[236:237], v217 offset:46560\n"
#define G18 "ds_read_b64_tr_b16 a[238:239], v217 offset:47072\n"
#define F3l "ds_read_b128 a[212:215], v215 offset:50752\n"
#define F3m "ds_read_b128 a[208:211], v215 offset:50688\n"
#define F3n "ds_read_b128 a[204:207], v215 offset:54976\n"
#define F3o "ds_read_b128 a[200:203], v215 offset:54912\n"
#define F3p "ds_read_b128 a[196:199], v215 offset:59200\n"
#define F3q "ds_read_b128 a[192:195], v215 offset:59136\n"
#define G19 "ds_read_b128 a[192:195], v215 offset:59648\n"
#define G2A "ds_read_b128 a[196:199], v215 offset:59712\n"
#define G2B "ds_read_b128 a[200:203], v215 offset:55424\n"
#define G2C "ds_read_b128 a[204:207], v215 offset:55488\n"
#define G2D "ds_read_b128 a[208:211], v215 offset:51200\n"
#define G2E "ds_read_b128 a[212:215], v215 offset:51264\n"
#define E8N "s_cbranch_scc1 Lb6_2\n"
#define G2H "s_addk_i32 m0, 16896\n"
#define G2I "s_addk_i32 m0, -19008\n"
#define E8O "s_cbranch_scc1 Lb6_3\n"
#define G2J "ds_write_b64 v220, v[148:149] offset:33792\n"
#define G2K "ds_write_b64 v220, v[150:151] offset:33824\n"
#define G2L "ds_write_b64 v220, v[156:157] offset:50688\n"
#define G2M "ds_write_b64 v220, v[158:159] offset:50720\n"
#define G2N "ds_write_b64 v220, v[156:157] offset:50752\n"
#define G2O "ds_write_b64 v220, v[148:149] offset:33856\n"
#define G2P "ds_write_b64 v220, v[158:159] offset:50784\n"
#define G2Q "ds_write_b64 v220, v[150:151] offset:33888\n"
#define G2R "ds_read_b128 a[204:207], v215 offset:29632\n"
#define G2S "ds_read_b128 a[200:203], v215 offset:29568\n"
#define G2T "ds_read_b128 a[196:199], v215 offset:33856\n"
#define G2U "ds_read_b128 a[192:195], v215 offset:33792\n"
#define F3t "ds_read_b64_tr_b16 a[232:233], v217 offset:38016\n"
#define F3u "ds_read_b64_tr_b16 a[234:235], v217 offset:38528\n"
#define F3v "ds_read_b64_tr_b16 a[236:237], v217 offset:42240\n"
#define F3w "ds_read_b64_tr_b16 a[238:239], v217 offset:42752\n"
#define G2V "ds_read_b128 a[192:195], v215 offset:34304\n"
#define G2W "ds_read_b128 a[196:199], v215 offset:34368\n"
#define G2X "ds_read_b128 a[200:203], v215 offset:30080\n"
#define G2Y "ds_read_b128 a[204:207], v215 offset:30144\n"
#define G2Z "ds_read_b64_tr_b16 a[216:217], v217 offset:38048\n"
#define G2a "ds_read_b64_tr_b16 a[218:219], v217 offset:38560\n"
#define G2b "ds_read_b64_tr_b16 a[220:221], v217 offset:42272\n"
#define G2c "ds_read_b64_tr_b16 a[222:223], v217 offset:42784\n"
#define G2d "ds_read_b64_tr_b16 a[224:225], v217 offset:38080\n"
#define G2e "ds_read_b64_tr_b16 a[226:227], v217 offset:38592\n"
#define G2f "ds_read_b64_tr_b16 a[228:229], v217 offset:42304\n"
#define G2g "ds_read_b64_tr_b16 a[230:231], v217 offset:42816\n"
#define G2h "ds_read_b64_tr_b16 a[232:233], v217 offset:38112\n"
#define G2i "ds_read_b64_tr_b16 a[234:235], v217 offset:38624\n"
#define G2j "ds_read_b64_tr_b16 a[236:237], v217 offset:42336\n"
#define G2k "ds_read_b64_tr_b16 a[238:239], v217 offset:42848\n"
#define G2l "ds_read_b64_tr_b16 a[216:217], v217 offset:59136\n"
#define G2m "ds_read_b64_tr_b16 a[218:219], v217 offset:59648\n"
#define G2n "ds_read_b64_tr_b16 a[224:225], v217 offset:59168\n"
#define G2o "ds_read_b64_tr_b16 a[226:227], v217 offset:59680\n"
#define G2p "ds_read_b64_tr_b16 a[232:233], v217 offset:59200\n"
#define G2q "ds_read_b64_tr_b16 a[234:235], v217 offset:59712\n"
#define G2r "ds_read_b64_tr_b16 a[216:217], v217 offset:59232\n"
#define G2s "ds_read_b64_tr_b16 a[218:219], v217 offset:59744\n"
#define G2t "ds_read_b64_tr_b16 a[48:49], v216 offset:50688\n"
#define G2u "ds_read_b64_tr_b16 a[50:51], v216 offset:51200\n"
#define G2v "ds_read_b64_tr_b16 a[52:53], v216 offset:54912\n"
#define G2w "ds_read_b64_tr_b16 a[54:55], v216 offset:55424\n"
#define G2x "ds_read_b64_tr_b16 a[60:61], v216 offset:54944\n"
#define G2y "ds_read_b64_tr_b16 a[62:63], v216 offset:55456\n"
#define G2z "ds_read_b64_tr_b16 a[56:57], v216 offset:50720\n"
#define G20 "ds_read_b64_tr_b16 a[58:59], v216 offset:51232\n"
#define G21 "ds_read_b64_tr_b16 a[64:65], v216 offset:50752\n"
#define G22 "ds_read_b64_tr_b16 a[66:67], v216 offset:51264\n"
#define G23 "ds_read_b64_tr_b16 a[68:69], v216 offset:54976\n"
#define G24 "ds_read_b64_tr_b16 a[70:71], v216 offset:55488\n"
#define G25 "ds_read_b64_tr_b16 a[72:73], v216 offset:50784\n"
#define G26 "ds_read_b64_tr_b16 a[74:75], v216 offset:51296\n"
#define G27 "ds_read_b64_tr_b16 a[76:77], v216 offset:55008\n"
#define G28 "ds_read_b64_tr_b16 a[78:79], v216 offset:55520\n"
#define G3B "v_mfma_f32_16x16x32_bf16 v[168:171], a[216:219], a[144:147], v[184:187]\n"
#define F30 "s_cbranch_scc1 Lb7_0\n"
#define F31 "v_mfma_f32_16x16x32_bf16 v[160:163], a[212:215], a[164:167], v[184:187]\n"
#define G3C "v_cvt_pk_bf16_f32 v144, v160, v161\n"
#define G3D "v_cvt_pk_bf16_f32 v145, v162, v163\n"
#define G3E "s_mov_b32 s92, 393216\n"
#define E8T "s_cbranch_scc1 Lb7_1\n"
#define E8U "s_cbranch_scc1 Lb7_2\n"
#define E8V "s_cbranch_scc1 Lb7_3\n"
#define G3X "s_add_u32 m0, s88, 93056\n"
#define H1N "s_movk_i32 s15, 1024\n"
#define H1M "s_movk_i32 s15, 2048\n"
#define H1L "s_movk_i32 s15, 3072\n"
#define H1K "s_movk_i32 s15, 4096\n"
#define H1C "s_movk_i32 s15, 5120\n"
#define H1J "s_movk_i32 s15, 6144\n"
#define H1I "s_movk_i32 s15, 7168\n"
#define H1H "s_movk_i32 s15, 8192\n"
#define H1G "s_movk_i32 s15, 9216\n"
#define H1F "s_movk_i32 s15, 10240\n"
#define H1E "s_movk_i32 s15, 11264\n"
#define H1D "s_movk_i32 s15, 12288\n"
#define G3Z "v_mfma_f32_16x16x32_bf16 v[160:163], v[232:235], a[96:99], 0\n"
#define G3a "v_mfma_f32_16x16x32_bf16 v[160:163], v[236:239], a[100:103], v[160:163]\n"
#define G3b "v_mfma_f32_16x16x32_bf16 v[160:163], v[240:243], a[104:107], v[160:163]\n"
#define G3c "v_mfma_f32_16x16x32_bf16 v[160:163], v[244:247], a[108:111], v[160:163]\n"
#define G3d "v_mfma_f32_16x16x32_bf16 v[160:163], v[248:251], a[112:115], v[160:163]\n"
#define G3e "v_mfma_f32_16x16x32_bf16 v[160:163], v[252:255], a[116:119], v[160:163]\n"
#define G3f "v_mfma_f32_16x16x32_bf16 v[164:167], v[232:235], a[96:99], 0\n"
#define G3g "v_mfma_f32_16x16x32_bf16 v[164:167], v[236:239], a[100:103], v[164:167]\n"
#define G3h "v_mfma_f32_16x16x32_bf16 v[164:167], v[240:243], a[104:107], v[164:167]\n"
#define G3i "v_mfma_f32_16x16x32_bf16 v[164:167], v[244:247], a[108:111], v[164:167]\n"
#define G3j "v_mfma_f32_16x16x32_bf16 v[164:167], v[248:251], a[112:115], v[164:167]\n"
#define G3k "v_mfma_f32_16x16x32_bf16 v[164:167], v[252:255], a[116:119], v[164:167]\n"
#define G3l "v_mfma_f32_16x16x32_bf16 v[168:171], v[232:235], a[96:99], 0\n"
#define G3m "v_mfma_f32_16x16x32_bf16 v[168:171], v[236:239], a[100:103], v[168:171]\n"
#define G3n "v_mfma_f32_16x16x32_bf16 v[168:171], v[240:243], a[104:107], v[168:171]\n"
#define G3o "v_mfma_f32_16x16x32_bf16 v[168:171], v[244:247], a[108:111], v[168:171]\n"
#define G3p "v_mfma_f32_16x16x32_bf16 v[168:171], v[248:251], a[112:115], v[168:171]\n"
#define G3q "v_mfma_f32_16x16x32_bf16 v[168:171], v[252:255], a[116:119], v[168:171]\n"
#define G3r "v_mfma_f32_16x16x32_bf16 v[172:175], v[232:235], a[96:99], 0\n"
#define G3s "v_mfma_f32_16x16x32_bf16 v[172:175], v[236:239], a[100:103], v[172:175]\n"
#define G3t "v_mfma_f32_16x16x32_bf16 v[172:175], v[240:243], a[104:107], v[172:175]\n"
#define G3u "v_mfma_f32_16x16x32_bf16 v[172:175], v[244:247], a[108:111], v[172:175]\n"
#define G3v "v_mfma_f32_16x16x32_bf16 v[172:175], v[248:251], a[112:115], v[172:175]\n"
#define G3w "v_mfma_f32_16x16x32_bf16 v[172:175], v[252:255], a[116:119], v[172:175]\n"
#define G3x "v_add_f32 v204, v204, v176\n"
#define H1O "v_mul_f32 v205, s1, v204\n"
#define G3y "v_sub_f32 v176, v176, v204\n"
#define G3z "v_sub_f32 v177, v177, v204\n"
#define G30 "v_sub_f32 v178, v178, v204\n"
#define G31 "v_sub_f32 v179, v179, v204\n"
#define D32 "s_cmp_eq_u32 s14, 1\n"
#define G32 "v_mul_f32 v0, v0, v205\n"
#define G33 "v_mul_f32 v1, v1, v205\n"
#define G34 "v_mul_f32 v2, v2, v205\n"
#define G35 "v_mul_f32 v3, v3, v205\n"
#define G36 "v_mul_f32 v4, v4, v205\n"
#define G37 "v_mul_f32 v5, v5, v205\n"
#define G38 "v_mul_f32 v6, v6, v205\n"
#define G39 "v_mul_f32 v7, v7, v205\n"
#define G4A "v_mul_f32 v8, v8, v205\n"
#define G4B "v_mul_f32 v9, v9, v205\n"
#define G4C "v_mul_f32 v10, v10, v205\n"
#define G4D "v_mul_f32 v11, v11, v205\n"
#define G4E "v_mul_f32 v12, v12, v205\n"
#define G4F "v_mul_f32 v13, v13, v205\n"
#define G4G "v_mul_f32 v14, v14, v205\n"
#define G4H "v_mul_f32 v15, v15, v205\n"
#define G4I "v_mul_f32 v16, v16, v205\n"
#define G4J "v_mul_f32 v17, v17, v205\n"
#define G4K "v_mul_f32 v18, v18, v205\n"
#define G4L "v_mul_f32 v19, v19, v205\n"
#define G4M "v_mul_f32 v20, v20, v205\n"
#define G4N "v_mul_f32 v21, v21, v205\n"
#define G4O "v_mul_f32 v22, v22, v205\n"
#define G4P "v_mul_f32 v23, v23, v205\n"
#define G4Q "v_mul_f32 v24, v24, v205\n"
#define G4R "v_mul_f32 v25, v25, v205\n"
#define G4S "v_mul_f32 v26, v26, v205\n"
#define G4T "v_mul_f32 v27, v27, v205\n"
#define G4U "v_mul_f32 v28, v28, v205\n"
#define G4V "v_mul_f32 v29, v29, v205\n"
#define G4W "v_mul_f32 v30, v30, v205\n"
#define G4X "v_mul_f32 v31, v31, v205\n"
#define G4Y "v_mul_f32 v32, v32, v205\n"
#define G4Z "v_mul_f32 v33, v33, v205\n"
#define G4a "v_mul_f32 v34, v34, v205\n"
#define G4b "v_mul_f32 v35, v35, v205\n"
#define G4c "v_mul_f32 v36, v36, v205\n"
#define G4d "v_mul_f32 v37, v37, v205\n"
#define G4e "v_mul_f32 v38, v38, v205\n"
#define G4f "v_mul_f32 v39, v39, v205\n"
#define G4g "v_mul_f32 v40, v40, v205\n"
#define G4h "v_mul_f32 v41, v41, v205\n"
#define G4i "v_mul_f32 v42, v42, v205\n"
#define G4j "v_mul_f32 v43, v43, v205\n"
#define G4k "v_mul_f32 v44, v44, v205\n"
#define G4l "v_mul_f32 v45, v45, v205\n"
#define G4m "v_mul_f32 v46, v46, v205\n"
#define G4n "v_mul_f32 v47, v47, v205\n"
#define G4o "v_add_f32 v161, v161, v176\n"
#define G4p "v_add_f32 v162, v162, v176\n"
#define G4q "v_add_f32 v163, v163, v176\n"
#define G4r "v_add_f32 v164, v164, v176\n"
#define G4s "v_add_f32 v165, v165, v176\n"
#define G4t "v_add_f32 v166, v166, v176\n"
#define G4u "v_add_f32 v167, v167, v176\n"
#define G4v "v_add_f32 v168, v168, v176\n"
#define G4w "v_add_f32 v169, v169, v176\n"
#define G4x "v_add_f32 v170, v170, v176\n"
#define G4y "v_add_f32 v171, v171, v176\n"
#define G4z "v_add_f32 v172, v172, v176\n"
#define G40 "v_add_f32 v173, v173, v176\n"
#define G41 "v_add_f32 v174, v174, v176\n"
#define G42 "v_add_f32 v175, v175, v176\n"
#define G43 "v_mfma_f32_16x16x32_bf16 v[160:163], v[232:235], a[120:123], 0\n"
#define G44 "v_mfma_f32_16x16x32_bf16 v[160:163], v[236:239], a[124:127], v[160:163]\n"
#define G45 "v_mfma_f32_16x16x32_bf16 v[160:163], v[240:243], a[128:131], v[160:163]\n"
#define G46 "v_mfma_f32_16x16x32_bf16 v[160:163], v[244:247], a[132:135], v[160:163]\n"
#define G47 "v_mfma_f32_16x16x32_bf16 v[160:163], v[248:251], a[136:139], v[160:163]\n"
#define G48 "v_mfma_f32_16x16x32_bf16 v[160:163], v[252:255], a[140:143], v[160:163]\n"
#define G49 "v_mfma_f32_16x16x32_bf16 v[164:167], v[232:235], a[120:123], 0\n"
#define G5A "v_mfma_f32_16x16x32_bf16 v[164:167], v[236:239], a[124:127], v[164:167]\n"
#define G5B "v_mfma_f32_16x16x32_bf16 v[164:167], v[240:243], a[128:131], v[164:167]\n"
#define G5C "v_mfma_f32_16x16x32_bf16 v[164:167], v[244:247], a[132:135], v[164:167]\n"
#define G5D "v_mfma_f32_16x16x32_bf16 v[164:167], v[248:251], a[136:139], v[164:167]\n"
#define G5E "v_mfma_f32_16x16x32_bf16 v[164:167], v[252:255], a[140:143], v[164:167]\n"
#define G5F "v_mfma_f32_16x16x32_bf16 v[168:171], v[232:235], a[120:123], 0\n"
#define G5G "v_mfma_f32_16x16x32_bf16 v[168:171], v[236:239], a[124:127], v[168:171]\n"
#define G5H "v_mfma_f32_16x16x32_bf16 v[168:171], v[240:243], a[128:131], v[168:171]\n"
#define G5I "v_mfma_f32_16x16x32_bf16 v[168:171], v[244:247], a[132:135], v[168:171]\n"
#define G5J "v_mfma_f32_16x16x32_bf16 v[168:171], v[248:251], a[136:139], v[168:171]\n"
#define G5K "v_mfma_f32_16x16x32_bf16 v[168:171], v[252:255], a[140:143], v[168:171]\n"
#define G5L "v_mfma_f32_16x16x32_bf16 v[172:175], v[232:235], a[120:123], 0\n"
#define G5M "v_mfma_f32_16x16x32_bf16 v[172:175], v[236:239], a[124:127], v[172:175]\n"
#define G5N "v_mfma_f32_16x16x32_bf16 v[172:175], v[240:243], a[128:131], v[172:175]\n"
#define G5O "v_mfma_f32_16x16x32_bf16 v[172:175], v[244:247], a[132:135], v[172:175]\n"
#define G5P "v_mfma_f32_16x16x32_bf16 v[172:175], v[248:251], a[136:139], v[172:175]\n"
#define G5Q "v_mfma_f32_16x16x32_bf16 v[172:175], v[252:255], a[140:143], v[172:175]\n"
#define G5R "v_add_f32 v204, v204, v180\n"
#define G5S "v_sub_f32 v180, v180, v204\n"
#define G5T "v_sub_f32 v181, v181, v204\n"
#define G5U "v_sub_f32 v182, v182, v204\n"
#define G5V "v_sub_f32 v183, v183, v204\n"
#define G5W "v_mul_f32 v48, v48, v205\n"
#define G5X "v_mul_f32 v49, v49, v205\n"
#define G5Y "v_mul_f32 v50, v50, v205\n"
#define G5Z "v_mul_f32 v51, v51, v205\n"
#define G5a "v_mul_f32 v52, v52, v205\n"
#define G5b "v_mul_f32 v53, v53, v205\n"
#define G5c "v_mul_f32 v54, v54, v205\n"
#define G5d "v_mul_f32 v55, v55, v205\n"
#define G5e "v_mul_f32 v56, v56, v205\n"
#define G5f "v_mul_f32 v57, v57, v205\n"
#define G5g "v_mul_f32 v58, v58, v205\n"
#define G5h "v_mul_f32 v59, v59, v205\n"
#define G5i "v_mul_f32 v60, v60, v205\n"
#define G5j "v_mul_f32 v61, v61, v205\n"
#define G5k "v_mul_f32 v62, v62, v205\n"
#define G5l "v_mul_f32 v63, v63, v205\n"
#define G5m "v_mul_f32 v64, v64, v205\n"
#define G5n "v_mul_f32 v65, v65, v205\n"
#define G5o "v_mul_f32 v66, v66, v205\n"
#define G5p "v_mul_f32 v67, v67, v205\n"
#define G5q "v_mul_f32 v68, v68, v205\n"
#define G5r "v_mul_f32 v69, v69, v205\n"
#define G5s "v_mul_f32 v70, v70, v205\n"
#define G5t "v_mul_f32 v71, v71, v205\n"
#define G5u "v_mul_f32 v72, v72, v205\n"
#define G5v "v_mul_f32 v73, v73, v205\n"
#define G5w "v_mul_f32 v74, v74, v205\n"
#define G5x "v_mul_f32 v75, v75, v205\n"
#define G5y "v_mul_f32 v76, v76, v205\n"
#define G5z "v_mul_f32 v77, v77, v205\n"
#define G50 "v_mul_f32 v78, v78, v205\n"
#define G51 "v_mul_f32 v79, v79, v205\n"
#define G52 "v_mul_f32 v80, v80, v205\n"
#define G53 "v_mul_f32 v81, v81, v205\n"
#define G54 "v_mul_f32 v82, v82, v205\n"
#define G55 "v_mul_f32 v83, v83, v205\n"
#define G56 "v_mul_f32 v84, v84, v205\n"
#define G57 "v_mul_f32 v85, v85, v205\n"
#define G58 "v_mul_f32 v86, v86, v205\n"
#define G59 "v_mul_f32 v87, v87, v205\n"
#define G6A "v_mul_f32 v88, v88, v205\n"
#define G6B "v_mul_f32 v89, v89, v205\n"
#define G6C "v_mul_f32 v90, v90, v205\n"
#define G6D "v_mul_f32 v91, v91, v205\n"
#define G6E "v_mul_f32 v92, v92, v205\n"
#define G6F "v_mul_f32 v93, v93, v205\n"
#define G6G "v_mul_f32 v94, v94, v205\n"
#define G6H "v_mul_f32 v95, v95, v205\n"
#define G6I "v_add_f32 v160, v160, v180\n"
#define G6J "v_add_f32 v161, v161, v180\n"
#define G6K "v_add_f32 v162, v162, v180\n"
#define G6L "v_add_f32 v163, v163, v180\n"
#define G6M "v_add_f32 v165, v165, v180\n"
#define G6N "v_add_f32 v166, v166, v180\n"
#define G6O "v_add_f32 v167, v167, v180\n"
#define G6P "v_add_f32 v168, v168, v180\n"
#define G6Q "v_add_f32 v169, v169, v180\n"
#define G6R "v_add_f32 v170, v170, v180\n"
#define G6S "v_add_f32 v171, v171, v180\n"
#define G6T "v_add_f32 v172, v172, v180\n"
#define G6U "v_add_f32 v173, v173, v180\n"
#define G6V "v_add_f32 v174, v174, v180\n"
#define G6W "v_add_f32 v175, v175, v180\n"
#define G6X "v_mfma_f32_16x16x32_bf16 v[160:163], v[232:235], a[144:147], 0\n"
#define G6Y "v_mfma_f32_16x16x32_bf16 v[160:163], v[236:239], a[148:151], v[160:163]\n"
#define G6Z "v_mfma_f32_16x16x32_bf16 v[160:163], v[240:243], a[152:155], v[160:163]\n"
#define G6a "v_mfma_f32_16x16x32_bf16 v[160:163], v[244:247], a[156:159], v[160:163]\n"
#define G6b "v_mfma_f32_16x16x32_bf16 v[160:163], v[248:251], a[160:163], v[160:163]\n"
#define G6c "v_mfma_f32_16x16x32_bf16 v[160:163], v[252:255], a[164:167], v[160:163]\n"
#define G6d "v_mfma_f32_16x16x32_bf16 v[164:167], v[232:235], a[144:147], 0\n"
#define G6e "v_mfma_f32_16x16x32_bf16 v[164:167], v[236:239], a[148:151], v[164:167]\n"
#define G6f "v_mfma_f32_16x16x32_bf16 v[164:167], v[240:243], a[152:155], v[164:167]\n"
#define G6g "v_mfma_f32_16x16x32_bf16 v[164:167], v[244:247], a[156:159], v[164:167]\n"
#define G6h "v_mfma_f32_16x16x32_bf16 v[164:167], v[248:251], a[160:163], v[164:167]\n"
#define G6i "v_mfma_f32_16x16x32_bf16 v[164:167], v[252:255], a[164:167], v[164:167]\n"
#define G6j "v_mfma_f32_16x16x32_bf16 v[168:171], v[232:235], a[144:147], 0\n"
#define G6k "v_mfma_f32_16x16x32_bf16 v[168:171], v[236:239], a[148:151], v[168:171]\n"
#define G6l "v_mfma_f32_16x16x32_bf16 v[168:171], v[240:243], a[152:155], v[168:171]\n"
#define G6m "v_mfma_f32_16x16x32_bf16 v[168:171], v[244:247], a[156:159], v[168:171]\n"
#define G6n "v_mfma_f32_16x16x32_bf16 v[168:171], v[248:251], a[160:163], v[168:171]\n"
#define G6o "v_mfma_f32_16x16x32_bf16 v[168:171], v[252:255], a[164:167], v[168:171]\n"
#define G6p "v_mfma_f32_16x16x32_bf16 v[172:175], v[232:235], a[144:147], 0\n"
#define G6q "v_mfma_f32_16x16x32_bf16 v[172:175], v[236:239], a[148:151], v[172:175]\n"
#define G6r "v_mfma_f32_16x16x32_bf16 v[172:175], v[240:243], a[152:155], v[172:175]\n"
#define G6s "v_mfma_f32_16x16x32_bf16 v[172:175], v[244:247], a[156:159], v[172:175]\n"
#define G6t "v_mfma_f32_16x16x32_bf16 v[172:175], v[248:251], a[160:163], v[172:175]\n"
#define G6u "v_mfma_f32_16x16x32_bf16 v[172:175], v[252:255], a[164:167], v[172:175]\n"
#define G6v "v_add_f32 v204, v204, v184\n"
#define G6w "v_sub_f32 v184, v184, v204\n"
#define G6x "v_sub_f32 v185, v185, v204\n"
#define G6y "v_sub_f32 v186, v186, v204\n"
#define G6z "v_sub_f32 v187, v187, v204\n"
#define G60 "v_accvgpr_read_b32 v206, a0\n"
#define n0 "v_mul_f32 v206, v206, v205\n"
#define G61 "v_accvgpr_write_b32 a0, v206\n"
#define G62 "v_accvgpr_read_b32 v206, a1\n"
#define G63 "v_accvgpr_write_b32 a1, v206\n"
#define G64 "v_accvgpr_read_b32 v206, a2\n"
#define G65 "v_accvgpr_write_b32 a2, v206\n"
#define G66 "v_accvgpr_read_b32 v206, a3\n"
#define G67 "v_accvgpr_write_b32 a3, v206\n"
#define G68 "v_accvgpr_read_b32 v206, a4\n"
#define G69 "v_accvgpr_write_b32 a4, v206\n"
#define G7A "v_accvgpr_read_b32 v206, a5\n"
#define G7B "v_accvgpr_write_b32 a5, v206\n"
#define G7C "v_accvgpr_read_b32 v206, a6\n"
#define G7D "v_accvgpr_write_b32 a6, v206\n"
#define G7E "v_accvgpr_read_b32 v206, a7\n"
#define G7F "v_accvgpr_write_b32 a7, v206\n"
#define G7G "v_accvgpr_read_b32 v206, a8\n"
#define G7H "v_accvgpr_write_b32 a8, v206\n"
#define G7I "v_accvgpr_read_b32 v206, a9\n"
#define G7J "v_accvgpr_write_b32 a9, v206\n"
#define G7K "v_accvgpr_read_b32 v206, a10\n"
#define G7L "v_accvgpr_write_b32 a10, v206\n"
#define G7M "v_accvgpr_read_b32 v206, a11\n"
#define G7N "v_accvgpr_write_b32 a11, v206\n"
#define G7O "v_accvgpr_read_b32 v206, a12\n"
#define G7P "v_accvgpr_write_b32 a12, v206\n"
#define G7Q "v_accvgpr_read_b32 v206, a13\n"
#define G7R "v_accvgpr_write_b32 a13, v206\n"
#define G7S "v_accvgpr_read_b32 v206, a14\n"
#define G7T "v_accvgpr_write_b32 a14, v206\n"
#define G7U "v_accvgpr_read_b32 v206, a15\n"
#define G7V "v_accvgpr_write_b32 a15, v206\n"
#define G7W "v_accvgpr_read_b32 v206, a16\n"
#define G7X "v_accvgpr_write_b32 a16, v206\n"
#define G7Y "v_accvgpr_read_b32 v206, a17\n"
#define G7Z "v_accvgpr_write_b32 a17, v206\n"
#define G7a "v_accvgpr_read_b32 v206, a18\n"
#define G7b "v_accvgpr_write_b32 a18, v206\n"
#define G7c "v_accvgpr_read_b32 v206, a19\n"
#define G7d "v_accvgpr_write_b32 a19, v206\n"
#define G7e "v_accvgpr_read_b32 v206, a20\n"
#define G7f "v_accvgpr_write_b32 a20, v206\n"
#define G7g "v_accvgpr_read_b32 v206, a21\n"
#define G7h "v_accvgpr_write_b32 a21, v206\n"
#define G7i "v_accvgpr_read_b32 v206, a22\n"
#define G7j "v_accvgpr_write_b32 a22, v206\n"
#define G7k "v_accvgpr_read_b32 v206, a23\n"
#define G7l "v_accvgpr_write_b32 a23, v206\n"
#define G7m "v_accvgpr_read_b32 v206, a24\n"
#define G7n "v_accvgpr_write_b32 a24, v206\n"
#define G7o "v_accvgpr_read_b32 v206, a25\n"
#define G7p "v_accvgpr_write_b32 a25, v206\n"
#define G7q "v_accvgpr_read_b32 v206, a26\n"
#define G7r "v_accvgpr_write_b32 a26, v206\n"
#define G7s "v_accvgpr_read_b32 v206, a27\n"
#define G7t "v_accvgpr_write_b32 a27, v206\n"
#define G7u "v_accvgpr_read_b32 v206, a28\n"
#define G7v "v_accvgpr_write_b32 a28, v206\n"
#define G7w "v_accvgpr_read_b32 v206, a29\n"
#define G7x "v_accvgpr_write_b32 a29, v206\n"
#define G7y "v_accvgpr_read_b32 v206, a30\n"
#define G7z "v_accvgpr_write_b32 a30, v206\n"
#define G70 "v_accvgpr_read_b32 v206, a31\n"
#define G71 "v_accvgpr_write_b32 a31, v206\n"
#define G72 "v_accvgpr_read_b32 v206, a32\n"
#define G73 "v_accvgpr_write_b32 a32, v206\n"
#define G74 "v_accvgpr_read_b32 v206, a33\n"
#define G75 "v_accvgpr_write_b32 a33, v206\n"
#define G76 "v_accvgpr_read_b32 v206, a34\n"
#define G77 "v_accvgpr_write_b32 a34, v206\n"
#define G78 "v_accvgpr_read_b32 v206, a35\n"
#define G79 "v_accvgpr_write_b32 a35, v206\n"
#define G8A "v_accvgpr_read_b32 v206, a36\n"
#define G8B "v_accvgpr_write_b32 a36, v206\n"
#define G8C "v_accvgpr_read_b32 v206, a37\n"
#define G8D "v_accvgpr_write_b32 a37, v206\n"
#define G8E "v_accvgpr_read_b32 v206, a38\n"
#define G8F "v_accvgpr_write_b32 a38, v206\n"
#define G8G "v_accvgpr_read_b32 v206, a39\n"
#define G8H "v_accvgpr_write_b32 a39, v206\n"
#define G8I "v_accvgpr_read_b32 v206, a40\n"
#define G8J "v_accvgpr_write_b32 a40, v206\n"
#define G8K "v_accvgpr_read_b32 v206, a41\n"
#define G8L "v_accvgpr_write_b32 a41, v206\n"
#define G8M "v_accvgpr_read_b32 v206, a42\n"
#define G8N "v_accvgpr_write_b32 a42, v206\n"
#define G8O "v_accvgpr_read_b32 v206, a43\n"
#define G8P "v_accvgpr_write_b32 a43, v206\n"
#define G8Q "v_accvgpr_read_b32 v206, a44\n"
#define G8R "v_accvgpr_write_b32 a44, v206\n"
#define G8S "v_accvgpr_read_b32 v206, a45\n"
#define G8T "v_accvgpr_write_b32 a45, v206\n"
#define G8U "v_accvgpr_read_b32 v206, a46\n"
#define G8V "v_accvgpr_write_b32 a46, v206\n"
#define G8W "v_accvgpr_read_b32 v206, a47\n"
#define G8X "v_accvgpr_write_b32 a47, v206\n"
#define G8Y "v_add_f32 v161, v161, v184\n"
#define G8Z "v_add_f32 v162, v162, v184\n"
#define G8a "v_add_f32 v163, v163, v184\n"
#define G8b "v_add_f32 v164, v164, v184\n"
#define G8c "v_add_f32 v165, v165, v184\n"
#define G8d "v_add_f32 v166, v166, v184\n"
#define G8e "v_add_f32 v167, v167, v184\n"
#define G8f "v_add_f32 v169, v169, v184\n"
#define G8g "v_add_f32 v170, v170, v184\n"
#define G8h "v_add_f32 v171, v171, v184\n"
#define G8i "v_add_f32 v172, v172, v184\n"
#define G8j "v_add_f32 v173, v173, v184\n"
#define G8k "v_add_f32 v174, v174, v184\n"
#define G8l "v_add_f32 v175, v175, v184\n"
#define G8m "v_mfma_f32_16x16x32_bf16 v[160:163], v[232:235], a[168:171], 0\n"
#define G8n "v_mfma_f32_16x16x32_bf16 v[160:163], v[236:239], a[172:175], v[160:163]\n"
#define G8o "v_mfma_f32_16x16x32_bf16 v[160:163], v[240:243], a[176:179], v[160:163]\n"
#define G8p "v_mfma_f32_16x16x32_bf16 v[160:163], v[244:247], a[180:183], v[160:163]\n"
#define G8q "v_mfma_f32_16x16x32_bf16 v[160:163], v[248:251], a[184:187], v[160:163]\n"
#define G8r "v_mfma_f32_16x16x32_bf16 v[160:163], v[252:255], a[188:191], v[160:163]\n"
#define G8s "v_mfma_f32_16x16x32_bf16 v[164:167], v[232:235], a[168:171], 0\n"
#define G8t "v_mfma_f32_16x16x32_bf16 v[164:167], v[236:239], a[172:175], v[164:167]\n"
#define G8u "v_mfma_f32_16x16x32_bf16 v[164:167], v[240:243], a[176:179], v[164:167]\n"
#define G8v "v_mfma_f32_16x16x32_bf16 v[164:167], v[244:247], a[180:183], v[164:167]\n"
#define G8w "v_mfma_f32_16x16x32_bf16 v[164:167], v[248:251], a[184:187], v[164:167]\n"
#define G8x "v_mfma_f32_16x16x32_bf16 v[164:167], v[252:255], a[188:191], v[164:167]\n"
#define G8y "v_mfma_f32_16x16x32_bf16 v[168:171], v[232:235], a[168:171], 0\n"
#define G8z "v_mfma_f32_16x16x32_bf16 v[168:171], v[236:239], a[172:175], v[168:171]\n"
#define G80 "v_mfma_f32_16x16x32_bf16 v[168:171], v[240:243], a[176:179], v[168:171]\n"
#define G81 "v_mfma_f32_16x16x32_bf16 v[168:171], v[244:247], a[180:183], v[168:171]\n"
#define G82 "v_mfma_f32_16x16x32_bf16 v[168:171], v[248:251], a[184:187], v[168:171]\n"
#define G83 "v_mfma_f32_16x16x32_bf16 v[168:171], v[252:255], a[188:191], v[168:171]\n"
#define G84 "v_mfma_f32_16x16x32_bf16 v[172:175], v[232:235], a[168:171], 0\n"
#define G85 "v_mfma_f32_16x16x32_bf16 v[172:175], v[236:239], a[172:175], v[172:175]\n"
#define G86 "v_mfma_f32_16x16x32_bf16 v[172:175], v[240:243], a[176:179], v[172:175]\n"
#define G87 "v_mfma_f32_16x16x32_bf16 v[172:175], v[244:247], a[180:183], v[172:175]\n"
#define G88 "v_mfma_f32_16x16x32_bf16 v[172:175], v[248:251], a[184:187], v[172:175]\n"
#define G89 "v_mfma_f32_16x16x32_bf16 v[172:175], v[252:255], a[188:191], v[172:175]\n"
#define G9A "v_add_f32 v204, v204, v188\n"
#define G9B "v_sub_f32 v188, v188, v204\n"
#define G9C "v_sub_f32 v189, v189, v204\n"
#define G9D "v_sub_f32 v190, v190, v204\n"
#define G9E "v_sub_f32 v191, v191, v204\n"
#define G9F "v_accvgpr_read_b32 v206, a48\n"
#define G9G "v_accvgpr_write_b32 a48, v206\n"
#define G9H "v_accvgpr_read_b32 v206, a49\n"
#define G9I "v_accvgpr_write_b32 a49, v206\n"
#define G9J "v_accvgpr_read_b32 v206, a50\n"
#define G9K "v_accvgpr_write_b32 a50, v206\n"
#define G9L "v_accvgpr_read_b32 v206, a51\n"
#define G9M "v_accvgpr_write_b32 a51, v206\n"
#define G9N "v_accvgpr_read_b32 v206, a52\n"
#define G9O "v_accvgpr_write_b32 a52, v206\n"
#define G9P "v_accvgpr_read_b32 v206, a53\n"
#define G9Q "v_accvgpr_write_b32 a53, v206\n"
#define G9R "v_accvgpr_read_b32 v206, a54\n"
#define G9S "v_accvgpr_write_b32 a54, v206\n"
#define G9T "v_accvgpr_read_b32 v206, a55\n"
#define G9U "v_accvgpr_write_b32 a55, v206\n"
#define G9V "v_accvgpr_read_b32 v206, a56\n"
#define G9W "v_accvgpr_write_b32 a56, v206\n"
#define G9X "v_accvgpr_read_b32 v206, a57\n"
#define G9Y "v_accvgpr_write_b32 a57, v206\n"
#define G9Z "v_accvgpr_read_b32 v206, a58\n"
#define G9a "v_accvgpr_write_b32 a58, v206\n"
#define G9b "v_accvgpr_read_b32 v206, a59\n"
#define G9c "v_accvgpr_write_b32 a59, v206\n"
#define G9d "v_accvgpr_read_b32 v206, a60\n"
#define G9e "v_accvgpr_write_b32 a60, v206\n"
#define G9f "v_accvgpr_read_b32 v206, a61\n"
#define G9g "v_accvgpr_write_b32 a61, v206\n"
#define G9h "v_accvgpr_read_b32 v206, a62\n"
#define G9i "v_accvgpr_write_b32 a62, v206\n"
#define G9j "v_accvgpr_read_b32 v206, a63\n"
#define G9k "v_accvgpr_write_b32 a63, v206\n"
#define G9l "v_accvgpr_read_b32 v206, a64\n"
#define G9m "v_accvgpr_write_b32 a64, v206\n"
#define G9n "v_accvgpr_read_b32 v206, a65\n"
#define G9o "v_accvgpr_write_b32 a65, v206\n"
#define G9p "v_accvgpr_read_b32 v206, a66\n"
#define G9q "v_accvgpr_write_b32 a66, v206\n"
#define G9r "v_accvgpr_read_b32 v206, a67\n"
#define G9s "v_accvgpr_write_b32 a67, v206\n"
#define G9t "v_accvgpr_read_b32 v206, a68\n"
#define G9u "v_accvgpr_write_b32 a68, v206\n"
#define G9v "v_accvgpr_read_b32 v206, a69\n"
#define G9w "v_accvgpr_write_b32 a69, v206\n"
#define G9x "v_accvgpr_read_b32 v206, a70\n"
#define G9y "v_accvgpr_write_b32 a70, v206\n"
#define G9z "v_accvgpr_read_b32 v206, a71\n"
#define G90 "v_accvgpr_write_b32 a71, v206\n"
#define G91 "v_accvgpr_read_b32 v206, a72\n"
#define G92 "v_accvgpr_write_b32 a72, v206\n"
#define G93 "v_accvgpr_read_b32 v206, a73\n"
#define G94 "v_accvgpr_write_b32 a73, v206\n"
#define G95 "v_accvgpr_read_b32 v206, a74\n"
#define G96 "v_accvgpr_write_b32 a74, v206\n"
#define G97 "v_accvgpr_read_b32 v206, a75\n"
#define G98 "v_accvgpr_write_b32 a75, v206\n"
#define G99 "v_accvgpr_read_b32 v206, a76\n"
#define H0A "v_accvgpr_write_b32 a76, v206\n"
#define H0B "v_accvgpr_read_b32 v206, a77\n"
#define H0C "v_accvgpr_write_b32 a77, v206\n"
#define H0D "v_accvgpr_read_b32 v206, a78\n"
#define H0E "v_accvgpr_write_b32 a78, v206\n"
#define H0F "v_accvgpr_read_b32 v206, a79\n"
#define H0G "v_accvgpr_write_b32 a79, v206\n"
#define H0H "v_accvgpr_read_b32 v206, a80\n"
#define H0I "v_accvgpr_write_b32 a80, v206\n"
#define H0J "v_accvgpr_read_b32 v206, a81\n"
#define H0K "v_accvgpr_write_b32 a81, v206\n"
#define H0L "v_accvgpr_read_b32 v206, a82\n"
#define H0M "v_accvgpr_write_b32 a82, v206\n"
#define H0N "v_accvgpr_read_b32 v206, a83\n"
#define H0O "v_accvgpr_write_b32 a83, v206\n"
#define H0P "v_accvgpr_read_b32 v206, a84\n"
#define H0Q "v_accvgpr_write_b32 a84, v206\n"
#define H0R "v_accvgpr_read_b32 v206, a85\n"
#define H0S "v_accvgpr_write_b32 a85, v206\n"
#define H0T "v_accvgpr_read_b32 v206, a86\n"
#define H0U "v_accvgpr_write_b32 a86, v206\n"
#define H0V "v_accvgpr_read_b32 v206, a87\n"
#define H0W "v_accvgpr_write_b32 a87, v206\n"
#define H0X "v_accvgpr_read_b32 v206, a88\n"
#define H0Y "v_accvgpr_write_b32 a88, v206\n"
#define H0Z "v_accvgpr_read_b32 v206, a89\n"
#define H0a "v_accvgpr_write_b32 a89, v206\n"
#define H0b "v_accvgpr_read_b32 v206, a90\n"
#define H0c "v_accvgpr_write_b32 a90, v206\n"
#define H0d "v_accvgpr_read_b32 v206, a91\n"
#define H0e "v_accvgpr_write_b32 a91, v206\n"
#define H0f "v_accvgpr_read_b32 v206, a92\n"
#define H0g "v_accvgpr_write_b32 a92, v206\n"
#define H0h "v_accvgpr_read_b32 v206, a93\n"
#define H0i "v_accvgpr_write_b32 a93, v206\n"
#define H0j "v_accvgpr_read_b32 v206, a94\n"
#define H0k "v_accvgpr_write_b32 a94, v206\n"
#define H0l "v_accvgpr_read_b32 v206, a95\n"
#define H0m "v_accvgpr_write_b32 a95, v206\n"
#define H0n "v_add_f32 v160, v160, v188\n"
#define H0o "v_add_f32 v161, v161, v188\n"
#define H0p "v_add_f32 v162, v162, v188\n"
#define H0q "v_add_f32 v163, v163, v188\n"
#define H0r "v_add_f32 v165, v165, v188\n"
#define H0s "v_add_f32 v166, v166, v188\n"
#define H0t "v_add_f32 v167, v167, v188\n"
#define H0u "v_add_f32 v168, v168, v188\n"
#define H0v "v_add_f32 v169, v169, v188\n"
#define H0w "v_add_f32 v170, v170, v188\n"
#define H0x "v_add_f32 v171, v171, v188\n"
#define H0y "v_add_f32 v173, v173, v188\n"
#define H0z "v_add_f32 v174, v174, v188\n"
#define H00 "v_add_f32 v175, v175, v188\n"
#define C0k "s_addc_u32 vcc_hi, s99, 0\n" "s_swappc_b64 s[100:101], vcc\n"
#define C0l F37 H01
#define F39 B7f B0K
#define I7 A0 p0
#define A1C e0 s0
#define A12 P1 U4
#define B0B q1 S1
#define D2N F1 x3
#define B96 m0 h0
#define A13 w3 z3
#define c7 S0 W0
#define F3 o4 p4
#define B7H V0 U0
#define E2L q0 f0
#define A4 m5 n5
#define y3 P4 q3
#define B3V B1 J2
#define B2N Y5 N7
#define E0I b0 X0
#define l3 q4 d5
#define X5 O0 E0
#define V4 k4 V6
#define b7 K2 M0
#define A2d L3 Y0
#define A1y u2 x0
#define A7 k3 Y2
#define Z4 s3 n3
#define B1U F0 G0
#define B1v B9o B9n
#define B3f B5g B5f
#define B5k B3h B3g
#define D8S S2 x4
#define v4 Q0 B0
#define T5 u3 r3
#define A0j P0 I0
#define E9 S4 B4
#define A1b C92 C9x
#define B81 E2X E2Y
#define R6 J0 C0
#define t6 K4 Y3
#define A1a F4z F40
#define D83 U6 v0
#define G7 "s_cmp_eq_u32 0, 0\n" "s_setpc_b64 s[2:3]\n"
#define g7 "s_add_u32 s2, s2, 16384\n" "s_addc_u32 s3, s3, 0\n"
#define B1E D8G D8I
#define A3c D4R D4S
#define B4H G1i G1j
#define A3F B82 B83
#define A19 a4 b4
#define W8 B8w B8x
#define U8 L0 Z0
#define A6B B4h B4i
#define I9 a0 R0
#define j9 D7I D7J
#define i9 D3H D3I
#define B0A D7C D7D
#define d9 G9 H9
#define A7L D4P D4Q
#define C8r D4z D40
#define A72 E9n E9o
#define D38 "v_pk_mul_f32 v[140:141], v[140:141], v[230:231]\n" "v_pk_mul_f32 v[142:143], v[142:143], v[230:231]\n"
#define D37 "v_pk_mul_f32 v[136:137], v[136:137], v[230:231]\n" "v_pk_mul_f32 v[138:139], v[138:139], v[230:231]\n"
#define G3M "v_lshlrev_b32 v132, 16, v102\n" "v_and_b32 v133, 0xffff0000, v102\n"
#define G3L "v_lshlrev_b32 v130, 16, v101\n" "v_and_b32 v131, 0xffff0000, v101\n"
#define B5d "v_lshlrev_b32 v128, 16, v100\n" "v_and_b32 v129, 0xffff0000, v100\n"
#define B0E D3E D3F
#define A0I e8 f8
#define A0D D3Q D3S
#define x9 B7B B7C
#define v9 B0q E1x
#define B0w S5 w4
#define q9 B4E B4F
#define o9 A8H A8I
#define l9 d0 c0
#define t9 A3y A3v
#define C2v G3Q G3R
#define F32 G3O G3P
#define E8J G3J G3K
#define G1e G3H G3I
#define G1d G3F G3G
#define A0m C0Z C0a
#define A0g Q8 R8
#define A0h M8 N8
#define B2R B93 B94
#define A0V D60 D6z
#define C7K D5s D5t
#define A0P B2Y B2Z
#define A0Z A5O D2H
#define B3l F3W F3X
#define D4F F33 F34
#define E4a F38 H02
#define A1K B6B B6D
#define A08 W2 g1
#define A0r A70 A71
#define E14 g3 t4
#define B4Y A7p A7q
#define B4X A7r A7s
#define A0p R5 W5
#define E37 E2R E2S
#define A1o X7 Y7
#define A1n V7 W7
#define A1m T7 U7
#define B3I A4j E1
#define A1q D4N D4O
#define A1Y B9q B9r
#define A1X B8t B8v
#define A1W B69 B6J
#define B0n D66 D67
#define A1Q A3B A3C
#define A1P A3e A3f
#define B73 I1 H1
#define D5R A5x A5y
#define B6f A7t A7u
#define A1z C3V A7b
#define C2y E1P E1Q
#define B3k E2P E2Q
#define E9g E11 E10
#define D4H E1V E1W
#define A2i B3O B3P
#define A2Y C7 D7
#define C4k E7 F7
#define A2I B95 A3g
#define A2D A6s A6t
#define A2B A1f A1g
#define D4G D7P D7O
#define A3k C0P A7Q
#define A3P o6 p6
#define A23 B1c B1d
#define A22 B04 B05
#define A21 B1B B4c
#define A20 B4k B4l
#define A2x B0l B0m
#define B8K B42 B43
#define A2w B9y B9z
#define B4G B1Y B1Z
#define A2t A3Z A3a
#define B6U A56 A57
#define A2s A58 A59
#define A2r A54 A55
#define A3L U1 B6h
#define A3X A0n A0o
#define A3Y H0 t0
#define B7l D6w D6x
#define B4J A8i A8j
#define D36 H03 B1w
#define A4d A7M A7N
#define A4q A9O A9P
#define A4K B5G F36
#define A4J A9v A9w
#define A4I A9x A9y
#define A4H A2V A2W
#define D4y B23 B24
#define A4G B25 B26
#define A4F B2z B20
#define A4E B21 B22
#define A4B A1l B0p
#define A4e F4 t3
#define A39 A8k A8l
#define A38 A6J A6K
#define B6H B2t B2u
#define A36 E2J F3g
#define A33 A4Z A4a
#define A32 C5E A4X
#define A4n A7v p9
#define A31 A09 A1A
#define A4m m9 n9
#define A30 j4 q6
#define A3s C2L C2M
#define A6Z M9 o5
#define A5o B5P B5Q
#define A5n A7R A7S
#define A6i D4L D4M
#define A5a E4H D5Q
#define A5Z B1l B1m
#define A5Y B1i B1j
#define D41 A8m A8n
#define A6U A0w B0T
#define A5V B5C B5D
#define A5U B46 B47
#define A5T B48 B49
#define A5S A3G A3H
#define A5Q B44 B45
#define A7C A2u A2v
#define A6l c1 D0
#define A5P A3A B4C
#define B8n A4f A4g
#define B98 A4b A4c
#define A7z A3K A3M
#define A7y B5E A3d
#define A8B B2P B2Q
#define A7x Z5 a5
#define A7o B3E B3F
#define A8G B35 E8Z
#define A7k B0F B0G
#define A7j B6Z A2c
#define E4S T3 U3
#define A8J A7h w9
#define A7g A74 A0e
#define A7f A0c A0d
#define A7e B3J B3K
#define D9K G3 H3
#define A8D A0s A0t
#define A7d B4D B2U
#define A7Z A9 A4W
#define A8C A3z q8
#define B3B C0k M7
#define H05 "v_mov_b32 v192, v196\n" "v_mov_b32 v193, v197\n" "v_mov_b32 v194, v198\n" "v_mov_b32 v195, v199\n"
#define H04 C0l "s_add_u32 vcc_lo, s98, Lrare1_1-Lrare0_0\n"
#define q5 A0 P0
#define B5O q1 N1
#define A10 e0 a0
#define A8g A12 x4
#define B3S Y0 I7
#define B9u S0 V0
#define B1g t4 S1
#define C0g J2 B1
#define A5D m0 p0
#define E4B E0I i4
#define F0H B96 E2L
#define F4A F37 F38
#define D4v d0 B7H
#define D7G B0B A13
#define A0l v0 b0
#define D3A O3 P1
#define E8S x0 J1
#define X6 N7 k3
#define S6 s0 J0
#define u4 q3 M1
#define A8v f0 X0
#define C5 Q1 P4
#define C2H "v_pk_mul_f32 v[128:129], v[128:129], v[230:231]\n" "v_pk_mul_f32 v[130:131], v[130:131], v[230:231]\n" "v_pk_mul_f32 v[132:133], v[132:133], v[230:231]\n"
#define B2v h0 i0
#define F9 A1y R1
#define A1R D8S U1
#define s6 c3 F1
#define B7 C1 B0
#define B5Y B5k B1v
#define A2p B3f A1N
#define B8 A73 q0
#define S8 N0 C0
#define k9 V2 Z0
#define B03 B5 D83
#define F35 G3L G3M
#define A0F t0 M0
#define A0C Q0 T0
#define B3R a3 D2N
#define A1B I1 L0
#define G1f E8J E2N
#define B1S E1z H01
#define B7R Y2 G4
#define A1t R0 O0
#define A2A j7 L3
#define D39 D37 D38
#define A2q H0 W0
#define F5S I0 i3
#define E4R D4I E37
#define E2T D6y E9g
#define D7A A1h E1
#define A4o g1 F0
#define D5j G0 S2
#define E1K o3 m3 X3
#define F3f C2v D4F
#define A7c B81 f2
#define A8f A0a c0
#define A7Y M4 H4
#define E0G E0 F4
#define E1M A3D A3E
#define B0C A3p B5M
#define A99 A5F A5G
#define A9m V5 o8
#define A93 A2S A2T
#define A9p A1c A1d
#define A9o A2L A2M
#define A9u A4Q A4R
#define A9F E4E B2g
#define A9E E1L D68
#define A9C A8d A8e
#define A9B A8K D9J
#define A88 A4s A3n
#define A87 B3M B3L
#define A92 u9 A5W
#define A9s A2F A2H
#define A84 A2R B6E
#define A83 B9 C9
#define A8z a8 b8
#define A9q j4 T4
#define A8y A2a Z8
#define B1f C2y D4G
#define G3T A1b A2U
#define D9Q A0j B1U
#define F48 e5 g9
#define E13 c9 r2
#define B0j K9 L9
#define B0i A14 A15
#define B0r A1i A1j
#define B00 A9S A9U
#define B0g D64 E1F
#define B0z A9Q A9R
#define B0y A03 A04
#define B07 D0 X5
#define B0a A8E A8F
#define B0u B9Q E4H
#define B0X A2X B6b
#define B0W A1r A1s
#define B0V D20 A4t
#define B0s A0x A0y
#define B0P A2k A2l
#define B02 A1S A1T
#define B09 h4 e3
#define F8N A7 L1
#define B2f y7 Z7
#define B2w m4 n4
#define B2j B2J B2K
#define B2G C0I A6d
#define B2T p1 C4k
#define B2e B4n B2F
#define B2p A2z r8
#define B2a D9G D9O
#define B2n r9 s9
#define B2X F3g A2E
#define B3Y Q2 w0
#define B3W S3 E4S
#define B2s A80 A7a
#define B3H K4 k1
#define B2l A29 T8
#define r1 F39 "v_mov_b32 v196, v192\n" "v_mov_b32 v197, v193\n" "v_mov_b32 v198, v194\n" "v_mov_b32 v199, v195\n"
#define H06 "s_add_u32 vcc_lo, s98, Lrare0_0-Lrare0_0\n" C0k
#define Q4 A0 B1
#define L4 H04 B3B
#define B7U B5O A8g
#define A2e V0 a0
#define E1R F0H E4B
#define A8r B2N J2
#define A2G D4v A10
#define A5H N1 D7G
#define E4I p0 i0
#define V8 L3 m0
#define H08 Y5 C0g
#define P5 A5D Y0
#define C8 x0 u2
#define c5 S0 t0
#define A5C e0 W0
#define B2o q0 h0
#define E8F H0 B9u
#define C0i p3 F1
#define A8 B0 Z0
#define A7n N7 D1
#define A8R g1 P0
#define E3x b0 f0
#define F0E c3 x4
#define A0i d0 R0
#define E8I F3c G1d G1e
#define D35 F35 F32
#define A00 B0B w3
#define A18 B3V E8S
#define A1V M3 L0
#define B41 Z2 D2N
#define A1O U0 C0
#define D4E C2H H03
#define B5u I0 S2
#define F8M F5S F0
#define D9W E14 S1
#define F3h v2 q5
#define E6x E2T E4R
#define D2x m1 E1
#define A8t D4H F3f
#define A9z K3 M0
#define A85 U1 A2g
#define A81 G1 D5R K1
#define A86 W5 s0
#define F2Y G0 A16
#define B0M A4 I1
#define B1n A0Y A0l
#define B0Y B0t B9Q
#define E0H E4 i3
#define B0o l0 r2
#define B1s D36 A2p
#define B2D B73 A1R
#define E39 A1C B03
#define F3J "v_pk_mul_f32 v[144:145], v[144:145], v[230:231]\n" "v_pk_mul_f32 v[146:147], v[146:147], v[230:231]\n" "v_pk_mul_f32 v[148:149], v[148:149], v[230:231]\n"
#define E1S d3 T4
#define B2A S3 E0
#define B28 A1 O0
#define B2b d5 K4
#define E1N v0 I7
#define B33 D4y K1
#define B3t B6f P2
#define B3p B4X i2
#define B3q B4Y U2
#define E4V B0A J4
#define B3r A3c j1
#define F3z G29 G3A
#define B5B A3I c0
#define B4B O8 P8
#define B5K A06 A9T
#define B5H B0b B0c
#define B4f A4N A4O
#define B37 A4k B7O
#define B34 A76 A77
#define B5N A25 L1
#define B4N y9 A7i
#define B4d A0T A0U
#define B3x D9O A4i
#define B3w A4h D9G
#define E9P H1 D5j
#define B4T H7 J7
#define B4S q6 z2
#define B3m A0O h3
#define B3u N0 z0
#define B7T D9K B1g
#define B5j B7l B3k
#define B66 B0E v3
#define B51 A72 o2
#define B54 C8r q2
#define B5x A6B R1
#define B5y A3F c1
#define F4u B4H B3
#define G3W A1a N4
#define G1b F3d F3e
#define G3N F3Y F3b
#define B6y r4 s4
#define B6O o8 y7
#define v1 F39 H05
#define W4 C0l "s_add_u32 vcc_lo, s98, Lrare0_1-Lrare0_0\n" C0k
#define N3 C0l H06
#define D6m H07 B3B
#define Q6 G4 A0
#define A7B B1 O1
#define E4e B3S E1R
#define D88 p0 m0
#define A0k V0 d0
#define A1p U1 x0
#define A5c e0 U0
#define i7 a0 s0
#define A4r A2d E4I
#define F2X F1 b0
#define J9 D3A A5H
#define h9 H08 V2
#define E12 B1g B7U
#define A4u h0 A8v
#define f9 B0 X0
#define A0R j7 q0
#define C0F Z0 L1
#define B3A f0 b3
#define A0Q Y0 B2v
#define D2R E3x i4
#define D9F F0E C0i
#define C0S k3 D1
#define D7B S0 u1
#define D5N t0 C0
#define C2N E8I G1f
#define D4T Q4 R1
#define A2J A73 L3
#define A2Z W0 L0
#define A3q A8r E8S
#define A3b B9u A1C
#define A24 C3 R0
#define A3h M0 H0
#define A5B Y5 J1
#define D8X x4 A11
#define A34 I0 G1
#define E6z F8M B1a
#define F0F F3h R3
#define A7V B5d D35
#define A8h P1 N1
#define E2K w2 D2N
#define A9t B2 X0 C2
#define A82 P0 H1
#define D5A F0 S2
#define B0Q R5 A2G
#define E93 G0 l1
#define F56 i0 I7
#define F3i F2Y G3T
#define B1u E6x B3l D39
#define B1t D4E B5Y B1w
#define E15 E1K D9W
#define B4p f5 r0 k7 T0
#define B5L A7n F8N
#define B4W B0w v0
#define G3V A0K B2R A07
#define B5V c8 A0l
#define B31 T4 E0G
#define B32 q6 t3
#define B3i r0 T0
#define E4g E1 E1M
#define B7a F3J F3K
#define B62 r1 "s_mov_b32 s12, 319488\n"
#define B55 D41 K1
#define E9m B81 B3
#define B5w V4 I1
#define B6Y E1F A4U
#define B64 A01 B0f
#define B7E A9M A9N
#define B6Q B0d B0e
#define B6P A9K A9L
#define B58 A78 A79
#define B7D F36 A2K
#define B56 B1C B4n
#define B6c A9D z9
#define B6m D9J D20
#define B7L S9 B6Z
#define B60 A0S A4s
#define B6l D8 B95
#define B6G A0v E2J
#define B6F O9 P9
#define B7Z B4 w1
#define B6T B6E C5E
#define B6k a7 A1e
#define B6N K2 g1
#define B7A s3 I2
#define B5c D63 j2
#define B5Z D4J D4K
#define C0f G1b B1w
#define B7r E0H O0
#define C0U A7L j3
#define B7v B1E q2
#define C0e E8G E8H
#define G1g E8K G3N
#define C0d E8R E8Q
#define C0c E8P F3z
#define B9l E1z F38
#define B8T Z7 M9
#define C0J A1k c0
#define C0D A3I w0
#define B8y X9 Y9
#define F59 A0G Y2
#define B8Z B39 A28
#define B97 A4S A4T
#define B8k A2N A2O
#define B91 A0E P4
#define d4 C0l D6m
#define I4 A0 O0
#define p5 H04 C0k
#define T6 q0 D88
#define y6 R1 A7B
#define C0L e0 d0
#define B4Z V0 W0
#define B1b B1 D1
#define C4j S0 a0
#define A8Q v0 F2X
#define A0B i0 h0
#define B4I F1 u2
#define A05 R0 Z0
#define A02 B0 p0
#define A2C B96 Y0
#define A2Q x0 J2
#define A3j F37 E4a B3B
#define B6V K2 L0
#define A2y t0 c7
#define A3J U0 A10
#define D3B B0D m0
#define A37 g1 X0
#define A4P b0 D0
#define A6z T0 H0
#define E4D D9F E4e
#define D9L c2 D2N
#define D7H D7B A2e
#define D7K B2o D2R
#define A9k b3 f0
#define E4j F0F E6z
#define B1W H08 J1
#define B1h a9 M0
#define F3s x2 q5 y2
#define F3k g2 q5
#define B0Z G0 G1
#define F6E I0 f3
#define B27 F48 D8X
#define B29 S5 A2G
#define B4u g8 K0 f5 u0
#define B5U E12 B3R
#define E3y B7U E2K
#define E3w D7A E93
#define E4C A0L F0
#define F5V A2h C0
#define B4s A11 A2g
#define B30 C2 P0
#define B3y Q3 B3A
#define B4b w4 s0
#define B67 B1g B5O
#define B5z B8K K1
#define B61 F39 H05 "s_mov_b32 s12, 294912\n"
#define B6M D5Q B1C
#define B6L B6b A9D
#define B6R c1 r0
#define B5b D62 j2
#define B9D D7E E0 D22
#define B9j v1 "s_mov_b32 s12, 344064\n"
#define B8X E8F A1C
#define B9k r1 "s_mov_b32 s12, 368640\n"
#define C0E E13 E1
#define C0M D3A N1
#define C0X A4V x4
#define C0N B0f P2
#define B8U A9H A9I
#define B8V A9G A9J
#define B8z A4L A4M
#define B8S E8Z E4E
#define B8C A2f A8W
#define B8i A0A B0t
#define B9p A7i u8
#define B8s A3n R9
#define B8h B0T A0S
#define B8Q E1x A7h
#define B8O A4C A4D
#define B8d B5A A1U
#define B71 A2 F4u
#define B7z h4 g3
#define F4B S7 C8
#define B7t A1e A7v
#define B88 U5 B9X
#define C0H u3 U2
#define B7x H1 E9
#define B9x Y3 x1
#define B8m A4W A3z
#define B9B q4 f1
#define B7n D2H A29
#define B8B M4 J4
#define C0K n3 G2
#define F4x K0 O1
#define D3c G1g E2O
#define D2X D2x E1S
#define D1o B1S H06
#define D2V X6 F59
#define C33 B6H N2
#define C34 B4G I2
#define b5 A0 Z0
#define F3y H07 C0k
#define A7A S0 U0
#define Q9 B0 B1
#define C0W q0 m0
#define A1F H0 a0
#define D87 b0 p0
#define C0b d0 W0
#define B2S V0 A1C
#define B4w h0 D3
#define B2x f0 i0
#define A3o F4A E1A B3B
#define B3o E8F e0
#define A3U L0 X0
#define A4A V3 R0
#define A35 t0 A5C
#define B8H Y0 C0
#define E0B x0 c1
#define H1P "v_mov_b32 v205, v204\n" F1
#define A7P J1 B1b
#define A95 D5N c7
#define B0J D1 C0F
#define A94 D0 I0
#define B1A u2 D4T
#define F6A F6E F0
#define B19 A1p B4I
#define B3e Q6 O0
#define B2L Y5 J2
#define B3T K2 Q4
#define B1r c0 C2N C2H
#define B5S F3k I3
#define E3t N2 P0
#define B7W E4j F3i
#define B7K E12 B41
#define B52 A8R G1
#define B7J B7U D9L
#define B7Y B5O P1
#define B6z h8 M0
#define F0f L1 E1L
#define B6A B2h L1 B2i
#define B50 v0 B5A
#define B5a D61 j2
#define B87 H1B E0 B9W
#define B7w B6U K1
#define B85 B2F A8W
#define B7y m3 E0
#define B8g U5 A3
#define C3u F4x u0
#define D26 D7H A5c
#define D2T B4Z C0L
#define D2s E15 E3y
#define D1p v1 "s_mov_b32 s12, 393216\n"
#define D13 r1 "s_mov_b32 s12, 417792\n"
#define C8z S6 c2
#define C39 B0n s1
#define C5w C7K B3
#define C5y C8r o2
#define C72 B4H f2
#define D2n y3 u1
#define D30 "s_mov_b32 s93, -1.0\n" "v_max_f32 v204, 0, v204\n"
#define D3X "v_pk_mul_f32 v[156:157], v[156:157], v[230:231]\n" "v_pk_mul_f32 v[158:159], v[158:159], v[230:231]\n"
#define D3K l8 Z9
#define D3g B5M W2
#define D3f C0P A0J
#define D2S A2o P4
#define R4 A0 X0
#define r6 C0l F3y
#define E2U c1 Z0
#define A2b B0 H0
#define D9P H09 C0k
#define C0Q B1X q0
#define C9F k7 B1
#define C0O A9V m0
#define A5E h0 s2
#define F58 b0 K0
#define A6h L0 f0
#define A6v S0 d0
#define H1Q F1 "v_max_f32 v204, v204, v205\n"
#define A5b W0 R0
#define A5L x0 C0
#define A8s C0l E1A B3B
#define F0g C0W Y0
#define A8x a0 M0
#define D9R A2e A1C
#define B0v O1 E0B
#define F4o i2 p0
#define B1D e0 t0
#define B0S I0 z0
#define B3Q J1 A8r
#define G3S F6A A3m G0
#define B5W B7R b5
#define G2F A7T F0
#define B4e D0 P0
#define E38 U0 C4j
#define G2G A0b G0
#define B68 A0u L1
#define B5t i0 j0
#define B8Y A19 v0
#define B7s U5 U3
#define B8D K2 I4
#define B8A K1 g1
#define B9S V0 c3
#define B70 N0 B8H
#define B7e C4J j2
#define C2i E4g B98 B4J B8n A0M
#define C2b B5Y "v_accvgpr_write_b32 a128, v136\n" "v_accvgpr_write_b32 a129, v138\n" "v_accvgpr_write_b32 a130, v140\n"
#define D27 E3t A1L
#define C4D C7K f2
#define C7y B1E o2
#define C4i B1U Q6
#define D3C B7H C0L
#define D3z H1O "v_cmp_lt_f32 vcc, 0x41000000, v205\n" "s_nop 4\n"
#define D24 k8 A2o
#define D2C A7O N3 A8o d4
#define D2D A49 W4 A7O L4
#define D1l "v_cvt_pk_bf16_f32 v110, v204, v205\n" "v_cvt_pk_bf16_f32 v111, v206, v207\n"
#define D1b "v_cvt_pk_bf16_f32 v110, v124, v125\n" "v_cvt_pk_bf16_f32 v111, v126, v127\n"
#define D1Y "v_cvt_pk_bf16_f32 v108, v120, v121\n" "v_cvt_pk_bf16_f32 v109, v122, v123\n"
#define D1V "v_cvt_pk_bf16_f32 v106, v116, v117\n" "v_cvt_pk_bf16_f32 v107, v118, v119\n"
#define D1U "v_mfma_f32_16x16x32_bf16 v[68:71], a[92:95], v[140:143], v[68:71]\n" A1
#define D1T "v_fmac_f32 v118, v54, v211\n" "v_fmac_f32 v119, v55, v211\n"
#define D1O "v_cvt_pk_bf16_f32 v104, v112, v113\n" "v_cvt_pk_bf16_f32 v105, v114, v115\n"
#define D1I "v_cvt_pk_bf16_f32 v134, v44, v45\n" "v_cvt_pk_bf16_f32 v135, v46, v47\n"
#define D1C "v_mul_f32 v46, v46, v210\n" "v_mul_f32 v47, v47, v210\n"
#define D05 "v_mul_f32 v43, v43, v210\n" "v_mfma_f32_16x16x32_bf16 v[52:55], a[56:59], v[136:139], v[52:55]\n"
#define D04 "v_mul_f32 v41, v41, v210\n" "v_mul_f32 v42, v42, v210\n"
#define D0t "v_cvt_pk_bf16_f32 v128, v32, v33\n" "v_cvt_pk_bf16_f32 v129, v34, v35\n"
#define D0s "v_mul_f32 v36, v36, v210\n" "v_mul_f32 v37, v37, v210\n"
#define D0o "v_cmp_nge_f32 vcc, s93, v197\n" B6t
#define D0i "v_cvt_pk_bf16_f32 v132, v24, v25\n" "v_cvt_pk_bf16_f32 v133, v26, v27\n"
#define D0d "v_mul_f32 v24, v24, v210\n" "v_mul_f32 v25, v25, v210\n"
#define D0X "v_cvt_pk_bf16_f32 v128, v16, v17\n" "v_cvt_pk_bf16_f32 v129, v18, v19\n"
#define D0E "v_cvt_pk_bf16_f32 v133, v10, v11\n" "v_cvt_pk_bf16_f32 v132, v8, v9\n"
#define C91 "v_cvt_pk_bf16_f32 v128, v0, v1\n" "v_cvt_pk_bf16_f32 v129, v2, v3\n"
#define D3e R7 E0
#define D2m Q7 c0
#define C8G D9 B5E
#define D2P U9 F2
#define D3M T9 m8
#define D2c A9T W1
#define C7J A2P D64
#define Y4 p3 A0
#define B5T B0h q0
#define B38 B1 Z0
#define A4l B0 h0
#define E3p H1A C0k
#define A5m x0 F1
#define F3x H02 B3B
#define A8A t0 R0
#define E25 V8 K0 Y0
#define A9n K1 L0
#define E2h f0 h2
#define D69 b0 N0
#define A9j W0 A0k
#define B1o C0l D9P
#define B0k d0 D0
#define F8O E3 q5 Z3 I0
#define B2y F4o X2
#define B3X F0g F56
#define B2M V0 e0
#define B2I T0 p0
#define B40 B3S m0
#define B4t h8 b5
#define B5F v0 C0
#define B3z X0 B2
#define B4A U0 M0
#define B3j a0 k0
#define B7I G3S G3V
#define B7N C0Q D3B A4r
#define B53 E2U b7
#define B6g G0 z0
#define B7G K0 i0
#define B92 N9 O0 A0H
#define C0C G2G B3I
#define F41 C0O A2d
#define F4q x1 P0
#define F4s f3 O0
#define F6D B4D L1
#define F5T c1 E0
#define B75 H0 p2
#define B7d C4H j2
#define C93 "v_mul_f32 v7, v7, v210\n" "v_mul_f32 v4, v4, v210\n" "v_mul_f32 v5, v5, v210\n" B6n
#define D1B "v_cvt_pk_bf16_f32 v132, v40, v41\n" "v_cvt_pk_bf16_f32 v133, v42, v43\n" "v_mul_f32 v44, v44, v210\n" "v_mul_f32 v45, v45, v210\n"
#define D1W "v_fmac_f32 v120, v56, v211\n" "v_fmac_f32 v121, v57, v211\n" "v_fmac_f32 v122, v58, v211\n" "v_fmac_f32 v123, v59, v211\n"
#define D1c "v_fmac_f32 v144, v64, v211\n" "v_fmac_f32 v145, v65, v211\n" "v_fmac_f32 v146, v66, v211\n" "v_fmac_f32 v147, v67, v211\n"
#define C6d E38 B2S
#define D3R D7K R4
#define D3P C0g J1
#define D2B A8o W4 B0H L4
#define D2A B0H N3 B1k d4
#define D19 B1k W4 B3Z
#define D18 B3Z N3 B5X
#define D2F A3i L4
#define D1g "v_cvt_pk_bf16_f32 v106, v148, v149\n" "v_cvt_pk_bf16_f32 v107, v150, v151\n" A7F
#define D0p "v_cvt_pk_bf16_f32 v134, v28, v29\n" "v_cvt_pk_bf16_f32 v135, v30, v31\n" B6u
#define D0A "v_cvt_pk_bf16_f32 v130, v4, v5\n" "v_cvt_pk_bf16_f32 v131, v6, v7\n" H1B
#define D2W u1 C4j
#define C4P A1w A1x
#define C4O A1u A1v
#define C6C B7O C0I
#define C8R A26 A27
#define C8K X8 Y8
#define C7M A5X R9
#define C8A B0p A2z
#define D2h B6J A74
#define C5p A5W B69
#define C37 d2 d8
#define C7x A2 E9m
#define C32 i8 j8
#define C7v O4 m3
#define D25 S4 w1
#define D2z r3 k2
#define C6Y u2 y0
#define C4I H1 D5A
#define C6e B4C C3V
#define C9s L3 "v_mfma_f32_16x16x32_bf16 v[164:167], a[152:155], a[128:131], v[164:167]\n"
#define C4v A2a q3
#define C8W V1 Q0
#define C77 S0 m2
#define C5x H4 b2
#define C3I E9h E9i
#define C22 "v_accvgpr_write_b32 a140, v136\n" "v_accvgpr_write_b32 a141, v138\n"
#define C21 "v_accvgpr_write_b32 a142, v140\n" "v_accvgpr_write_b32 a143, v142\n"
#define C3Q g0 c2
#define C2Y "v_accvgpr_write_b32 a109, v130\n" "v_accvgpr_write_b32 a110, v132\n"
#define C2G "v_accvgpr_write_b32 a98, v132\n" "v_accvgpr_write_b32 a99, v134\n"
#define c4 S2 A0
#define B74 c1 B1
#define A5N F58 q0
#define A5M o0 B0
#define A7K b0 h0
#define A8q F4A H1A C0k
#define A9i L0 F0
#define A9A i0 R0
#define A89 m0 f0
#define A8w W0 N0
#define B1p C0l F3x
#define B0U Y0 E4I
#define B0R C0 F1
#define E1O H0 d0
#define F4m F8O W3 F0
#define B4v E25 C1
#define B5R Q6 Z0
#define E92 D2 E2h
#define B4a t0 B4Z
#define G3Y A2h x0
#define B7Q e0 u1
#define G3U X0 a2
#define C0B h7 M0
#define B9v M1 U0
#define B90 D0 O0
#define B8e z0 G0
#define F6B A80 L1
#define B8a P0 G1
#define B7c C4G j2
#define C2K "v_accvgpr_write_b32 a100, v128\n" "v_accvgpr_write_b32 a101, v130\n" "v_accvgpr_write_b32 a102, v132\n" "v_accvgpr_write_b32 a103, v134\n"
#define C2U "v_accvgpr_write_b32 a104, v128\n" "v_accvgpr_write_b32 a105, v130\n" "v_accvgpr_write_b32 a106, v132\n" "v_accvgpr_write_b32 a107, v134\n"
#define C2W "v_accvgpr_write_b32 a124, v136\n" "v_accvgpr_write_b32 a125, v138\n" "v_accvgpr_write_b32 a126, v140\n" "v_accvgpr_write_b32 a127, v142\n"
#define C2p "v_accvgpr_write_b32 a112, v128\n" "v_accvgpr_write_b32 a113, v130\n" "v_accvgpr_write_b32 a114, v132\n" "v_accvgpr_write_b32 a115, v134\n"
#define D3G F0f D1 D68 J1
#define D1e "v_fmac_f32 v148, v68, v211\n" "v_fmac_f32 v149, v69, v211\n" "v_fmac_f32 v150, v70, v211\n" "buffer_store_dwordx4 v[36:39], v224, s[44:47], s92 offen offset:3328 nt\n" "v_fmac_f32 v151, v71, v211\n" A7D w0
#define D2U F6D B2U D1
#define C80 F4q s1
#define D3N B5T F41
#define D3h "s_waitcnt vmcnt(0) lgkmcnt(0)\n" B0K "s_lshl_b32 s15, s22, 2\n" "s_add_u32 s15, s15, s23\n" "s_lshl_b32 s15, s15, 14\n" "s_add_u32 s15, s15, 0x100000\n" "s_add_u32 s16, s20, s15\n" "s_addc_u32 s17, s21, 0\n" "s_mov_b32 s18, 0x4000\n" "s_mov_b32 s19, 0x20000\n"
#define D17 B5X W4 A5A
#define C38 e2 E0
#define C3x O4 F4s
#define C8s V6 S4
#define C3l m2 T0
#define C2l "ds_read_b128 a[228:231], v214 offset:4800\n" "ds_read_b128 a[232:235], v214 offset:8960\n" "ds_read_b128 a[236:239], v214 offset:9024\n"
#define C7R K0 v0
#define C2B "s_andn2_b64 s[8:9], s[10:11], s[6:7]\n" "s_and_b64 s[10:11], s[10:11], s[6:7]\n"
#define C2A "v_mov_b32 v199, 0\n" "v_accvgpr_write_b32 a252, v20\n"
#define C18 "v_mov_b32 v196, 0\n" "v_mov_b32 v197, 0\n"
#define C1z "v_cmp_le_i32_e64 s[64:65], 16, v17\n" "v_cmp_le_i32_e64 s[66:67], 17, v17\n"
#define C1y "v_cmp_le_i32_e64 s[60:61], 2, v17\n" "v_cmp_le_i32_e64 s[62:63], 3, v17\n"
#define C1v "v_add_u32 v219, v13, v14\n" "v_add3_u32 v221, v13, v16, v228\n"
#define C1t "v_add_u32 v229, 160768, v8\n" "v_add_u32 v216, 256, v14\n"
#define C1n "v_cmp_le_u32_e64 s[6:7], 8, v9\n" "v_cmp_eq_u32_e64 s[10:11], v24, v10\n"
#define C1a "v_lshl_add_u32 v8, v1, 4, v2\n" "v_lshl_add_u32 v14, v5, 8, v14\n"
#define C1O "v_add_u32 v214, 256, v11\n" "v_add_u32 v215, 63616, v11\n"
#define C1N "v_accvgpr_write_b32 a250, v20\n" "v_accvgpr_write_b32 a251, v20\n"
#define C1M "v_accvgpr_write_b32 a248, v20\n" "v_accvgpr_write_b32 a249, v20\n"
#define C1I "v_mov_b32 v25, 0xffff\n" "v_mov_b32 v26, 0xffff0000\n"
#define C1G "v_accvgpr_write_b32 a246, v20\n" "v_accvgpr_write_b32 a247, v20\n"
#define C1F "v_accvgpr_write_b32 a244, v20\n" "v_accvgpr_write_b32 a245, v20\n"
#define C1A p8 "v_cmp_eq_u32 vcc, 1, v15\n"
#define C05 "s_mov_b32 s34, 0xC0000\n" "s_mov_b32 s35, 0x20000\n"
#define C0v D2G "v_lshlrev_b32 v27, 7, v27\n"
#define C0u "v_lshl_add_u32 v27, v0, 7, v27\n" "s_mov_b64 s[52:53], s[100:101]\n"
#define C0r "s_mov_b32 s27, 0x20000\n" "v_mad_u32_u24 v223, v6, s0, v228\n"
#define C0n "v_lshrrev_b32 v2, 3, v0\n" "v_and_b32 v4, 1, v2\n"
#define g4 v0 A0
#define D86 B1 L1
#define A7l D87 q0
#define A8p F4A H09 C0k
#define A7w y0 B0
#define B1q C0l E3p
#define B06 g1 f0
#define B0N a3 L0
#define B1V W0 M0
#define B3G A4V F1
#define B2m R0 E0
#define B2E d0 C0
#define E0A a0 N0
#define F6S F3s I0 l2
#define B4j Q3 h0
#define E30 Z0 d3
#define E21 X0 i0
#define F3r b0 I7
#define B7V F4m A2n
#define B6j E2L m0
#define F0G I0 G2F
#define B6p x0 F5V
#define B9R G3Y g8
#define F4Q B74 K2
#define B8P G2 D0
#define B9w D3 G3U
#define F4t O1 O0
#define C0G V0 t0
#define B8F H0 U0
#define B9C G0 l4
#define B7b C4F j2
#define C0x "s_add_u32 s28, s6, s96\n" "s_addc_u32 s29, s7, 0\n" "s_add_u32 s32, s8, s96\n" "s_addc_u32 s33, s9, 0\n"
#define C1B "v_accvgpr_write_b32 a240, v20\n" "v_accvgpr_write_b32 a241, v20\n" "v_accvgpr_write_b32 a242, v20\n" "v_accvgpr_write_b32 a243, v20\n"
#define D2Q F6B A7a D1
#define D2o E1O c7
#define D21 B9u e0
#define D16 A7J W4 A8c
#define D2E A3i N3 A49 d4
#define D1k "v_cvt_pk_bf16_f32 v107, v190, v191\n" "v_fmac_f32 v204, v92, v211\n" "v_fmac_f32 v205, v93, v211\n" "v_cvt_pk_bf16_f32 v108, v200, v201\n" "v_cvt_pk_bf16_f32 v109, v202, v203\n" "v_fmac_f32 v206, v94, v211\n" "v_fmac_f32 v207, v95, v211\n"
#define C9y "v_cndmask_b32_e64 v176, 0, v240, s[8:9]\n" "v_cndmask_b32_e64 v177, 0, v241, s[8:9]\n" "v_cndmask_b32_e64 v178, 0, v240, s[10:11]\n" "v_cndmask_b32_e64 v179, 0, v241, s[10:11]\n" "v_mul_f32 v0, v0, v210\n" "v_mul_f32 v1, v1, v210\n" "v_mul_f32 v2, v2, v210\n" "v_mul_f32 v3, v3, v210\n" t1
#define C3h Q2 c0
#define D2v P0 A4Y
#define C2d A1I B8J B8L w0
#define C2J Q0 F6C
#define C2O K0 Q0 u0
#define C15 "global_load_ushort v210, v21, s[16:17]\n" "s_mov_b32 s90, 294912\n" D23
#define C13 "v_cmp_le_i32_e64 s[78:79], 35, v17\n" "v_cmp_le_i32_e64 s[80:81], 48, v17\n" "v_cmp_le_i32_e64 s[82:83], 49, v17\n"
#define C10 "v_cmp_le_i32_e64 s[68:69], 18, v17\n" "v_cmp_le_i32_e64 s[70:71], 19, v17\n" "v_cmp_le_i32_e64 s[72:73], 32, v17\n"
#define C1r "s_mov_b32 s42, 0xC0000\n" "s_mov_b32 s43, 0x20000\n" "s_mov_b32 s46, 0xC0000\n"
#define C0w "v_and_b32 v29, 31, v0\n" "v_lshl_add_u32 v29, v5, 7, v29\n" "v_lshl_add_u32 v29, v1, 5, v29\n"
#define C0p "s_and_b32 s96, s22, 31\n" "s_mul_i32 s96, s96, 0xC0000\n" E1
#define A1H A0 f0
#define A75 v0 B0
#define E8a O1 B1
#define A91 q0 D0
#define B3U C0S D86
#define B2q R2 L0
#define B2d g1 h0
#define B3N R0 G0
#define E9O Z0 c1
#define B5I d0 U0
#define B7S F6S F0 A2m
#define B7M L8 F1
#define B6a I0 K1
#define F3j X0 E2
#define B6S b0 J0
#define B80 R1 x0
#define F8P O0 h1
#define B77 F0 E9P
#define B7m i0 C0
#define C0Y m0 I7
#define B9A Q0 H0
#define D1i "v_fmac_f32 v156, v76, v211\n" "v_fmac_f32 v157, v77, v211\n" A7G c0 "v_fmac_f32 v184, v80, v211\n" "v_fmac_f32 v185, v81, v211\n" "v_cvt_pk_bf16_f32 v110, v156, v157\n" "buffer_store_dwordx4 v[52:55], v224, s[44:47], s92 offen offset:3072 nt\n" "v_cvt_pk_bf16_f32 v104, v184, v185\n" "v_cvt_pk_bf16_f32 v105, v186, v187\n" "v_fmac_f32 v188, v84, v211\n" "v_fmac_f32 v189, v85, v211\n" "v_fmac_f32 v190, v86, v211\n" "v_fmac_f32 v191, v87, v211\n" "v_fmac_f32 v200, v88, v211\n" "v_fmac_f32 v201, v89, v211\n" "v_fmac_f32 v202, v90, v211\n" "v_fmac_f32 v203, v91, v211\n" T1 "buffer_store_dwordx4 v[48:51], v224, s[44:47], s92 offen offset:0 nt\n"
#define C0y "s_add_u32 s36, s10, s96\n" "s_addc_u32 s37, s11, 0\n" "s_add_u32 s40, s12, s96\n" "s_addc_u32 s41, s13, 0\n" "s_add_u32 s44, s18, s96\n" "s_addc_u32 s45, s19, 0\n"
#define D2J E0A s0
#define D33 "v_mul_f32 v160, s1, v160\n" "v_mul_f32 v161, s1, v161\n" "v_mul_f32 v162, s1, v162\n" "v_mul_f32 v163, s1, v163\n" "v_mul_f32 v164, s1, v164\n" "v_mul_f32 v165, s1, v165\n" "v_mul_f32 v166, s1, v166\n" "v_mul_f32 v167, s1, v167\n" "v_mul_f32 v168, s1, v168\n" "v_mul_f32 v169, s1, v169\n" "v_mul_f32 v170, s1, v170\n" "v_mul_f32 v171, s1, v171\n" "v_mul_f32 v172, s1, v172\n" "v_mul_f32 v173, s1, v173\n" "v_mul_f32 v174, s1, v174\n" "v_mul_f32 v175, s1, v175\n" p0 A4u D9Q B38 X5 B3o
#define C4L E2U K2
#define D2q B0I F0G
#define D15 A98 W4 B1T
#define D1h "v_fmac_f32 v152, v72, v211\n" "v_fmac_f32 v153, v73, v211\n" "v_fmac_f32 v154, v74, v211\n" "v_fmac_f32 v155, v75, v211\n" D22 e9 "v_fmac_f32 v187, v83, v211\n" "v_cvt_pk_bf16_f32 v111, v158, v159\n" "v_cvt_pk_bf16_f32 v108, v152, v153\n" "v_cvt_pk_bf16_f32 v109, v154, v155\n" A7H c0
#define D2K B39 L1
#define C6c W3 E30
#define D2O E0 e3
#define C4g P0 J1
#define D1m M0 "buffer_store_dwordx4 v[64:67], v224, s[44:47], s92 offen offset:128 nt\n"
#define C9G Y0 E21
#define C9T W0 C0L
#define C4Q K0 u0
#define C1C B79 j2
#define C03 "v_mul_u32_u24 v11, 1056, v11\n" "v_bfe_u32 v12, v0, 3, 1\n" "v_lshl_add_u32 v11, v12, 8, v11\n" "v_and_b32 v12, 1, v0\n" "v_lshrrev_b32 v10, 4, v0\n"
#define C01 "s_mov_b32 s50, 0x4000\n" "s_mov_b32 s51, 0x20000\n" "v_mov_b32 v21, s22\n" "v_and_b32 v21, 31, v21\n" "v_lshlrev_b32 v21, 1, v21\n"
#define C0q "s_mul_i32 s89, s23, 4224\n" "s_movk_i32 s0, 384\n" "s_add_u32 s24, s4, s96\n" "s_addc_u32 s25, s5, 0\n" "s_mov_b32 s26, 0xC0000\n"
#define E8W A0 "buffer_load_dwordx4 v[232:235], v202, s[28:31], s15 offen offset:0\n" "buffer_load_dwordx4 v[236:239], v202, s[28:31], s15 offen offset:64\n" "buffer_load_dwordx4 v[240:243], v202, s[28:31], s15 offen offset:128\n" "buffer_load_dwordx4 v[244:247], v202, s[28:31], s15 offen offset:192\n" "buffer_load_dwordx4 v[248:251], v202, s[28:31], s15 offen offset:256\n" "buffer_load_dwordx4 v[252:255], v202, s[28:31], s15 offen offset:320\n" A47
#define D85 i4 A1H
#define D82 q0 C0
#define B0L B0 S0
#define B2C E8a c1
#define B2c T2 L0
#define E2d f0 a2
#define B3v n2 R0
#define F47 B1 g8
#define B3n p0 h0
#define B5e d0 T0
#define F4n E92 X0
#define F57 z1 q5 E2 I0
#define B8N O1 E9O
#define C0A G0 X2
#define C8B E1N b0
#define C7L B2o m0
#define D14 B3D W4 B4x
#define D2r b9 H0
#define D2Y N7 L1
#define D2l D1 D0
#define D2j q1 x0
#define C81 z1 F3j
#define D2M q2 F8P
#define D2I s0 N0
#define D2b F0 A7T
#define C6n U0 D9R
#define C04 "v_lshl_add_u32 v11, v12, 7, v11\n" "v_mul_u32_u24 v13, 4224, v1\n" "v_lshl_add_u32 v11, v10, 4, v11\n" "v_add_u32 v13, 93184, v13\n" "v_add_u32 v218, v13, v11\n" "v_mov_b32 v230, 0x3dd53b95\n" "v_mov_b32 v231, 0x3dd53b95\n"
#define C02 "v_lshl_add_u32 v7, v5, 4, v4\n" "v_lshl_add_u32 v7, v1, 1, v7\n" "s_mul_i32 s88, s23, 1056\n" "s_mov_b32 s30, 0xC0000\n" "s_mov_b32 s31, 0x20000\n" "v_mad_u32_u24 v222, v7, s0, v228\n" "v_bfe_u32 v11, v0, 1, 2\n"
#define C00 B78 j2
#define C0z "v_lshlrev_b32 v232, 2, v29\n" "s_mul_i32 s96, s23, 512\n" "s_add_u32 s96, s96, 160768\n" "s_and_b32 s50, s22, 31\n" "s_lshl_b32 s50, s50, 14\n" "s_add_u32 s48, s14, s50\n" "s_addc_u32 s49, s15, 0\n"
#define C0o "v_bfe_u32 v5, v0, 4, 1\n" "v_lshl_add_u32 v4, v5, 3, v4\n" "v_lshrrev_b32 v5, 5, v0\n" "v_and_b32 v3, 7, v0\n" "v_lshl_add_u32 v6, v5, 6, v4\n" "v_lshlrev_b32 v228, 4, v3\n" "v_lshl_add_u32 v6, v1, 4, v6\n"
#define z7 c3 A0
#define B0O B0 f0
#define B2O b1 L0
#define B4V R0 O2
#define B65 K8 Z0 M3
#define B6d A1 h0
#define B7P q0 X0
#define B6X T0 Z0
#define C0T B4w E2d
#define B8b B6V B1
#define B7q O1 C0
#define B7p I0 B1U
#define B7u d0 A7A A2e E39
#define D3U V9 O0 n8
#define D2a F57 B0I
#define D3v B0K "s_add_u32 s15, s12, 6144\n" E8W
#define C55 D69 D82
#define D2Z L1 D0
#define C5S m1 H0
#define C35 G0 F9
#define C3s F0 z0
#define D2u x0 Q6
#define A3l E4e A0
#define B2B q0 B0
#define B2k T0 f0
#define B4g H1 L0
#define B4O R0 B1
#define B8R X0 Y0
#define B8G W0 C0
#define B8o d0 A5c
#define D3w B0K "s_add_u32 s15, s12, 12288\n" E8W
#define D2g D0 F0
#define D2w A5X L1
#define D29 G0 W9
#define C9E x0 A2h
#define D3d A0 B3V
#define B3s A1 f0
#define B4L L0 O0
#define B4m B0 E0
#define B6I I1 R0
#define B76 U2 h0 k2
#define B8l f3 Z0 o3
#define C0R A3l G3W
#define B86 F3r q0
#define B8r b7 B1
#define F4H h0 h2
#define B7o d0 M0
#define B8c a0 C0
#define D3J A1M I0 A4Y
#define D3x B0K "s_add_u32 s15, s12, 18432\n" E8W
#define D3O B2x X0
#define D3T L1 Z0
#define C8l t2 D0
#define C3j G0 H2
#define D3D I0 E4C E3w
#define D2f U0 T0
#define A0W N4 A0
#define A3r J1 D3d
#define B4R L0 P0
#define B4z B0 O0
#define B6W K1 R0
#define B5v f0 s2
#define B8I C0 Z0
#define D3u "v_mul_u32_u24 v202, 384, v200\n" "v_lshl_add_u32 v202, v201, 4, v202\n" "s_lshl_b32 s15, s23, 4\n" "s_add_u32 s15, s15, s13\n" "v_add_u32 v203, s15, v200\n" "v_lshlrev_b32 v201, 2, v201\n" "v_sub_u32 v203, v203, v201\n" "s_add_u32 s15, s12, 0\n" E8W
#define C6b Z3 D0
#define D3L D2 F4H
#define C9w G0 k2
#define C4h I0 L1
#define D2y h0 F4n i2
#define C4E X0 t1
#define D2i q0 Q0
#define C2C K0 T0
#define A0z A0 h0
#define B4o L0 p0
#define B4U B0 P0
#define C0h A1k R0
#define F5L f0 K3
#define B8M C0 I0
#define C36 b7 Z0
#define C47 d1 D0
#define C8y X0 s2
#define D2e d0 Q0
#define A1Z v3 A0
#define B6x A2h y0 F47 B0
#define B6e i1 L0
#define B9s U0 R0
#define B84 C0 f0
#define C8P D0 Z0
#define C4C j1 F5L
#define D2t h0 D85
#define C8O X0 D2
#define C5R A0 Q1
#define B57 Q3 L0
#define B7F W0 B0
#define C3t F4B C0
#define C4B T2 R0
#define C5m E2 f0
#define C5k Z0 F4t
#define C8m i0 D0
#define C0j "s_movk_i32 s15, 0\n" A0
#define B5J Y2 C5R
#define B6K U0 L0
#define B8q R1 B0
#define D1n R0 "buffer_store_dwordx4 v[68:71], v224, s[44:47], s92 offen offset:3200 nt\n" T1 "buffer_store_dwordx4 v[80:83], v224, s[44:47], s92 offen offset:256 nt\n" E1 "buffer_store_dwordx4 v[84:87], v224, s[44:47], s92 offen offset:3328 nt\n" "s_endpgm\n"
#define D2L Z0 R2
#define C60 b0 C0
#define A7U B1e A0
#define B8j s1 L0
#define B8u Y0 B0
#define D2k C0 G0
#define A5R J4 A0
#define B89 O2 L0
#define B99 B0 V0
#define A6L A0 H0
#define B8f P2 L0
#define C8t t0 B0
#define A7W B0 A0
#define B8W t0 L0
#define A7m A0 E0
#define C71 K3 L0
#define C8Q m0 B0
#define A9h j3 A0
#define C6Q e1 L0
#define A8u b0 A0
#define D28 E4V F1
#define B08 w2 A0
#define B3d C0b E4D A0
#define D3y B0K "v_cmp_le_i32 vcc, 0, v203\n" F1 "v_cndmask_b32 v160, v209, v160, vcc\n" "v_cmp_le_i32 vcc, 1, v203\n" F1 "v_cndmask_b32 v161, v209, v161, vcc\n" "v_cmp_le_i32 vcc, 2, v203\n" F1 "v_cndmask_b32 v162, v209, v162, vcc\n" "v_cmp_le_i32 vcc, 3, v203\n" F1 "v_cndmask_b32 v163, v209, v163, vcc\n" "v_cmp_le_i32 vcc, 16, v203\n" F1 "v_cndmask_b32 v164, v209, v164, vcc\n" "v_cmp_le_i32 vcc, 17, v203\n" F1 "v_cndmask_b32 v165, v209, v165, vcc\n" "v_cmp_le_i32 vcc, 18, v203\n" F1 "v_cndmask_b32 v166, v209, v166, vcc\n" "v_cmp_le_i32 vcc, 19, v203\n" F1 "v_cndmask_b32 v167, v209, v167, vcc\n" "v_cmp_le_i32 vcc, 32, v203\n" F1 "v_cndmask_b32 v168, v209, v168, vcc\n" "v_cmp_le_i32 vcc, 33, v203\n" F1 "v_cndmask_b32 v169, v209, v169, vcc\n" "v_cmp_le_i32 vcc, 34, v203\n" F1 "v_cndmask_b32 v170, v209, v170, vcc\n" "v_cmp_le_i32 vcc, 35, v203\n" F1 "v_cndmask_b32 v171, v209, v171, vcc\n" "v_cmp_le_i32 vcc, 48, v203\n" F1 "v_cndmask_b32 v172, v209, v172, vcc\n" "v_cmp_le_i32 vcc, 49, v203\n" F1 "v_cndmask_b32 v173, v209, v173, vcc\n" "v_cmp_le_i32 vcc, 50, v203\n" F1 "v_cndmask_b32 v174, v209, v174, vcc\n" "v_cmp_le_i32 vcc, 51, v203\n" F1 "v_cndmask_b32 v175, v209, v175, vcc\n" "v_max_f32 v204, v160, v161\n" "v_max3_f32 v204, v204, v162, v163\n" "v_max3_f32 v204, v204, v164, v165\n" "v_max3_f32 v204, v204, v166, v167\n" "v_max3_f32 v204, v204, v168, v169\n" "v_max3_f32 v204, v204, v170, v171\n" "v_max3_f32 v204, v204, v172, v173\n" "v_max3_f32 v204, v204, v174, v175\n" H1P "v_permlane32_swap_b32 v204, v205\n" H1Q H1P "v_permlane16_swap_b32 v204, v205\n" H1Q
#define B4q D7F A0
#define B4r g9 A0
#define B36 U1 A0
#define B6q C9F h8 A0
#define B59 A8Q A0
#define B7X A2j A0
#define B6C U4 A0
#define B6i Z2 A0
#define B72 B5u A0
#define C0V A17 A0
#define B9t V2 A0
#define B9T s0 A0
#define B8p A0 u2
#define C2D J0 T0 A0 N0
#define C3c F4Q A0
#define C5l F5T K2 A0
#define D0m "v_mul_f32 v30, v30, v210\n" "v_mul_f32 v31, v31, v210\n" w2 A0
#define D31 H1O "v_exp_f32 v205, -v205\n" A0
#define D34 C0j "buffer_load_dwordx4 v[160:163], v207, s[16:19], s15 offen sc0 sc1\n" H1N A0 "buffer_load_dwordx4 v[164:167], v207, s[16:19], s15 offen sc0 sc1\n" H1M A0 "buffer_load_dwordx4 v[168:171], v207, s[16:19], s15 offen sc0 sc1\n" H1L A0 "buffer_load_dwordx4 v[172:175], v207, s[16:19], s15 offen sc0 sc1\n" H1K A0 "buffer_load_dwordx4 v[200:203], v207, s[16:19], s15 offen sc0 sc1\n" H1J A0 "buffer_load_dwordx4 v[208:211], v207, s[16:19], s15 offen sc0 sc1\n" H1I A0 "buffer_load_dwordx4 v[232:235], v207, s[16:19], s15 offen sc0 sc1\n" H1H A0 "buffer_load_dwordx4 v[236:239], v207, s[16:19], s15 offen sc0 sc1\n" H1G A0 "buffer_load_dwordx4 v[240:243], v207, s[16:19], s15 offen sc0 sc1\n" H1F A0 "buffer_load_dwordx4 v[244:247], v207, s[16:19], s15 offen sc0 sc1\n" H1E A0 "buffer_load_dwordx4 v[248:251], v207, s[16:19], s15 offen sc0 sc1\n" H1D A0 "buffer_load_dwordx4 v[252:255], v207, s[16:19], s15 offen sc0 sc1\n" H1C A0 "buffer_load_dwordx4 v[204:207], v207, s[16:19], s15 offen sc0 sc1\n" A47 "s_nop 7\n" "s_setpc_b64 s[100:101]\n"
#define D3t H1D A0
#define D3s H1E A0
#define D3r H1F A0
#define D3q H1G A0
#define D3p H1H A0
#define D3o H1I A0
#define D3n H1J A0
#define D3m H1C A0
#define D3l H1K A0
#define D3k H1L A0
#define D3j H1M A0
#define D3i H1N A0
#define D2p A1J A0
#define D1H "v_mfma_f32_16x16x32_bf16 v[60:63], a[76:79], v[140:143], v[60:63]\n" A0
#define D1D "v_mfma_f32_16x16x32_bf16 v[44:47], a[44:47], v[176:179], v[44:47]\n" A0
#define D0n "v_mfma_f32_16x16x32_bf16 v[28:31], a[28:31], v[176:179], v[28:31]\n" A0 "v_mul_f32 v32, v32, v210\n" "v_mul_f32 v33, v33, v210\n" B6s
#define C9m "v_mfma_f32_16x16x32_bf16 v[160:163], a[192:195], a[120:123], v[160:163]\n" A0
#define D0q B6v A0 "v_mul_f32 v34, v34, v210\n" "v_mul_f32 v35, v35, v210\n" B6w
#define D0j B6r A0
#define D0F B6o A0
#define D1d f1 A0 "v_fmac_f32 v158, v78, v211\n" "v_fmac_f32 v159, v79, v211\n" "buffer_store_dwordx4 v[32:35], v224, s[44:47], s92 offen offset:256 nt\n" "v_cvt_pk_bf16_f32 v104, v144, v145\n" "v_cvt_pk_bf16_f32 v105, v146, v147\n" A7E
#define D1a x1 A0
#define D0e q2 A0
#define D0c w1 A0

__global__ void __launch_bounds__(256, 1) attn_mem_kernel(const void* q, const void* k, const void* v, const void* mk,
                                                          const void* mv, const void* idx, const void* gb, void* out, void* ws) {
#if defined(__HIP_DEVICE_COMPILE__)
    __shared__ __attribute__((aligned(16))) char lds[163840];
    unsigned tid = threadIdx.x, bid = blockIdx.x;
    unsigned long long karg = (unsigned long long)__builtin_amdgcn_kernarg_segment_ptr();
    asm volatile("" ::"s"((unsigned)(size_t)lds));
    asm volatile(
"s_load_dwordx16 s[4:19], %1, 0\n" "s_load_dwordx2 s[20:21], %1, 64\n" "v_lshrrev_b32 v1, 6, v0\n" "s_bfe_u32 s3, %2, 0x20003\n"
"s_lshr_b32 s22, %2, 7\n" "s_lshl2_add_u32 s3, s22, s3\n" "s_lshl_b32 s3, s3, 2\n" "s_lshr_b32 s3, 0x32546710, s3\n" "s_and_b32 s3, s3, 7\n"
"s_bfe_u32 s22, %2, 0x20005\n" "s_lshl2_add_u32 s3, s3, s22\n" "s_and_b32 s22, %2, 7\n" "s_lshl3_add_u32 s22, s3, s22\n" "s_getpc_b64 s[98:99]\n"
"Lpc:\n" "v_readfirstlane_b32 s23, v1\n" "s_lshr_b32 s2, s22, 5\n" "s_mul_i32 s2, s2, 98304\n" "s_add_u32 s2, s2, Lv0-Lpc\n"
"s_add_u32 s100, s98, s2\n" "s_addc_u32 s101, s99, 0\n" "s_lshl_b32 s2, s23, 12\n" "s_add_u32 s2, s100, s2\n" "s_addc_u32 s3, s101, 0\n"
"s_add_u32 s98, s98, Lrare0_0-Lpc\n" "s_addc_u32 s99, s99, 0\n" G7 ".p2align 12\n" "Lv0:\n" "Lb0_0:\n" C0m C0n C0o C0p C0q C0r E8X A0N C0s C0t B7b C0u
"s_mov_b32 s54, Lend0-Lv0\n" C0v C0w C0x B7c C0y C0z C00 C01 C02 B5Z C03 C04 B7d C05 C06 C07 C08 C09 C1A C1B C1C p8 C1D C1E C1F C1G C1H C1I p8 C1J B7e
C1K C1L C1M C1N C1O C1P C1Q C1R C1S A5I p8 C1T C1U C1V C1W C1X C1Y C1Z C1a C1b C1c C1d C1e "s_mov_b32 s92, 15360\n" C1f C1g C1h C1i C1j C1k C1l C1m
C1n C1o C1p A5J "s_addk_i32 s92, -15360\n" C1q C1r C1s C1t C1u C1v C1w C1x A5J C1y C1z C10 C11 C12 C13 C14 C15 C16 C17 C18 C19 C2A B7f C2B C2C A0 C2D
P3 B7g E8X B7h B7i B7j B5a B7k B5b B1r B5c B1s C2E C2F C2G E1 B5d A5K D35 C2H D36 P3 C2I C2J B3f S8 A1N C2K A8t C2L R6 C2M E1 C2N D37 C2O D38 B1t N0
C2P A2p C2Q C2R C2S C2T C2U B1u C2V T1 A7V B1t A2p C2W C2X C2Y C2Z E1 A8t A3s D39 C2a C2b C2c C2d B1u B1v C2e C2f B3g C2g B3h C2h C2i A5L A3t A3u R6
A3v A3w A3x C2j A3y C2k C2l C2m K0 C2n A7W j0 A7X D4A D4B C2o B0 D4C D4D B3i A0 g0 B1r B1s C2p E1 A7V D4E D7N B0 D4F B3j U0 Q0 A3s B5e B7l W0 B3k B5f
A1N B1w B5g C2q C2r C2s C2t S8 z7 B1v C2u C2v A8u B5h C2w q0 B5i C2x J3 D4G D4H C2y m0 K6 E2R h3 E2S B3l B3g B5j D4I D7O B3h C2z C20 E2T C2y D7P Y0 J3
B1v A0O C21 B5k C22 J0 h3 B7m C23 C24 C25 C26 C27 C28 B1x B5l B5m B1y B5n C29 B1z K0 J3 B10 u0 K6 C3A h3 B11 J3 C3B B12 F1 B3m C3C C3D C3E C3F A5M B13
P3 C3G C3H B14 x0 C3I B5o B15 B5p C3J B16 B5q B5r B17 B5s B18 l0 A8v B3n v4 F3 c0 "Lp0_1:\n" E8Y t0 C3K C3L A8w l9 C3M C3N A1O I0 A8x F0 S6 G0 P0 C0
Z4 v0 C3O C3P A5N C1 B1 m0 A8 Y0 X5 B5t A7Y w0 B1x y0 H0 B1y B0L B1z r0 B10 D7Q V0 B11 x0 T0 e0 B12 A0 A0n A19 V1 C3Q B0 A0o A5O B13 p0 B14 B7n B15 k0
B16 Q0 T8 A3z B17 T0 h0 B18 A0 A8v l3 R0 t0 A8y W0 U8 B7o A7Z U0 E0 A8x P0 B1 O0 s0 B7p B3o A30 t6 v0 q8 A0p b0 C3R q0 m9 C3S m0 n9 Y0 N0 B7m C3T m1
H7 C3U Z2 I7 J7 R6 A8z A80 A2A A7a A5P C3V "s_cbranch_vccnz Lc0_0\n" "Lr0_0:\n" u6 C3W A7b D2 K0 C3X u0 B3p h0 C3Y B3q o0 C3Z B0 B3r B3s C3a A7c l0
C3b Q0 B3t g5 X0 A81 B3u C0 B0M A82 M0 B5u A0q F0 B19 A1P R1 J0 B7q o9 C3c A31 C3d K0 v2 C1 Z0 C3e B0 a7 B7r C3f e4 u3 B2A C3g f4 B7s j0 C3h B7t C3i
y0 C3j B0 p9 r3 C3k r0 C3l A1Q t0 A2q A83 B7u A2B b0 g0 B2B A7d A2C A84 i0 k0 B0N A32 C3m Q0 B5v T0 X0 C3n A33 E2 N0 C3o C0 A2r w1 p0 C3p q9 b1 J0 C3q
L0 B7v P0 C3r A2s W1 A34 B7w C3s B5w G0 B7x A1R C3t B5x C3u B5y B1 b7 A2D C3v o0 g2 Z0 C3w O9 C3x C3y A2t B7y C3z P9 B7z H0 r2 A0r C30 S0 F2 B0 A2u S5
C31 V0 C3 M0 A2v A0s A35 A0t w4 d0 l0 U0 C32 a0 v4 s0 w2 x4 g4 A0u b0 v6 B8 A0v V8 B70 A36 i0 "s_cbranch_vccnz Lc0_1\n" "Lr0_1:\n" O2 R0 A2E D3 J0 a2
C0 C33 h0 X1 C34 K0 G2 C1 A2w Y1 B0O B71 j0 d1 A5Q e1 A37 G1 B5z p0 z0 F3 I1 A82 B72 F0 A85 C35 y0 A38 O1 Q9 c1 A0P C36 A5R r9 M4 x2 r0 y2 T0 s9 s3 l2
O0 C37 n3 C38 G3 h5 H4 H3 g0 V1 B0 w6 x6 b2 H0 n1 A5S o1 k0 B3v A2x A2y A2F a4 A2G A2H b4 s0 A1S c2 A1T B50 b0 A1U T6 B3w A0Q L0 B3x B3y B3z A5T B30
l1 A5U k1 I0 x1 C39 F0 A1V B51 Q0 G0 f1 T0 A7e W2 l3 G1 A5V "Lp0_2:\n" E8Y K1 B3u t6 B73 C0 A1R A0q C8 A39 R1 J0 B7q t9 B74 b7 B0P v3 K0 E3 u0 Z0 Z3
u9 j4 W3 O0 d3 A7f B31 A5W B32 o0 m1 B0 A1W z2 B75 A7g y1 l0 A4A A4B t0 C4A c7 B0 A2z B0Q r8 A86 N0 Z2 B0R g4 s8 b0 A4C A0R A4D V8 J0 Y0 A87 i0
"s_cbranch_vccnz Lc0_2\n" "Lr0_2:\n" C0 C4B t8 D2 p0 h2 A4E i2 K0 X2 A4F B76 A20 C4C C4D C4E A4G P2 B52 B33 B0S B0M B77 A0 T5 U1 d7 C8 A4H y6 v9 B53
A7h E4 N4 I4 v2 w9 R3 E0 i3 y4 U5 L2 H0 S3 z4 A5 T3 S0 U3 D8 B5 Q2 V0 A3 A2I H2 "s_mov_b32 s92, 0\n" e0 M2 A0N A0w U6 m2 L0 B0T t0 A0S W0 C4F d0 A88
U0 a0 j2 R9 s0 a3 v0 A5X b0 C4G q0 j2 A21 A89 Y0 A1X A9A X0 i1 e7 V6 s2 B78 z1 j2 A4I E2 p0 Z1 A4J w1 D4J a1 D4K A22 b1 h0 R2 B54 C4H h1 j2 A23 W1 B52
B55 B79 z0 j2 k4 I1 I0 C4I C4J U1 d7 C35 W8 B2C x9 C36 j3 j2 A0T O4 g2 O0 I3 A0U S4 f3 E0 o3 y9 h4 m3 A5I X3 C1 A7i B4 g3 C4K H0 r2 u8 S9 e3 j0 F2 y0
A7j t2 B0L A24 A0x S5 t0 r0 W0 T0 A0y i5 A2G g0 s0 X8 w4 v0 Y8 b0 j5 q0 R4 m0 A25 f7 B0U A9B w2 A0z B0V f0 F3 P0 A2J B7p "s_cbranch_vccnz Lc0_3\n"
"Lr0_3:\n" M0 O2 Z4 D3 a2 A5Y N2 X1 A7k I2 G2 A1Y Y1 A2 A0V B3 d1 A5Z e1 G1 A9C B8A z0 B2D F4B B0W y6 B0X C4L A0 A9D B8B I4 x2 K7 z9 y2 E0 l2 M0 k5 H4
d2 H0 e2 k0 G3 Q0 l5 W6 H3 T0 S0 V1 A0A a4 b2 N0 n1 C0 B0Y o1 V0 n2 A5a t0 J0 A5b B56 B2E A5c B2F b4 a0 K0 s0 P3 D7R D7S c2 v0 D7T D7U A7l B8C A2C A9E
i0 f0 B57 A4K b3 u0 B3z A2K v8 B30 l1 o0 k1 B0 B34 x1 I0 s1 w8 E8Z A1V F0 o2 l0 f1 Q0 A9F W2 B0 B0Z B35 A26 K1 B3u C0 A27 A4L I1 l3 H1 F4C D7V D7W S2
J0 B36 D7X D7Y u2 t6 R1 g7 A4M E8a E2U B8D A1Z E0 E3 A30 Z3 H0 W3 T1 S0 d3 V0 T4 e0 F4 A0 "Lp0_3:\n" E8Y A0p t3
"v_cndmask_b32_e64 v168, v209, 0, s[56:57]\n" "v_cndmask_b32_e64 v169, v209, 0, s[58:59]\n" m1 "v_cndmask_b32_e64 v170, v209, 0, s[60:61]\n"
"v_cndmask_b32_e64 v171, v209, 0, s[62:63]\n" z2 E1 p2 y1 V3 A5d A0 B5a A0 B5b A0 B5c A0 A5K F4D p1 x0 B37 K0 C4M B0 B2G B58 E8 F8 G8 H8 B0a A4N j0
C4N B0 A4O B8E t0 E2V C4O W0 B8E d0 E2V C4P U0 B8E a0 B2H s0 r0 Z2 x4 B59 A0R V8 B2I Y0 "s_cbranch_vccnz Lc0_4\n" "Lr0_4:\n" A0B T2 E1 f0 D2 g0 h2 A9G
m5 i2 X0 X2 A9H n5 U2 P0 A9I A9J B7p T5 A7W k0 A0C A0 S8 A0 R6 A0 C4Q A0 A5M A0 l0 v4 p1 D7Z D7a S8 D7b D7c c0 U4 w0 P1 J0 N1 x8 A00 A5e D7d D0 A0 B6A
A0 D7e D7f J3 D1 A5f K6 h3 J3 J1 w0 A9K B3m C4R C4S C4T C4U J2 D0 A9L D7g V2 A5g k2 D0 B2J D7h j1 D7i A0 D7j D7k J3 K3 B2K K6 h3 J3 f2 A5h B3m C4V t1
L7 D4L C4W P2 c0 C4X C4Y u1 D4M Y2 a0 A5i U0 D0 B0b C4Z l9 C4a D7l W0 B0c D7m c3 A5j p3 D0 B0d D7n Y0 X6 m0 B0e Q1 q0 A5k A4P A9M P4 i4 A7m A0W A9N q3
v2 A5l R3 M1 C4b i3 c0 p0 L2 h0 Y5 B38 O0 S3 A0X A01 C4c C4d B0f C4e C4f F4E f0 w3 X0 S1 H0 q1 S0 N1 V0 P1 e0 U4 A5m A1a A0D x3 A4Q B2L C4g A4R A7n
C4h C4i A9O k3 A1b Q2 K0 a3 C1 A9P Y2 T3 Q9 U3 w0 A2L Q1 A3 Z0 H2 O0 M2 E0 m2 Y6 A0E A2M t0 y3 W0 w0 I8 M1 d0 B8F w0 C4j B2M z6 T1 A5n s0 J8 y8 A0Y j0
v0 y0 z8 A2N b0 A02 q0 A2O A4S m0 A4T D7o Y0 r0 i0 L0 A1c D7p i1 R0 D7q D7r s2 A1d A03 z1 h0 E2 R0 A04 k4 Z1 M0 f0 w1 A5o U4 X0 P1 A9Q V6 N1 P0 q1 A9R
S1 I0 w3 T0 F0 z3 A0 B39 S4 L1 G0 D1 A2P A28 J1 g0 J2 B0 B0g V2 B2N a1 A4U k3 B2O B1 R2 D4N B4 q2 k0 h1 Q0 D4O Y2 W1 A05 u1 T0 A7o E0A B4A O0 d0 P3
C4k G7 "Lb0_1:\n" M7 B8G E0 z7 A9S A06 p3 J0 B8H A9T C4l m0 C4m C4n q0 C4o Q1 b0 A9U P4 i4 w0 p0 C4p A0 Z5 q3 C4q K0 j3 u0 a5 M1 g2 h0 I3 B2P D4P f3
o0 o3 B2Q m3 f0 w3 X0 S1 H0 q1 S0 N1 D4Q B6B P1 V0 B6C e0 r2 E1 A0 B6D C4r p1 m4 Y5 C4s P0 x3 A2Q B0 n4 N7 J1 I0 D1 F0 L1 G0 G4 T1 B2R C4t C4 A0n w2
w0 A7 C4u D4 A0o X3 B1 g3 A0Z e3 Z0 F2 O0 t2 E0 A29 A24 T8 T9 U9 A0F C5 W0 c0 C4v l9 M1 D7s U0 H0 Z6 w0 C4j T1 B2S B4B C4w Z8 D7t A4V l0 C4x O7 A9 D7u
v0 p0 b0 A4W D7v q0 A5p m0 D0 A3z B0h A0Q q8 A9V C4y A5q C4z D0 m9 L3 C40 P0 C41 n9 H7 C42 "s_cbranch_vccnz Lc0_5\n" "Lr0_5:\n" o4 C43 M0 J7 a8 O2 A5r
"Lp0_4:\n" E2W D3 D0 A7b b8 a2 I0 N2 C3V B4C E8b A5s a6 A9W A3A A7a E8c F0 D5 L0 A80 s3 X1 A5t I2 D0 A7p C44 G2 G0 Y1 A7q n3 E8d A5u V9 D0 A7r C45 E8e
f0 E5 A7s C46 A2 M0 X0 B3 A5v C47 E2X p4 e1 C48 Y5 E8f E2Y N7 b6 A5w E8g Q0 D4R k3 F5 g5 B1 D7w D4S Y2 D7x N0 D7y B8I D7z O0 D70 E0 D71 A0 A5x E8h D72
J0 A9X C0 A5y E8i A9Y V0 A9Z e0 A9a A7t E8j A9b K0 A5z F1 A7u A50 H0 A51 p0 A52 h0 A53 f0 C49 w0 S0 J4 R4 x2 E8k A0 D73 V1 D74 D75 y2 E8l E8m l2 D76
D77 E8n P0 W9 I0 E8o F0 G5 G0 c2 E2Z E2a d2 x0 e2 A1P G3 D78 D79 H3 o9 c8 D8A E8p A31 c6 E8q e4 H5 f4 b2 L0 a7 n1 A1e o1 A7v n2 p9 E8r A3B d6 A3C E8s
B7 A1f h7 B9 D8B C9 H1 A1 A1g I1 j0 z0 A7w B8J K1 C5A D8C r0 c1 O1 T0 R1 u2 Q4 D8D Z0 C5B A4X E2b C5C g0 C5D B0 C5E B6E C5F V0 C5G B2T C5H k0 Q3 Q0
A2R B2U b3 T0 e0 B2 B4D E8t C2 N0 E8u C0 O0 A4Y E0 E8v E2c C5I A1h c0 S0 l1 A54 C5J k1 J0 x1 C0 A55 D8E s1 H0 E8w A4Z E8x A0G K0 E8y u0 A4a C5K K8 p0
M3 C5L D8F o2 D8G D8H C5M o0 C5N A7x f1 h0 W2 D8I D8J E8z f0 e6 B4E E80 E81 X0 A0a B4F E82 D8K A56 A3D D8L A57 A3E D8M R0 A58 A4b D8N A59 A4c D8O D8P
x4 C5O P3 m4 C5P A0 I0 D8Q n4 A0 X9 A9c F0 A9d G0 A9e P0 A9f E83 E84 A9g A0 Y9 A1i V2 A1j A2S J2 b5 J1 O0 A2T D9 D1 E0 A7y L1 B1 B2V B2W G4 E1 E85 E86
L6 M6 C5Q D4 C5R B0i v3 y3 A4d E3 M1 A7z N6 C5S C5T C5U C5V C4 C5W Z3 S0 W3 V0 E87 e0 f6 B0j E88 P7 C5X C5Y I5 A6A Z2 D0 C5Z d3 x0 T4 E1 A6B C5a A6C
C5b D0 A3F A4e A2D L8 A6D E89 D0 A2t Z9 E9A B6F J5 A6E z2 L7 A70 E9B p2 A71 L3 y1 A2u V3 A6F E9C D0 A2v C5c g6 C5d C5e E9D C32 K5 L0 C5f t0
"s_cbranch_vccnz Lc0_6\n" "Lr0_6:\n" A0s W0 A6G B0k A0t U0 i7 A6H v0 D0 A0u b0 v6 q0 A0z m0 B6G Y0 A6I i0 F4F B2X T2 f0 D2 X0 h2 l0 i2 Q0 p0 L5 B0 B4G
h6 M0 P0 X2 B6H U2 N0 k2 C0 A4 j1 I0 M5 B4H i6 J0 K3 A2w f2 F0 t1 G0 P2 T5 Q7 B8K j6 C5g C5h G1 A5Q K1 C5i C5j z0 B8L B6I H1 A1R B4I D4T C5k C5l E4 U5
N4 E1 B4J v2 H0 R3 S0 i3 x0 N5 A6J B5 A2U V0 L2 A6K B2Y S3 e0 Q2 B2Z U6 T3 r9 A4f U3 s9 A4g O5 d8 k6 h5 A3 w6 H2 C0 x6 A0 "Lp0_5:\n" E2W a3 A3G M2 A3H
m2 B0l a9 K0 l6 L0 B0m A2F t0 B7 W0 A2H A1S d0 j0 U0 A7w A1T i7 B50 b0 r0 q0 T0 A1U A4h P5 B2T A9A h0 i1 B2a s2 g0 z1 B0 A4i k4 C5m A4j A5U A0b k0 Z1
Q0 A5T w1 T0 X0 a1 V6 b1 N0 R7 C0 A72 A0H P0 R2 B0n q2 J0 h1 B8M W1 F0 A3I A5V b9 L0 B0Z A7e K1 K0 z0 u0 E9 B73 S2 o0 U1 x4 u2 R1 Q4 B8N O0 K2 A7m A9h
O4 h4 g2 A6L C5n Z5 C5o S0 I3 a5 S5 f3 x0 c9 B0 A39 A07 M0 V0 o3 t9 m3 B0o Q0 B0P X3 B0 e0 g3 A0c w4 Q5 N0 m6 C0 A0d u9 e3 F2 C5p w2 A1 C5q S7 m4 B6J
C5r J0 t2 C0 n4 A73 C3 A74 L3 A1k A0e n6 K0 t0 "s_cbranch_vccnz Lc0_7\n" "Lr0_7:\n" A1l W0 B7 B0p d0 A2z B6K r8 a0 j0 s0 C5s M0 C4 C5t D4 v0 s8 A7l
B8O A2C x8 A87 i0 A6M O2 D0 t8 o4 D3 E2d A4E N2 A6N X1 D0 A4F I2 A1 X0 C5u p4 C5v A6O B8P A20 Y1 P0 A2 C5w A6P d1 B4K A4G e1 A34 B33 A6Q z0 A9i I1 D0
G0 H1 D8R Z4 S2 A5m U1 A2V X9 u2 A6R R1 D0 A2W B0q B2C B8Q K2 A6S J4 D0 w9 Y9 x2 Z0 y2 A5 M4 l2 B4L d2 y4 z4 e2 A6T G3 y0 E0 H3 E2e D8 C5x H0 n1 A2I
V1 r0 o1 T0 A6U n2 c5 R0 A0S a4 A9j A88 A3J R9 b4 s0 C3Q B4M B0r v0 A5X A2S A7l A21 m0 k0 Y0 A1X A0B Q3 R0 e7 q4 A9k B2 A4I C2 X0 l1 A4J k1 P0 x1 A22
s1 I0 M3 C5y F0 f1 A23 A08 B0Z B55 B2b A2T D9 z0 Y3 A7y B2V I1 B2W L6 M6 H1 B0i S2 F1 A4d A3K A3L A3M N6 C5z C50 F9 W8 O1 Q0 B1 c1 T0 x9 C36 A1Z A0T
j4 E3 N0 Z3 A0f A0U u8 W3 O0 d3 B4N B31 S9 B32 J0 m1 A9l A7j z2 B75 B0s y1 K0 A4A i5 X8 t0 u0 c7 Y8 B0Q j5 A86 o0 Z2 P3 E2f E2g A75 C51 C52 b0 A25 A0R
f7 V8 l0 Y0 Q0 A9B i0 "s_cbranch_vccnz Lc0_8\n" "Lr0_8:\n" C53 B2c B0V D2 B0 E2h A5Y i2 N0 X2 C0 A7k U2 X0 k2 A1Y j1 J0 K3 C0 A0V f2 p0 t1 A5Z P2 B2d
G1 A9C K1 K0 z0 C1 E2i E2j I1 B0 A82 A4 c4 E2k C54 U1 j0 u2 I0 y6 E0B y0 B0W K2 c0 Z0 A0 "Lp0_6:\n" E2W N4 B0 A2X E4 v2 r0 R3 T0 B6L i3 O0 A6V z9 u3
L2 E0 S3 E2l U5 T3 g0 U3 B0 E2m E2n Q2 F0 A3 k5 l5 H2 k0 M2 Q0 W6 A0A C3l G0 A6W L0 B0t r3 t0 A2q B0u B7u B6M C55 B2e m0 B8R B8C E9E i0 J0 A6X C7 B0N
A9E D7 o6 i1 p0 p6 T7 s2 A4K U7 A6Y h0 z1 A3N C5m C56 C57 Z1 "s_waitcnt vmcnt(17) lgkmcnt(3)\n" A2K E2o C58 C59 w1 P0 A9m a1 E2p E2q B2f b1 I0 A6Z A6a
A3O R2 F0 q2 E2r A76 h1 G0 W1 A77 v8 G1 E0C K1 w8 B35 z0 A1 B5w B8S B7x c4 B2g E2s A6b C6A C6B U1 E2t A26 u2 Z0 R1 S7 A27 A4L B0v C0 A4k A4M B6N O0
A6c A0 C6C j3 K0 g2 u0 A6d A78 C6D C6E I3 E0 A2Y f3 A79 h4 A3P o3 B4O A1m m3 A3Q C6F C6G X3 O4 C6H C6I V5 g3 E2u E2v B6O r2 o0 B8T e3 B0 E8 F8 o5 A6e
F2 H0 A1n t2 A3R A0I C3 l0 A1o A6f Q0 E2w E2x A8A c7 B0 A3S A2G B0w s0 G8 H8 w2 N0 g4 B0a C6J C6K b0 A73 C6L C6M M8 q0 M0 A4N L3 N8 G9 m0 A4O H9 A0g
Y0 "s_cbranch_vccnz Lc0_9\n" "Lr0_9:\n" C6N i0 A3T A6g C6O O2 c0 C4O D3 C4P a2 C6P N2 C0 B2h B2i X1 A6h I2 B6P G2 J0 Y1 L0 B2j A2 A3U B3 C0 B8U d1 K0
C6Q B8V C6R C6S G1 C1 p0 C6T V7 A9n A6i W7 e8 z0 A4l f8 X7 I1 M0 E9F E9G Y7 H1 j0 S2 B0x A3V C6U C6V A1p C6W C6X C6Y B6Q A0h R1 Q9 d9 O1 E9H E9I A0g
B6R K2 T0 A3W J4 A05 x2 E2y E2z y2 O0 l2 A3x o4 d2 E0 e2 A3w p4 G3 A7Y H3 B4P B4Q V1 B4R b2 A9o n1 g0 o1 e9 I8 J8 n2 A1 I0 B8W D4U s3 A2Z F0 A0i D4V
A2N B6K G0 I9 B8X A2O n3 B8Y C6Z b0 D4W D4X T6 A9p Y0 C6a i0 B0 B0y c2 L0 h0 Q3 B8Z b3 k0 B2 Q0 D4Y D4Z C2 B2k l1 L0 E0D E0E k1 N0 x1 C0 A5o s1 X0 A1V
B0z o2 J0 f1 P7 A1q W2 B8a A7o B8A B0S B00 I1 A6j H1 D0 A8B c4 F0 U1 B01 l3 F9 A3X O1 A6k A6l A0Z B8b A1Z B2l E3 A6m C6b A9 K4 C6c A2a A9q A6n F4 B4K
X5 t3 Z8 B4S c0 G0 p2 A4W Y3 g7 m1 A6o y1 D0 A8C V3 A3Y A4m W0 A6p B0k B4T C6d a8 A0p v0 A6q A4P A80 b8 A0 "Lp0_7:\n" E2W T6 A7a A3A Y0 A6r i0 C6e Z2
A0z A7b f0 A0X C4K X0 m5 A2A n5 A0j F0 "s_cbranch_vccnz Lc0_10\n" "Lr0_10:\n" c0 T2 K0 D2 u0 G0 h2 B3p T5 X2 B3q o0 k2 B0 B3r C6f C6g K3 A7c l0 t1 B3t
C6h C6i A81 C6j C6k z0 L0 C6l C6m B2D "s_waitcnt vmcnt(25)\n" C8 A1P y6 o9 C4L A0 A09 E4 N4 I4 v2 A1A R3 E0 i3 M0 e4 U5 L2 H0 S3 f4 a7 T3 S0 U3 A1e B5
Q2 V0 A3 A4n H2 e0 M2 Q0 A3B U6 m2 D4a A3C t0 B9 A2Z C9 d0 N0 U0 A2B B8c s0 a3 v0 B4D B6S q0 A0f B2U A2R m0 B8R B6T i0 K0 i1 R0 C1 A4X s2 A9r p0 z1 P3
A4Z D4b E2 j0 Z1 D4c A54 w1 y0 P0 a1 B0 A55 A4a b1 I0 R2 B7v r0 h1 T0 q9 W1 A4o G1 D4d D4e K1 g0 z0 B0 D4f D4g I1 h0 H1 D4h D4i c4 f0 B36 B6U F9 k0 O1
Q0 A2s c1 T0 B1 B6V A6B j3 N0 g2 A3F I3 B8I f3 A6s O4 o3 O0 B7y X3 A6t B7z M0 G0 V4 e3 A3Z S4 F2 J0 r2 C0 A3a B4 A6u H0 t2 B6F C3 K0 t0 u0 A0r A2Z A6v
A2u S5 C6n A2v w4 A6w o0 w2 d7 D4j D4k A75 f0 b0 D4l B8 D4m V8 l0 Y0 Q0 D4n D4o i0 "s_cbranch_vccnz Lc0_11\n" "Lr0_11:\n" C7 L0 O2 B0 D4p D4q D3 A8D
a2 C32 N2 N0 D7 o6 X1 C0 A0u v6 p6 T7 I2 X0 U7 A6V A3N G2 J0 Y1 M0 B6G A2 R0 p0 B3 C0 B2X d1 K0 e1 R0 B4G C6o C6p G1 C1 h0 G7 "Lb0_2:\n" M7 C6q C6r
B6W B6H A9m z0 B4R B2f I1 M0 B4H A6Z H1 B0 B72 A3O A6W j0 A85 A2w F9 y0 B8K O1 Q9 c1 A5Q K2 r0 A6x L0 A6J o4 J4 B6X x2 A6K p4 y2 O0 l2 B2Y M4 d2 B2m
e2 B2Z H4 G3 g0 H3 B0 B2n C6s C6t V1 F0 C6u C6v A6y d8 s3 b2 M0 G0 A2Y n1 h5 n3 A3P o1 k0 C6w C6x n2 Q0 w6 x6 C6y C6z t0 A6z A9m W0 A5S B2f A0i S0 A1m
U0 A3Q A6Z a0 B2S A3R B8Y N0 A1n C60 D4r D4s A0I B2o A1o m0 A3S Y0 A2x i0 J0 C61 C62 c2 A4p A9s C63 C64 A6b R0 f0 A0h Q3 B02 d9 b3 C65 A0g A6c A3T A9t
B8d l1 R0 p0 k1 B3w x1 C66 s1 B3x A1V P0 o2 A5U f1 I0 W2 L0 A5T G1 F0 A9n A72 B8e A1B B0n H1 l3 c4 A5V A85 t6 F9 C0 A7e O1 K0 A0 "Lp0_8:\n" E20 c1 u0
A9u C67 C68 B8b C69 C7A v3 A4q A1n E3 o0 A0I Z3 B0 A3u A3t A1o W3 Z0 C7B C7C d3 A3V C7D C7E T4 O0 A0h F4 E0 d9 t3 w0 A30 A0g m1 A3W z2 l0 p2 Q0 D4t
D4u y1 A2b V3 A7f A2y u9 R5 D4v e0 I9 A5W A86 N0 Z2 e9 A1W v0 C7F C7G b0 M0 A74 A0R C7H m0 A0e A4r A4B C7I C0 "s_cbranch_vccnz Lc0_12\n" "Lr0_12:\n"
T2 c0 h0 D2 B2p h2 J0 i2 C0 D4w D4x X2 A6h U2 C7J k2 K0 j1 L0 B6Y K3 A3U f2 C1 A4F t1 B4U B8f A4E G1 j0 A9n C7K z0 O7 I0 A1B A20 H1 A60 S2 D0 D4y U1
D8R F0 F9 A4G O1 A61 A6l A2V m5 K2 B4O A0W A2W n5 v2 A62 R3 D0 v9 i3 Z0 L2 A7h E4 S3 A63 T3 B4K X5 U3 M0 w9 B8g C3j y4 u3 Q2 A64 M2 D0 z4 r3 m2 A3Y c0
A5 D8 W0 A65 B0k A2I U0 S0 I9 B2S A0w B03 A66 A4P B8h C7L A88 Y0 A67 i0 y0 C7M a3 A1 p0 i1 E2e A21 s2 r0 z1 T0 A1X C5m Z1 L0 e7 A9v w1 g0 a1 B4M A9w
A9x b1 X0 B2q A9y D4z q2 k0 h1 D40 B04 W1 B8a A68 B05 K1 B0S L0 D41 I1 F0 H1 A23 c4 G0 U1 E0F V4 F9 W8 B2C x9 K2 b5 j3 M0 A0T O4 g2 O0 I3 A0U S4 f3 E0
o3 y9 h4 m3 S0 X3 A7i B4 g3 V0 r2 u8 S9 e3 e0 F2 B6Z w4 t2 H0 C3 M0 A2c S5 t0 A0x C7N W0 A0y C7O d0 i5 A3D U0 X8 A3E a0 Y8 A4b s0 Q0 j5 C7P w2 T0 A69
C7Q g4 f0 b0 A25 B8 N0 m0 A0f f7 A2d E21 A9B "s_cbranch_vccnz Lc0_13\n" "Lr0_13:\n" B4V J0 D3 A9l B0V a2 p0 N2 A7k X1 K0 I2 u0 A5Y G2 h0 Y1 A0V A2 o0
B3 P3 D42 D43 d1 B4U C6Q D44 D45 G1 B6a D46 D47 z0 l0 I1 Q0 D48 D49 H1 B0 D5A A0 A1Y U1 N0 u2 A9C R1 A5L O1 A5Z C3c A1r o4 J4 A1 Z0 x2 A1s p4 y2 O0 l2
O6 A2X D5B d2 L0 E0 e2 D5C D5D G3 A7Y H3 D5E D5F V1 J0 b2 L0 D5G D5H B6b n1 C0 B6c o1 G0 n2 D5I s3 t0 A2q g1 k5 n3 B7o A7A c0 A2e w0 A1C A19 A6Y l5 W6
c2 C7R C1 B8i b0 A02 q0 B0u m0 j0 Y0 y0 B6M i0 B6d Q3 R0 B2e b3 B0O A6a B8C B2 r0 C2 L0 A9E l1 A1 X0 k1 T0 A4K x1 g0 B8j A2K A76 A1V P0 o2 B0 A77 v8
f1 k0 W2 Q0 "Lp0_9:\n" E20 w8 B35 A2Y G1 g1 I0 A3P K1 T0 B8S A1m z0 A9i I1 A3N H1 N0 S2 C0 B2g A26 A6u A0 G0 U1 A27 A4L u2 J0 R1 S7 A4M q4 B0v C0 A4k
d5 B8b C7S C7T v3 C6C C7U C7V E3 K0 A9m Z3 u0 A6d A78 B2f W3 Z0 A6Z A6w A3O d3 A1t T4 A79 j4 A6f E0G q6 K4 t3 E8 Y3 m1 o0 z2 B0 F8 G8 p2 H0 y1 H8 A8E
V3 l0 A8A A8F A4N W0 E9J A6v B0 A4O R5 C7W C7X U0 V0 C7Y C7Z a0 e0 A2Y s0 A1u W5 A3P Z2 N0 A1m A6g C0 A3Q g4 j7 C7a b0 A1v L3 C7b C7c q0 J0 C7d V5 m0
"s_cbranch_vccnz Lc0_14\n" "Lr0_14:\n" A1w B6O B8H h0 B8T i0 w0 B2h A1x o5 C7e T2 K0 C7f V7 D2 C1 A3R W7 e8 h2 f9 f8 X7 i2 D5J D5K Y7 A6x j0 X2 A3S U2
p0 k2 A4p B2i A9K j1 R0 f0 C7g C7h A9z A9L B2J C7i C7j f2 B2K m5 A0h t1 C7k d9 P2 B8U A0g A6y R0 B8a A3T K1 C7l z0 c0 B8V I1 I0 H1 B0x A6i S2 A5m U1
y0 E9F E9G u2 Q9 R1 B6Q O1 r0 c1 T0 E9H E9I B6N Z0 A0W A3x n5 v2 g0 R3 B2r A3w E0H B4L L2 B4P B4Q C7m C7n B2A C7o C7p T3 A2L U5 A1n U3 k0 A0I C3h A2M
u3 A1o A3 Q0 F0 C7q C7r H2 T0 A3V C7s C7t M2 N0 A0h m2 I8 J8 d9 t0 G0 A0g W0 c0 A3W d0 B8F D5L D5M a0 A3b r3 B03 e9 D4U D4V E0I A4c q0 c0 B8k m0 D4W
D4X Y0 C7u i0 x8 A9p a3 p0 B6e B0y s2 A5e z1 D0 B8Z E2 L0 h0 Z1 D4Y D4Z w1 A5f a1 D0 E0D E0E b1 B06 B2q A5o q2 A5g h1 D0 B0z W1 B52 A1q B8A B0S A7o I1
A4o H1 B00 S2 A5h A3L A8B A1y L7 R1 A0n k4 O1 A5i A6l A0o V6 B8b A9h A0Z g2 A5j I3 D0 B2l B8l A2a C7v A5k X3 B07 g3 R0 h4 S4 e3 Z8 A9 F2 A5l r2 B8m t2
G0 C3 q8 B4 t0 C7w A5b A4m d0 F4G B8F B4T a0 J0 A3b A9l B4W A8z A5N B2s P5 A5P A0B B08 A1z A8v F3 P0 A2J B7p "s_cbranch_vccnz Lc0_15\n" "Lr0_15:\n" M0
O2 Z4 D3 B4J a2 B4X N2 B8n X1 B4Y I2 G2 A3c Y1 C7x d1 B6f e1 A81 g1 z0 B2D A90 C8 A1P y6 u0 o9 C4L A0 A09 B8B I4 x2 o0 y2 B0 A1A a7 l2 B2m d2 e4 H4 e2
l0 G3 Q0 f4 A1e H3 D4a H0 V1 A7v A0 "Lp0_10:\n" E20 p9 b2 N0 n1 A1Q o1 S0 n2 B9 a4 D5N B4Z R0 C9 A1f B8o A1g b4 a0 J0 s0 P3 C4k c2 A0f v0 B4D A5N E9K
B2U A2R m0 A9r p0 Y0 B6T B5t Q3 E9L B0 A4X b3 h0 B2 A33 C2 B3s l1 A2r k1 r0 x1 T0 q9 s1 X0 M3 C7y g0 f1 B0 A2s W2 B8a L0 B7w k0 z0 Q0 l3 I1 T0 I0 C4I
N0 A1p C0 B8p B5x A7B B5y J0 K2 C0 A2D v3 b5 E3 O0 Z3 O9 j4 W3 B2m d3 A3Z q6 T4 K0 F4 u0 A3a K4 t3 G0 m1 P9 Y3 z2 o0 p2 B0 A0r y1 H0 V3 A7C A2y R0 A0s
B0Q A0t A86 l0 Z2 Q0 C32 A75 A0 b0 A0u A0R v6 V8 B70 B6G i0 "s_cbranch_vccnz Lc0_16\n" "Lr0_16:\n" B2c B2X D2 J0 C7z C0 Z5 C70 F4H a5 B2t i2 K0 X2 C1
B4G U2 B0O k2 B2u F4I j1 j0 C71 C72 C4E F4J F4K P2 p0 G1 B5z P0 z0 F4L m5 I1 I0 H1 n5 u3 c4 F0 C73 "s_waitcnt vmcnt(21) lgkmcnt(3)\n" A0 m4 C74 Q4 A85
x0 n4 C6Y Z0 B8q D5O E4 O1 r0 c1 T0 D5P E22 B8D A0W E23 r9 v2 L0 E0 C75 R0 C4 U5 C76 g0 R3 B0 D4 s9 i3 G0 L2 d8 r3 S3 k0 T3 Q0 h5 F4M U3 A6z Q2 F4N
A3G A3 N0 H2 C0 A3H F4O M2 C77 L0 F4P B5 C78 J0 C79 O7 A9s B4a B02 d0 A5p U0 D0 A10 U6 s0 B50 b0 A5q A91 A1U E9M A2C E9N E0J i0 A5r a3 R0 D0 E0K i1
B5v X0 z1 A5T E2 A5s Z1 A9W A5U w1 p0 a1 B0n b1 A5t R2 D0 A72 q2 P0 h1 A7e W1 g1 A34 A5V K1 A5u z0 D0 B5w B77 A5v A3L E9 A1y D0 R1 A9u O1 A5w c1 E0L
A4q B8r A9h A3u A3t g2 K0 I3 u0 A92 B8l A0c C7v O0 X3 E0 g3 A0d B09 o0 F2 B0 A1W r2 H0 t2 A7g A24 c5 A1l S5 A9j C8A A3J r8 B4b l0 w2 F1 A68 g4 D4w b0
D4x B8 A2P L3 P5 B0g i0 "s_cbranch_vccnz Lc0_17\n" "Lr0_17:\n" L0 O2 R0 A4U A3D D3 h0 a2 A4E N2 f0 X1 A4F I2 X0 G2 A20 Y1 P0 A2 C5w I0 d1 A4G e1 A4o
G1 B33 B6g F3 I1 Z4 H1 Q0 A3E A4b S2 B2r A0 A4c B36 A2V B1A A2W O1 B6h E9O x0 K2 w0 B0q B8B N0 x2 C0 B8Q y2 O0 l2 w9 y4 d2 E0 e2 z4 H4 G3 J0 H3 A0f A5
D8 V1 H0 b2 A2I "Lp0_11:\n" E20 n1 K0 o1 B2T n2 A1 c5 C1 A0w a4 W0 A9r A0k B8h A3J A4s b4 s0 j0 g7 c2 B8s C8B A5X B1B q0 A0X m0 y0 B4c B8t B8u B2v L0
B8v e7 Q3 r0 b3 B2k B2 A4I C2 X0 l1 A4J k1 g0 x1 B4M A22 s1 P0 C8C Z5 D4z C8D k0 A1V a5 D40 o2 Q0 I0 f1 T0 A23 W2 N0 G1 A0f B55 C3s l3 I1 J0 E9P A0 t6
U1 B4I D4T C5k c1 x0 K2 E1 B8w j4 C8E A9l E0 C8F A0q B2w v3 K0 E3 u0 B8x q6 Z3 H0 W3 x9 d3 o0 T4 B0 B4d B8y F4 S0 B0r t3 y9 R5 A93 m1 R0 V0 C8G C8H D4
A7i A3d L6 z2 l0 M6 p2 Q0 u8 S9 B0i y1 B0 e0 A4d V3 B6Z W5 A7z t0 N0 N6 C8I W0 c0 A2c A0x C8J B2E U0 A0y i5 a0 J0 s0 C0 C8K C8L B6i C4 j5 C8M C7R C1
A25 j7 b0 B0 f7 L3 B6j A9B Y0 "s_cbranch_vccnz Lc0_18\n" "Lr0_18:\n" A9A B0V C8N j0 T2 P7 C8O p0 h2 A5Y i2 A6A X2 D0 A7k B76 A1Y j1 A6C C71 A0V f2 B4e
t1 A5Z P2 A6D G1 D0 A9C K1 B0S B0M A6E H1 L7 D5A D8R G0 B19 B0W R1 A6F O1 D0 B0X B74 b7 B6c N4 A6G v2 C8P R3 W6 B7r L2 k5 u3 S3 A6H T3 D0 E0 U3 U5 r3
A3 l5 A0A H2 A6I Q2 y0 A69 B0t M2 E9Q H0 m2 M0 B0u A2y D5Q B5 A2G B1C U6 s0 r0 a3 T0 B2e v0 M0 B4J b0 B8C q0 g0 C8Q A9E B0U A4K i1 k0 s2 A2K A4f z1 h0
E2 B34 Z1 B3s w1 v8 w8 a1 X0 b1 A8G R2 P0 q2 A9F h1 I0 W1 C8R G1 A4o K1 G7 "Lb0_3:\n" M7 B8z B8e I1 V4 B7x S2 F1 A4g A3L C8 A38 y6 A0P C4L A0 E0M O4
j3 A1t g2 E0N I3 E0 f3 E8 h4 o3 H0 m3 F8 G8 X3 Q0 S0 g3 T0 H8 S5 r2 V0 e3 B0a F2 N0 t2 C0 B4f C3 A1 B1D A1u w4 W0 J0 B2E A1v A1w U0 a0 A1x s0 K0 w2 u0
F1 g4 E9R b0 E9S B8 p1 E7 V8 o0 B8u F7 A4h i0 "s_cbranch_vccnz Lc0_19\n" "Lr0_19:\n" B4V B2a D3 l0 a2 Q0 A4i A9G N2 A02 X1 B8U I2 N0 G2 C0 A9J E9F Y1
h0 A2 A6i B3 f0 d1 E9G E9H e1 A37 G1 B6Q K1 J0 z0 C0 E9I o4 I1 A82 p4 S2 K0 C8S B7 Z5 C8T I0 U1 a5 s3 u2 j0 B80 O1 y0 A3x A3w c1 Q9 b7 B4P B4Q J4 r0
x2 T0 "Lp0_12:\n" E24 A9o y2 Z0 l2 L0 I8 M4 C8U g0 C8V S7 B2w d2 B2r O0 C38 G3 J8 H4 H3 k0 C8W D4U D4V b2 T0 F0 n1 B8k o1 N0 B3v D4W D4X t0 A0f G0 W0
A1c n3 d0 H0 C6d A19 C8X C4 A1d c2 J0 C8Y D4 A03 v0 p0 b0 B39 A04 C7L A28 D4Y Y0 x8 B2x L0 D4Z E0D Q3 A6M b3 A1D E0E q4 C8Z A1 X0 C8a d5 B2 B0z C2 A6Q
l1 D0 A5o k1 P0 x1 A7o s1 A6R A1V A1q o2 A94 f1 A8B W2 A6S G1 D0 B00 K1 F0 B8e I1 A6T H1 A1E t6 c4 B8y U1 B01 B0r F9 A3X O1 A6N A6l A0Z B8r A1Z B2l E3
A6O C6b A7Z C6c A2a A9q A6P F4 C4K X5 t3 Z8 B4S B75 A8C m1 K0 y1 u0 A4m A4A c5 H7 R5 A9j J7 a8 A3J b8 A86 o0 Z2 B0 A93 v0 A80 D9 b0 A7a A0R l0 m0 A3A
A2d B2x C6e "s_cbranch_vccnz Lc0_20\n" "Lr0_20:\n" R0 T2 C8O A7b n5 h2 I0 i2 B4Y X2 F0 U2 B4X k2 G0 j1 B81 K3 p0 f2 A3c t1 h0 A7y B8f D5R L6 M6 G1 P0
B0i K1 B6f A4d z0 m5 u3 A7z I1 r3 N6 C8b C8c H1 C8d C8e c4 C8f C8g U1 B4I b5 R1 B6h A7B x0 Q0 D4a c1 A3e E4 B8D N4 T1 A3f A8H v2 N0 R3 C0 A8I A09 i3
E0 L2 A1A U5 S3 J0 T3 A0f e4 f4 U3 H0 Q2 B6k A3 K0 H2 C1 A4n M2 A9r C77 L0 A3B B5 B4a A3C B9 d0 j0 U0 y0 A10 B0 C9 U6 s0 p1 D5S a3 r0 v0 T0 D5T D5U
A7l D5V D5W m0 g0 B8u D5X D5Y i0 B6d i1 D5Z A1f s2 A1g k4 z1 k0 E2 Q0 A7d Z1 B2k w1 p1 D5a D5b a1 A2R b1 B6E B2q C5E q2 A4X h1 N0 W1 L0 D5c D5d G1 C0
X0 B6W D5e D5f z0 B4R B6I D5g D5h B4g A2r c4 A33 U1 J0 u2 P7 B1E y6 L0 q9 c1 x0 B6V B6U j3 U8 g2 A2s I3 A6j f3 L0 B4h V6 o3 A1t m3 A1D E0 X3 B4i O4 g3
M0 B82 B09 c0 I0 F2 B83 A6s r2 A6o C8h D0 A6t S4 C8i F0 C8j A2t C8k A6p C8l B6F C3 A1 G0 t0 A70 B4 W0 A6q A0i A71 A2u U0 D0 A1F A3b A2v B4W A6r b0 A1E
A8D T6 C32 Y0 A6k C8m A0u v6 w2 A0z A0v A36 A6m D0 A2E A2J A8v F3 "s_cbranch_vccnz Lc0_21\n" "Lr0_21:\n" L0 O2 A6n D3 D5i P0 a2 C33 I0 X1 C34 K0 G2 u0
F4I F4J Y1 F0 B71 o0 "Lp0_13:\n" E24 d1 B0 F4K F4L e1 g1 B0Z B5z l0 z0 Z4 B73 D8S A3L C8 D5O D5P y6 E22 E23 B53 r9 B8B I4 x2 s9 y2 E0 l2 d8 H4 d2 H0
e2 h5 F4M G3 S0 H3 F4N a4 V1 V0 b2 A5S n1 e0 o1 F4O b4 n2 L0 F4P t0 A2F W0 A2H d0 A1S U0 A1T i7 c2 v4 B50 b0 A1U T6 E9M E9N Y0 N0 i0 M0 E0J E0K B4j b3
B84 B2 A5T C2 X0 l1 A5U k1 J0 x1 C0 C39 P0 M3 B51 K0 f1 C1 A7e A08 A34 B0 A5V K1 j0 z0 l3 I1 y0 F0 H1 B0 D5j A0 t6 U1 A1G C8 r0 R1 T0 A9u B2C A4q C36
v3 g0 E3 B0 A3u j4 Z3 O0 W3 A3t A0c d3 k0 T4 Q0 E0G T0 A0d B32 H0 m1 A92 z2 N0 p2 C0 A1W y1 S0 A4A A74 R5 B4a A0e A1l B8o B0p W5 a0 J0 s0 C0 B2p Z2 x4
v0 d7 D5k D5l b0 A0R D5m E25 u0 D5n D5o i0 "s_cbranch_vccnz Lc0_22\n" "Lr0_22:\n" A1 T2 D5p D5q D2 o0 h2 B0 D4w D5r B2y D4x A2P U2 l0 k2 Q0 B0g j1 B2r
h0 C71 A4U B2z f2 N0 t1 C0 B20 B21 P2 B06 G1 L0 B22 D5s K1 J0 z0 L0 D5t B4k I1 A0f X0 H1 "s_waitcnt vmcnt(17) lgkmcnt(6)\n" B4l D5u S2 K0 U1 C1 D5v
D5w u2 A9r P0 R1 d7 D5x D5y B0v D5z B23 B6N O0 A0W B24 B25 v2 j0 R3 y0 B26 D50 i3 B4m L2 L0 A2V D51 S3 B1 T3 Z0 U3 R0 A4 E4 U5 A2W A3 M0 v9 H2 A8J Q2
r0 M2 T0 y4 z4 m2 A1 I0 B8W A5 u3 A2Z F0 A0i B6l B6K G0 I9 B8X r3 B5 U6 C8n A3g A0w a3 g0 C8o B4M B8h C8p p0 C8q A88 v0 k0 b0 Q0 C7M q0 T0 h0 m0 A21
Y0 N0 i0 L0 A1X i1 A0f B5v e7 k4 z1 J0 E2 O7 A4J Z1 X0 w1 A4I a1 A60 b1 R0 C8r R2 A1D P0 q2 A22 h1 A64 W1 D0 D41 G1 B6a A23 z0 A65 I1 D0 C8s B4g D5A
A0 G0 U1 A1G x0 B4 u2 W8 R1 A66 O1 D0 x9 C3c B4d j3 A67 g2 A1E Z0 I3 u8 C3x o3 R0 B4N m3 A61 X3 D0 E0 g3 S9 B09 H0 F2 A7j r2 A62 C8l B0s C3 c5 i5 S5
A2Z A0k C8K U0 A63 a0 E26 A1C j5 w4 v0 A25 A5N u0 f7 A8K P5 B6m i0 o0 w2 A7W A4t A4u F3 A2J l0 A0j "s_cbranch_vccnz Lc0_23\n" "Lr0_23:\n" O2 w0 F0 D3
G0 a2 A5Y N2 Z4 X1 A7k I2 G2 A1Y Y1 A2 A0V B3 "Lp0_14:\n" E24 d1 A5Z e1 G1 A9C K1 z0 A1B H1 D8S A3L C8 B0W y6 B0X C4L A0 A9D B8B I4 x2 z9 y2 E0 l2 M0
k5 H4 d2 H0 e2 Q0 l5 W6 G3 B0L H3 A0A a4 V1 V0 b2 B0Y n1 N0 o1 C0 A5a n2 B1D B1C b4 A2Z B4n d0 J0 A1O B85 i7 c2 C7R B7 A2f B86 A9E m0 j0 Y0 y0 A4K i0
A4l Q3 R0 A2K q4 b3 r0 B2 T0 v8 w8 C2 f0 l1 B34 k1 X0 x1 A9F s1 g0 M3 B0 A8G o2 P0 f1 B8z A08 A34 C8R K1 k0 z0 Q0 B2b I1 T0 B77 A0q D52 Y3 A1p B8p A38
R1 N0 B7q A0P F4Q R0 E0M E0N v3 J0 E3 C0 D53 D54 Z3 Z0 W3 D55 j4 d3 O0 B31 D56 B32 K0 m1 u0 D57 D58 z2 B75 D59 E8 y1 o0 A4A F8 G8 C8t c7 H8 B0Q S7 D6A
A86 l0 Z2 Q0 D6B D6C A75 A0 b0 D6D D6E q0 N0 m0 C0 D6F D6G Y0 j7 i0 D6H A8E C8u L3 C8v L0 A8F A4N C8w "s_cbranch_vccnz Lc0_24\n" "Lr0_24:\n" C8x A4O
A1u T2 A1v D2 C4P h2 J0 i2 L0 F4R F4S X2 B4o U2 C0 D8T D8U k2 K0 j1 L0 E0O E0P C71 h0 f2 C1 E0Q E0R t1 B0O B8f E0S E0T P6 j0 l4 L0 E0U E0V r4 X0 s4 L0
E27 E28 e5 A2g P0 g9 A5m A11 y0 E0W E0X A2h Q9 g8 E29 E3A B4p A3x m5 h8 A05 B4q A3w n5 A4v g0 A4w A9r B4P E4 B6n O0 A4x B4Q A2L B87 A2M B88 k0 Q2 M0
I8 J8 A4y Q0 I0 B6o "s_addk_i32 m0, -8576\n" D4U u3 A4z N0 A40 D4V A2N t0 F0 A5b A2O D4W d0 G0 U0 A1F A3b r3 B03 D4X A1c B86 A1d A03 A2C D8V A04 i0 f0
B0N D8W E3B i1 C8y E3C E3D z1 P0 E2 E3E D6I Z1 g1 I0 w1 D6J D6K a1 F0 b1 D6L D6M R2 g1 G0 q2 D6N D6O h1 V4 W1 D6P D6Q P6 E9 l4 D6R D6S r4 g1 s4 D6T
B27 A0q B6p A3X g8 J0 f5 "s_addk_i32 m0, 8576\n" A0Z B6q B2l A41 U8 B6r A2a O4 B6s O0 B6t Z8 A9 B6u E0 B6v A4W h4 B6w K0 A42 u0 A8C r2 H0 A43 A4m A44
o0 A45 B0 B4T A46 A1 c5 a8 S5 A9j E3F b8 A3J E3G B4b l0 w2 Q0 E3H E3I v0 D4a A0 b0 E3J B8 E3K V8 B70 E3L E3M i0 "s_cbranch_vccnz Lc0_25\n" "Lr0_25:\n"
B89 E3N E3O D3 J0 a2 A0f E3P E3Q N2 p0 X1 E9T E9U I2 K0 G2 C1 E3R E3S Y1 A9r h0 A2 E9V E9W B3 j0 d1 f0 e1 X0 P6 F3 l4 M0 P0 r4 I0 s4 F0 e5 G0 A0
"Lp0_15:\n" E24 B4r Z4 B4s x0 e4 B6x A1P B4p o9 B4t B1F A0 A09 M4 B1G g0 B1H B0 A1A f4 B1I O0 B1J B6k B1K E0 B1L A7v H4 B1M k0 V1 R0 p9 A3B B1N B9A
B1O T0 A3C B9 B1P N0 B1Q C0 C9 A1f A2y A1g a4 D4v e0 g7 I9 E3T b4 C8z E3U E0Y A0l E0Z E3V q0 A0X m0 E3W E0a B0U E0b E0c B4j b3 E0d E0e B2 f0 C2 E0f
E0g l1 A37 k1 E0h E0i C80 E0j E0k M3 I0 o2 E0l B9B F0 W2 B2b P6 B9C Y3 B6y e5 T1 g9 D8X d7 B6p A6B B4u A3F B6q A2D A8L o0 A8M B0 A2t A7D Z0 A7E O9 j4
A8N O0 B9D P9 q6 A7F l0 m1 Q0 A0r A8O A3h A8P B0 A7C A7G N0 A7H C0 A8D A2y i8 B0Q j8 W5 S6 Z2 C0 E3X E3Y A8Q E0m A0R E0n B4v E0o E0p i0
"s_cbranch_vccnz Lc0_26\n" "Lr0_26:\n" B0 B2c E0q E0r D2 j0 h2 E0s E0t B2y E0u E0v B76 F4T F4U C4C E0w E0x f2 C4E F4V F4W P2 A8R P6 A4 l4 I0 r4 F0 s4
G0 e5 T5 g9 F1 d8 A11 A1G x0 h5 B6x B37 B4p B2G B4t B4q r9 E4 A4v g0 A4w B0 s9 F4X B6n B28 A4x F4Y A3G B87 A3H B88 k0 Q2 Q0 F4Z F4a A4y A6z B6o A9s
A4z N0 A40 R0 B02 A95 D8Y B5 A2G D8Z U6 S6 a3 E3Z E3a A0l E3b E3c T6 D8a D8b A0Q D8c D8d i1 B5v D8e D8f C81 L0 D8g D8h Z1 P0 w1 D8i D8j a1 I0 b1 D8k
D8l R2 F0 q2 V4 h1 G0 W1 E9 P6 l4 B6y B27 d7 B6p A9u B4u A4q k7 G7 "Lb0_4:\n" M7 B1 B6z A3u A3t A41 o0 B6r B0 A7f B6s Z0 B6t u9 O4 B6u O0 B6v E0 B6w
A5W h4 A42 B0o Q0 A1W A43 A9r H0 A44 A7g A45 N0 A46 R0 A4B t0 A0f c7 A2z B29 r8 w4 S6 w2 C0 F4b F4c A8Q E3d B8 E3e B4v E3f E3g i0
"s_cbranch_vccnz Lc0_27\n" "Lr0_27:\n" B2r B89 C82 C83 D3 j0 a2 C84 C85 N2 p0 X1 C86 C87 I2 h0 G2 C88 C89 Y1 f0 A2 C9A C9B B3 X0 d1 C9C C9D e1 A8R P6
F3 l4 I0 r4 F0 s4 G0 e5 Z4 g9 F1 y4 B4s x0 z4 B6x A4H B4p v9 B4t B1F A0 A7h M4 B1G g0 B1H B4M w9 A5 B1I B28 B1J B6l B1K E0 B1L A3g H4 A0 "Lp0_16:\n"
E9X B1M k0 C8W A6U B1N A6z B1O B60 B1P N0 B1Q R0 B8s t0 A0f c7 D6U a4 A2G D6V b4 C8z D6W D6X A0l D6Y D6Z T6 B9E B9F A0Q B9G B9H B3y B9I B9J A9t L0 B9K
B9L l1 P0 k1 B9M B9N x1 I0 s1 B9O B9P M3 F0 o2 l3 f1 G0 W2 t6 P6 l4 B6y B27 S7 C9E A9l W8 B4u x9 C9F B6z B4d A8L o0 A8M B0 B4N A7D Z0 A7E u8 j4 A8N O0
B9D S9 q6 A7F l0 m1 Q0 A7j A8O A2b A8P B0s A7G N0 A7H R0 i5 X8 A95 Y8 B0Q j5 W5 S6 Z2 C0 E3h E3i A8Q E3j A0R E3k B4v E0y E0z i0
"s_cbranch_vccnz Lc0_28\n" "Lr0_28:\n" B0 B2c E00 E01 D2 j0 h2 E02 E03 B2y E04 E05 B76 E9Y E9Z C4C E06 E07 f2 C4E E9a E9b P2 A8R P6 A4 l4 I0 r4 F0 s4
G0 e5 T5 g9 F1 k5 B4s x0 l5 B6x B0W B4p B0X B4t B4q A9D E4 A4v g0 A4w B0 z9 W6 B6n B28 A4x B8i B87 B9Q B88 k0 Q2 Q0 A5a A4y A6z B6o B56 A4z N0 A40 R0
B85 A95 E08 B5 A2G E09 U6 S6 a3 E3l E3m A0l E3n E3o B6j D8m D8n C9G D8o D8p i1 p0 s2 D8q D8r z1 h0 E2 L0 D8s D8t Z1 P0 w1 D8u D8v a1 I0 b1 D8w D8x R2
F0 q2 V4 h1 C9H C9I W1 C9J C9K P6 B9C E9 B6y B27 S7 B6p D5O D5P B4u E22 E23 C9F B6z F4d F4e A41 o0 B6r B0 E8 F8 B6s Z0 B6t G8 O4 B6u O0 B6v E0 B6w H8
h4 A42 B0o C1 B0a A43 D8y H0 A44 B4f A45 j0 A46 R0 C4O c3 y0 c7 B0 A1w B29 A1x B4b r0 w2 T0 "ds_read_b128 a[144:147], v215 offset:42752\n"
"ds_read_b128 a[148:151], v215 offset:42816\n" v0 C9L C9M b0 "ds_read_b128 a[152:155], v215 offset:38528\n" B8 g0 C8Q
"ds_read_b128 a[156:159], v215 offset:38592\n" A4r F4f F4g "s_cbranch_vccnz Lc0_29\n" "Lr0_29:\n" B89 B4w E0Q C9N a2 k0 N2 E0R C9O X1 f0 I2 E0S E0T G2
X0 Y1 E0U E0V A2 P0 B3 E27 E28 d1 I0 C6Q E0W E0X A96 F0 l4 E29 E3A r4 G0 s4 F3 e5 C9P C9Q B4r C9R C9S B4s Z4 B9R T0 A3x A3w f5 o0 k7 B0 B4P B4Q h8 Q4
B1F M0 A9o B1G l0 B1H I8 J8 B1I Z0 B1J D4U M4 B1K O0 B1L E0 B1M D4V H4 B1N H0 B1O B8k V1 S0 B1P D4W a4 B1Q M0 B9S D4X A1c C9T A1d b4 U0 A03 C9U a0 A04
C9V B9T D8V C9W c2 D8W C9X v0 R4 b0 E3B A0 "Lp0_17:\n" E9X E3C T6 E3D E3E A0Q A0 D6I D6J Q3 A1 B3A D6K D6L B2 G0 C2 D6M D6N l1 F0 k1 D6O D6P C80 D6Q
D6R M3 I0 o2 D6S B9B D6T d5 W2 Y3 C9Y A96 L0 C9Z C9a l4 C9b C9c r4 C9d K4 s4 C9e C9f B27 P3 C9E A3X g8 Z0 f5 A0Z B6q A29 j4 A8L I4 A8M T8 E3K A7D V0
A7E E3J E3I A8N A1 e0 B9D q6 W5 A7F E3H E3G C5S A8O E3F A2a A8P S0 A7G Z8 R5 A7H A9 C9g A9b A4W C9h A9a A1 A3z C9i A9Z q8 C9j A9Y m9 C9k A9X n9 C9l
C9m H7 C9n B6i J7 C9o C9p R4 C9q a8 j7 C9r L0 b8 C9s p0 C9t E3L E3M C9u "s_cbranch_vccnz Lc0_30\n" "Lr0_30:\n" A0 T2 A0 E3N E3O D2 C9v h2 E3P A6X i2
A9i X2 E3Q E9T U2 A0 C9w A0 E9U C9x j1 h0 A9z E3R B9U f2 A1H C9y A47 E3S B9V P2 x0 C9z A0 E9V C90 B4q A0j A4v A0 C91 C92 A4w A8S C93 E9W C94 C95 A0
A3e C96 C97 c0 C98 C99 A4x D0A A0 A8T D0B A0 D0C D0D B9W A3f A8H B9X A0 D0E A4y M0 A8U A8I D0F A31 A4z D0G D0H A40 E3W E3V D0I g1 D0J D0K A9g A1 E0Z
E0Y A9f g1 D0L D0M A9e L0 A8V A9d L0 D6a D6b A9c A1 E3U E3T D0N g1 D0O D0P D0Q A1 e4 f4 A2h g1 B6k g8 u6 E0 f5 R0 A4n D0R k7 L0 A1Q h8 E8X D0S D0T D0U
D0V L0 A83 B6e D0W s2 A0 D0X D0Y B9Y D0Z Q4 z1 A0 A2B E2 R0 D0a D0b Z1 B9Z D0c D0d a1 E0a E0b B2O E0c E0d R2 A05 D0e E0e D0f D0g h1 x0 W1 O0 E0f D0h
M0 E0g B9a A41 c0 E0h B9b H0 D0i S0 B9c B2M D0j B0w D0k D0l D0m E0i E0j D0n E0k D0o M0 E0l D0p B9d D6c D6d D0q
"ds_read_b64_tr_b16 a[48:49], v217 offset:12672\n" "ds_read_b64_tr_b16 a[50:51], v217 offset:13184\n" A42
"ds_read_b64_tr_b16 a[52:53], v217 offset:16896\n" "ds_read_b64_tr_b16 a[54:55], v217 offset:17408\n" D0r u6 D0s A43 A1
"ds_read_b64_tr_b16 a[60:61], v217 offset:16928\n" "ds_read_b64_tr_b16 a[62:63], v217 offset:17440\n" A44 A1
"ds_read_b64_tr_b16 a[56:57], v217 offset:12704\n" D0t D0u D0v A8S "ds_read_b64_tr_b16 a[58:59], v217 offset:13216\n" D0w D0x A45 A0
"ds_read_b64_tr_b16 a[64:65], v217 offset:12736\n" "ds_read_b64_tr_b16 a[66:67], v217 offset:13248\n" A46 "s_cbranch_vccnz Lc0_31\n" "Lr0_31:\n"
"ds_read_b64_tr_b16 a[68:69], v217 offset:16960\n" "ds_read_b64_tr_b16 a[70:71], v217 offset:17472\n" D0y A1 D0z D00 D01
"ds_read_b64_tr_b16 a[72:73], v217 offset:12768\n" "ds_read_b64_tr_b16 a[74:75], v217 offset:13280\n" A8T D02 A1
"ds_read_b64_tr_b16 a[76:77], v217 offset:16992\n" "ds_read_b64_tr_b16 a[78:79], v217 offset:17504\n" D03 D04 D05
"ds_read_b64_tr_b16 a[80:81], v217 offset:21120\n" "ds_read_b64_tr_b16 a[82:83], v217 offset:21632\n"
"ds_read_b64_tr_b16 a[84:85], v217 offset:25344\n" "ds_read_b64_tr_b16 a[86:87], v217 offset:25856\n" D06
"ds_read_b64_tr_b16 a[92:93], v217 offset:25376\n" "ds_read_b64_tr_b16 a[94:95], v217 offset:25888\n"
"ds_read_b64_tr_b16 a[88:89], v217 offset:21152\n" "ds_read_b64_tr_b16 a[90:91], v217 offset:21664\n" D07 B8E D08 D09 A0
"ds_read_b64_tr_b16 a[220:221], v217 offset:25408\n" A6e D1A B9e "ds_read_b64_tr_b16 a[222:223], v217 offset:25920\n" D1B A8U D1C
"ds_read_b64_tr_b16 a[216:217], v217 offset:21184\n" "ds_read_b64_tr_b16 a[218:219], v217 offset:21696\n" D1D D1E D1F D1G R0
"ds_read_b64_tr_b16 a[224:225], v217 offset:21216\n" "ds_read_b64_tr_b16 a[226:227], v217 offset:21728\n" D1H
"ds_read_b64_tr_b16 a[228:229], v217 offset:25440\n" "ds_read_b64_tr_b16 a[230:231], v217 offset:25952\n" D1I D1J B56 A8V D1K D1L D1M A0 B2F D1N D6e
D1O D6f D1P A8W D1Q D1R D1S D1T D1U D8m D8n D1V A8X C2 R0 D8o D1W D8p B2 A0 D8q D1X D1Y D8r A8Y s1 A8Z D1Z D8s D8t D1a D8u D1b D8v A08 A8a D1c D8w D8x
D1d B9f D1e D6g D1f D6h D1g B9g D1h B9h D1i "Lp0_18:\n" E9X B9i D6i D1j A8X D1k D6j A8Y D1l A8Z D1m A8a D6k D6l D1n "Lend0:\n"
".fill (Lv0+77824-.)/4, 4, 0xBF800000\n" "Lp0_19:\n" "s_branch Lb0_4\n" "Lc0_0:\n" B61 A97 D1o B1R B1S D6m "s_branch Lr0_0\n" "Lc0_1:\n" B62 A8b W4
A97 L4 "s_branch Lr0_1\n" "Lc0_2:\n" B9j A7I N3 A8b d4 "s_branch Lr0_2\n" "Lc0_3:\n" B9k A48 W4 A7I L4 "s_branch Lr0_3\n" "Lc0_4:\n" D1p B4x N3 A48 d4
"s_branch Lr0_4\n" "Lc0_5:\n" r1 D1q F4h W4 "s_mov_b32 s13, 1984\n" p5 A3i B9l E3p A49 B9l E1A B3B "s_branch Lr0_5\n" "Lc0_6:\n" v1 D1r E9c N3 F4h r6
A3i A3j "s_branch Lr0_6\n" "Lc0_7:\n" r1 D1s E3q W4 E9c L4 "s_branch Lr0_7\n" "Lc0_8:\n" v1 D1t E1B N3 E3q d4 "s_branch Lr0_8\n" "Lc0_9:\n" r1 D1u D8z
W4 E1B L4 "s_branch Lr0_9\n" "Lc0_10:\n" v1 D1v D6n N3 D8z d4 "s_branch Lr0_10\n" "Lc0_11:\n" r1 D1w D1x W4 D6n L4 "s_branch Lr0_11\n" "Lc0_12:\n" v1
D1y B9m N3 D1x d4 "s_branch Lr0_12\n" "Lc0_13:\n" r1 D1z B63 W4 B9m L4 "s_branch Lr0_13\n" "Lc0_14:\n" v1 D10 B4y N3 B63 d4 "s_branch Lr0_14\n"
"Lc0_15:\n" r1 D11 B3C W4 B4y L4 "s_branch Lr0_15\n" "Lc0_16:\n" v1 D12 B1R N3 B3C d4 "s_branch Lr0_16\n" "Lc0_17:\n" D13 D14 L4 "s_branch Lr0_17\n"
"Lc0_18:\n" v1 D6o B1T N3 B3D d4 "s_branch Lr0_18\n" "Lc0_19:\n" r1 D6p D15 L4 "s_branch Lr0_19\n" "Lc0_20:\n" v1 D80 A8c N3 A98 d4
"s_branch Lr0_20\n" "Lc0_21:\n" r1 D81 D16 L4 "s_branch Lr0_21\n" "Lc0_22:\n" v1 E1C A5A N3 A7J d4 "s_branch Lr0_22\n" "Lc0_23:\n" r1 E1D D17 L4
"s_branch Lr0_23\n" "Lc0_24:\n" v1 E3r D18 d4 "s_branch Lr0_24\n" "Lc0_25:\n" r1 E3s D19 L4 "s_branch Lr0_25\n" "Lc0_26:\n" v1 E9d D2A
"s_branch Lr0_26\n" "Lc0_27:\n" r1 E9e D2B "s_branch Lr0_27\n" "Lc0_28:\n" v1 F4i D2C "s_branch Lr0_28\n" "Lc0_29:\n" r1 F4j D2D "s_branch Lr0_29\n"
"Lc0_30:\n" v1 "s_mov_b32 s12, 737280\n" D2E "s_branch Lr0_30\n" "Lc0_31:\n" r1 "s_mov_b32 s12, 761856\n" D2F "s_branch Lr0_31\n"
".fill (Lv0+98304-.)/4, 4, 0xBF800000\n" "Lv1:\n" "Lb1_0:\n" C0m C0n C0o C0p C0q C0r E9f A0N C0s C0t F4k B7b C0u "s_mov_b32 s54, Lend1-Lv1\n" C0v C0w
C0x B7c C0y C0z C00 C01 C02 B5Z C03 C04 B7d C05 C06 C07 C08 C09 C1A C1B C1C C1D p8 C1H C1E C1I C1F C1G C1J p8 B7e C1O C1K C1P C1Q C1L D6q C1T C1c C1R
C1S C1U A5I C1V C1M C1N D6r p8 D6s C1Z C1a C1b C1d C1e "s_mov_b32 s92, 14336\n" C1f C1g C1h C1i C1j C1k C1l C1m C1n C1o C1p A5J
"s_addk_i32 s92, -13312\n" C1q C1r C1s C1t C1u C1v C1w C1x A5J C1y C1z C10 C11 C12 C13 C14 C15 C16 C17 C18 C19 C2A C1W C1X C1Y B7f C2C C2B D6t D6u D6v
D2G C2D P3 B7g E9f B7h B7i B7j B5a B7k B5b B1r B5c B1s C2E C2F C2G E1 B5d A5K D35 C2H D36 P3 C2I C2J B3f S8 A1N C2K A8t C2L R6 C2M E1 C2N D37 C2O D38
B1t N0 C2P A2p C2Q C2R C2S C2T C2U B1u C2V T1 A7V B1t A2p C2W C2X C2Y C2Z E1 A8t A3s D39 C2a C2b C2c C2d B1u B9n B3g B9o C2f B3h C2g C2e C2h C2i A5L
A3t A3u R6 A3v A3w A3x A3y C2j C2k C2m C2l K0 C2n A7W j0 A7X D4A D4B C2o B0 D4C D4D B3i A0 g0 B1r B1s C2p E1 A7V D4E D7N B0 A3s B3j U0 Q0 C2v B5e D4F
W0 B3k B5f A1N B5g C2s C2t B1w C2q C2r S8 z7 D4H C2u D4G A8u B5h D6w q0 B5i D6x J3 C2y m0 K6 B9n h3 B3g B9o C2x B3h C20 D4I C2w E2S B3l E9g D7O E2R
D6x B3k C2z D6y C2y D7P D6w Y0 J3 B5k A0O B1v C22 C21 R6 i0 h3 C3A C23 C24 C25 C26 B5m C29 B1x B5l C28 B1y K0 B1z u0 B5n C27 J3 B10 A0 K6 C3B h3 B11
p1 x0 J3 B12 o0 B3m C3E C3D C3F C3C E9h B0 B13 E9i B5r B14 B5o C3G B15 B5p C3H B16 B5q C3J B17 B5s B18 l0 p0 "Lp1_1:\n" E9j h0 X0 f0 v4 F3 c0 t0 C3K
C3L A8w l9 C3M C3N A1O P0 A8x I0 S6 B1U C0 Z4 v0 C3O C3P A5N C1 Z0 m0 B4z Y0 E0 B1 B5t A7Y w0 B1x y0 H0 B1y O6 S0 B1z x0 B0 B10 E1 V0 B11 r0 e0 B12 T0
A0n A19 V1 h0 c2 g0 B13 B0 A0o A5O B14 D2H B15 A29 B16 T8 B17 k0 B18 Q0 A8v p0 T0 l3 M0 t0 A8y B1V P0 d0 A7Z B4A I0 a0 A3z K4 B38 X5 D2I B1U B3o A30
C0 Y3 v0 q8 A0p B6S D82 A4m m0 C3R Y0 H7 C3S i0 K0 C3T m1 u0 J7 C3U Z2 I7 a8 A5M b8 B2s A2A A5P A1z "s_cbranch_vccnz Lc1_0\n" "Lr1_0:\n" u6 C3W l0 D2
Q0 h0 C3X g5 B3p f0 C3Y B3q N0 C3Z C0 B3r A3U C3a A7c J0 C3b C0 B3t P0 A81 K0 z0 B0M I0 C4I d7 G0 B19 C1 A1P B8q A7B o9 c1 j0 b7 A31 C3d y0 Z0 v2 B4z
C3e a7 E0H E0 C3f e4 U5 S3 r0 C3g T0 f4 u3 U3 H0 Q2 A1e r3 C3i g0 H2 B0 A4n C3k C77 M0 A3B B5 B4a A3C B9 B8o C9 U6 B3j s0 Q0 A2B a3 T0 v0 B4D C55 B2U
A2R P5 B6T i0 B6d C3m A4X k4 s2 J0 C3n A33 C5m C3o A2r w1 X0 C3p q9 b1 P0 C3q L0 B7v I0 C3r A2s W1 F0 G1 B7w B6g C8s I1 B4 H1 A1R C3t B5x C3u B5y B1
b7 A2D C3v o0 g2 A8 C3w O9 C3x C3y A2t B7y C3z P9 B7z B0o Q0 A0r C30 A2b F2 A7C C31 N0 A24 A8D A95 i8 B29 j8 w4 S6 w2 B0R g4 A0u b0 v6 B8 A0v B4v A36
i0 "s_cbranch_vccnz Lc1_1\n" "Lr1_1:\n" B0 O2 R0 A2E D3 j0 a2 C33 h0 X1 C34 X0 G2 A2w Y1 p0 B71 f0 d1 A5Q e1 B52 B5z B0S F3 I1 B77 A0 Z4 A85 C8 y0 A38
B8q A7B A0P B6R b7 B2n J4 B6X x2 O0 y2 w6 M4 l2 E0 C37 H4 e2 g0 G3 B0 h5 x6 H3 H0 V1 A5S b2 k0 n1 Q0 A2x o1 T0 S0 n2 L0 A2F a4 B4a A2H A1S B8o A1T b4
D2J C0 c2 B50 B6S q0 A1U A4h A89 Y0 B2a A9A X0 Q3 A4i d5 b3 p0 B2 A5T C2 h0 l1 A5U k1 P0 x1 C39 I0 M3 B51 F0 f1 A7e A0 "Lp1_2:\n" E9j A08 B0Z A5V K1
q4 K4 z0 Y3 B2D C3t A39 R1 C3u t9 B74 b7 B0P v3 o0 E3 A8 Z3 u9 j4 W3 O0 d3 A7f B31 A5W B32 l0 m1 C4A A1W z2 A2b p2 A7g y1 N0 A4A A4B A95 A2z B0Q r8 W5
S6 Z2 B0R g4 s8 b0 A4C A0R A4D B4v A87 i0 "s_cbranch_vccnz Lc1_2\n" "Lr1_2:\n" B0 C4B t8 D2 j0 h2 A4E B2y A4F B76 A20 C4C C4D C4E A4G P2 B52 B33 B0S
B0M F0 H1 E9k D5j A0N T5 A85 C8 A4H R1 C4F O1 j2 v9 B74 b7 A8J N4 C4G v2 j2 Z0 R3 A5 B7r L2 y4 z4 S3 B78 T3 j2 E0 U3 D8 B8g H0 H2 A2I Q2 D4J M2 D4K
A6U m2 R0 c5 A0S B5 W0 C4H d0 j2 A88 C6n R9 D83 B79 b0 j2 A5X B1B T6 B4c B8t Y0 C4J i0 j2 B8v e7 a3 h0 i1 A6h s2 A5I z1 y0 A4I E2 E2e X0 Z1 A4J w1 r0
a1 T0 A22 b1 P0 R2 B54 g0 h1 K7 A23 W1 g1 A34 B55 k0 z0 B5w B77 A0 E9 A3L C8 W8 y6 x9 B53 A0T O4 j3 I4 g2 A0U I3 E0 f3 y9 h4 o3 H0 m3 B9p X3 S0 g3 S9
S5 r2 V0 e3 A7j F2 e0 t2 A0x w4 C3 L0 A0y t0 i5 W0 A0C X8 d0 Y8 U0 j5 i7 N0 w2 B0R g4 A25 b0 f7 B8 A8K V8 J0 B8H B6m i0 "s_cbranch_vccnz Lc1_3\n"
"Lr1_3:\n" B4V A4t D3 K0 a2 u0 A5Y N2 f0 X1 A7k I2 X0 G2 p1 D7R D7S Y1 o0 A2 B0 D7T D7U B3 p0 d1 D6z B9q e1 B2d G1 B9r A8d K1 l0 z0 A5d A8e D60 I1 A82
L0 A5Z S2 D61 A1p j2 B8p A1r o4 y6 A1s p4 c1 D62 K2 p1 D7V D7W J4 j2 Z0 x2 D7X D7Y y2 D63 l2 R0 A2X M4 d2 j2 O0 C38 G3 B6b H4 H3 A5K C8W B6c b2 E9l I0
n1 k5 s3 o1 N0 n2 C0 l5 W6 t0 A9i W0 B8i d0 A1 G0 B9s A1F A3b n3 B8Y B0u B6S D82 B6M P5 B2e i0 K0 c2 L0 B8C Q3 C4M h0 b3 B0 A9E B2 j0 C2 L0 A4K l1 C4N
f0 k1 B0 A2K A76 x1 r0 B8j A77 v8 M3 T0 X0 o2 w8 B35 f1 g0 W2 B0 B8S G1 g1 B6a B2g A26 z0 k0 I1 A27 A4L H1 A4o c4 A4M q4 A85 C35 A0 "Lp1_3:\n" E9j A4k
d5 B2C C6C K2 b5 v3 M0 A6d j4 E3 O0 Z3 B58 W3 E0 d3 E8 q6 T4 H0 F4 F8 Y3 t3 S0 m1 G8 R5 z2 R0 V0 p2 H8 A8E y1 P0 V3 A8F K4 A35 A4N W5 g7 d0 A4O B9s
A1u a0 A1v s0 F1 A1w Z2 A0C A1x g4 h0 A8u A0R V8 N0 Y0 "s_cbranch_vccnz Lc1_4\n" "Lr1_4:\n" C0 i0 R4 T2 T1 p0 D2 J0 h2 C0 P0 i2 A9H m5 X2 I0 U2 A9I u3
C4Q B1U f0 r3 n5 B8V A5M P3 D7Z D7a D7b D7c c0 E1E w0 P1 l0 N1 x8 A00 A5e D7d D0 A0 B6A A0 D7e D7f J3 D1 A5f K6 h3 J3 J1 w0 A9K B3m C4R C4S C4T C4U J2
D0 C4b D7h B9t A9L D7i k2 A5g j1 D0 B2J D7j D7k J3 K3 B2K K6 h3 J3 f2 A5h B3m C4V t1 L7 D4L C4Z P2 c0 C4W D7g u1 D4M Y5 a0 A5i U0 D0 B0b N7 l9 C4X C4Y
W0 B0c k3 c3 A5j p3 D0 B0d Y2 Y0 C4a D7l m0 B0e D7m q0 A5k A4P A9M D7n i4 Q4 A0W A9N Q1 v2 A5l R3 Z0 i3 c0 O0 L2 E0 S3 A0X Q0 D84 B64 t4 y3 S1 I8 M1
q1 p0 N1 h0 P1 x0 A8v U4 A6L A1a B9u Q2 e0 A0D x3 A4Q B2L C4g A4R A7n C4h C4i A1b T3 A9O k3 U3 A9P Y2 a3 c0 B1 A3 A9o H2 Z0 M2 X5 m2 Y6 A0E A0F J8 Q1
B1V y3 l9 y8 B9v c0 A1F w0 A3b A0 A5n z6 z8 A2N A0Y N0 v0 "s_addk_i32 s92, 16384\n" A2O A4S A7K q0 A4T A03 m0 X4 Y0 A04 A1c i0 B06 B6e A0 A1d s2 X0 z1
A9Q V6 E2 A9i Z1 M0 A5o w1 G0 O3 A9R B4 P1 p0 N1 k4 q1 P0 S1 I0 w3 F1 S4 z3 A0 D2K A28 Y5 D1 A2P N7 J1 D64 k3 J2 E1F Y2 V2 Q4 a1 A7X A4U E7 b1 A1 D2L
F7 D4N D2M D4O B3E W1 A1 E0 u1 B3F Q1 a0 B8F y3 A0i M1 A06 B8G A9S A9T z7 S0 Y4 A9U B6B Y0 J0 m0 C0 A8B q0 p0 A7K i4 K0 j3 A1H g2 X0 I3 V0 f3 w0 e0 o3
C1 A7L m3 B0 G7 "Lb1_1:\n" M7 B6D t4 P0 S1 j0 q1 I0 N1 F0 P1 G0 B6C B2R r2 D2N A2Q A0n A5B X6 D1 A0o Y2 L1 B1 Q6 A0Z w2 b5 C4p Z5 T9 C4q O0 X3 a5 A29
g3 D2O R0 T8 D2P C5 t2 C4v C3 Z8 M1 Z6 R0 H0 C4r F4l m4 A9 C4s c5 n4 O8 A9j B8m B6K A10 q8 P8 s0 m9 B3G n9 g4 H7 A7l J7 B0h m0 a8 A9V Y0 b8 L3 i0 L0
h0 C4t C4 o4 C4u "s_cbranch_vccnz Lc1_5\n" "Lr1_5:\n" D4 O2 f0 B9w p4 E3t E8b I0 a6 F0 E8c G0 D5 A7p s3 X1 T1 A7q n3 I2 A7r G2 A7s Y1 C4w C4x J9 D2Q
A3A J1 B4C h9 y0 A1z A0 "Lp1_4:\n" E3u E8d B0 X6 V9 Y2 Q1 E8e A3c E5 r0 A2 T0 E9m Z0 d1 O0 e1 g0 E8f B4m b6 A99 E8g y3 F5 w0 A5x M1 u1 k0 a0 O7 B8F
A6v B4Z A5p c3 D0 e0 Y4 A0m B40 A5q A91 h0 D2R R4 J4 A5r x2 D0 B0A y2 P0 l2 A5y A7t E8n A5s W9 A9W A7u E8o I0 G5 F0 V1 c0 G0 d2 A5t e2 D0 i9 G3 e4 H3
f4 t4 a7 S1 A5u q1 D0 A1e N1 B8J A12 A5m c2 A5v x3 D0 A3e B1W A3f A7n A5w L1 Q0 A8H k3 G4 E3v A8I Y2 E8p b5 c6 A09 k8 E8q O0 H5 M0 A1A Q1 b2 E0 n1 D2S
o1 A7v q3 n2 N0 A0J C0 p9 M1 E8r H0 d6 A1Q E8s J0 h7 C0 S0 A0F D2T A3k U0 B9 A3D a0 K0 s0 C9 A3E c8 A1f A4b v0 A1g A4c E0I q0 p0 A2C B2x l3 Q3 b3 B2
C2 E8u q5 A4Y I0 E8v F0 E3w u0 A54 B3H A55 B9x A4Z s1 A4a O3 o0 P1 B0 N1 B0B l0 A13 D2U A2R J1 B6E h9 A32 E8w D2V C5R E8y q9 B65 C7y
"s_mov_b32 s92, 114688\n" O0 f1 E0 W2 B0C E8z y3 e6 X4 E81 A58 M1 A8f H0 D2W B2T U0 A0k A5C A2i s6 A56 Y4 A57 A3l B66 A0 A59 F4m E87 G0 f6 j9 E88 I5
D2X A4e B67 Q0 g5 A8g B41 A2Q B4h A5B N0 D1 C0 B4i D2Y A7 Q6 A3F E89 J0 Z9 C0 A2D E9A B1 J5 A3Z Q1 z2 Z0 p2 O0 y1 E0 V3 l8 r5 E9C A3a P4 g6 K0 s5 O9
q3 E9D P9 M1 K5 c0 A3Y A0r W0 A6v A2u K9 C6n A2v L9 v0 E1G E1H b0 E1I E1J q0 A0s P5 A0t A0B L8 A0 i8 m5 C5M A1H C5N A7x B1X B0D L3 j8 X0 P0
"s_cbranch_vccnz Lc1_6\n" "Lr1_6:\n" M0 T2 I0 D2 n5 u3 h2 F0 i2 G0 l7 r3 t5 h6 B2t L5 B1Y X2 B1Z U2 c0 B2u C5O O6 m4 C5P n4 k2 j1 B6C X9 A1i P1 Y9 A1j
N1 A93 q1 D9 S1 A7y w3 B2V B2W z3 B68 L6 D1 v6 A0v J1 M6 J2 A36 A14 V2 R0 A15 C5Q D4 A2E A7M m7 B2N A7N u5 A7 A3K i6 B1 A3M M5 B4H N6 K3 Z0 C5T f2 O0
C5U C5V C1 C4 A01 t1 B4m P2 B9y B0f n7 B9z C4c v5 M0 C4d C4e C5b j0 j6 y0 C4f Q1 Q7 w0 y3 C5a B0 B42 M1 u1 r0 a0 A6z U0 A6v B4Z g0 c3 B0 e0 Y4 A0D B40
k0 q0 P7 h0 D2R R4 N4 A6A v2 D0 A1a R3 B43 i3 P0 B1a B44 "Lp1_5:\n" E3u B45 A16 A6C A2U A94 N5 F0 Q2 G0 L2 c0 A1b S3 A6D T3 D0 d8 U3 h5 w3 w6 S1 A6E
q1 L7 x6 N1 A1I x0 A12 A6F a3 D0 D2N A6J A0 B1W A6K A7n A6G D2Z B2Y k3 Q6 B2Z Y2 A1J b5 w5 r9 Y6 k6 A6H O5 M0 s9 Q1 A3 B90 H2 E0 M2 A6I m2 Q0 B91 o7
D65 A3G q3 x5 A3H M1 z6 N0 l6 C0 A2x a9 A3Y M0 c7 A0k A5c A5n a0 A2F D7o S6 A0Y C0 D7p D7q v0 A2H D7r E3x q0 A1S m0 K0 C9G A1T V6 i1 p0 s2 h0 D2a D2b
G0 A0b k4 S4 B3I B46 B4 Z1 B47 w1 B48 a1 B49 b1 B8L U4 A8h B0B u0 w3 x4 z3 E1 B5A A8i L1 A1U A7n o0 J1 B3w J2 Z0 V2 B3x p7 A7 B92 E0 R7 E9n Y5 R2 B1
q2 E9o A06 h1 D66 D2c D67 C4l q7 C4m C4n y5 B0 C4o Q1 b9 y3 B5B B5C M1 u1 A1F l0 U0 "s_mov_b32 s92, 163840\n" A6v B4Z e0 c3 X4 Y4 A1K Y0 B5D A8j A5D
q0 B2T A7K D85 j3 R4 g2 A7L I3 P0 f3 I0 A3m B3J B93 A0K F0 A07 B3K A4f c9 G0 r2 w0 B94 A4g E1K g3 w3 S1 E3y A2Q A8k A5B B1b A8l D2Y Q0 A7 G4 B0 t9 A2j
b5 m8 A2k T9 m6 O0 Q5 A2l Q1 e3 B2m F2 A0c U9 t2 N0 C3 C0 A0d P4 Z6 H0 r7 u9 q3 z5 J0 n6 C0 A5W M1 A1k c5 c0 B69 O8 A9j B6J D7s A3J P8 D7t s0 A74 D7u
A4V C7R A0 A0e D7v A7l A1l B0h A2C B0p A9V i0 f0 C4y A2z L3 C4z X0 C40 t8 r8 C41 "s_cbranch_vccnz Lc1_7\n" "Lr1_7:\n" o4 C42 B3L B3M C43 A9i O2 A4D
A4C D3 G0 a2 s8 n3 N2 I0 A1L p4 C46 a6 C48 Y5 A6 B21 N7 D5 P0 X1 B2z B7A B1 G2 B20 C44 Y1 Z0 s7 B22 C45 V9 A7 n8 B4k E8h E5 O0 C5n Z5 C5W C5o M0 E0 A2
a5 E8i B3 D5s C5X d1 D5t C5Y e1 B4l C5Z t7 b6 B6 B25 F5 B23 D7w B24 D7x B26 D7y D7z D70 D71 M0 F1 C5q O6 m4 C5r A6L D72 n4 A9X S0 A9Y V0 A9Z e0 A9a
E8k E8j A9b x4 A5z F1 A50 I7 A51 C1 h0 A52 B0O A53 X0 C49 E1 E8l E8m C5s C4 D77 C5t j0 J4 y0 D4 D73 x2 B0 D74 D75 V1 D76 D78 y2 r0 l2 T0 P0 A2m D2d I0
W9 x0 C6 g0 G5 B0 A2V E2Z c2 T1 F0 C5u A2W B0q C5v k0 d2 Q0 B8Q e2 T0 G0 G3 w9 E2a H3 N0 c8 C0 D79 D8A A17 y4 X9 c6 z4 Y9 D6 J0 H5 C0 A5 A1i b2 D8 A1j
n1 B95 A2S o1 K0 n2 u0 A3g A2T u7 A0w D9 d6 B0T B5E E6 o0 h7 R0 A0S A3d D8B B0 A4s B2V H1 "Lp1_6:\n" E3u L6 I1 A3n B2W z0 M6 K1 l0 C5A Q0 R9 A14 D8C
B0 A15 c1 Q4 O1 N0 R1 B8I u2 O0 D8D E0 C5B e7 E2b C5C J0 C5D C0 B8v B8t C5F V0 C5G B4c B1B C5H K0 Q3 R0 A5X E2c b3 C1 e0 B2 A2b C2 j0 A1M y0 S0 A4Y B0
E8x E8t A0L D8H D8J A1h A9v C5K l1 r0 k1 T0 C5L C5I x1 A9w C5J s1 f0 v7 A9x D8E A0G g0 F6 K7 A9y D8F K8 X0 M3 D4z E82 o2 k0 f1 Q0 D40 A7M W2 B2I w7
B04 A7N e6 N0 G6 C0 B05 A3K A0a M0 h0 D8K A8m E80 D8L J0 A3M D8M C0 A8n N6 C5z D8N B1c C50 D8O B1d D8P K0 D8Q u0 F1 A9c A9d q5 A9e o0 A9f x8 I0 A9g A0
F0 B9t G0 J2 A6M J1 D0 E83 E84 D1 D86 Z0 G4 A6N v3 B90 E3 E0 m1 A0 u4 Z3 A6O W3 D0 C5 A2n H0 f6 S0 H6 A6P I5 "s_waitcnt vmcnt(19) lgkmcnt(0)\n" V0 E85
E86 d3 x0 B4K T4 e0 Z2 B8w K9 F4 A6Q t3 D0 B8x L9 B1e B7B Z9 B7C L8 A6R I6 D0 B4d J5 z2 B4N p2 A6S y1 D0 u8 S9 V3 E9B x7 B6Z L3 g6 A6T J6
"s_add_u32 m0, s88, 21120\n" A2c A0x K5 A1 C5c C5d t0 "s_cbranch_vccnz Lc1_8\n" "Lr1_8:\n" A0y W0 l0 D2e i5 X8 U0 B0 E9E a0 Y8 j5 D2I B5F A25 A6X b0
E0C q0 f7 A8K m0 J0 Y0 B6m i0 B6d T2 A4t C5e F4n F4o L5 A7k h6 P0 X2 A5Y U2 I0 k2 A4 j1 F0 M5 A0V i6 G0 A9z A1Y f2 T5 t1 C5f A6e P2 C5g C5h Q7 A9C j6
C5i C5j G1 A5Z K1 C51 C52 z0 C53 C54 B2D A0 A1r B1A A1s B8N O0 K2 A7m N4 w0 E4 U5 v2 A6L R3 S0 i3 V0 N5 A90 k5 B5 A2U A5L L2 B0X S3 e0 Q2 A9D U6 T3 K0
U3 C1 z9 l5 O5 B0 k6 W6 A0A A3 H2 B0Y a3 j0 M2 y0 A5a m2 B0 a9 B56 l6 g1 t0 B85 W0 r0 d0 p1 E2f E2g D2f a0 E2i E2j s0 g0 A75 E2k E2l b0 q0 E2m E2n m0
k0 Y0 Q0 A2f E1L i0 B2k B6e D68 B5G s2 N0 z1 C0 B7D E2 X0 A4j B34 A0b J0 Z1 p1 E2o E2p w1 C0 p0 a1 E2q E2r b1 K0 A6V u0 E2s E2t R7 h0 A0H E2u E2v R2
o0 q2 v8 w8 h1 P0 W1 A8G A3I I0 b9 A9F A6W A4o G1 C8R K1 B6g A4L k4 I1 A4M V6 B7x c4 A2Y U1 A3P B1A C5k C5l O4 h4 j3 E1 A1m g2 A3N A6Y H0 I3 S0 f3 V0
C56 c9 x0 g7 C57 A07 A4k S5 C58 o3 A0X C59 m3 B0 C6C V5 r2 e0 o8 X3 A6d w4 y7 g3 l0 Z7 Q5 Q0 B58 M9 m6 B0 o5 C6A A6a A3O e3 N0 F2 C0 E8 F8 B08 C6B C6D
t2 G8 H8 C3 J0 A1k "Lp1_7:\n" E3u C0 A8E A73 n6 A8F L3 A6b A4N C6E A6c A1 A4O C7 t0 "s_cbranch_vccnz Lc1_9\n" "Lr1_9:\n" D7 W0 C4O d0 K0 U0 C1 C4P a0
B0 A3P s0 A1m C6F v0 A3Q C6G b0 j0 C6H q0 B2h B2i C6I B96 V5 Y0 B6P o8 A9A X0 y7 O2 B2j Z7 D3 p0 M9 a2 B8V o5 N2 f0 V7 X1 A3R W7 I2 P0 e8 G2 E2w E2x
f8 Y1 I0 X7 A2 B8U Y7 B3 F0 C6J A6f E9k A3S C6K d1 M0 G0 C6L e1 A6i C6M G1 C6O M8 K1 B5H N8 z0 R0 F3 G9 I1 B6Q H9 H1 C6P Q8 S2 B01 B7E R8 A6g x0 U1 y0
A3T u2 Q9 R1 A9u O1 r0 c1 T0 A4q K2 b5 A5R A2L M4 x2 g0 y2 B0 A2M s3 l2 B4L C6N d2 I8 n3 C6R C38 C6S G3 J8 H4 C6T H3 k0 V7 C8W y8 z8 W7 b2 A6z e8 n1
B8k f8 o1 N0 X7 B3v B97 Y7 D5N S0 C6U W0 A3V C6V d0 V0 C6W U0 e0 C6X a0 A19 M8 s0 A9p N8 c2 J0 G9 v0 B0y H9 D87 Q8 q0 B8Z R8 A2C A3W A9A f0 Q3 E2y E2z
b3 B3z C7J B30 l1 B6Y k1 P7 I0 x1 A5o s1 A6j A1V B0z o2 D2g f1 A1q W2 A6k G1 D0 A7o B8A B6g B00 I1 A6m H1 D0 A8B c4 l3 A3L t6 F9 A3X O1 A6n c1 B4K A0Z
B8b A1Z B2l E3 A6o C6b A7Z C6c A2a A9q A6p F4 B07 t3 Z8 B4S A3h p2 A8C m1 A6q y1 D0 A4m V3 c5 H7 R5 W0 A6r d0 e9 J7 a8 C6n b8 W5 v0 C6Z b0 B2s T6 A5P
Y0 C6a i0 C4K A1z Z2 A0z f0 K0 X0 A4 P0 A2A B7p "s_cbranch_vccnz Lc1_10\n" "Lr1_10:\n" w0 T2 T5 D2 C6f C6g h2 B3p G7 "Lb1_2:\n" M7 C6h C6i X2 B3q C6j
C6k k2 B3r C6l C6m K3 A7c t1 B3t A81 z0 A1B H1 A1R u0 B8p A90 B1 B80 O1 A1P c1 o0 K2 B0 o9 N4 b5 v2 A09 E4 R3 l0 i3 R0 A1A e4 L2 Q0 O0 S3 g5 E0 T3 f4
B7s N0 Q2 C0 B6k A3 H0 H2 A4n M2 C77 F4p D4d D4e B4a D4f D4g d0 J0 A1O D4h D4i a0 A1C D4b D4c B03 K0 b0 C1 A1Q B2B D88 A83 Y0 j0 i0 y0 A2B a3 B2d i1
B0 A7d s2 r0 z1 p1 A2R D4j E2 T0 P0 Z1 L0 D4k D4l w1 g0 a1 B0 D4m D4n b1 B6E k4 R2 A1 C5E q2 A4X h1 k0 W1 Q0 D4o D4p G1 T0 B6a A1 D4q A54 z0 A9i B6I
A55 A4Z B4g D5j A0 A4a D8G U1 "Lp1_8:\n" E3z N0 u2 C0 D8I B4E R1 E0 B0v B4F A56 K2 J0 A6u L0 A57 A58 j3 A6h g2 C0 B4h A59 I3 K0 f3 u0 B4i B82 A6w g1
H0 o3 L0 B83 A6s m3 S0 X3 A6t S4 X0 B38 O0 g3 R0 V0 V6 O4 B09 A3Z B4 F2 o0 C7 r2 B0 A3a S5 D7 t2 e0 o6 C3 O9 w4 p6 t0 c0 P9 A1m W0 l0 D2e A3N U0 B0
C6o C6p a0 A0r D2I A6V C0 A7C B08 C6q C6r v0 A8D V5 b0 A73 B6O q0 i8 L3 Z7 m0 J0 M9 Y0 "s_cbranch_vccnz Lc1_11\n" "Lr1_11:\n" j8 o5 B7m h0 A6W M0 A3O
O2 K0 D3 C1 A0u v6 A6x f9 a2 B6G N2 j0 X1 B2X I2 p0 G2 B4G Y1 B06 A2 B6H B3 P0 C6s A6y B4H C6t d1 I0 C6u C6Q A2w C6v G1 F0 C7 K1 B8K D7 B8e o6 I1 A5Q
p6 H1 F3 T7 S2 A2g Z4 U7 A1p C6w C6Y A3Q C6x R1 Q9 C6y O1 A38 C6z B6R V5 K2 c0 A0P o8 J4 B6X y7 x2 r9 M4 Z7 y2 O0 M9 l2 s9 d8 o5 d2 E0 V7 e2 A3R W7 G3
H4 A0I H3 D4r D4s X7 V1 g0 Y7 A6b B0 A3S C61 b2 H0 C62 n1 h5 w6 C63 o1 k0 C64 n2 M0 x6 A3G M8 t0 A4p S0 N8 W0 A3H a4 G9 d0 C65 H9 U0 V0 Q8 a0 c0 e0 R8
s0 A3T b4 C67 A6c C66 C68 c2 Q0 A2x C69 v0 B2I C7A b0 A9s V7 q0 N0 W7 m0 C0 B02 e8 Y0 h0 f8 i0 B8d X7 Q3 A6h Y7 b3 A3V C7B B2 J0 C7C C2 B3w C7D l1 X0
C7E k1 B3x M8 F4q N8 s1 M0 A5U G9 M3 I0 H9 o2 A5T Q8 f1 F0 R8 W2 A3W G1 G0 B6W D4t D4u z0 l3 I1 A72 H1 t6 c4 B0n A85 C7F C7G A1y O7 R1 A5V O1 A60 A6l
A7e B8b A1Z A39 E3 A61 C6b t9 C6c L0 A2k A9q A62 F4 B07 t3 A2l B4S A3h p2 A7f m1 A63 y1 B4K A68 u9 V3 c5 A5W R5 A2Z A0k A1W U0 A64 I9 A1C D0 A74 W5 v0
A0e C7N b0 A65 A91 A4B P5 B2p i0 A66 Z2 D0 s8 B8O A87 t8 h0 A2A f0 A67 e9 "s_cbranch_vccnz Lc1_12\n" "Lr1_12:\n" T2 A37 D2 A4 h2 A4E i2 C7H X2 A4F U2
P0 k2 A20 j1 C7I C71 C4D E0L I0 t1 A4G P2 K0 G1 B33 F0 B8e I1 T5 H1 A69 C7O c4 E1M U1 A90 A4b C7P F9 A4H B2C v9 K2 A05 A0W A7h E4 v2 O0 R3 w9 A5 i3 E0
L2 y4 U5 S3 H0 T3 z4 D8 U3 S0 Q2 B95 B5 A3 u0 V0 H2 A3g A0w M2 e0 m2 R0 B0T U6 t0 o0 B7F A0S A0 "Lp1_9:\n" E3z A4s d0 C7Q C7e U0 B8s a0 l0 s0 Q0 C7f
A4c a3 B0 B4J v0 A5X A4f D69 q0 A21 m0 B84 Y0 A1X A9A X0 i1 p1 e7 D43 s2 J0 z1 D44 D45 E2 B8M Z1 D46 D47 w1 p0 a1 D48 D49 b1 K0 R2 C1 D42 A9v q2 A4l
h1 A9w A9x W1 A4o G1 A9y D4z K1 j0 z0 y0 D40 B04 I1 B0 G0 H1 B05 A8m S2 r0 A1p T0 B8p p1 D5B D5C y6 D5D D5E c1 g0 B6V D5F D5G j3 K7 Z0 g2 D5H D5I I3
A8n B1c f3 B1d V6 o3 u6 O0 m3 k0 X3 Q0 E0 B8w g3 c0 B8x O4 h4 k4 e3 w0 x9 F2 T0 B4d r2 N0 t2 C0 y9 B4 C3 L0 A3Y R0 B9p A2Z S0 A0i S9 S5 B9s A2e M0 e0
P0 s0 B6Z S4 w4 A6Y J0 w2 C0 A2c A0x E1N C7 b0 A0y A73 D7 q0 K0 o6 m0 u0 i5 L3 p6 Y0 h0 T7 i0 "s_cbranch_vccnz Lc1_13\n" "Lr1_13:\n" X8 U7 A6a c0 f0
C7S O2 A3N C7T D3 o0 C7U a2 B0 Y8 j5 C7V N2 X0 V5 X1 M0 A25 f7 o8 I2 l0 y7 G2 Q0 A9B Z7 Y1 B4U M9 A6u L0 B0V o5 A2 L0 I0 C7W B3 A3O C7X d1 N0 C7Y e1
M0 A7k C7Z G1 C0 F0 C7 K1 A5Y D7 z0 B3N o6 I1 A0V p6 H1 J0 T7 S2 C0 A1Y U7 A6w F3 C7a U1 S7 A3Q C7b A1y C7c R1 A9C C7d O1 K0 V5 c1 C1 A5Z o8 K2 Q9 y7
A6f R0 B0W Z7 J4 j0 M9 x2 y0 B0X o5 y2 A8 V7 l2 A3R W7 d2 O0 e8 e2 D5J D5K f8 A6g E0 X7 G3 A7Y Y7 H3 A3S C7g V1 c0 H0 C7h b2 B6c C7i n1 r0 C7j o1 A4p
k5 s3 M8 n2 S0 N8 t0 c0 l5 n3 G9 W0 C7k H9 d0 W6 a4 Q8 U0 c0 V0 R8 a0 A3T e0 C7m s0 C7l b4 C7n A6x T0 B8i C7o c2 h0 C7p v0 B0u V7 b0 g0 W7 B2B B6M e8
A89 f8 Y0 B2e X7 i0 k0 Y7 A6y L0 A3V C7q Q3 Q0 X0 C7r b3 T0 B8C C7s B2 N0 C7t C2 M0 A9E M8 l1 C0 p0 N8 k1 A4K G9 x1 J0 H9 s1 A2K A76 Q8 A1V P0 R8 o2
A3W f1 I0 W2 D5L D5M G1 R0 F0 K1 A77 v8 B8e I1 w8 B35 H1 A1 l3 S2 x8 B8S A85 t6 F9 B2g A26 O1 A5e A6l A27 A4L B8b A1Z A4k A4M E3 A5f C6b C6C W3 g1 E30
L0 A6d A9q A5g F4 B07 t3 R0 A78 B4S A3h p2 A79 E8 m1 A5h y1 L7 F8 G8 V3 c5 R0 H8 R5 W0 A5i B0k B0a U0 A1 V0 I9 A1C A4N W5 v0 A5j A4P A4O A1u C7L A1v
A1w Y0 A5k C8m B2h A1x Z2 A1H B2i B6P A5l e9 B2J A2A B2K X0 p0 "s_cbranch_vccnz Lc1_14\n" "Lr1_14:\n" A1 T2 C7u D2 A4 A0 "Lp1_10:\n" E3z h2 B8V i2 C7w
X2 F4r B8U U2 P0 k2 B5H j1 K0 C71 A6i f2 I0 t1 B7E P2 F0 G1 B6Q K1 B6g T5 I1 A4g H1 A1R A90 C8 A9u y6 A4q B53 A2L E4 N4 I4 v2 A2M R3 E0 i3 I8 U5 L2 H0
S3 J8 y8 T3 S0 U3 z8 B5 Q2 u0 V0 A3 B8k H2 e0 M2 A4S U6 m2 L0 A4T t0 o0 B7F A9p B5I B0y a0 l0 s0 E9J B0 a3 v0 B39 C55 A28 A2P P5 B0g A9A h0 i1 B2T s2
J0 z1 C0 A4U A9Q C5m Z1 A5o w1 K0 a1 C1 A9R B3E b1 f9 R2 A1q q2 j0 h1 y0 B3F B2P W1 B52 B0 B00 K1 r0 z0 T0 B2Q k4 I1 I0 H1 C8s S2 g0 A1p A7W u2 A3X y6
k0 c1 Q0 A0Z C36 j3 T0 A29 O4 g2 N0 I3 C0 T8 A9 F4s o3 A8y B7y X3 A4W B7z J0 r2 C0 A8C e3 F0 F2 A4m t2 K0 A24 B4T t0 u0 G0 W0 a8 B4 d0 H0 C6d B4W A80
b8 b0 o0 B2B A7a A3A P5 C6e i0 l0 w2 Q0 g5 A7b A4u F3 A2J S8 P0 "s_cbranch_vccnz Lc1_15\n" "Lr1_15:\n" O2 c0 I0 D3 F0 a2 B4X N2 J0 X1 C0 B4Y I2 G0 G2
A3c Y1 K0 A2 E9K E9m B0 Z4 d1 B6f e1 j0 A81 z0 L0 B73 D8S C7z A0 Z5 C70 Q4 U1 a5 u2 Z0 R1 A2g O0 B0v E9L A3e M4 K2 B4m A5R A3f H4 x2 c0 H0 C73 w0 m4
A8H C74 r0 y2 T0 n4 A8I l2 S0 d2 A31 e2 g0 G3 B0 e4 a4 H3 V0 V1 f4 a7 b2 k0 n1 Q0 B7t o1 T0 e0 B3v p9 b4 C75 N0 C76 C0 C4 D4 t0 W0 A1Q d0 J0 U0 A83 a0
O7 s0 A2B c2 A5p v0 D0 B4D A7l B2U A2R m0 A5q Y0 D0 B6T i0 B6d C78 A4X q4 C79 A5r Q3 D0 B3A B3z A33 C2 A5s l1 A9W A2r k1 P0 x1 q9 s1 A5t A1V C7y A94
f1 A2s W2 A5u G1 D0 B7w C3s B2b I1 A5v H1 D0 D5j A0 A68 Y3 A3L C8 B5x A5w O1 D5i B5y B1 K2 R0 A2D v3 K0 E3 u0 Z0 Z3 O9 j4 W3 O0 d3 A2t B31 P9 B32 o0
m1 B0 A0r z2 B75 A7C y1 l0 A4A A8D A2y i8 B0Q j8 A86 E1M B6i B98 g4 A0u b0 v6 A0R A0v V8 f0 Y0 A36 i0 "s_cbranch_vccnz Lc1_16\n" "Lr1_16:\n" C4B A2E
D2 X0 h2 B6H A0 "Lp1_11:\n" E3z B2y B4G B76 A2w j1 P0 K3 C72 Q0 I0 t1 B0 A5Q P2 A4o g7 G1 B5z B3u B0M G0 H1 T5 S2 A0X U1 C0 A0 A6J u2 A6K y6 J0 c1 E31
Z0 K2 w0 O0 N4 A5L A0 v2 B2Y E4 R3 E0 i3 B2T L2 U5 S3 B2Z r9 T3 K0 U3 C1 s9 d8 Q2 A2b A3 h5 w6 H2 j0 M2 y0 x6 A3G m2 g1 c5 B0 A3H B5 W0 V0 A0i A2x A3J
A2F U6 s0 r0 a3 T0 A2H A1S A0l B5A A1T q0 g0 C8Q A1U A4h B0U B2a C8C k0 C8D L0 A7x i1 Q0 A5E T0 A4i k4 z1 N0 E2 C0 A5U Z1 f0 w1 A5T a1 J0 b1 A72 R2 A1
X0 q2 B0n C8E P0 C8F A0q B2w h1 I0 W1 A5V G1 F0 K1 A7e B8e I1 C8s B4g B4 X9 c4 Y9 A1i U1 A1j A2S B1A Z0 A2T F4t D9 c1 x0 B5E K2 E1 A8k O4 A3d C8H C0
E0 L6 j3 D4 h4 M6 g2 K0 A14 I3 u0 A8l A3y A15 f3 H0 A7M o3 A3v A2k A7N m3 o0 A3K X3 B0 A2l A0c A3M g3 S0 N6 r2 R0 A0d S5 C8I C8M l0 C8J C8L Q0 C4 u9
e3 B99 F2 C5p t2 N0 C3 C0 D2h A35 L0 A0e w4 d0 J0 U0 P7 A4B i7 R0 B2p C8N A6A w2 D0 F1 g4 s8 b0 A4C B8 A6C m0 D0 A4D A4r A87
"s_cbranch_vccnz Lc1_17\n" "Lr1_17:\n" O2 A6D D3 D0 t8 a2 h0 N2 M0 A4F X1 A6E I2 L7 A4E G2 f0 Y1 C7K A2 A6F B3 D0 A20 d1 X0 e1 D4y G1 A6G A9n A4G z0
B4e I1 F3 H1 A6H S2 A94 B36 F0 u2 A90 G0 B80 O1 A4H c1 A6I K2 "s_add_u32 m0, s88, 110080\n" A69 B0q J4 Q4 x2 B8Q y2 Z0 l2 L0 w9 M4 d2 O0 C38 G3 H4 s3
H3 y4 n3 V1 K0 b2 C1 z4 A5 n1 A2b o1 G7 "Lb1_3:\n" M7 B6l B3v c5 A3g a4 A9j A6U A3J A0S b4 s0 j0 c2 y0 A88 A75 B4J b0 C7M q0 r0 A4f m0 A21 A4g B0U L0
A1X B4j b3 e7 q4 B2 f0 C2 A4J l1 X0 k1 A4I C80 R0 C8r M3 I0 o2 T0 A22 f1 F0 W2 D41 G1 G0 K1 A23 z0 g0 I1 K7 B2b B4g Y3 A1R k0 u2 A0C R1 B6h A7B E0B W8
K2 N0 v3 C0 x9 E3 Z0 Z3 B2T W3 O0 d3 L0 A0T A9q E0G A0U B32 J0 m1 c0 B4N C8S A3h C8T C0 A7x z2 K0 p2 u0 u8 S9 y1 S0 V3 B6Z R5 B4a A2c A0x B8o R0 A0y
W5 a0 o0 s0 B0 i5 X8 Z2 x4 C8U O6 m4 Y8 "Lp1_12:\n" E32 C8V l0 v0 Q0 n4 j7 b0 B0 j5 L3 B6j A25 f7 Y0 "s_cbranch_vccnz Lc1_18\n" "Lr1_18:\n" i0 A9B T2
A3U D2 B0V h2 N0 i2 C0 A7k X2 p0 U2 A5Y C8X B2d C8Y C4 D4 k2 J0 j1 C0 A0V K3 G0 f2 A1Y t1 K0 P2 C1 A9C G1 B4U A9n A5Z z0 j0 I1 x8 A4 H1 B5u A5m C8Z
A6M C8a A1D F0 U1 A1r u3 u2 B1 R1 A1s A2X O1 A6Q A6l B6L K2 b5 A0W z9 E4 v2 A6R R3 D0 W6 r3 i3 B4L L2 k5 l5 S3 A6S T3 D0 E0 U3 A0A B8g S0 H2 B0Y Q2
A6T M2 A1E A5a m2 V0 A8A B56 W0 A6N B0k B85 A3J H0 s0 A6O B03 D0 A2f X9 A7l A9E m0 A6P Y0 y0 A4K i0 E9Q h0 a3 A2K k4 i1 A6h s2 r0 z1 T0 v8 w8 E2 X0 Z1
B34 w1 g0 a1 B0 A9F b1 P0 R2 A8G q2 k0 h1 B8z W1 g1 A34 C8R K1 C3s C8s I1 G0 H1 B4 Y9 c4 B0r A3L A93 F9 B37 B2C B2G C36 A9h A78 O4 g2 O0 I3 A79 G8 D9
f3 E0 B5E o3 E8 h4 A3d m3 H0 L6 X3 F8 H8 M6 g3 S0 A14 r2 Q0 A8E S5 A15 e3 T0 V0 A7M F2 A8F A4N A7N t2 N0 A3K C3 c0 A4O A1u A3M D5N e0 N6 W0 A1v w4 C8b
d0 J0 C8c A1O C4P a0 C8d C8e s0 C8f C8g w2 C7R u0 A0 B2h A7l B2i A73 m0 A9K A2d o0 i0 B0 A9L B2J "s_cbranch_vccnz Lc1_19\n" "Lr1_19:\n" O2 B4w p1 D5Y
D5Z a2 l0 N2 L0 D5S D5T X1 Q0 f0 I2 B0 D5U D5V G2 N0 Y1 C0 D5W D5X A2 X0 B3 B2K A9H d1 J0 e1 C0 A9I A9G G1 A8R K1 p1 D5a D5b z0 K0 I1 C1 D5c D5d H1 A1
B5u B0 D5e D5f U1 A9J o4 u2 j0 B80 O1 y0 D5g D5h c1 Q9 K2 R0 A6i J4 M0 Z0 x2 B5H y2 r0 l2 T0 B6Q C8h O0 C8i B7E C8j g0 C8k L0 A4Q p4 d2 B2m e2 B0 A4R
M4 G3 A9O H4 H3 k0 V1 R0 A9P A2L b2 R0 F0 n1 Q0 A2M s3 o1 T0 G0 B3v I8 n3 A8A A2q M0 J8 y8 A0i A7A M0 A2e c0 A1C B8Y z8 A2N C55 A2O A4S m0 A4T B0U A9p
c2 B6d B57 B0y b3 J0 B2 P7 B8Z C2 B3s l1 L0 C7J k1 A6j x1 A1D B6Y s1 A37 A1V A5o o2 A6o f1 D0 B0z A08 B8a A1q B8A B0S A7o I1 A4o H1 B00 S2 A6p U1 A1G
A8B A1y D0 R1 A0n q4 O1 A6q A6l A0o d5 B8b A1Z A0Z E3 A6r Z3 A1E B2l C6c A2a A9q A6k F4 B07 t3 "Lp1_13:\n" E32 R0 q6 K4 z2 Z8 A9 p2 A6m m1 D0 B8m y1
G0 V3 q8 Y3 t0 A6n A5b A4m d0 C4K H0 C6d A0p v0 B4T A5N u0 A8z P5 B2s i0 o0 Z2 B0 A5P A1z A4u A2A l0 A4 P0 "s_cbranch_vccnz Lc1_20\n" "Lr1_20:\n" L0
T2 I0 D2 F0 h2 B3p C0A B3q T5 k2 B3r K3 A7c t1 B3t g1 A81 z0 B73 S2 Q0 g5 A0 U1 B4I I4 R1 B6h E0 B0v A3e U5 K2 T1 B1 A0W A3f A8H v2 N0 R3 C0 A8I A09
i3 Z0 L2 A1A E4 S3 J0 T3 C0 e4 f4 U3 H0 Q2 B6k A3 K0 H2 C1 A4n M2 B0 C77 L0 A3B B5 B4a A3C B9 B8o C9 U6 a0 j0 s0 p1 D5k D5l a3 A7w v0 D5m D5n b0 r0 q0
T0 D5o D5p P5 D5q D5r i0 B2d i1 A2B s2 g0 z1 B0 A7d C5m Z1 O6 D5u D5v w1 k0 a1 Q0 D5w D5x b1 T0 A84 R2 g1 C5E k4 q2 A4X h1 N0 W1 g1 D5y D5z G1 C0 X0
B6W D50 D51 z0 B4R B6I A2r B4g B72 A33 C8n J0 C8o O7 B1E C8p B1 C8q L0 q9 U1 C8 B6U R1 A60 O1 A1D A2s E2U B6V B4h V6 j3 A64 g2 D0 B4i O4 I3 O0 f3 A3F
o3 L0 E0 m3 A6s h4 X3 A65 g3 D0 A6t S4 r2 F0 e3 M0 A2t F2 A66 C8l B6F C3 G0 t0 A70 B4 A2Z E1O A71 A2u U0 A67 I9 A3b A1E B4W A2v A0s b0 A61 A91 A0t i8
P5 A0u j8 i0 A62 w2 D0 v6 A0v A36 A2E h0 f0 A2J A63 D5i X0 "s_cbranch_vccnz Lc1_21\n" "Lr1_21:\n" O2 A1 P0 D3 F3 a2 C33 K0 X1 u0 C34 I0 G2 A2w Y1 o0
A2 L0 F4u B0 F0 d1 A5Q e1 l0 G1 B5z B6g Z4 B73 D8S A3L C8 A38 y6 A0P B53 r9 B8B I4 x2 s9 y2 E0 l2 d8 H4 d2 H0 e2 h5 w6 G3 S0 H3 x6 a4 V1 V0 b2 A5S n1
Q0 e0 o1 B0 B0l b4 n2 L0 B0m t0 A2F A8w B2E A2H A1S U0 a0 A1T C8z C0 B50 b0 A1U T6 B3w Y0 B7G M0 B3x Q3 C1 h0 b3 B0O B2 A5T C2 j0 l1 y0 A5U k1 f9 x1
C39 r0 M3 T0 B51 P0 f1 A7e A08 A34 A5V K1 g0 z0 B0 l3 I1 B77 k0 A3L t6 A1y A0C R1 A39 O1 N0 c1 C0 t9 B8r A1Z B0P E3 J0 Z3 d7 D52 D53 W3 C0 E30 D54 A9q
O0 "Lp1_14:\n" E32 F4 E0 t3 D55 B4S K0 p2 u0 D56 D57 C5S y1 D58 D59 A4A c5 A0c R5 A9j A0d u9 A3J A5W A86 o0 Z2 S7 D6A D6B A75 A0 b0 D6C D6D q0 l0 m0
Q0 D6E D6F B8u j7 i0 D6G D6H C8u g1 L3 C8v A1W C8w "s_cbranch_vccnz Lc1_22\n" "Lr1_22:\n" C8x A7g T2 A4B D2 B2p h2 N0 i2 L0 D89 D9A X2 B4o C0 U2 D9B
D9C k2 J0 j1 L0 D9D D9E C71 h0 f2 C0 C82 C83 t1 K0 B8f C84 C85 P6 C1 f0 l4 L0 C86 C87 r4 f9 s4 L0 C88 C89 e5 d7 P0 g9 A5m A11 C9A C9B A2h j0 g8 y0 C9C
C9D f5 Q9 k7 A2V m5 h8 A05 B4q A2W n5 A4v r0 A4w T0 B0q E4 B6n O0 A4x B8Q B87 w9 B88 g0 Q2 M0 y4 z4 A4y B4M I0 B6o A5 u3 A4z k0 A40 Q0 B6l t0 T0 F0
A5b A3g A0w d0 G0 U0 A1F A3b r3 B03 B8h D69 q0 A88 P5 D6U R9 A0B B0N D6V D6W i1 B5v D6X D6Y C81 D6Z B9E Z1 A8R w1 B9F B9G a1 I0 b1 B9H B9I R2 A4o q2
B9J B9K h1 G0 W1 B9L B9M P6 V4 l4 B9N B9O r4 g1 E9 s4 B9P B27 A0q C9E A0f W8 g8 J0 f5 A9l x9 B6q B4d A41 U8 B6r y9 O4 B6s O0 B6t B9p B6u E0 B6v S9 h4
B6w K0 A42 u0 A7j r2 H0 A43 B0s A44 o0 A45 B0 i5 X8 A46 A1 c5 Y8 S5 A9j E3h j5 A3J E3i B4b l0 w2 Q0 E3j E3k A75 A0 b0 E0y B8 E0z V8 B70 E00 E01 i0
"s_cbranch_vccnz Lc1_23\n" "Lr1_23:\n" B89 E02 E03 D3 J0 a2 C0 E04 E05 N2 p0 X1 E9Y E9Z I2 K0 G2 C1 E06 E07 Y1 A4l A2 E9a E9b B3 j0 d1 f0 e1 X0 P6 F3
l4 M0 P0 r4 I0 s4 F0 e5 G0 B4r Z4 B4s x0 k5 B6x B0W B4p B0X B4t B1F A0 A9D M4 B1G g0 B1H B0 z9 l5 B1I O0 B1J W6 A0A B1K E0 B1L B0t H4 B1M k0 V1 R0 B0u
B1N B9A B1O T0 B6M B1P N0 B1Q C0 B2e A2y A8W a4 D4v e0 I9 E08 b4 C8z E09 E3l A0l E3m E3n T6 E3o D8m A0Q D8n D8o B3y D8p D8q A9t D8r D8s l1 A8R k1 D8t
D8u x1 I0 s1 D8v D8w M3 F0 o2 D8x B9B G0 W2 B2b P6 Y3 l4 B6y e5 T1 g9 D8X S7 B6p B37 B4u B2G B6q B58 A8L o0 A8M B0 E8 F8 A7D Z0 A7E G8 j4 A8N O0 B9D
H8 q6 A7F l0 m1 Q0 B0a A8O A3h A8P B0 B4f A0 "Lp1_15:\n" E32 A7G N0 A7H C0 C4O A2y A1w B0Q A1x W5 S6 Z2 C0 E9p E9q A8Q E3Z A0R E3a B4v E3b E3c i0
"s_cbranch_vccnz Lc1_24\n" "Lr1_24:\n" B0 B2c E0Q E0R D2 j0 h2 E0S E0T B2y E0U E0V B76 E27 E28 C4C E0W E0X f2 C4E E29 E3A g7 P2 A8R P6 A0X l4 A4 r4 I0
s4 F0 e5 G0 B4r T5 A11 A1G x0 I8 B6x A3x A3w B4p B4P B4Q B4t B4q A2L E4 A4v g0 A4w B0 A2M J8 B6n B28 A4x D4U D4V B87 A2N B88 k0 Q2 Q0 A2O D4W A4y A6z
B6o D4X A1c A4z N0 A40 R0 A1d A03 A95 A04 B5 A2G D8V U6 S6 a3 D8W E3B A0l E3C E3D T6 E3E D6I A0Q D6J D6K i1 B5v D6L D6M C81 L0 D6N D6O Z1 P0 w1 D6P
D6Q a1 I0 b1 D6R D6S R2 F0 q2 D6T k4 h1 G0 W1 C8s P6 B4 l4 B6y e5 T1 g9 D8X d7 B6p A3X B4u A0Z B6q B2l A41 o0 B6r B0 A8y B6s Z0 B6t A9 O4 B6u O0 B6v
E0 B6w A4W h4 A42 B0o Q0 A8C A43 A3h A44 D4a A4m A45 N0 A46 C0 B4T A2y a8 B29 b8 w4 S6 w2 A0f E3F E3G A8Q E3H B8 E3I B4v E3J E3K i0
"s_cbranch_vccnz Lc1_25\n" "Lr1_25:\n" A9r B89 E3L E3M D3 j0 a2 E3N E3O N2 p0 X1 E3P E3Q I2 h0 G2 E9T E9U Y1 f0 A2 E3R E3S B3 X0 d1 E9V E9W e1 A8R P6
F3 l4 I0 r4 F0 s4 G0 e5 Z4 g9 F1 e4 A11 A1G x0 f4 B6x A1P B4p o9 B4t B1F A0 A09 M4 B1G g0 B1H B0 A1A a7 B1I B28 B1J B7t B1K E0 B1L p9 H4 B1M k0 C8W
A1Q B1N A6z B1O A83 B1P N0 B1Q R0 A2B A95 E3T a4 d0 G7 "Lb1_4:\n" M7 B7H A10 E3U b4 C8z E0Y E0Z A0l E3V E3W T6 E0a E0b A0Q E0c E0d B3y E0e E0f A9t L0
E0g E0h l1 P0 k1 E0i E0j x1 I0 s1 E0k E0l M3 F0 o2 l3 f1 G0 W2 t6 P6 l4 B6y B27 S7 B6p A6B B4u A3F C9F B6z A2D A8L o0 A8M B0 A2t A7D Z0 A7E O9 j4 A8N
O0 B9D P9 q6 A7F l0 m1 Q0 A0r A8O A2b A8P A7C A7G N0 A7H R0 A8D A95 i8 B0Q j8 W5 S6 Z2 C0 E3X E3Y A8Q E0m A0R E0n B4v E0o E0p i0
"s_cbranch_vccnz Lc1_26\n" "Lr1_26:\n" B0 B2c E0q E0r A0 "Lp1_16:\n" F4v D2 j0 h2 E0s E0t B2y E0u E0v B76 E33 E34 C4C E0w E0x f2 C4E E35 E36 P2 A8R P6
A4 l4 I0 r4 F0 s4 G0 e5 T5 g9 F1 d8 B4s x0 h5 B6x A38 B4p A0P B4t B4q r9 E4 A4v g0 A4w B0 s9 w6 B6n B28 A4x x6 A3G B87 A3H B88 k0 Q2 Q0 A2x A4y A6z
B6o A9s A4z N0 A40 R0 B02 A95 D8Y B5 A2G D8Z U6 S6 a3 D8T D8U A0l E0O E0P B6j D8a D8b C9G D8c D8d i1 p0 s2 D8e D8f z1 h0 E2 L0 D8g D8h Z1 P0 w1 D8i
D8j a1 I0 b1 D8k D8l R2 F0 q2 V4 h1 C9H C9I W1 C9J C9K P6 B9C E9 B6y B27 S7 B6p A9u B4u A4q C9F B6z A3u A3t A41 o0 B6r B0 A7f B6s Z0 B6t u9 O4 B6u O0
B6v E0 B6w A5W h4 A42 B0o C1 A1W A43 D8y H0 A44 A7g A45 j0 A46 R0 A4B c3 y0 c7 B0 A2z B29 r8 B4b r0 w2 T0 F4b F4c v0 C9L C9M b0 E3d B8 g0 C8Q E3e A4r
E3f E3g "s_cbranch_vccnz Lc1_27\n" "Lr1_27:\n" B89 B4w C82 C9N a2 k0 N2 C83 C9O X1 f0 I2 C84 C85 G2 X0 Y1 C86 C87 A2 P0 B3 C88 C89 d1 I0 C6Q C9A C9B
A96 F0 l4 C9C C9D r4 G0 s4 F3 e5 C9P C9Q B4r C9R C9S B4s Z4 B9R T0 A4H f5 o0 k7 B0 v9 h8 Q4 B1F M0 A8J B1G l0 B1H y4 z4 B1I Z0 B1J A5 M4 B1K O0 B1L E0
B1M D8 H4 B1N H0 B1O A2I V1 S0 B1P A0w a4 B1Q M0 B9S B8h C9T A4s b4 U0 A3n C9U a0 R9 C9V B9T D6U C9W c2 D6V C9X v0 R4 b0 D6W D6X T6 D6Y D6Z A0Q A0 B9E
B9F Q3 A1 B3A B9G B9H B2 G0 C2 B9I B9J l1 F0 k1 B9K B9L C80 B9M B9N M3 I0 o2 B9O B9B B9P d5 W2 Y3 C9Y A96 L0 C9Z C9a l4 C9b C9c r4 C9d K4 s4 C9e C9f
B27 P3 C9E W8 g8 Z0 f5 x9 B6q A0T j4 A8L I4 A8M A0U E0z A7D V0 A7E E0y E3k A8N A1 e0 B9D q6 W5 A7F E3j E3i C5S A8O E3h y9 A8P S0 A7G A7i R5 A7H u8 C9g
A9b S9 C9h A9a A1 B6Z C9i A9Z A2c C9j A9Y A0x C9k A9X A0y C9l C9m i5 C9n B6i X8 C9o C9p R4 C9q Y8 j7 C9r L0 j5 C9s p0 C9t E00 E01 C9u
"s_cbranch_vccnz Lc1_28\n" "Lr1_28:\n" A0 T2 A0 E02 E03 D2 C9v h2 E04 A6X i2 A9i X2 E05 E9Y U2 A0 C9w A0 E9Z C9x j1 h0 A9z E06 B9U f2 A0 "Lp1_17:\n"
F4v f0 C9y A47 E07 B9V P2 x0 C9z A0 E9a C90 B4q A0j A4v A0 C91 C92 A4w A8S C93 E9b C94 C95 A0 A1r C96 C97 c0 C98 C99 A4x D0A A0 A8T D0B A0 D0C D0D B9W
A1s A2X B9X A0 D0E A4y M0 A8U B6b D0F B6c A4z D0G D0H A40 E3o E3n D0I g1 D0J D0K A9g A1 E3m E3l A9f g1 D0L D0M A9e L0 A8V A9d L0 D6a D6b A9c A1 E09
E08 D0N g1 D0O D0P D0Q A1 k5 l5 A2h g1 W6 A0A g8 u6 E0 f5 R0 B0Y D0R k7 L0 A5a h8 E9f D0S D0T D0U D0V L0 B56 B6e D0W s2 A0 D0X D0Y B9Y D0Z Q4 z1 A0
B85 E2 R0 D0a D0b Z1 B9Z D0c D0d a1 D8m D8n B2O D8o D8p R2 A05 D0e D8q D0f D0g h1 x0 W1 O0 D8r D0h M0 D8s B9a A41 c0 D8t B9b H0 D0i S0 B9c B2M D0j B0w
D0k D0l D0m D8u D8v D0n D8w D0o M0 D8x D0p B9d D6c D6d D0q "ds_read_b64_tr_b16 a[48:49], v217 offset:38016\n"
"ds_read_b64_tr_b16 a[50:51], v217 offset:38528\n" A42 "ds_read_b64_tr_b16 a[52:53], v217 offset:42240\n"
"ds_read_b64_tr_b16 a[54:55], v217 offset:42752\n" D0r u6 D0s A43 A1 "ds_read_b64_tr_b16 a[60:61], v217 offset:42272\n"
"ds_read_b64_tr_b16 a[62:63], v217 offset:42784\n" A44 A1 "ds_read_b64_tr_b16 a[56:57], v217 offset:38048\n" D0t D0u D0v A8S
"ds_read_b64_tr_b16 a[58:59], v217 offset:38560\n" D0w D0x A45 A0 "ds_read_b64_tr_b16 a[64:65], v217 offset:38080\n"
"ds_read_b64_tr_b16 a[66:67], v217 offset:38592\n" A46 "s_cbranch_vccnz Lc1_29\n" "Lr1_29:\n" "ds_read_b64_tr_b16 a[68:69], v217 offset:42304\n"
"ds_read_b64_tr_b16 a[70:71], v217 offset:42816\n" D0y A1 D0z D00 D01 "ds_read_b64_tr_b16 a[72:73], v217 offset:38112\n"
"ds_read_b64_tr_b16 a[74:75], v217 offset:38624\n" A8T D02 A1 "ds_read_b64_tr_b16 a[76:77], v217 offset:42336\n"
"ds_read_b64_tr_b16 a[78:79], v217 offset:42848\n" D03 D04 D05 "ds_read_b64_tr_b16 a[80:81], v217 offset:46464\n"
"ds_read_b64_tr_b16 a[82:83], v217 offset:46976\n" "ds_read_b64_tr_b16 a[84:85], v217 offset:50688\n"
"ds_read_b64_tr_b16 a[86:87], v217 offset:51200\n" D06 "ds_read_b64_tr_b16 a[92:93], v217 offset:50720\n"
"ds_read_b64_tr_b16 a[94:95], v217 offset:51232\n" "ds_read_b64_tr_b16 a[88:89], v217 offset:46496\n"
"ds_read_b64_tr_b16 a[90:91], v217 offset:47008\n" D07 B8E D08 D09 A0 E9r A6e D1A B9e E9s D1B A8U D1C E9t E9u D1D D1E D1F D1G R0 E9v E9w D1H E9x E9y
D1I D1J A9s A8V D1K D1L D1M A0 A1S D1N D6e D1O D6f D1P A1T D1Q D1R D1S D1T D1U D8a D8b D1V A8X C2 R0 D8c D1W D8d B2 A0 D8e D1X D1Y D8f A8Y s1 A8Z D1Z
D8g D8h D1a D8i D1b D8j A08 A8a D1c D8k D8l D1d B9f D1e D6g D1f D6h D1g B9g D1h B9h D1i B9i D6i D1j A8X D1k D6j A8Y D1l A8Z D1m A8a D6k D6l D1n
"Lend1:\n" ".fill (Lv1+73728-.)/4, 4, 0xBF800000\n" "Lp1_18:\n" F4w ".fill (Lv1+77824-.)/4, 4, 0xBF800000\n" "Lp1_19:\n" F4w "Lc1_0:\n" B61 A7I D1o
A8b B1S D6m "s_branch Lr1_0\n" "Lc1_1:\n" B62 A48 W4 A7I L4 "s_branch Lr1_1\n" "Lc1_2:\n" B9j B4x N3 A48 d4 "s_branch Lr1_2\n" "Lc1_3:\n" B9k D14 L4
"s_branch Lr1_3\n" "Lc1_4:\n" D1p B1T N3 B3D d4 "s_branch Lr1_4\n" "Lc1_5:\n" r1 D1q E3q W4 E9c p5 A7O B9l E3p A8o B9l E1A B3B "s_branch Lr1_5\n"
"Lc1_6:\n" v1 D1r E1B N3 E3q r6 A49 A8p A7O A3j "s_branch Lr1_6\n" "Lc1_7:\n" r1 D1s D8z W4 E1B p5 A3i A8q A49 A3o "s_branch Lr1_7\n" "Lc1_8:\n" v1
D1t D6n N3 D8z r6 A3i A3j "s_branch Lr1_8\n" "Lc1_9:\n" r1 D1u D1x W4 D6n L4 "s_branch Lr1_9\n" "Lc1_10:\n" v1 D1v B9m N3 D1x d4 "s_branch Lr1_10\n"
"Lc1_11:\n" r1 D1w B63 W4 B9m L4 "s_branch Lr1_11\n" "Lc1_12:\n" v1 D1y B4y N3 B63 d4 "s_branch Lr1_12\n" "Lc1_13:\n" r1 D1z B3C W4 B4y L4
"s_branch Lr1_13\n" "Lc1_14:\n" v1 D10 B1R N3 B3C d4 "s_branch Lr1_14\n" "Lc1_15:\n" r1 D11 A97 W4 B1R L4 "s_branch Lr1_15\n" "Lc1_16:\n" v1 D12 A8b
N3 A97 d4 "s_branch Lr1_16\n" "Lc1_17:\n" D13 D15 L4 "s_branch Lr1_17\n" "Lc1_18:\n" v1 D6o A8c N3 A98 d4 "s_branch Lr1_18\n" "Lc1_19:\n" r1 D6p D16
L4 "s_branch Lr1_19\n" "Lc1_20:\n" v1 D80 A5A N3 A7J d4 "s_branch Lr1_20\n" "Lc1_21:\n" r1 D81 D17 L4 "s_branch Lr1_21\n" "Lc1_22:\n" v1 E1C D18 d4
"s_branch Lr1_22\n" "Lc1_23:\n" r1 E1D D19 L4 "s_branch Lr1_23\n" "Lc1_24:\n" v1 E3r D2A "s_branch Lr1_24\n" "Lc1_25:\n" r1 E3s D2B
"s_branch Lr1_25\n" "Lc1_26:\n" v1 E9d D2C "s_branch Lr1_26\n" "Lc1_27:\n" r1 E9e D2D "s_branch Lr1_27\n" "Lc1_28:\n" v1 F4i D2E "s_branch Lr1_28\n"
"Lc1_29:\n" r1 F4j D2F "s_branch Lr1_29\n" ".fill (Lv0+196608-.)/4, 4, 0xBF800000\n" "Lv2:\n" "Lb2_0:\n" C0m C0n C0o C0p C0q C0r E9z A0N C0s C0t g7 G7
"Lb2_1:\n" M7 "s_sleep 20\n" B7b C0u "s_mov_b32 s54, Lend2-Lv2\n" C0v C0w C0x B7c C0y C0z C00 C01 C02 B5Z C03 C04 B7d C05 C06 C07 C08 C09 C1A C1B C1C
C1D p8 C1H C1E C1I C1F C1G C1K C1J B7e p8 C1c C1O C1T C1P C1Q C1L C1i C1Z C1b C1U A5I C1l C1m C1R C1S C1M C1N p8 D6q C1V C1Y D6r C1e
"s_mov_b32 s92, 13312\n" C1f D6s C1a C1d C1g C1h C1j C1k C1n C1o A5J "s_addk_i32 s92, -11264\n" C1q C1p C1r C1s C1t C1u C1v C1w A5J C1x C1y C1z C10
C11 C12 C13 C15 C14 C16 C17 C18 C19 C2A C1W C1X B7f C2C C2B D6t D6u D6v D2G C2D P3 B7g E9z B7h B7i B7j B5a B7k B5b B1r B5c B1s C2E C2F C2G E1 B5d A5K
D35 C2H D36 P3 C2I C2J B3f S8 A1N C2K A8t C2L R6 C2M E1 C2N D37 C2O D38 B1t N0 C2P A2p C2Q C2R C2S C2T C2U B1u C2V T1 A7V B1t A2p C2W C2X C2Y C2Z E1
A8t A3s D39 C2a C2b C2c C2d B1u B1v B5k C2e C2f C2h C2g C2i A5L A3t A3v R6 A3y A3w A3x A3u C2j C2k C2m C2l K0 C2n A7W j0 A7X D4A D4B C2o B0 D4C D4D
B3i A0 g0 B1r B1s C2p E1 A7V D4E D7N B0 D4F B3j U0 Q0 A3s B5e D4H W0 C2v B5f B1w B5g C2s A1N C2t C2q C2r S8 z7 B7l C2u B3k A8u B5h E1P q0 B5i E1Q J3
D4G m0 K6 B9o h3 B9n B5k C2w C2x C2z C20 E37 B3l E2T D4I B5j B1f Y0 J3 B5k A0O B1v C22 C21 J0 h3 B7m C23 C24 C25 C26 B5m C29 B1x B5l C27 B1y B5n C28
B1z K0 J3 B10 u0 K6 C3A h3 B11 J3 C3B B12 F1 B3m C3E C3C C3D C3F A5M B13 P3 B5r C3G B14 x0 C3I B5o B15 B5p C3H B16 B5q C3J B17 A0 "Lp2_1:\n" E90 B5s
B18 l0 p0 A4u v4 F3 c0 t0 C3K C3L A8w l9 C3M C3N A1O P0 A8x I0 S6 B1U C0 Z4 v0 C3O C3P A5N C1 B1 m0 A8 Y0 X5 B5t A7Y w0 B1x y0 H0 B1y B0L B1z r0 B10
D7Q V0 B11 x0 T0 e0 B12 A0 A19 V1 C3Q B0 A3X B13 A5O B14 D2H B15 A29 B16 T8 B17 k0 B18 Q0 A8y p0 A4u T0 l3 R0 t0 Z0 W0 A7Z B7o O0 U0 E0 a0 P0 B1 s0
B7p B3o A30 S8 m1 t6 v0 A0p b0 A8C q0 C3R m0 m9 C3S Y0 n9 i0 J0 C3T Z2 C0 H7 C3U J7 A8z B2s C4Q A2A A5P A1z "s_cbranch_vccnz Lc2_0\n" "Lr2_0:\n" B2H
C3W o0 D2 A4l C3X B3p f0 C3Y B3q l0 C3Z Q0 B3r A3U C3a g5 A7c N0 C3b C0 B3t p0 A81 J0 z0 C0 B0M A82 B5u S7 F0 B19 A1P R1 F4x C1 o9 c1 Q9 b7 A31 C3d j0
v2 y0 Z0 C3e B0 a7 B7r C3f e4 u3 B2A C3g f4 B7s r0 Q2 T0 B7t C3i C3j p9 r3 C3k g0 m2 M0 A1Q C8t A2q A83 B7u A2B b0 k0 D2i A7d m0 B2I Y0 A84 i0 N0 B0N
A32 C3m C0 A5E f0 C3n A33 E2 J0 C3o A2r w1 X0 C3p q9 b1 P0 C3q L0 B7v I0 C3r A2s W1 F0 G1 B7w B6g B5w E9 H1 A1R C3t B5x C3u B5y B1 b7 A2D C3v o0 g2 A8
C3w O9 C3x C3y A2t B7y C3z P9 B7z B0o Q0 A0r C30 A2b F2 A7C C31 N0 A24 A8D A95 i8 B29 j8 w4 S6 w2 B0R g4 A0u b0 v6 B8 A0v B4v A36 i0
"s_cbranch_vccnz Lc2_1\n" "Lr2_1:\n" B0 O2 R0 A2E D3 j0 a2 C33 p0 X1 C34 h0 G2 A2w Y1 f0 B71 X0 d1 A5Q e1 B52 B5z B0S F3 I1 B77 A0 Z4 A85 C8 y0 A38
B8q A7B A0P B6R b7 B2n J4 B6X x2 O0 y2 w6 M4 l2 E0 C37 H4 e2 g0 G3 B0 h5 x6 H3 H0 V1 A5S b2 k0 n1 Q0 A2x o1 T0 S0 n2 L0 A2F a4 B4a A2H A1S B8o A1T b4
D2J C0 c2 B50 B6S q0 A1U A4h m0 B8R B2a A9A p0 Q3 A4i b3 h0 B2 A5T C2 f0 l1 A5U k1 P0 x1 C39 I0 M3 B51 F0 f1 B3J "Lp2_2:\n" E90 B3K A08 B0Z A5V K1 l3
z0 t6 B2D C3t A39 R1 C3u t9 B74 b7 B0P v3 o0 E3 A8 Z3 u9 j4 W3 O0 d3 A7f B31 A5W B32 l0 m1 C4A A1W z2 A2b p2 A7g y1 N0 A4A A4B A95 A2z B0Q r8 W5 S6 Z2
B0R g4 s8 b0 A4C A0R A4D E25 E91 A87 i0 "s_cbranch_vccnz Lc2_2\n" "Lr2_2:\n" A0N C4B t8 D2 C4F h2 j2 A4E B2y A4F U2 C4G k2 j2 A20 j1 h0 K3 C4D B78 t1
j2 A4G P2 B06 G1 B33 D4J z0 D4K X0 I1 A4 H1 P0 S2 C4H A3L I0 A1y j2 R1 A4H O1 B79 c1 j2 v9 B8r A0W A8J v2 C4J R3 j2 A5 u3 i3 Z0 L2 y4 E4 S3 A5I T3 C1
X5 U3 C4K z4 B8g F0 H2 B6l Q2 j0 M2 y0 A3g A0w m2 B3N C8t B0T r3 W0 E1O B60 E38 V0 E39 B8s b0 r0 q0 T0 A5X B1B P5 B4c B8t i0 g0 B0N B8v e7 i1 A5E f0
z1 A4I E2 X0 Z1 A4J w1 P0 a1 A22 b1 I0 B2q B54 F0 h1 A23 W1 B0Z B55 V4 z0 E9 B2D B01 C8 W8 y6 x9 B53 A0T O4 j3 I4 g2 A0U I3 E0 f3 K7 y9 h4 o3 H0 m3
B9p X3 S0 g3 k0 r2 Q0 S9 S5 e3 T0 V0 F2 A7j t2 N0 A24 B0s D5N A5C i5 w4 d0 J0 A1O C8K i7 j5 w2 C7R P3 D7R D7S b0 u0 f0 q0 D7T D7U m0 A73 Y0 A25 L3 i0
A5M f7 "s_cbranch_vccnz Lc2_3\n" "Lr2_3:\n" L0 O2 A9B B9w B0V N2 l0 X1 A5d A7k I2 p0 G2 A1 A5Y Y1 D61 A2 j2 A0V B3 h0 d1 A1Y e1 D62 G1 L0 A9C K1 j2 P0
z0 A5Z I1 D63 H1 A1I D7V o4 S2 j2 I0 B19 A1r p4 R1 A5K O1 Q0 A1s A2X c1 E9l B1 K2 A0 B6L J4 A05 x2 D7W M4 y2 O0 l2 D7X D7Y d2 E0 e2 z9 H4 G3 N0 H3 C0
k5 s3 V1 F0 b2 l5 W6 n1 J0 o1 C0 B8i n2 L0 G0 t0 B9Q n3 W0 E1O A5a U0 S0 I9 B2S B8Y A0 B56 A5N C4M B85 m0 f9 Y0 A2f E1L B5t c2 C4N D68 B5G Q3 g1 p0 b3
B0 B7D B2 r0 C2 T0 B34 l1 h0 k1 w8 v8 x1 g0 B8j A8G M3 B0O o2 A9F f1 k0 W2 C8R G1 B6a B8z z0 F0 A1B l3 E9P q5 A85 A0 "Lp2_3:\n" E90 t6 F9 B37 B8N B2G
B8D A1Z B58 E3 E0 Z3 G8 q6 W3 B1 d3 R0 E8 A9q H0 F4 F8 H8 t3 S0 m1 A8E R5 z2 V0 p2 A8F A4N y1 e0 V3 A4O W5 t0 A1u A5b A1v d0 A1x U0 A1w i7 x4 Z2 x4 v0
R4 b0 A0C A0R L3 P5 "s_cbranch_vccnz Lc2_4\n" "Lr2_4:\n" i0 N0 T2 C0 h0 E92 q5 i2 E1 A9H m5 X2 J0 U2 C0 A9I n5 B7p T5 A9G C4Q A9J p1 D7Z D7b D7c D7a
c0 E1E E1 P1 o0 N1 x8 A00 A5e D7d D0 A0 B6A A0 D7e D1 A9K D7f J1 A5f J2 D0 A9L J3 V2 B2J K6 k2 B2K h3 j1 A5g K3 D0 D4L J3 f2 D4M A0O t1 B0b h3 B8f C4R
C4S u1 B0c C4T a0 A5h B4A C4U C4b d0 L7 D7j D7g W0 D7k D7h z7 B0d J3 p3 A5i Y0 D0 B0e K6 m0 h3 D7i q0 A9M J3 b0 A5j i4 D0 A9N A0O N4 h3 Y5 v2 C4Z C4W
R3 A5k i3 w0 N7 C4X L2 D0 C4Y k3 S3 Y2 C4a B38 X5 A5l F4y B64 t4 D7l D7m S1 D7n Q1 D2j N1 y3 P1 I8 M1 p0 A4u U4 A6L A1a B9u Q2 e0 A0D x3 A4Q B2L C4g
A4R A7n C4h C4i A1b T3 A9O k3 U3 A9P Y2 a3 B1 A3 c0 A9o H2 Z0 M2 X5 m2 Y6 A0E A0F J8 Q1 B1V y3 l9 y8 B9v c0 A1F w0 A3b A0 A5n z6 z8 A2N A0Y l0 v0 Q0
A2O A4S b0 A02 q0 A4T A1c m0 B70 A1d A03 i0 B2d B6e A04 k4 s2 J0 z1 B0z E2 B3s Z1 R0 A5o w1 X0 O3 F0 P1 D2k N1 V6 B4 q1 P0 S1 I0 w3 K0 z3 A0 B39 S4 L1
"s_addk_i32 s92, -16384\n" A28 C4V D1 A2P Y2 J1 E0 J2 B0g V2 X4 a1 A0 A4U B3E b1 A1 Y5 Q1 R2 D4N P4 q2 D4O q3 h1 H0 W1 L0 B3F N7 D2W O6 E7 k3 B4A A0k
F7 M1 W0 B1 z7 A9S B6B p3 b5 Y0 A9U A06 A5D q0 A8B A7K D85 j3 R4 g2 O0 I3 e0 f3 w0 A9T D4P o3 D4Q B6D m3 P0 t4 I0 S1 F0 D2j N1 G0 P1 B2R U4 F1 A2a r2
B7 Z8 x3 A8r A0n k3 J1 A0o Y2 D1 j0 L1 y0 A0Z G4 Q9 B08 B2l X3 Z0 g3 A9 T9 e3 B4L F2 E0 t2 U9 Q1 C3 A4W P4 Z6 r0 t0 w0 A3z q3 W0 T0 q8 M1 d0 H0 C6d
B4B v0 A4m b0 g0 q0 H7 P5 J7 A0B A4V A0 a8 o4 C4p A1H C4q A7x B0h A9V L3 b8 X0 P0 "s_cbranch_vccnz Lc2_5\n" "Lr2_5:\n" O2 M0 I0 D3 F0 a2 G0 N2 p4 s3
E8b n3 a6 E8c A7r D5 T1 A7p C4r A0M m4 C4s n4 X1 A7q I2 A7s G2 Y1 J9 D2Q A3A J1 B4C h9 M0 A1z C4t X6 C4u C4 D4 E8d Z0 V9 Y2 A5F A0 "Lp2_4:\n" E4A E8e
D4R Q1 E5 O0 C7x E0 d1 D4S A5G e1 A3h C4w y3 C4x M1 E8f S0 b6 V0 E8g e0 F5 A0m u1 A5x a0 A5y U0 A7t d0 A7u W0 D9F Y0 A02 E1R k0 J4 O7 B0A x2 y2 q5 l2
A5p E8n A94 W9 F0 E8o G0 G5 A5q V1 E1 i9 d2 D0 e4 e2 f4 G3 A5r H3 D0 a7 t4 A1e S1 q1 A5s N1 A9W A68 A12 A1I x0 A0 c2 A5t x3 D0 A3e B1W A3f A7n A5u D2Z
A8H k3 Q6 A8I Y2 E8p b5 c6 A09 k8 E8q A5v H5 A1A Q1 b2 B90 n1 E0 o1 D2S n2 A7v q3 A0J A5w E8r Q0 p9 M1 d6 E3v H0 E8s A1Q C0B c5 D2T A3k U0 B9 A3D D2J
C0 C9 A3E c8 A1f A4b g4 h0 b0 A1g A4c q0 J0 m0 B84 C9G p0 Q3 K0 b3 l3 B2 q5 C2 I0 E8u F0 A4Y G0 E8v t6 D7A A54 l1 A55 k1 A4Z x1 A4a s1 J9 D2U A2R J1
B6E h9 A32 E8w D2V C5R E8y q9 B65 u0 C7y O0 f1 E0 W2 B0C E8z o0 e6 B0 y3 E81 A58 M1 A8f H0 u1 l0 a0 Q0 A7A g5 A0k A5C A56 B3O c3 N0 p3 C0 A57 B3P Y0
A59 A5D q0 B2T B6S i4 A0z v3 A1H E3 X0 Z3 B0E W3 P0 E87 I0 f6 F0 E88 G0 I5 j9 D2X F4 C0 t3 B1g q1 K0 N1 B7 A8g Z2 j0 x3 A7w J2 B2N J1 A7 D1 r0 L1 x0
T0 Q6 A6B C5M Q4 C5N A7x E89 g0 Z9 B0 A3F E9A Z0 J5 A6s l8 z2 O0 p2 E0 y1 A6t r5 V3 k0 s5 M0 A3Z Q1 C5O B9A C5P A1I B2w E9C T0 y3 g6 A3a M1 E9D N0 K5
C0 B6F A2y R0 A70 K9 D4v e0 X9 a0 A71 L9 Y9 S6 A1i L8 C0 A7C A1j v0 p0 A2S b0 A0s B1X A2T q0 A0t B0D D9 m0 i8 L3 B5E Y0 K0 A3d A9A j8 B2V B2W C5Q
"s_cbranch_vccnz Lc2_6\n" "Lr2_6:\n" D4 L6 T2 u0 h0 M6 D2 o0 A14 h2 B0O A15 i2 X0 A7M l7 l0 A7N t5 E1 P0 A3K C5b Q0 I0 A3M h6 B0 F0 N6 L5 B1Y m5 C5T
X2 N0 C5U U2 D2k C5V C4 n5 k2 B1Z u3 j1 J0 O3 C0 B2t r3 P1 B2u N1 q1 K0 S1 P7 w3 c0 C5a z3 B68 A6A D2l v6 A0v B3Q A36 V2 A6C m7 D0 A2E k3 u5 B1 i6 B9y
Y2 M5 A6D K3 D0 C72 Z0 t1 B9z A01 P2 A6E n7 L7 O0 v5 E0 j6 B0f Q1 D2m D2n A6F a0 D0 B42 B9v D2o A6G c3 D0 V0 Y4 e0 Y0 A0D m0 A6H A91 p0 A7K D85 N4 A6I
v2 C1 X0 R3 F4r F4z "Lp2_5:\n" E4A F40 i3 B43 B44 B1a j0 A16 B45 A2U P0 N5 I0 C3h F0 L2 G0 S3 A1b T3 d8 U3 h5 t4 w6 S1 x6 q1 A69 N1 A8g B3R A0 A8r A7
J1 E0F B1b x0 L1 y0 A38 G4 A8 D2p B2Y Y6 w5 r0 k6 T0 B2Z r9 O5 A1t A3 s9 Q1 H2 E0 M2 B91 m2 A3G q3 z6 g0 o7 A3H M1 x5 H0 l6 A2x B1h c5 D2T A5n U0 A2F
A8i a0 A2H A8j s0 A1S A4f A0Y A1T A4g A0l I7 q0 "s_mov_b32 s92, 131072\n" h0 A89 C9G V4 i1 X4 s2 z1 E2 q5 D2q C0C E9 Z1 B46 w1 B47 a1 B48 b1 B49 O3
A0M E7 P1 F7 A5H B5A L1 A1U D1 A4h J1 D9G h9 B3x p7 X6 N9 B5J A0H Z0 R7 A72 R2 O0 q2 B0n h1 E0 W1 B0 B5K q7 y3 y5 M1 D2r C0D D7B B5C B6B B3j U0 Q0 A0k
T0 A5C B5D B6D c3 N0 p3 C0 A0 B3J Y0 B3K A89 q0 J0 E4B I7 C5n A0 Z5 D4Q C5o h0 j3 a5 D4P g2 P0 I3 B7I C0E C5q A7X m4 C5r n4 D2s A0 A8r A7 A7P C0F x0
G4 E1 A8k T9 C5s O0 C5t C4 D4 A2j E0 m8 A8l U9 m6 A3y Q1 Q5 H0 e3 A3v A2k F2 y3 t2 A2l M1 C3 S0 Z6 R0 A0c O8 C5u V0 C5v A0d u9 r7 e0 z5 A5W P8 n6 B69
X9 A1k B6J Y9 B8W A74 A1i W0 A0e A1j d0 A1l A2S U0 B0p A2T a0 A2z D9 s0 r8 B5E A4V A0 A3d B2V C8B A0 B0h B2W q0 F41 B2v "s_cbranch_vccnz Lc2_7\n"
"Lr2_7:\n" o4 E1 O2 f0 B9w P0 N2 I0 A1L F0 a6 x8 G0 A6 p4 s3 D5 B2z n3 X1 L6 I2 A5e B8P B20 Y1 M6 O3 B21 A14 P1 A5f N1 D0 B22 q1 A15 S1 A7M w3 A5g z3
D0 s8 A7N L1 A4C A3K D1 A4D A5B A5h J2 L7 A87 V2 B1 s7 t8 N7 V9 A5i n8 D0 B4k k3 E5 Z0 A2 C5w A5j C47 B4l Y2 e1 O0 t7 E0 b6 A5k B6 D0 A99 F5 c0 C5 u1
u4 a0 A5l U0 D2o E0L B9S A0 e0 Y4 A0m B3S A3M m0 B23 N6 C5z q0 K0 C50 b0 u0 D2t J4 R4 x2 B24 D7C y2 o0 l2 B0 B25 D7D A2m P0 W9 B26 C6 l0 G5 I0 V1 c0
F0 d2 G0 e2 i9 G3 y4 H3 z4 t4 A5 S1 D8 B7J A2V B1W A2W B5L D2u v9 A17 b5 c6 A7h k8 D6 O0 H5 M0 w9 Q1 b2 E0 n1 D2S o1 u4 n2 A2I A0J H0 u7 A6U d6 S0 E6
V0 h7 Q0 e0 A0F "Lp2_6:\n" E4A A3k B7F A0S d0 A4s U0 N0 B8c A3n s0 R9 c8 v0 J0 C60 q0 A0z m0 B8R B7G C1 p0 Q3 B0 B3A l3 B2 B2T C2 j0 A1M y0 D2v B0 I0
E4C A1h w0 E93 A9v B3H r0 x1 A9w Y3 s1 A9x O3 A9y P1 A5H D2w B1B D1 "s_mov_b32 s92, 778240\n" B4c J1 B8t h9 M0 B8v e7 C7z X4 C70 A7x v7 D2V Q1 F6 B04
P4 B65 D4z A3p o2 O0 f1 D40 B05 W2 M0 E0 C73 O6 m4 B5M C74 H0 w7 n4 q3 e6 S0 G6 M1 B3O A0a V0 u1 A10 A8m B3P U0 A8n d0 B1c W0 B1d E4D T1 B0E C75 A0 C4
C76 q5 v3 D4 E3 T0 I0 Z3 F0 W3 G0 A2n j9 f6 g0 H6 K7 I5 D2x C78 k0 C79 A0C E1S F4 N0 t3 B7K A2Q B8w A5B B1b B8x D2Y A7 Q6 x9 B1e b5 Z9 A0T l8 I6 O0 J5
A0U Q1 z2 E0 p2 y9 r5 y1 H0 V3 A7i P4 s5 S0 x7 u8 q3 g6 M1 K9 J6 B7L K5 R0 C0G A2c A0x C9T A0y L9 U0 E1G E1H a0 E1I E1J s0 i5 B7M X8 g4 Y8 b0 j5 B7N
"s_cbranch_vccnz Lc2_8\n" "Lr2_8:\n" M0 T2 D2y P0 l7 I0 t5 F0 h6 G0 L5 A4 X2 B0F C0H B0G D2z B1i j1 B1j U4 A8h A00 x4 z3 A0 B5N f7 Y5 D1 B1 J1 A9B J2
X6 V2 B0V m7 Z0 u5 Y2 A01 i6 O0 M5 M0 A0V K3 E0 f2 A1Y t1 O7 B0f C4c P2 C4d C4e n7 C4f Q1 v5 y3 j6 A5p Q7 D0 A8d M1 u1 A1F T1 A7A A5q B0k B4Z e0 z7
A0D p3 A5r Y0 D0 A8e A5D B2o b0 A5s i4 A9W f0 N4 R4 v2 A1a R3 A5t i3 B4e B1a A5Z A16 I0 A2U A5u N5 D2g Q2 G0 L2 A1b S3 A5v T3 D0 k5 U3 l5 w3 W6 S1 A5w
q1 A0A N1 "s_add_u32 m0, s88, 8576\n" B8J A8g B3R A1r B2L J0 J1 C0 A1s B5L B0x B1 G4 A5m A1J B0X w5 K0 k6 u0 B6c O5 A05 A3 O0 H2 E0 M2 Y6 A0E m2 B0t
Q1 z6 o0 o7 y3 x5 B9Q M1 l6 H0 a9 A5a A0F c7 A0k A5c A5n a0 D7o D7p s0 D7q D7r A0Y B1C A3D v0 B4n A3E E3x q0 B2F A4b m0 B8R A8W V6 i0 p0 i1 A5E k4 A4c
D2a D2b G0 A0b E9 B3I A76 Z1 A77 w1 v8 a1 w8 b1 B0 U4 A8h q1 l0 S1 v4 A13 A2f L1 N0 D1 C0 A9E B3Q A4K V2 J0 p7 C0 A2K k3 N9 B1 A0H E4E Y2 "Lp2_7:\n"
E4A R7 K0 R2 P3 C4k q2 C1 Z0 h1 B0 A8G W1 j0 q7 B2g A06 y5 O0 b9 E0 A3I M0 A9T C4l g7 C8C c0 Z5 C4m C8D a5 C4n u1 C4o Q1 a0 y3 U0 A26 M1 d0 A2q S0 c3
w0 V0 C8E T1 e0 C8F A1K p3 p0 Y0 h0 X9 m0 A0X Y9 q0 y0 f0 A1i b0 f9 A1j i4 A0 A27 D4P A2S j3 r0 A2T g2 T0 A4L D4Q D9 I3 P0 B5E f3 p1 B2w A3d A3m g0 L6
A0K B0 A4M M6 A07 I0 A14 c9 k0 A15 T1 C8H Q0 D4 B93 A7M r2 T0 F0 A7N o3 N0 A3K m3 D2k A3M X3 B94 N6 C8I g3 E1 C8J C8L C4 C8M J0 w3 C0 S1 B5O K0 P1 P7
U4 E1 F1 C8N F1 w2 A6A x3 x0 D0 J2 A4k A5B B1b B7O D2Y A6C G4 D0 C0I k3 A2j A6d Y2 m8 Z0 m6 A78 T9 Q5 A6D e3 D0 A79 Q1 F2 O0 t2 E0 C3 E8 U9 r7 A6E z5
L7 F8 P4 Z6 u4 n6 G8 D7s C0J A3Y H8 D7t W0 A6F B0k A8E D7u C6d A8F B4B v0 A6G A4P A4N D7v T6 A4O A1u Y0 A6H C8m A1v A1w A4V A0z C4y A0 B2K A1x C4z A6I
C40 "s_add_u32 m0, s88, 67584\n" B2J B0h C41 A9L A9V C42 A9K L3 C43 A5M P0 "s_cbranch_vccnz Lc2_9\n" "Lr2_9:\n" B4V I0 D3 B2i B2h a2 l0 N2 Q0 F0 A1L
B0 G0 a6 f0 A6 A9G s3 D5 N0 X1 X0 I2 A9H C0K F3 Y1 A9I C44 s7 C45 C46 V9 A9J C48 n8 B2N E5 D4L k3 A2 M0 B1 B3 D4M Y2 d1 Z0 e1 B0b E8h t7 O0 b6 B0c C5W
B6 E0 F5 B0d E8i D7w B0e C5X D7x A9M C5Y D7y A9N C5Z D7z B8L D70 D71 x4 D72 F1 A9X A0 V0 A9Y e0 A9Z H0 A9a S0 A9b A0 E8k E8j A5z E1 B4J A50 p0 A51 h0
A52 f0 A53 X0 C49 A0 E8l E8m J4 D77 D78 x2 C0 D73 D74 V1 D75 D76 y2 P0 l2 I0 A2m J0 W9 C0 F0 C6 G0 G5 E2Z E2a c2 I8 D79 d2 K0 e2 E0F J8 D8A G3 x0 H3
C1 A9u c8 B0 B8n A17 A4q c6 G7 "Lb2_2:\n" M7 D6 A9o H5 j0 b2 A7w y8 n1 z8 o1 p1 E7 n2 r0 u7 T0 F7 d6 A2N E6 A2O h7 L0 A4S D8B g0 H1 B0 A4T I1 A1c z0
A1d K1 k0 C5A R0 Q0 A03 C8S T0 Z5 C8T Q4 D8C a5 c1 N0 B7q A04 R1 Z0 u2 O0 D8D w0 E0 C8U "s_waitcnt vmcnt(10) lgkmcnt(1)\n" m4 E2b C8V J0 C5B C0 n4 A4U
C5C S0 C5D E1F D64 C5F K0 C5G u0 A2P E2c C5H V0 Q3 e0 b3 R0 A28 E8t C8X A3h C8Y C4 D4 B2 o0 C2 B39 E8x A1M C5K C5L A4Y C5I C5J A0L x8 A9Q D8E A1h A6M
l1 D0 B5P D8F k1 p0 x1 B5Q D8H s1 A6N v7 D0 A9R D8J A0G M0 h0 C8Z f0 C8a A6O F6 D0 B3E E80 K8 X0 M3 D4N E82 o2 A6P f1 B4K D4O B3F W2 B8y w7 B0r e6 A6Q
G6 D0 B2P A2S A0a A9S A2T D8K A9U D9 D8L A6R D8M D0 B2Q B5E D8N L6 D8O A3d D8P A0 "Lp2_8:\n" E4F A6S D8Q D0 M6 A9c A14 A9d A0 G0 A9e A6T A9f P0 A9g A0
I0 B9t F0 J2 E83 E84 A7P C0F O0 G4 E1 E0 A1Z E85 E86 E3 A0 C5 m1 u4 Z3 H0 W3 S0 A2n V0 f6 e0 H6 B0j I5 x0 Z2 "s_add_u32 m0, s88, 122496\n" A3X d3 l0
T4 Q0 A0Z F4 g5 A15 t3 B2l L8 N0 B1e C0 A8y Z9 A7M A0 E9B I6 A7Z J5 J0 z2 A3z L3 p2 q8 A7N y1 m9 A3K V3 n9 A3M x7 N6 C8b g6 C8c C5c J6 A8z K5 L0 C5d
C5e t0 "s_cbranch_vccnz Lc2_10\n" "Lr2_10:\n" H7 W0 E9E d0 J7 A6X U0 E0C a0 C5f C51 s0 C52 C53 v0 A80 C54 b0 A7a A6e C7L A5P Y0 B2x A1z T2 C8O p0 h2
A4 i2 P0 L5 B4Y h6 M0 I0 X2 C0 B4X U2 F0 k2 G0 j1 T5 M5 B81 i6 K0 K3 C1 A3c f2 B0 C5g C5h t1 C5i C5j P2 j0 Q7 D5R j6 C8d C8e G1 B6f K1 C8f C8g z0 I1
M0 H1 O6 E2f S2 F1 E2g B36 E2i B1A C5k C5l E4 U5 A0W E2j v2 A6L R3 S0 i3 x0 N5 y0 A3e B5 A2U B99 L2 A3f A8H S3 r0 Q2 T0 A8I A09 T3 e0 U3 A1A U6 O5 g0
k6 R0 e4 f4 A3 B0 H2 B6k a3 k0 M2 Q0 A4n C3l a9 A1Q l6 N0 B8W A83 B8G d0 A2B U0 J0 B8c E2k s0 E2l v0 B4D A5N u0 B2U A2R P5 B6T i0 o0 B6e A4X s2 h0 z1
E2m k4 E2 P0 A4j A2r A0b I0 Z1 A33 w1 F0 a1 E2n S4 b1 G0 A6V f0 R7 B1E A0H X0 R2 q9 q2 V6 B4 h1 A1I E2o C7 W1 E2p D7 A3I A56 o6 b9 A57 p6 A6W L0 A58
T7 G1 A59 U7 K1 E2q C56 z0 E2r C57 I1 E2s C58 H1 E2t C59 c4 E2u V5 U1 E2v o8 B1A A3N B8N O0 K2 A7m A6Y A0 O4 h4 j3 A6L g2 S0 I3 V0 f3 x0 c9 B0 B4h S5
A07 l0 y7 o3 T1 B4i B82 Z7 m3 Q0 e0 M9 r2 B0 B83 w4 o5 A6a N0 X3 C0 A3O g3 C6A C6B Q5 A2D m6 J0 e3 C0 A2t F2 C6D C6E B08 B6F t2 K0 C3 C1 A70 A73 A1k
B0 A71 L3 n6 A2u C7 A6b A1 A2v D7 t0 "s_cbranch_vccnz Lc2_11\n" "Lr2_11:\n" o6 W0 A8D d0 j0 U0 C32 a0 p6 T7 s0 U7 C6F A6c A3Q v0 C6G C6H b0 A0u v6 C6I
B7P V5 m0 B6G o8 Y0 p0 y7 A9A B2X Z7 O2 h0 M9 D3 f0 o5 a2 A3R V7 E3t W7 X1 E2w E2x e8 I2 I0 f8 G2 B4G X7 Y1 F0 Y7 A6f E91 A3S A2 M0 G0 B3 B6H C6J d1
C6O C6K e1 B4H C6L G1 R0 F3 C6M K1 A2w M8 z0 C6P N8 I1 B8K G9 H1 Z4 H9 S2 B0x A5Q Q8 A1p R8 C6Y A3T R1 "Lp2_9:\n" E4F B0 A7B A38 B6R K2 T0 A0P A6g b5
J4 R0 r9 M4 x2 g0 y2 B0 s9 w6 l2 O0 C37 h5 C6N C38 C6R G3 x6 H4 C6S H3 k0 C6T C8W A5S V7 b2 A6z W7 n1 A2x e8 o1 N0 f8 B3v A9s X7 D5N S0 Y7 W0 A3V C6U
d0 V0 C6V U0 e0 C6W a0 A19 C6X s0 B02 M8 c2 J0 N8 B50 d9 D87 Q8 q0 A1U A4h R8 A2C A3W i0 f0 Q3 M0 E2y E2z b3 B3z B2a B30 l1 A4i B46 k1 P7 I0 x1 B47
B48 s1 A6j M3 D0 B49 E9n o2 F0 f1 E9o D66 A08 B0Z D67 B5C K1 A6k z0 D0 B5D B3J I1 l3 B4g B3K K4 S2 A6m U1 B01 Y3 C6f A1y D0 R1 A39 O1 A6n c1 B4K t9
B3T A1Z B0P E3 A6o C6b A92 W3 g1 E30 A0c A9q A6p F4 B07 t3 A0d B4S B75 A1W m1 A6q y1 D0 A7g A4A c5 A1l R5 W0 A6r d0 e9 C8A C6n r8 W5 v0 C6Z b0 s8 A4C
T6 A4D B3M Y0 C6a i0 E0L B3L t8 Z2 A0z f0 K0 X0 A4 P0 A2A B7p "s_cbranch_vccnz Lc2_12\n" "Lr2_12:\n" w0 T2 T5 D2 C6g C6h h2 A4E i2 C6i C6j X2 A4F U2
C6k C6l k2 A20 j1 C6m K3 C4D t1 A4G P2 G1 B33 z0 A1B H1 A1R A90 C8 A4H y6 v9 C4L A0 A7h E4 N4 I4 v2 w9 R3 E0 i3 M0 y4 U5 L2 u0 H0 S3 z4 A5 T3 S0 U3 o0
Q2 D8 B5 A3 B99 H2 A2I M2 e0 m2 A0w U6 t0 l0 B1V B8h d0 v4 U0 A88 D2J C0 R9 a3 v0 A5X B6S D82 A21 P5 A1X i0 K0 i1 R0 C1 e7 s2 A4l z1 A4I E2 j0 Z1 y0
A4J w1 B4U a1 A22 b1 I0 R2 B54 r0 h1 T0 A23 W1 A4o G1 B55 g0 z0 d7 D4d k4 I1 K7 G0 H1 D4e S4 S2 k0 A1p A0C u2 W8 y6 x9 c1 N0 K2 R0 B4d j3 B8I g2 D4f
O4 I3 O0 f3 y9 B4 o3 E0 m3 A7i h4 X3 J0 A8v g3 C0 D4g V6 e3 H0 F2 D4h D4i r2 K0 t2 u0 D4b D4c A24 c5 u8 S5 A9j B7L A3J A2c B4b o0 w2 B0 B0s A6u F1 v0
i5 A73 b0 l0 D2i X8 V8 f9 Y0 Y8 j5 i0 "s_cbranch_vccnz Lc2_13\n" "Lr2_13:\n" C7 O2 M0 A25 f7 D3 N0 a2 B0x D4j D4k N2 C0 p0 A6w D4l D4m X1 J0 I2 C0 D4n
D4o G2 h0 Y1 L0 D4p D4q A2 A9B B3 D20 o4 d1 A4t D7 e1 K0 o6 G1 C1 A5Y p6 B6W f0 T7 z0 M0 A0 "Lp2_10:\n" E4F A0V U7 B6I A82 M0 A3N S2 B0 A7k A6V j0 U1
A1Y u2 A2g B1 B80 O1 y0 A9C c1 A8 b7 A5Z C6o J4 r0 C6p x2 T0 A1r p4 C6q y2 O0 C6r l2 A1s M4 V5 d2 M0 E0 o8 e2 A2X H4 y7 A6x g0 Z7 G3 B0 B6L M9 H3 I0
o5 A6W A3O V1 k0 b2 M0 z9 s3 n1 Q0 F0 o1 T0 k5 l5 n2 N0 D5N W6 A0A A2Z G0 d0 B0t n3 C6s U0 H0 C6t I9 D21 C6u s0 A19 C6v A6y B0u C7 c2 J0 D7 v0 B6M o6
A7K p6 q0 B2e T7 m0 X0 U7 Y0 A3Q C6w A9A p0 V7 Q3 B8C W7 A9k e8 B2 A9E f8 B30 X7 l1 M0 A4K Y7 k1 I0 C6x A6b A3S C6y x1 F0 C6z s1 A2K A76 V5 M3 B3N o8
o2 A77 v8 y7 f1 l3 Z7 W2 w8 B35 M9 G1 t6 o5 K1 A3R C61 z0 R0 C62 C63 C64 I1 D4r D4s M8 H1 N8 d9 c4 B8S Q8 U1 S7 R8 C67 A1y A4p R1 A3T O1 M0 B2g C68 c1
C65 K2 A0 C8R A6c Q4 v3 M0 B8z E3 C66 Z3 C0 B37 C6c L0 C0I j4 C69 T4 O0 C7A F4 E0 V7 t3 c0 A6d q6 W7 z2 A78 A0I p2 A79 C7B C7C m1 K0 C7D y1 u0 E8 F8
C7E A4A H0 M8 t0 G8 H8 N8 W0 S0 G9 A0i A8E R5 H9 U0 V0 X7 I9 e0 Y7 s0 A3V W5 Q8 v0 o0 R8 b0 B0 A3W q0 A8F C7P m0 B4f Y0 l0 i0 E9J D4t D4u Z2 A02 C4O
A1w S8 A1x A2A B2h B2i B6P "s_cbranch_vccnz Lc2_14\n" "Lr2_14:\n" B2H T2 B2j D2 J0 h2 e9 B8V i2 h0 X2 u6 B8U U2 C7H k2 B5H C4C L0 A6i f2 C7I t1 C0 B7E
P2 X0 G1 B6Q K1 K0 z0 C1 B0M B0 A82 B5u j0 A3L F0 A1y O7 R1 A9u O1 A60 c1 A1D A4q B8r A0W A9o v2 A64 R3 D0 y8 u3 i3 Z0 L2 I8 E4 S3 A65 T3 B07 U3 J8
B8g C3j z8 r3 Q2 A66 M2 D0 B8k m2 M0 A3Y B97 W0 A67 d0 A1E A9p C6d A03 B03 A61 A4P B39 A04 T6 A28 A2P Y0 A62 C8m B0g a3 B6d i1 A4U k4 s2 A63 z1 y0 B0z
E2 F42 f0 Z1 A5o w1 r0 a1 T0 A7o b1 X0 B2q A1q q2 g0 h1 B0 A8B W1 B8a B00 K1 k0 z0 A69 V6 I1 I0 C4I A0 G0 U1 E31 E9 A1y g7 R1 A3X B2C A0Z C36 A9h A29
O4 g2 O0 I3 T8 A9 f3 E0 o3 A2a h4 m3 H0 X3 Z8 A4W g3 S0 r2 A3z S5 e3 V0 F2 q8 m9 t2 e0 A24 n9 w4 t0 H7 C7Q W0 J7 "Lp2_11:\n" E4F C7e d0 a8 C7f U0 b8
A8i a0 A68 C7F s0 C7G C7N B08 C7O A8j v0 A80 A4f b0 A7a B8 A3A V8 B8R C6e i0 "s_cbranch_vccnz Lc2_15\n" "Lr2_15:\n" A4g B4V A7b A3D D3 A0X a2 Q0 B4X
N2 B2I X1 B4Y I2 N0 G2 C0 A3c Y1 h0 C7x J0 d1 C0 B6f e1 B06 A81 K0 z0 u0 F3 I1 I0 C4I o0 U1 P3 D5G D5H u2 B0 G0 R1 D5I D5B B0v l0 b7 D5C D5D J4 Q0 Z0
x2 g5 D5E D5F y2 N0 l2 C0 A1P d2 O0 C38 G3 A8H H4 B1 H3 J0 M4 b2 C0 A8I A09 n1 A1A n3 V1 H0 o1 P3 D43 D44 n2 L0 c5 D45 a4 A2Z V0 A0i D46 e4 B6K e0 I9
P0 s0 c0 s3 b4 A6Y f4 a7 c2 C7R E9K B7t b0 A4l q0 p9 D47 m0 j0 Y0 E9L D48 D49 i0 g1 A1Q A6a B06 Q3 A1 D42 B9 b3 B0 C9 C7 B2 A2B C2 u6 X0 l1 R0 A7d k1
r0 x1 T0 A84 s1 B4o D7 M3 M0 A32 o6 o2 g0 p6 f1 B0 A2r T7 W2 B4R U7 A6u A3N G1 k0 B6W A33 z0 Q0 I0 B6I B1E H1 T0 D5A A0 q9 C7S U1 N0 C7T u2 C0 B6U C7U
R1 S7 B1 C7V O1 x0 V5 c1 A2s o8 K2 U8 y7 v3 B4h q4 Z7 E3 J0 M9 A6f C0 B4i d5 o5 A6w O0 Z3 A3O W3 E0 d3 c0 B82 A9q B83 q6 F4 K0 t3 u0 A2D m1 G0 z2 A3Z
K4 p2 o0 A6g B0 A3a Y3 y1 H0 C7W V3 M0 B6F C7X t0 S0 C7Y W0 A70 R5 C7Z d0 V0 C7 B4A e0 D7 a0 A71 W5 o6 s0 l0 p6 Z2 Q0 A7C T7 v0 A02 G7 "Lb2_3:\n" M7
V7 b0 A0s j7 W7 q0 A0t L3 e8 m0 N0 f8 Y0 "s_cbranch_vccnz Lc2_16\n" "Lr2_16:\n" i8 X7 i0 M0 h0 Y7 A6x C0 A3S U7 T2 J0 C7a D2 C0 A3Q C7g h2 j8 m5 C7h
i2 K0 C7i X2 c0 A0u v6 C7j U2 C1 f0 M8 k2 B0 B6G N8 j1 j0 G9 A9z B2X H9 f2 X0 Q8 t1 B4G R8 P2 P0 C7b A6y L0 A3T C7c G1 I0 C7d K1 M0 B6H V5 z0 F0 o8 I1
B4H y7 H1 B3N Z7 S2 A2g A2w M9 A1p o5 C6Y A3R B8q A7B D5J D5K B6R C7m K2 T0 B8K C7n N4 A05 C7o v2 A5Q C7p R3 O0 V7 i3 A6J n5 W7 L2 E0 e8 S3 A6K E4 f8
T3 B2Y U5 C7q U3 g0 C7r C3h B2Z r9 C7s A3 A4p H0 C7t H2 s9 u3 M8 M2 C7k N8 m2 c0 d8 r3 G9 t0 S0 H9 W0 h5 B5 X7 l9 V0 Y7 U0 A3V Q8 a0 c0 e0 R8 s0 A3W
D83 w6 x6 A3E b0 C7l A4b B2B D5L D5M A4c m0 A5S Y0 A2x i0 k0 B0N A9s i1 e9 p0 s2 A1 B02 z1 C7u E2 L0 B8d Z1 L0 h0 w1 B3w A0 "Lp2_12:\n" E4G a1 C7w B2O
B3x R2 A6h q2 Q0 A5U h1 T0 X0 W1 L0 A5T G1 N0 A9n A72 z0 C0 P0 A1B B0n H1 J0 S2 x8 A5V U1 B0x I0 F9 A7e O1 A6M c1 A1D A8k k4 K2 B4O A9h A8l V6 g2 A6Q
I3 D0 t9 B8l A2k C7v A6R X3 B07 g3 M0 A2l B09 F0 F2 A0c S4 r2 A6S C8l A0d u9 C3 G0 A0F A5W B4 W0 A6T d0 A1E A1W U0 H0 I9 A3b A74 B4W A6N A4P A0e A1l
B6j C8A Y0 A6O C8m s8 r8 w2 R4 B8O B3M A6P E0L B3L A2J t8 B3n "s_cbranch_vccnz Lc2_17\n" "Lr2_17:\n" A1 O2 K0 D3 u0 F3 a2 A4E N2 G0 X1 A4F I2 o0 G2 B0
A20 Y1 P0 A2 L0 C5w l0 d1 A4G e1 A34 B33 C3s Z4 B2D E0F C8 A4H y6 v9 B53 A7h B8B I4 x2 w9 y2 E0 l2 y4 H4 d2 S0 e2 z4 A5 G3 V0 H3 B6l V1 e0 b2 A3g b4
n1 H0 o1 A0w a4 n2 L0 B0T t0 A0S W0 A4s d0 A3n U0 R9 a0 v4 s0 c2 v0 A5X C55 A21 P5 A1X A9A h0 Q3 e7 q4 b3 J0 B2 C0 A4I C2 f0 l1 A4J k1 K0 x1 C1 A22 s1
f9 M3 C5y j0 f1 y0 A23 A08 B8a p1 A8m D5Y K1 B0 B0S D5Z D5S I1 r0 H1 T0 D5T D5U c4 F0 B19 D5V D5W R1 g0 O1 K7 D5X A8n B74 B6V B8w d5 v3 k0 E3 Q0 B8x
B7B Z3 B6X W3 p1 D5a D5b d3 O0 T4 D5c D5d F4 E0 t3 L0 D5e D5f A30 z2 B7C A0T p2 A0U K4 m1 N0 y1 C0 D5g D5h V3 G0 t0 A1 y9 Y3 W0 H0 A0i B9p B6K S0 I9
B2S A0p v0 B7L B6S D82 A2c A0x P5 A0y i5 i0 K0 Z2 u0 C8K C8h A0z C8i j5 j7 C8j A25 L3 C8k A5M f7 "s_cbranch_vccnz Lc2_18\n" "Lr2_18:\n" L0 T2 A9B E92
B0V i2 l0 X2 Q0 A7k U2 f9 k2 A5Y j1 A8R K3 A0V f2 N0 t1 C0 A1Y P2 A34 A9C K1 A4o z0 A5Z I1 J0 H1 S7 A4 S2 A5L A0 U1 A1r u3 u2 K0 R1 C1 A1s A2X O1 Q9
c1 B6L K2 j0 N4 R0 y0 z9 v2 A8 R3 W6 B7r L2 k5 l5 B2A T3 A0A B7s r0 Q2 T0 B0Y A3 C3j E4H r3 M2 g0 m2 M0 B6M t0 B2r A2q B2e B7u B8C b0 k0 D2i A9E m0
B2I Y0 A4K i0 N0 a3 P7 A2K i1 A1 A5E f0 z1 v8 A0 "Lp2_13:\n" E4G w8 E2 A6j Z1 A1D B34 w1 X0 a1 A9F b1 A6o B2q A8G q2 B4e h1 B8z W1 A6p G1 D0 C8R K1
B0S B5w A6q H1 D0 D5A A0 G0 U1 A1G E9 F9 B37 O1 A6r c1 A1E B2G B8r A9h B58 g2 A6k I3 D0 G8 H8 B8l E8 C7v A6m X3 B07 g3 F8 B09 H0 F2 B0a r2 A6n t2 E94
B4f A24 c5 A1u S5 A9j A1v A1w A3J A1x w4 S6 w2 A9l F1 g4 B2h b0 B2i B8 A9K E25 u0 A9L B2J i0 "s_cbranch_vccnz Lc2_19\n" "Lr2_19:\n" B4V B2K D3 o0 a2
B8V N2 p0 X1 B8U I2 h0 G2 B5H Y1 f0 A2 A6i B3 X0 d1 B7E e1 B52 B6Q K1 B0S F3 I1 B77 A0 Z4 U1 B4I I4 R1 B01 E0 B0v A4Q H4 K2 T1 B1 A5R A4R A9O x2 Z0 y2
A9P M4 l2 H0 d2 A9o e2 S0 G3 B0 I8 a4 H3 V0 V1 J8 y8 b2 e0 n1 l0 o1 Q0 z8 b4 n2 M0 B0 A2N t0 A2O A8w B2E B97 U0 a0 A9p C8z C0 B0y A0l B8Z q0 K0 m0 C7J
Y0 C1 E4I L0 B6Y Q3 A4l A9k B2 p1 D5k D5l C2 j0 l1 y0 D5m D5n k1 f9 x1 D5o D5p s1 r0 A1V D5q D5r o2 B4R C8n M0 A5o C8o T0 I0 f1 B0z W2 A9i C8p M0 A1q
C8q g0 G1 p1 D5u D5v K1 B0 B6g D5w D5x I1 k0 H1 Q0 D5y D5z S2 T0 l3 A1p B8p D50 D51 R1 N0 B7q A7o B74 B6V B00 v3 J0 E3 C0 A8B Z3 Z0 W3 A0n j4 d3 A1t
B31 A0o B32 A5O K4 m1 R0 H0 z2 c0 B7n p2 T8 Y3 y1 K0 A4A A8y B8W c7 R0 A9 R5 A0i B7H M0 A10 c0 A4W A86 u0 A3z Z2 o0 A75 q8 m9 A7l n9 j7 m0 H7 A2d l0
i0 Q0 J7 a8 g5 "s_cbranch_vccnz Lc2_20\n" "Lr2_20:\n" L0 T2 h0 D2 A80 b8 h2 N0 i2 R0 A7a A3A X2 O7 f0 U2 C6e k2 A60 j1 L0 A7b A7p K3 A3U f2 A1D A7q
A7r t1 A64 P2 D0 A7s E2X G1 A8R K1 E2Y "ds_read_b64_tr_b16 a[228:229], v216 offset:54912\n" z0 g1 I0 I1
"ds_read_b64_tr_b16 a[230:231], v216 offset:55424\n" A5x H1 A4o c4 A5y "ds_read_b64_tr_b16 a[236:237], v216 offset:54944\n" U1 d7 C35 E1T
"ds_read_b64_tr_b16 a[238:239], v216 offset:55456\n" O1 A65 A6l E1U m5 K2 B4O A0W E4J n5 v2 A66 R3 D0 E4K A09 i3 Z0 L2 A1A E4 S3 A67 T3 A1E X5 U3 M0
U5 u3 A3 e4 r3 H2 A61 Q2 D0 f4 F43 M2 H0 m2 F44 A7v t0 A62 A5b p9 F45 B0k A7A A2e F46 B5 A1C A63 D83 B4M A83 B86 A2B m0 J0 B8H A7d A0B B0N E95 E96 i1
K0 s2 C1 E4L "Lp2_14:\n" E4G E4M z1 B0O E2 A2r Z1 j0 w1 A33 a1 X0 B2O B1E R2 P0 q2 q9 h1 I0 W1 B6U G1 F0 K1 A2s B8e A1B V4 B7x A1R A1G C8 B5x A7B B5y
Z0 K2 A0 A6s O4 j3 I4 g2 A6t I3 E0 f3 A3Z h4 o3 R0 H0 m3 A3a O9 X3 S0 g3 P9 S5 r2 y0 V0 e3 B0 A0r F2 e0 t2 A2u w4 C3 r0 t0 T0 A2v A0s A2Z d0 A0t i8 U0
g0 a0 E3X j8 s0 B08 E3Y E0m A8Q E0n B8 D88 B0 E0o A2d B2v E0p E0q "s_cbranch_vccnz Lc2_21\n" "Lr2_21:\n" O2 A6h D3 k0 a2 Q0 E0r E0s N2 T0 X0 X1 E0t
E0u I2 N0 G2 C0 E0v E33 Y1 P0 A2 p1 E34 D52 B3 J0 d1 C0 D53 D54 e1 I0 P6 L0 D55 D56 l4 K0 r4 u0 D57 D58 s4 F0 e5 x0 g9 R0 D59 E0w C8u B3N C8v o0 A11
B0 E0x E35 A2h F47 p1 E36 D6A f5 l0 k7 Q0 D6B D6C h8 U8 C8w B2r D6D D6E C8x N0 B1F C0 D6F D6G B1G O0 B1H A6J D6H B1I E0 B1J A6K o4 B1K p4 M4 B1L B2Y
H4 B1M J0 V1 M0 B2Z r9 B1N A0f H0 B1O s9 s3 B1P K0 B1Q M0 d8 n3 t0 C1 c7 M0 h5 a4 B7o B7H c0 A10 w0 w6 b4 s0 A9r x6 c2 j0 v0 y0 A5S b0 A02 q0 A2x m0
r0 Y0 T0 A9s i0 B6d B57 B02 b3 g0 B2 B2r D8Y D8Z C2 B06 l1 L0 D8T D8U k1 k0 x1 Q0 E0O E0P s1 A37 A1V D8a D8b o2 T0 P0 f1 D8c D8d A08 I0 P6 D8e D8f l4
A4o r4 D8g D8h s4 g1 G0 e5 D8i D8j g9 N0 A11 A0q D8k D8l B9R A0f A8k q4 f5 J0 k7 A9l A8l d5 h8 L0 B1 A8L A0 t9 A8M K0 A7D u0 B0P A7E Z0 A8N A0c j4 D7E
O0 D22 E0 A7F R0 q6 K4 A8O A0d Y3 A8P o0 m1 B0 A92 A7G H0 A7H A1W A2y R0 A74 B0Q A0e A86 l0 Z2 Q0 A4B A75 A0 b0 A2z A0R r8 V8 B70 D89 D9A i0
"s_cbranch_vccnz Lc2_22\n" "Lr2_22:\n" L0 T2 D9B D9C D2 J0 h2 C0 D9D D9E B2y C82 C83 U2 K0 k2 C1 C84 C85 j1 B2d K3 B0 C86 C87 f2 j0 t1 C88 C89 P2 f0
P6 C9A C9B l4 A37 r4 C9C C9D s4 P0 e5 A4 B4r I0 B4s F0 B9R y0 A4H f5 Q9 k7 v9 h8 r0 D7F M0 A8J A4v B6X A4w y4 E4 B6n O0 A4x z4 u3 B87 A5 B88 g0 Q2 K7
B6l A4y G0 B6o A3g r3 A4z k0 A40 M0 A6U t0 Q0 A2q T0 B60 B7u B8s C55 D6U D6V P5 D6W D6X i0 J0 a3 D6Y D6Z i1 g1 A5E B9E B9F z1 f0 A0 "Lp2_15:\n" E4G E2
B9G B9H Z1 X0 w1 B9I B9J a1 P0 b1 B9K B9L R2 g1 I0 q2 B9M B9N h1 F0 W1 B9O B9P P6 B9C V4 r4 E9 s4 F48 g7 A0 A11 S7 C9E A1P g8 A0X f5 C0 o9 C9F B6z E97
E98 A41 K0 B6r u0 B4N B6s Z0 B6t u8 O4 B6u O0 B6v E0 B6w S9 h4 A42 o0 r2 B0 A7j A43 H0 A44 B0s A45 l0 A46 R0 i5 X8 t0 Q0 c7 B0 Y8 B29 j5 B4b N0 w2 C0
"ds_read_b128 a[144:147], v214 offset:51200\n" "ds_read_b128 a[148:151], v214 offset:51264\n" A8Q E0Y B8 E0Z V8 J0 B8H E3V E3W i0
"s_cbranch_vccnz Lc2_23\n" "Lr2_23:\n" B89 E00 E01 D3 K0 a2 C1 E02 E03 N2 A02 X1 E04 E05 I2 j0 G2 F49 F5A Y1 h0 A2 E06 E07 B3 f0 d1 F5B F5C e1 A37 P6
F3 l4 P0 r4 I0 s4 F0 e5 G0 B4r Z4 A11 A1G x0 k5 B6x E4N E4O B4p D9H D9I B4t B1F A0 A9D M4 B1G g0 B1H B2r z9 l5 B1I B28 B1J F5D F5E B1K E0 B1L B0t H4
B1M k0 C8W B9Q F5F B1N A6z B1O F5G B1C B1P N0 B1Q R0 B2e t0 A0f c7 A8W a4 A2G E08 b4 C8z E09 E4P A0l E4Q F5H T6 F5I D8m A0Q D8n D8o B3y D8p D8q A9t L0
D8r D8s l1 P0 k1 D8t D8u x1 I0 s1 D8v D8w M3 F0 o2 D8x B9B G0 W2 B2b P6 Y3 l4 B6y e5 T1 g9 D8X d7 C9E A9l B37 B4u B2G B6q B58 A8L o0 A8M B0 E8 F8 A7D
Z0 A7E G8 j4 A8N O0 B9D H8 q6 A7F l0 m1 Q0 B0a A8O A3h A8P B2r B4f A7G N0 A7H C0 C4O A2y A1w B0Q A1x W5 S6 Z2 A0f E9p E9q A8Q E3Z A0R E3a B4v E3b E3c
i0 "s_cbranch_vccnz Lc2_24\n" "Lr2_24:\n" A9r B2c E0Q E0R D2 j0 h2 E0S E0T B2y E0U E0V B76 F5J F5K j1 G7 "Lb2_4:\n" M7 F5L E0W E0X f2 C4E F5M F5N P2
A8R P6 A4 l4 I0 r4 F0 s4 G0 e5 T5 g9 F1 I8 B4s x0 J8 B6x A9u B4p A4q B4t B4q A2L E4 A4v g0 A4w B0 A2M y8 B6n B28 A4x z8 A2N B87 A2O B88 k0 Q2 Q0 B97
A4y A6z B6o A9p A4z N0 A40 R0 B0y A95 D8V B5 A2G D8W U6 S6 a3 E3d E3e A0l E3f E3g B6j D6I D6J C9G D6K D6L i1 p0 s2 D6M D6N z1 h0 E2 L0 D6O D6P Z1 P0
w1 D6Q D6R a1 I0 b1 D6S D6T R2 F0 q2 V4 h1 C9H C9I W1 C9J C9K P6 B9C E9 B6y B27 S7 B6p "Lp2_16:\n" F5O A3X B4u A0Z C9F B6z B2l A41 o0 B6r B0 A8y B6s
Z0 B6t A9 O4 B6u O0 B6v E0 B6w A4W h4 A42 B0o C1 A8C A43 D23 H0 A44 A4m A45 j0 A46 R0 B4T c3 y0 c7 B0 a8 B29 b8 B4b r0 w2 T0 E3F E3G v0 C9L C9M b0 E3H
B8 g0 C8Q E3I A4r E3J E3K "s_cbranch_vccnz Lc2_25\n" "Lr2_25:\n" B89 B4w E3L C9N a2 k0 N2 E3M C9O X1 f0 I2 E3N E3O G2 X0 Y1 E3P E3Q A2 P0 B3
"ds_read_b64_tr_b16 a[180:181], v216 offset:54912\n" "ds_read_b64_tr_b16 a[182:183], v216 offset:55424\n" d1 I0 C6Q E3R E3S A96 F0 l4
"ds_read_b64_tr_b16 a[188:189], v216 offset:54944\n" "ds_read_b64_tr_b16 a[190:191], v216 offset:55456\n" r4 G0 s4 F3 e5 C9P C9Q B4r C9R C9S B4s Z4
B9R T0 E1T E1U f5 o0 k7 B0 E4J E4K h8 Q4 B1F M0 A31 B1G l0 B1H e4 f4 B1I Z0 B1J F43 M4 B1K O0 B1L E0 B1M F44 H4 B1N H0 B1O A4n V1 S0 B1P F45 a4 B1Q M0
B9S F46 B9 C9T C9 b4 U0 A1f C9U a0 A1g C9V B9T E3T C9W c2 E3U C9X v0 R4 b0 "ds_read_b128 a[152:155], v214 offset:51200\n"
"ds_read_b128 a[156:159], v214 offset:51264\n" T6 "ds_read_b128 a[160:163], v214 offset:59648\n" "ds_read_b128 a[164:167], v214 offset:59712\n" A0Q A0
E0a E0b Q3 A1 B3A E0c E0d B2 G0 C2 E0e E0f l1 F0 k1 E0g E0h C80 E0i E0j M3 I0 o2 E0k B9B E0l d5 W2 Y3 C9Y A96 L0 C9Z C9a l4 C9b C9c r4 C9d K4 s4 C9e
C9f B27 P3 C9E B0W g8 Z0 f5 B0X B6q E99 j4 A8L I4 A8M F0A E3o A7D V0 A7E E3n E3m A8N A1 e0 B9D q6 W5 A7F E3l
"ds_read_b128 a[148:151], v215 offset:13248\n" C5S A8O "ds_read_b128 a[144:147], v215 offset:13184\n" A3Z A8P S0 A7G A3a R5 A7H O9 C9g A9b P9 C9h A9a
A1 A70 C9i A9Z A71 C9j A9Y A2u C9k A9X A2v C9l C9m A0s C9n B6i A0t C9o C9p R4 C9q i8 j7 C9r L0 j8 C9s p0 C9t E0q E0r C9u "s_cbranch_vccnz Lc2_26\n"
"Lr2_26:\n" A0 T2 A0 E0s E0t D2 C9v h2 E0u A6X i2 A9i X2 E0v E33 U2 A0 C9w A0 E34 C9x j1 h0 A9z E0w B9U f2 A1H C9y A47 E0x B9V P2 x0 C9z A0 E35 C90
B4q A0j A4v A0 C91 C92 A4w A8S C93 E36 C94 C95 A0 A6J C96 C97 c0 C98 C99 A4x D0A A0 A8T D0B A0 D0C D0D B9W A6K B2Y B9X A0 D0E A4y M0 A8U B2Z D0F B2n
A4z D0G D0H A40 E0P E0O D0I g1 D0J D0K A9g A1 D8U D8T A9f g1 D0L D0M A9e L0 A8V A9d L0 D6a D6b A9c A1 D8Z D8Y D0N g1 D0O D0P D0Q A1 d8 h5 A2h g1 w6 x6
g8 u6 E0 f5 R0 A5S D0R k7 L0 A2x h8 E9z D0S D0T D0U D0V L0 A9s B6e D0W s2 A0 D0X D0Y B9Y D0Z Q4 z1 A0 B02 E2 R0 D0a D0b Z1 B9Z D0c D0d a1 D8a D8b B2O
D8c D8d R2 A05 D0e D8e D0f D0g h1 x0 W1 O0 D8f D0h M0 D8g B9a A41 c0 D8h B9b H0 D0i S0 B9c B2M D0j B0w D0k D0l "Lp2_17:\n" F5O D0m D8i D8j D0n D8k D0o
M0 D8l D0p B9d D6c D6d D0q "ds_read_b64_tr_b16 a[48:49], v216 offset:0\n" "ds_read_b64_tr_b16 a[50:51], v216 offset:512\n" A42
"ds_read_b64_tr_b16 a[52:53], v216 offset:4224\n" "ds_read_b64_tr_b16 a[54:55], v216 offset:4736\n" D0r u6 D0s A43 A1
"ds_read_b64_tr_b16 a[60:61], v216 offset:4256\n" "ds_read_b64_tr_b16 a[62:63], v216 offset:4768\n" A44 A1
"ds_read_b64_tr_b16 a[56:57], v216 offset:32\n" D0t D0u D0v A8S "ds_read_b64_tr_b16 a[58:59], v216 offset:544\n" D0w D0x A45 A0
"ds_read_b64_tr_b16 a[64:65], v216 offset:64\n" "ds_read_b64_tr_b16 a[66:67], v216 offset:576\n" A46 "s_cbranch_vccnz Lc2_27\n" "Lr2_27:\n"
"ds_read_b64_tr_b16 a[68:69], v216 offset:4288\n" "ds_read_b64_tr_b16 a[70:71], v216 offset:4800\n" D0y A1 D0z D00 D01
"ds_read_b64_tr_b16 a[72:73], v216 offset:96\n" "ds_read_b64_tr_b16 a[74:75], v216 offset:608\n" A8T D02 A1
"ds_read_b64_tr_b16 a[76:77], v216 offset:4320\n" "ds_read_b64_tr_b16 a[78:79], v216 offset:4832\n" D03 D04 D05
"ds_read_b64_tr_b16 a[80:81], v216 offset:8448\n" "ds_read_b64_tr_b16 a[82:83], v216 offset:8960\n" "ds_read_b64_tr_b16 a[84:85], v216 offset:12672\n"
"ds_read_b64_tr_b16 a[86:87], v216 offset:13184\n" D06 "ds_read_b64_tr_b16 a[92:93], v216 offset:12704\n"
"ds_read_b64_tr_b16 a[94:95], v216 offset:13216\n" "ds_read_b64_tr_b16 a[88:89], v216 offset:8480\n" "ds_read_b64_tr_b16 a[90:91], v216 offset:8992\n"
D07 B8E D08 D09 A0 "ds_read_b64_tr_b16 a[220:221], v216 offset:12736\n" A6e D1A B9e "ds_read_b64_tr_b16 a[222:223], v216 offset:13248\n" D1B A8U D1C
"ds_read_b64_tr_b16 a[216:217], v216 offset:8512\n" "ds_read_b64_tr_b16 a[218:219], v216 offset:9024\n" D1D D1E D1F D1G R0
"ds_read_b64_tr_b16 a[224:225], v216 offset:8544\n" "ds_read_b64_tr_b16 a[226:227], v216 offset:9056\n" D1H
"ds_read_b64_tr_b16 a[228:229], v216 offset:12768\n" "ds_read_b64_tr_b16 a[230:231], v216 offset:13280\n" D1I D1J A9p A8V D1K D1L D1M A0 A03 D1N D6e
D1O D6f D1P A04 D1Q D1R D1S D1T D1U D6I D6J D1V A8X C2 R0 D6K D1W D6L B2 A0 D6M D1X D1Y D6N A8Y s1 A8Z D1Z D6O D6P D1a D6Q D1b D6R A08 A8a D1c D6S D6T
D1d B9f D1e D6g D1f D6h D1g B9g D1h B9h D1i B9i D6i D1j A8X D1k D6j A8Y D1l A8Z D1m A8a D6k D6l D1n "Lend2:\n"
".fill (Lv2+73728-.)/4, 4, 0xBF800000\n" "Lp2_18:\n" F5P ".fill (Lv2+77824-.)/4, 4, 0xBF800000\n" "Lp2_19:\n" F5P "Lc2_0:\n" B61 B4x D1o A48 B1S D6m
"s_branch Lr2_0\n" "Lc2_1:\n" B62 D14 L4 "s_branch Lr2_1\n" "Lc2_2:\n" B9j B1T N3 B3D d4 "s_branch Lr2_2\n" "Lc2_3:\n" B9k D15 L4 "s_branch Lr2_3\n"
"Lc2_4:\n" D1p A8c N3 A98 d4 "s_branch Lr2_4\n" "Lc2_5:\n" r1 D1q D8z W4 E1B p5 B0H B9l E3p B1k B9l E1A B3B "s_branch Lr2_5\n" "Lc2_6:\n" v1 D1r D6n
N3 D8z r6 A8o A8p B0H A3j "s_branch Lr2_6\n" "Lc2_7:\n" r1 D1s D1x W4 D6n p5 A7O A8q A8o A3o "s_branch Lr2_7\n" "Lc2_8:\n" v1 D1t B9m N3 D1x r6 A49
A8p A7O A3j "s_branch Lr2_8\n" "Lc2_9:\n" r1 D1u B63 W4 B9m p5 A3i A8q A49 A3o "s_branch Lr2_9\n" "Lc2_10:\n" v1 D1v B4y N3 B63 r6 A3i A3j
"s_branch Lr2_10\n" "Lc2_11:\n" r1 D1w B3C W4 B4y L4 "s_branch Lr2_11\n" "Lc2_12:\n" v1 D1y B1R N3 B3C d4 "s_branch Lr2_12\n" "Lc2_13:\n" r1 D1z A97
W4 B1R L4 "s_branch Lr2_13\n" "Lc2_14:\n" v1 D10 A8b N3 A97 d4 "s_branch Lr2_14\n" "Lc2_15:\n" r1 D11 A7I W4 A8b L4 "s_branch Lr2_15\n" "Lc2_16:\n" v1
D12 A48 N3 A7I d4 "s_branch Lr2_16\n" "Lc2_17:\n" D13 D16 L4 "s_branch Lr2_17\n" "Lc2_18:\n" v1 D6o A5A N3 A7J d4 "s_branch Lr2_18\n" "Lc2_19:\n" r1
D6p D17 L4 "s_branch Lr2_19\n" "Lc2_20:\n" v1 D80 D18 d4 "s_branch Lr2_20\n" "Lc2_21:\n" r1 D81 D19 L4 "s_branch Lr2_21\n" "Lc2_22:\n" v1 E1C D2A
"s_branch Lr2_22\n" "Lc2_23:\n" r1 E1D D2B "s_branch Lr2_23\n" "Lc2_24:\n" v1 E3r D2C "s_branch Lr2_24\n" "Lc2_25:\n" r1 E3s D2D "s_branch Lr2_25\n"
"Lc2_26:\n" v1 E9d D2E "s_branch Lr2_26\n" "Lc2_27:\n" r1 E9e D2F "s_branch Lr2_27\n" ".fill (Lv0+294912-.)/4, 4, 0xBF800000\n" "Lv3:\n" "Lb3_0:\n"
C0m C0n C0o C0p C0q C0r F0B A0N C0s C0t g7 G7 "Lb3_1:\n" M7 "s_sleep 36\n" B7b C0u "s_mov_b32 s54, Lend3-Lv3\n" C0v C0w C0x B7c C0y C0z C00 C01 C02
B5Z C03 C04 B7d C05 C06 C07 C08 C1D C1A C1B C1C C09 p8 C1H C1E C1I C1F C1G C1K C1J B7e p8 C1c C1O C1T C1P C1Q C1L C1i D6q C1Z C1U A5I C1l C1m C1R C1S
C1M C1N p8 C1V C1Y D6r D6s C1e "s_mov_b32 s92, 12288\n" C1f C1a C1b C1d C1g C1h C1j C1k C1n C1o A5J "s_addk_i32 s92, -9216\n" C1q C1p C1r C1s C1t C1u
C1v C1w A5J C1x C1y C1z C10 C11 C12 C13 C15 C14 C16 C17 C18 C19 C2A C1W C1X B7f C2C C2B D6t D6u D6v D2G C2D P3 B7g F0B B7h B7i B7j B5a B7k B5b B1r B5c
B1s C2E C2F C2G E1 B5d A5K D35 C2H D36 P3 C2I C2J B3f S8 A1N C2K A8t C2L R6 C2M E1 C2N D37 C2O D38 B1t N0 C2P A2p C2Q C2R C2S C2T C2U B1u C2V T1 A7V
B1t A2p C2W C2X C2Y C2Z E1 A8t A3s D39 C2a C2b C2c C2d B1u B9n B9o C2f B3g C2e B3h C2g C2h C2i A5L A3t A3u R6 A3v A3y A3x A3w C2j C2k C2m C2l K0 C2n
A7W j0 A7X D4A D4B C2o B0 D4C D4D B3i A0 g0 B1r B1s C2p E1 A7V D4E D7N B0 A3s B3j U0 Q0 D4F B5e B3k W0 C2v B1w B5f A1N C2q B5g C2r C2s C2t S8 z7 B7l
C2u B9n E1V A8u B5h E1W q0 B5i D7P J3 D7O C2y m0 K6 B9o h3 C2x B3g C2w B3l B3h E4R B3k C2z C20 E2T B1f B7l Y0 J3 B5k A0O B1v C22 C21 J0 h3 B7m C23 C24
C25 C26 C28 B5m B1x B5l C29 B1y B5n C27 B1z K0 J3 B10 u0 K6 C3A h3 B11 J3 C3B B12 F1 B3m C3C C3D C3F C3E A5M B13 P3 C3G C3H B14 x0 C3I B5o B15 B5p C3J
B16 B5q B5r B17 A0 "Lp3_1:\n" F0C B5s B18 l0 X0 B3n f0 v4 F3 c0 t0 C3K C3L A8w l9 C3M C3N A1O I0 A8x F0 S6 G0 P0 C0 Z4 v0 C3O C3P A5N C1 Z0 m0 B4m Y0
H0 B1 O0 B5t A7Y w0 B1x y0 S0 B1y B99 B1z r0 B10 D7Q e0 B11 x0 T0 B12 A0 A0n A19 V1 C3Q B0 A0o A5O B13 p0 B14 B7n B15 k0 B16 Q0 T8 A3z B17 T0 h0 B18
A0 A8v l3 R0 t0 A8y W0 U8 B7o A7Z U0 E0 A8x P0 B1 O0 s0 B7p B3o A30 t6 v0 q8 A0p b0 C3R q0 m9 C3S m0 n9 Y0 N0 B7m C3T m1 H7 C3U Z2 I7 J7 R6 A8z A80
A2A A7a A5P C3V "s_cbranch_vccnz Lc3_0\n" "Lr3_0:\n" u6 C3W A7b D2 K0 C3X u0 B3p h0 C3Y B3q o0 C3Z B0 B3r B3s C3a A7c l0 C3b Q0 B3t g5 X0 A81 B3u C0
B0M A82 M0 B5u A0q F0 B19 A1P R1 J0 B7q o9 C3c A31 C3d K0 v2 C1 Z0 C3e B0 a7 B7r C3f e4 u3 B2A C3g f4 B7s j0 C3h B7t C3i y0 C3j B0 p9 r3 C3k r0 C3l
A1Q t0 A2q A83 B7u A2B b0 g0 B2B A7d P5 A84 i0 k0 B0N A32 C3m Q0 A5E B2k C3n A33 E2 N0 C3o C0 A2r w1 X0 C3p q9 b1 J0 C3q L0 B7v P0 C3r A2s W1 A34 B7w
C3s B5w G0 B7x A1R C3t B5x C3u B5y B1 b7 A2D C3v o0 g2 A8 C3w O9 C3x C3y A2t B7y C3z P9 B7z B0o C4A A0r C30 A2b F2 A7C C31 N0 A24 A8D A95 i8 B29 j8 w4
S6 w2 B0R g4 A0u b0 v6 B8 A0v B4v A36 i0 "s_cbranch_vccnz Lc3_1\n" "Lr3_1:\n" B0 O2 R0 A2E D3 j0 a2 C33 X0 X1 C34 p0 G2 F0D A2w Y1 A0N h0 B71 C4F d1
j2 A5Q e1 B06 G1 B5z C4G z0 j2 F3 I1 A82 B5u B78 U1 B01 F0 A1y j2 R1 A38 O1 D4J c1 D4K A0P B8r A5R B2n x2 C4H y2 j2 w6 s3 l2 Z0 C37 M4 e2 B79 G3 j2 X5
H3 h5 C5x G0 n1 x6 n3 V1 C4J o1 j2 A5S n2 M0 A3Y A2x W0 A5I d0 y0 A9s U0 F5Q C4j B2S B8Y B02 b0 r0 q0 T0 B8d P5 B3w i0 C3Q L0 B3x Q3 A4l A9k B2 A5T C2
k0 l1 A5U "Lp3_2:\n" F0C k1 X0 x1 C39 P0 A1V B51 I0 f1 A7e W2 F0 G1 A5V K1 B6g l3 I1 t6 H1 D8S A3L C8 A39 y6 t9 B53 A2k j4 v3 I4 E3 A2l Z3 E0 W3 A0c
q6 d3 H0 T4 A0d u9 F4 S0 t3 A5W R5 m1 V0 z2 A1W p2 e0 y1 Q0 A74 W5 V3 L0 T0 A0e t0 A1l W0 B0p d0 N0 A1O B2p i7 Z2 J0 B5F A0 s8 B86 A4C j7 m0 A4D A2d
B7G u0 A87 "s_cbranch_vccnz Lc3_2\n" "Lr3_2:\n" R0 T2 h0 D2 p1 D7R D7S h2 o0 i2 B0 D7T D7U X2 f0 U2 t8 B2z k2 l0 j1 Q0 B20 B21 K3 f9 f2 B22 D5s t1 N0
B8f D5t B4k G1 A5d P0 K1 B4l B23 z0 D61 I1 j2 B24 B25 H1 g1 B72 B26 m5 A1p B8p A2V n5 R1 D62 O1 j2 A2W B0q C3c B8Q N4 U8 v2 w9 E4 R3 D63 i3 j2 y4 u3
L2 O0 B2A T3 z4 B7s A5K Q2 F5R A5 D8 A3 M0 F0 H2 A2I M2 J0 m2 C0 A6U t0 G0 W0 A0S r3 d0 H0 E38 V0 E39 A88 A5N C4M C7M m0 A02 Y0 A21 B5t B0N A1X i1 C4N
A5E A2g e7 D7X z1 B4U E2 A4J Z1 r0 w1 T0 A4I a1 I0 B2O C8r R2 g0 q2 K7 A22 h1 F0 W1 D41 G1 k0 K1 Q0 A23 z0 B2k I1 D7Y k4 H1 A1 X0 c4 D7V V6 A85 C35 W8
O1 N0 c1 x9 B8D A9h B4d g2 E0 I3 D7W h4 f3 B1 o3 R0 y9 S4 m3 Z0 X3 O4 B4 g3 B9p r2 H0 e3 B7L F2 S0 t2 A2c S5 C3 C0G B0s W0 A1 C0L i5 w4 U0 X8 a0 Y8 s0
F1 j5 w2 x4 g4 f0 A8u B8 V8 B8R "s_cbranch_vccnz Lc3_3\n" "Lr3_3:\n" p4 i0 T1 p0 O2 A0 B4w o4 a2 A5Y N2 F0 X1 D2k I2 B0F n3 A0j B0G s3 R6 A0 C4Q A0
A5M p1 D7Z D7a l0 v4 D7b D7c c0 E1E w0 P1 N0 N1 x8 A00 A5e D7d D0 A0 B5N f7 A0 D7e D1 A8K D7f J1 c0 D9J J3 J2 A5f V2 D0 D20 K6 G2 A4t h3 Y1 D60 J3 A2
A5g B3 D0 D6z A0O d1 B9q h3 e1 R0 C4T C4U u1 B9r C4R a0 A5h U0 L7 C4S D7h l9 D7j D7i W0 D7k C4b z7 A8d J3 p3 A5i Y0 D0 A8e K6 m0 h3 D7g q0 B1l J3 b0
A5j i4 D0 B1m A0O J4 h3 N7 x2 C4Z C4W y2 C4X C4Y l2 w0 A7 d2 C4a D7l e2 Y5 D7m Z0 X5 B1 A5k D0 A99 t4 D7n Q1 S1 y3 D2j N1 A5l P1 E1X k5 M1 p0 A4u U4
A6L B0A B9u V1 e0 A0m x3 A1r B2L C4g A1s A7n C4h C4i i9 G3 A2X k3 H3 B6b Y2 c2 B1 b2 c0 "Lp3_3:\n" F0C B6c n1 Z0 o1 X5 n2 D24 A0F l5 Q1 B1V y3 l9 W6
B9v c0 A1F w0 A3b A0 A3k A0J B8i c8 J0 B5F B0u A7K q0 B6M m0 K0 Y0 C1 B2e i0 B06 B57 A8W C4V b3 f9 B2 v8 d5 C2 B4R l1 M0 B34 k1 j0 O3 w8 P1 I0 N1 F0
q1 G0 S1 p0 w3 A0 q4 K4 z3 A2f Y3 L1 y0 E1L Y5 D1 Q9 J1 D68 B5G J2 X6 B9t B7D x1 r0 B8j B35 Y2 M3 B6X o2 B8S f1 g0 W2 R0 B2g Q1 u1 B4z a0 E0 U0 A26 P4
B7o A2q A27 q3 c3 k0 p3 E9k A4L M1 Y0 B0C m0 A4M q0 X4 D87 i4 A0z v3 A1H E3 X0 Z3 S0 W3 T1 V0 d3 e0 T4 B0E t4 A2i S1 P0 q1 D8R I0 N1 x0 P1 F0 B6C G0
m1 A0 j9 x3 A4k B2L X6 J1 B7O Y2 D1 D86 B2G B5R B6i A78 l8 F4 O0 t3 A79 Q1 z2 B2m p2 E8 r5 y1 Q0 y3 V3 T0 F8 M1 s5 A3Y c0 G8 H8 W0 A6v A8E K9 U0 A2e
M0 A1C A8F L9 v0 N0 C60 B4f T6 A1I C4k Y0 J0 B7m A1u L8 A0z A1v C4P K0 A8v B1X B0D L3 A4 A0j "s_cbranch_vccnz Lc3_4\n" "Lr3_4:\n" M0 T2 F0 D2 G0 h2 T5
i2 E1Y t5 E1Z A9G L5 A9H X2 A9I U2 A9J k2 j1 C0M A00 C4p Z5 C4q a5 z3 B6A Y5 D1 B1 J1 B6P J2 X6 V2 B2j E1a Z0 u5 Y2 A01 E1b B0b Q1 M5 A1t C4r p1 m4
D4L C4s E0 K3 n4 D4M f2 H0 t1 B0c C0N y3 E1c M1 v5 S0 E1d V0 Q7 B7Q A0D a0 T1 B0d U0 B0e d0 A9M W0 A9N F0E C4t A0 C4 C4u I7 p3 D4 Y0 h0 A89 B7P b0 A1a
i4 x4 N4 F1 F0F F5S E1 F0 C4w G0 C4x A1b E1e A16 u0 E1f y8 N5 I8 Q2 J8 L2 o0 S3 B0 z8 E4S t4 l0 S1 O7 B5O x0 P1 A5p U4 D0 F1 B3R A4Q B2L A5q J1 D0 A4R
B5L A4q G4 A5r E1g D0 A9o w5 B1 E1h Z0 O5 O0 A3 E0 H2 Y6 A0E M2 A5s m2 A9W A2N Q1 z6 H0 E1i A2O P4 x5 A5t E1j D0 A4S q3 a9 c0 A4T M1 A2y A0k A5c A5n
a0 A5u s0 D0 A1c A0Y A1d v0 A03 b0 A5v A91 A04 P5 B2v A5w i1 Q0 B5v D84 X0 z1 P0 E2 N0 E1k C0 F0G E1l G0 B3I V4 Z1 B5P D25 J0 a1 C0 B5Q B4 b1 A9Q O3
A9R P1 K0 N1 A68 D7G D2K A28 D1 w0 A2P A3D J1 D64 h9 B6Y E1m X6 N9 B5J E1n A7o R7 D2L A1q D2M E0 W1 B5K E1o P4 "Lp3_4:\n" E4T q3 y5 M1 A3E E1p H0 C0D
D26 A1K d0 A9S A4b W0 A9U A4c s6 B2P p3 B7 B2Q Y0 p0 F0H b0 j0 i4 y0 X0 j3 B0 A7L B5S r0 f3 T0 I0 E1q F0 A0K G0 E1r g0 c9 B2R r2 E1 D2s B6h A8r x0 E91
J1 A0n B3U A0o B7R X4 E1s A0 A0Z m8 Z0 E1t A29 T9 Q5 O0 e3 T8 Q1 F2 E0 t2 U9 P4 C3 C4v Z6 H0 E1u Z8 M1 z5 S0 E1v A9 O8 C0J C0G B8m C9T q8 P8 U0 m9 a0
n9 s0 H7 B3G J7 g4 a8 b0 b8 B5T P3 E7 C0O F7 A4r h0 "s_cbranch_vccnz Lc3_5\n" "Lr3_5:\n" B4V f0 B9w B0 F3 D27 I0 a6 F0 A6 A7r s3 D5 k0 X1 Q0 G0 I2 T0
A7p C0K A7q Y1 N0 O3 C0 A7s A8h q1 J0 S1 A13 D2Q A3A J1 B4C h9 A1z s7 X6 V9 B5J n8 D4R P4 E5 A05 C7x O0 d1 D4S A5F e1 E0 t7 A5G q3 b6 M1 B6 H0 F5 D7H
T1 A5c A0m d0 A5x W0 A5y s6 A7t Y4 A7u A3l D28 B7S D29 i9 C5M Z5 C5N a5 C6 a7 G5 e4 V1 f4 d2 R0 A1e e2 B7T B7U c2 E1 A0 C5O p1 m4 Y5 C5P B1 x3 A2Q n4
N7 J1 Z0 D1 A3e k3 L1 O0 Q6 A3f Y2 A17 A7m B8y c6 A8H k8 B0r D6 A8I A2o A93 H5 M0 A09 Q1 C8G C5Q C0 H0 A3d B2V b2 A1A P4 B2W L6 n1 S0 M6 o1 D4 q3 B0i
n2 K0 A4d A0J u0 M1 C0P A7z u7 A4n N6 C5T d6 o0 C5U C5V B0 C4 A3B E6 V0 C0B A3C A35 B9 A7Q d0 l0 U0 P7 C9 a0 A1f s0 A1g c8 R0 C5a A6A C5b D0 v0 X0 A7l
A6C m0 D0 h0 Y0 B2x l3 Q3 A6D b3 D0 B2 q5 C2 I0 A1M A6E A4Y L7 F0 A0L G0 A1h A54 K4 l1 T1 A55 Y3 k1 A6F x1 D0 A4Z s1 A4a D3A A6G N1 D0 A00 A6H z3 D0
D2U A2R A5B A6I J2 Q0 B6T V2 g5 B1 v7 A4X N7 A0G N0 F6 C0 B4E k3 B65 D8G Y2 o2 J0 f1 C0 D8I B4F W2 O0 w7 E0 e6 K0 G6 B0C A8f C5 u1 u4 a0 B8F A6v B4Z
e0 z7 A2i Y4 A56 A3l B66 A0 A57 B7V A58 D7I f6 G0 H6 A59 D7J I5 A69 m1 c0 E1S A4e B1g E1 B4J q1 B8n N1 A8g B41 d7 A8r x0 B7 J1 B4h k3 A0 "Lp3_5:\n"
E4T D1 D86 B4i B7R j0 B1e F0D A3F Z9 Z0 I6 A6s l8 J5 O0 z2 A6t Q1 p2 E0 y1 r5 P4 V3 A3Z q3 s5 X4 x7 A3a M1 g6 H0 J6 B6F K5 M0 c5 A70 K9 A9j A71 A2u
A3J A2v L9 s0 A0s B7M A0t g4 i8 b0 j8 C0Q O6 E7 D3B F7 A4r h0 "s_cbranch_vccnz Lc3_6\n" "Lr3_6:\n" R0 T2 f0 D2 X0 h2 P0 i2 I0 l7 F0 t5 G0 h6 A4 L5 B1Y
u3 X2 B1Z r3 U2 B2t k2 B2u j1 D3A A7w N1 A00 r0 z3 B68 T0 v6 D1 A0v A5B B3V A36 V2 g0 m7 B0 A2E N7 u5 Z0 i6 B9y k3 M5 A1t K3 C72 k0 t1 B9z Y2 P2 E0 n7
B64 v5 C5 j6 u4 Q7 H0 D2W T1 D3C A0D W0 B42 s6 B43 Y4 B44 C0R A0 B45 B7W w6 N5 d8 Q2 h5 L2 c0 x6 B3W B5U A6J B1W A6K B5L D2u A0P A1J b5 w5 r9 Y6 k6 O0
O5 s9 Q1 A3 E0 H2 B91 M2 u4 m2 H0 z6 S0 o7 V0 x5 e0 l6 A3G A7R a9 T1 A3H A7S t0 B0l W0 B0m d0 A2F U0 A2H a0 A1S s0 A1T A0Y C5n Z5 C5o a5 E1N A7K D2i
f0 m0 T0 B8R V4 i0 i1 N0 s2 C0 z1 E2 E1 C5q O6 m4 C5r J0 B0I C0 n4 A7T P0 A0b I0 A4j B46 S4 Z1 K0 w1 u0 F0 a1 G0 b1 B47 B4 O3 o0 P1 B0 B48 N1 B49 B0B
l0 w3 c0 v4 C5s C4 C5t D4 A0 Y5 z3 N0 L1 C0 B8d D1 B1 J1 B3w J2 J0 V2 x8 B3x p7 X6 N9 Y2 Q1 A0H B0n R7 U8 C5u A72 C5v A5e R2 B90 q2 E0 h1 B5K W1 A5f
q7 D0 y3 y5 M1 X9 D2r A3I A5g u1 D0 C4j E1 D3C B5C B6B W0 A5h c3 L7 B5D B6D p3 B3J Y9 B40 B3K A1i q0 A5i A4P D2t j3 R4 g2 A5j I3 D0 A7L f3 P0 A3m I0
A0K A5k A07 D2g c9 G0 r2 B2R o3 A5l m3 E4U A1j A2S X3 A2T g3 D9 t4 K0 S1 C1 A7y q1 B0 B2V B2W N1 L6 P1 j0 U4 y0 M6 w2 A7W A14 x3 A2g A3q A8k C0S r0 L1
A8l B7R Q4 B7X t9 m8 Z0 m6 A2k T9 Q5 A1t e3 A2l Q1 F2 E0 t2 U9 P4 C3 A0c q3 Z6 A0d M1 r7 H0 z5 A92 n6 S0 A1k M0 B69 O8 t0 V0 A15 W0 D2h A4d d0 e0 A7z
U0 A0e P8 N6 C5z a0 A1l C50 s0 B0p B3G A2z g4 r8 A8u B5T C0O A4r "s_cbranch_vccnz Lc3_7\n" "Lr3_7:\n" c0 O2 C0T X0 D27 I0 A0 "Lp3_6:\n" E4T a6 F0 A6
G0 D5 F3 X1 B2z B7A B20 C0K B21 Y1 B22 O3 T0 A8h B0B g0 w3 B0 z3 s8 L1 A4C D1 k0 J1 "s_mov_b32 s92, 212992\n" A4D B3M J2 B2N V2 B3L t8 s7 X4 V9 A7 n8
A20 E5 L0 B1 A2 C5w Z0 d1 O0 e1 E0 t7 A99 b6 C5 B6 u4 F5 H0 D2W E1 D3C A0m W0 B23 s6 B24 Y4 B25 A3l E4V A0 B26 B7S D29 i9 C6 A0C A5 G5 y4 V1 z4 d2 D8
e2 N0 G3 P3 C0 E7 H3 F7 B1g J0 q1 C0 N1 A12 K0 c2 x4 x3 A2V A0 B1W A2W B5L D2u v9 A17 b5 c6 A7h k8 D6 O0 H5 M0 w9 Q1 b2 E0 n1 D2S o1 u4 n2 A2I A0J H0
u7 A6U d6 S0 E6 V0 h7 e0 A0F A3k W0 A0S d0 A4s U0 A3n a0 R9 s0 B5V B3X B4j A9k A9t l3 A1M D2v D3D A9v B3H A9w B9x A9x s1 A9y J9 D2w B1B D1 B4c J1 B8t
h9 B8v e7 v7 D2V C5R F6 B04 P4 B65 C5y O0 C7z Z5 B05 C70 E0 f1 a5 A3p W2 H0 w7 B5M q3 e6 M1 G6 S0 A8f V0 u1 u0 A10 A2i U0 A8m d0 o0 B7F A8n c3 w0 A0
B1c C73 p1 m4 C74 l0 p3 v4 n4 Y0 p0 B96 q0 B1d D3E D69 i4 B84 v3 R4 E3 D3F Z3 J0 W3 C0 P0 A2n I0 f6 E1 F0 C75 C4 D7I C76 K0 H6 B7 D4 I5 G0 m1 D7J d3
j0 T4 A7w A4e t4 r0 S1 T0 B7Y g0 U4 K7 F1 Z2 E1 A0 C78 A0 B2N C79 k0 x3 A0q A7 J2 x0 O7 J1 W8 D1 A5p D2Z x9 G4 Q4 A7U B4d Z9 A5q I6 D0 y9 Q1 J5 Z0 z2
A7i l8 p2 O0 y1 E0 V3 r5 P4 x7 u8 q3 g6 A5r s5 D0 S9 M1 J6 H0 K5 w0 A7j A2y A0x K9 A2G A0y L9 s0 A5s L8 A9W A0 i5 g4 X8 b0 Y8 C0Q j5 D3B B8J A2d A5t
C8m f0 "s_cbranch_vccnz Lc3_8\n" "Lr3_8:\n" C4B C8O q5 h2 A5u i2 A94 l7 F0 t5 G0 h6 A5v L5 D0 p0 X2 h0 U2 A4 k2 A5w j1 Q0 B0F u3 O3 D4a B0G r3 P1 w0
B1i A3D N1 N0 q1 C0 B1j A3E S1 B98 A13 B5N J0 D1 C0 f7 A8K B3Q B6m V2 K0 m7 A4t k3 u5 B1 i6 B9q Y2 M5 Z0 K3 A0V f2 O0 t1 B9r A01 P2 E0 n7 B0f Q1 v5 y3
j6 B1l M1 Q7 A3h D2W D3C A0D W0 A8d s6 A8e p3 "Lp3_7:\n" E4T A0 B1m C0R F1 B7W W6 N5 k5 B3Y l5 L2 A0A B3W "s_mov_b32 s92, 745472\n" B67 X4 A8g B3R A0
A8r A7 J1 B01 B1b x0 L1 B0W B5R D2p A2X Y6 w5 O0 k6 B6L O5 E0 A3 z9 A0E H2 C5 M2 u4 m2 B0Y z6 H0 o7 A5a x5 S0 l6 V0 B1h B1D A5n W0 g7 B1C d0 B4n U0
B2F a0 A8W s0 A0M E7 A0Y F7 A0l q0 I7 A2C A0X i0 u0 f0 i1 C8y V4 z1 o0 E2 B4U D2q A0b v8 S4 A4j T1 G0 Z1 A76 B7Z l0 a1 v4 A77 b1 w8 D3A N0 A5H A2f D3G
B5G h9 B7D p7 X6 N9 B5J A0H A9F R7 D2L A8G D2M E0 W1 B5K q7 y3 y5 M1 D2r C0D D26 A1K d0 A26 W0 A27 s6 A4L Y4 A4M A3l C0U F1 B5S B7I C0E D2s A0 A8r A7
A7P C0F D2u A4k T9 C8C I4 C8D A7x A2j E0 m8 C0 B7O U9 m6 C0I Q1 Q5 H0 e3 A6d A78 F2 J0 t2 C0 A79 P4 C3 S0 Z6 R0 E8 q3 C8E K0 C8F p1 B2w r7 C1 V0 z5 B0
F8 M1 n6 j0 A1k y0 G8 O8 A8A A5C B0 H8 P8 d0 r0 D2f B0a B8y a0 E1G E1H B0r s0 B4f A93 A4V R0 E1I E1J C8G C8H D4 A3d L6 M6 v0 A1u B0h B0i b0 A1v A9V
A4d q0 A1w L3 A7z m0 g0 N6 C8I Y0 "s_cbranch_vccnz Lc3_9\n" "Lr3_9:\n" A1x C8J i0 B0O C8L C4 O2 c0 X0 D3 k0 a2 "s_mov_b32 s92, 417792\n" P0 N2 I0 A1L
F0 a6 E1 G0 C8N X4 C8M p0 A6 h0 D5 F3 X1 A9H B7A A9I C0K A9G Y1 A9J U4 G7 "Lb3_2:\n" M7 A8h A00 x4 z3 A0 B6A C0S O0 J1 B6P J2 Q0 Y2 Y5 V2 T0 B2j s7 E0
V9 N7 A5G n8 B0b C4e E5 N0 A2 C0 A6i B3 B1 d1 B0c C4f e1 J0 t7 B8I b6 A5F Q1 B6 C4c C4d F5 c0 D2n B0d M1 a0 K0 U0 D2o B9S A0 e0 Y4 A0m A3l E4V A0 B0e
B7S A9M D3H W9 G0 C6 A9N D3I G5 I8 V1 c0 J8 d2 y8 e2 z8 D9K w3 S1 B5O A12 P3 x0 A0 D9L A4Q B1W A4R B5L A4q B5R C0V A2L k8 c6 O0 D6 A2M Q1 H5 E0 b2 M0
D2S n1 q3 "Lp3_8:\n" E4W M1 o1 H0 n2 B8k A0J S0 u7 A4S C0P d6 V0 E6 A4T D7o h7 B1D A7Q D7p B1V D7q D7r d0 A1c U0 A1d a0 A03 s0 A04 B5V C0W B3S A0B B3y
B3z l3 B30 D3J F0 A0L G0 D7A B5P K4 l1 u0 B5Q Y3 k1 A9Q x1 A9R s1 o0 U4 B0 A8h q1 l0 S1 P7 A13 D2K A6A D2l A28 A2P B3Q B0g V2 A6C v7 D0 A4U k3 A0G B1
F6 B3E Y2 K8 A6D M3 D0 A1q o2 Z0 f1 B3F A3p W2 A6E w7 L7 O0 e6 E0 G6 B5M C4l A8f C4m C4n u1 A6F a0 D0 C4o Q1 U0 y3 d0 A9S M1 W0 A6G z7 H0 p3 D0 S0 Y0
V0 A5D B2o b0 A6H i4 D0 f0 v3 R4 E3 B0E Z3 A6I W3 Q0 A9U B3O A2n D84 P0 f6 A8B H6 N0 I5 B8M m1 c0 F0 d3 G0 T4 J0 F4 C0 j9 t3 e0 w3 B8L B3P S1 K0 B7U
B41 A1G A3q A0n B3U A0o B5W A7U A5O D3K O0 I6 B7n J5 B2m z2 T8 r5 p2 C5 y1 C4v V3 H0 s5 Z8 M1 x7 S0 g6 A9 K9 J6 V0 K5 c0 A4W D7s A35 A3z L9 d0 D7t D7u
U0 q8 D7v a0 m9 A8i s0 n9 A8j L8 A0 H7 A4f C8B J7 C0Q C1 h0 C8Q a8 B0D Y0 B2x b8 L3 C4y j0 C4z y0 A7b m5 C40 "s_cbranch_vccnz Lc3_10\n" "Lr3_10:\n"
A4g C41 B0 C3V B4C C42 r0 C43 M0 A3A A7a T2 T0 F0 D2 A80 Y5 h2 g0 i2 G0 l7 X0 t5 r3 n5 h6 A7r C48 L5 P0 X2 A7p C46 U2 I0 k2 A7q u3 j1 B1 m7 A7s C44 u5
C45 N7 i6 A7 M5 E2X C5c K3 c0 Z0 f2 B0 E2Y F0I t1 O0 P2 D4R C5d n7 E0 v5 B2T j6 k0 Q7 Q0 D4S F0J D7w T0 A5x C5e D7x A5y C5f D7y N0 D7z C0 A7t C5W D70
A7u C5X D71 R0 C5Y C5Z C8S A0 Z5 C8T J0 D72 C0 a5 A9X H0 A9Y S0 A9Z K0 A9a u0 V0 A9b T1 e0 C8U D7Q m4 F0K C8V o0 A5z B0 n4 F0L A50 h0 A51 f0 A52 l0
A53 Q0 X0 C49 T1 p0 C8X E1 C4 F0M C8Y g5 D4 D73 N4 N0 v2 C0 D74 D75 Q2 D76 F0N R3 x0 i3 T1 A1P C8Z w0 P0 C8a o9 B1a J0 A16 C0 A31 A2U I0 N5 F0 a3 G0
L2 D9M D9N S3 e4 D77 T3 K0 U3 C1 D78 D79 A0Y B0 f4 D8A A1J a7 X9 w5 j0 k6 y0 A1e Y9 O5 B0 A7v A1i A3 p9 A1j H2 r0 M2 T0 A3B A2S m2 A3C A2T o7 D9 x5 g0
l6 B0 A1f B5E B1h L6 D8B B9 A3d H1 k0 I1 x8 C9 z0 M6 K1 A1g A14 C5A A6M D8C A1D A15 c1 A7M O1 D4T A6Q u2 C8P D8D O0 C5B A4X E4X C5C A6R C5D D0 C5E B6E
C5F H0 C5G A2R A7N C5H A6S i1 D0 S0 s2 B2U B4D z1 V0 A0 "Lp3_9:\n" E4W E2 A6T B0I A1E e0 A7T E0 A0b F0O F0P A4j w0 E4Y C5K Z1 A54 C5L w1 A6N a1 D0 A55
C5I b1 A4Z C5J p7 A4a D8E N9 A6O A0H D0 D8F D8H R7 D8G D8J A7z R2 p0 N6 C8b q2 A6P C8c h1 Q0 D8I B4E W1 D65 h0 q7 B4F F0Q y5 N0 b9 B84 A3I X0 D8K A56
F0R D8L J0 D8M C0 A57 C8d D8N c0 A58 C8e D8O A59 C8f D8P K0 D8Q F1 C8g A9c A9d A0 F0 A9e G0 A9f P0 A9g A0 I0 B9t F0S F0T J2 Q4 J1 Z0 D1 O0 L1 E0 Q6
F0U F0V j3 F1 g2 A0 C5 r2 u4 I3 H0 f3 S0 A3m V0 A0K e0 A07 B4B c9 A90 x0 w2 A6B o3 w0 m3 A3F X3 g3 A2D A4V A2j A2t m8 m6 B6F Q5 F0W e3 A70 L3 F2 A71
t2 A2u C3 A2v r7 z5 n6 C32 A1k L0 "s_cbranch_vccnz Lc3_11\n" "Lr3_11:\n" t0 A8D W0 F0X d0 u0 A6e C51 U0 E1w a0 A6X C52 s0 C53 C54 v0 A0u b0 o0 B2B v6
A0v A2C A36 i0 l0 O2 v4 A2E D3 E2d X0 N2 N0 D5 C0 B4G A6 R0 p0 X1 B6H I2 J0 G2 C0 F3 Y1 P0 E5 B4H n8 K0 A2 C1 A2w B3 B0 I0 d1 F0 e1 P3 E2o s3 F5 j0 B6
B8K G1 G0 K1 A5Q z0 R0 n3 I1 E2p H1 E2q S2 F1 E2r B36 A6J B1A A6K B8N O0 K2 A7m A5R E2s M4 x2 E2t H4 y2 H0 l2 E2u E2v G5 x0 C6 y0 A0P d2 B0L e2 r9 a4
V1 r0 G3 T0 s9 d8 H3 V0 H5 h5 w6 D6 A1 e0 b2 x6 b4 n1 C3Q B0 A5S o1 n2 A2x h7 k0 E6 A1I E2f E2g t0 A0C A2Z E2i E2j d0 N0 A1O E2k E2l i7 E2m E2n v0 J0
C60 A9s B7P m0 B02 Y0 B7G u0 B8d Q3 p0 b3 B3w B2 o0 C2 B3x A1h h0 A0L L0 A5U l1 f0 k1 A5T C80 l3 A6V I0 K8 A72 F6 F0 M3 B0n o2 G0 f1 t6 W2 A2Y A0a A5V
G6 A3P A6W M0 A7e G1 A1m K1 A3N z0 C56 C57 I1 C58 C59 H1 A9m c4 B2f U1 A6Z u2 A3O R1 C5k F5T B3T A6Y A0 A30 A1Z H0 E3 S0 Z3 V0 W3 e0 A6a A0p I5 x0 H6
B0 A39 d3 w0 C6A C6B T4 t9 m1 l0 F4 Q0 B0P t3 B0 C6D C6E J5 A7f I6 N0 z2 C0 A92 p2 A2Y Z2 A1W y1 J0 V3 C0 A7g K5 j7 C5g J6 A1l L3 A6b K0 A6c R0 C8A t0
"s_cbranch_vccnz Lc3_12\n" "Lr3_12:\n" C5h W0 C1 r8 C5i d0 B0 C5j V7 U0 j0 a0 W7 e8 s0 f8 X7 A3P v0 s8 Y7 A1m D87 C6F C6G q0 A3S C6H C6I B96 A9m Y0
A3Q B2f i0 f0 A6Z A6f A3R T2 C8O E2w E2x "Lp3_10:\n" E4W h2 P0 i2 M0 B8O C6J C6K X2 I0 C6L C6M U2 F0D A87 A0h k2 R0 F0 d9 j1 t8 B2z A0g A6g C6O K3 A3T
f2 B3N t1 B20 B21 P2 C6P G1 B22 D5s K1 A4 z0 R0 D5t B4k I1 T5 H1 B4l B23 S2 B0x C6N C6R A1p A0 C6Y B24 B25 B8q A7B A2V B26 B6R B6V A2W B0q N4 B6X v2
E1x E4 R3 O0 i3 A8J C6S C6T L2 A1 E0 A1n S3 y4 U5 A0I T3 g0 A1o U3 K7 A3V C6U C6V Q2 H0 C6W C6X A3 w0 z4 A5 A0h H2 k0 d9 M2 Q0 B6l A0g C3l c5 A3W A9j
E2y E2z A3J B5 U6 s0 A3g A0w a3 N0 v0 e9 B8h A7l R0 A88 m0 C6Z Y0 C7M A0B B6e A21 s2 C6a z1 C0 A1X C5m Z1 L0 e7 A9v w1 J0 a1 P7 A9w A9x b1 X0 B2q A9y
D4z q2 A6j h1 A1D D40 B04 W1 B8a B05 A8m K1 A6o z0 L0 A8n B1c I1 A94 H1 B1d k4 S2 A6p U1 D8R C8s A1y D0 R1 W8 O1 A6q A6l x9 B3T j3 M0 B4d g2 A6r I3
A1E u8 S9 B8l y9 C7v A6k X3 B07 g3 A7i B09 F0 F2 A7j r2 A6m C8l B0s A24 G0 t0 i5 B4 W0 A6n d0 E26 C8K U0 A1F A3b B4W A25 j5 A5N u0 f7 A8K P5 B6m i0 o0
w2 B0 A4t C6j A4u B1U A2J l0 F3 P0 "s_cbranch_vccnz Lc3_13\n" "Lr3_13:\n" T1 O2 I0 D3 Z4 a2 A5Y N2 C6k C6l X1 A7k I2 C6m C6f G2 A1Y Y1 C6g C6h A2 A0V
B3 C6i d1 A5Z e1 G1 A9C K1 z0 B73 M0 A1R B4I E0F O0 B80 O1 B0W C5l A2X H4 J4 Q4 x2 B6L y2 H0 l2 z9 k5 d2 S0 e2 F0Y l5 a4 G3 B99 Z0 H3 R0 W6 M4 b2 e0
n1 A0A b4 V1 N0 o1 C0 B0Y n2 t0 A5a W0 J0 A0i B56 A1O a0 B85 s0 K0 c2 B7 v0 A2f b0 A1H q0 A9E m0 j0 Y0 y0 A4K i0 f9 Q3 F4p D4j D4k b3 r0 B2 T0 D4l D4m
C2 p0 l1 D4n D4o k1 g0 x1 B0 D4p D4q s1 h0 M3 A2K A76 o2 k0 f1 Q0 A77 v8 A08 B8a T0 w8 B35 K1 B3u P3 D4d D4e I1 B8M B4g D4f D4g S2 J0 B36 D4h D4i u2
F0 R1 D4b D4c O1 x0 g7 c1 B8S B6N B2g q4 A6u A0X v3 C0 C8R E3 B1 Z3 B8z W3 A1 Z0 A6w L0 A4k d5 d3 A1t B31 A30 t3 C6C m1 B3N z2 c0 A6d A78 p2 K0 y1 u0
A79 K4 A4A H0 A2Y A0F E8 Y3 A3P A0 "Lp3_11:\n" E4W B1V S0 A1m l9 A3N U0 c0 A2e w0 A1C A0p A6V F8 G8 Z2 o0 A75 H8 A8E C6o C6p b0 j7 C6q C6r V5 q0 A8F
L3 B6O m0 l0 B8T Y0 "s_cbranch_vccnz Lc3_14\n" "Lr3_14:\n" A4N o5 i0 Q0 A4O C6s A6W c0 p0 T2 B0 A3O D2 C4O A6x N0 h2 C0 C4P i2 B6d X2 R0 B2h B2i U2 J0
k2 C0 B6P j1 A6h A9z B2j f2 K0 A6y F0Z B8U t1 f9 B8f B8V G1 j0 A9n A6i C6t C6u z0 P0 C6v C7 A1B B5H D7 o6 H1 I0 p6 T7 S2 A1G B6Q A1n A1p A0I u2 F0a
B7E A1o R1 Q9 U7 C6w O1 A3S C6x C6y B6R C6z V5 K2 T0 A3Q B6O A6b w0 Z0 B8T N4 A4Q m5 o5 v2 g0 C61 C62 R3 B0 A3R C63 C64 i3 O0 A0h L2 D4r D4s d9 B2A
A0g A6c A3T T3 A4R n5 U3 A9O E4 B8g A9P A2L H2 A2M u3 Q2 k0 M2 A4p I8 J8 m2 A1 F0 B8W y8 z8 W0 A1 G0 d0 L0 A2N r3 B6K H0 I9 A3b A2O B03 C65 b0 B97 q0
p0 C67 C68 m0 A9p C69 C7A Y0 C66 A1n i0 Q0 B0y A0I a3 R0 h0 A1o i1 T0 A3V C7B C7C s2 N0 C7D C7E z1 C0 B8Z A0h C5m d9 Z1 c0 C7J A0g w1 J0 a1 A3W b1 X0
R2 D4t D4u q2 B6Y h1 P0 W1 R0 A5o G1 e9 B6a B0z z0 A1 F0 B6I A1q B4g D5j A0 A7o U1 A1G V4 F9 B00 O1 C7H c1 A8B B8b A9h A3X g2 C7I I3 C0 A0Z B8l L0 A29
C7v O0 X3 E0 g3 T8 B09 A3h F2 A2a S4 r2 K0 t2 u0 Z8 B4 C3 c5 A9 S5 B1V A0k B8m A3J M0 q8 B4b o0 w2 B0 A4m v0 G7 "Lb3_3:\n" M7 C7P C7Q b0 H7 B8 J7 V8
l0 Y0 O7 A8z i0 "s_cbranch_vccnz Lc3_15\n" "Lr3_15:\n" C7e L0 O2 B2s D3 A60 a2 A1D A5P N2 I0 X1 L0 A1z I2 A64 B8P B4Y Y1 F0 A2 L0 B4X B3 A65 C47 B81
e1 B0Z A3c B8A p0 z0 D5R I1 A66 H1 D0 B6f c4 h0 A3L f0 F9 A1P O1 A67 c1 A1E A8I A09 B6V O0 A5R A1A o4 x2 A61 y2 D0 A69 A8H l2 E0 d2 e4 H4 e2 A62 G3 D0
f4 n3 X0 B38 H3 H0 p4 M4 b2 w0 B6k n1 A63 C8W A4n o1 g5 S0 n2 A3B a4 t0 N0 B8G A3C B9 A2G C9 b4 P0 S6 s3 B5F A2B B86 A7d m0 K0 Y0 A84 A0B c2 L0 A32
B3y A68 q4 A9t A2r l1 P0 k1 A33 x1 I0 s1 B1E M3 A1 F0 o2 A0 "Lp3_12:\n" E4Z q9 f1 G0 W2 B6U G1 B2b K1 A2s z0 Y3 C7f I1 A8i C7F H1 C7G C7N c4 C7O A8j
A3L B8n F9 A6B B2C A3F C36 A1Z A6s j4 E3 O0 Z3 C1 A6t O9 W3 B4m d3 A3Z q6 T4 H0 F4 j0 t3 y0 A3a P9 m1 B0L z2 A70 R5 p2 r0 y1 T0 A71 A2u A4A C0G A2v
A0s W0 g0 d0 A0t i8 U0 B0 A10 j8 A86 E1M B6i B98 v0 k0 b0 A0u A0 A0R Q0 v6 V8 B2I Y0 B6G i0 "s_cbranch_vccnz Lc3_16\n" "Lr3_16:\n" M0 T2 p1 D5B D5C D2
N0 h2 C0 D5D D5E i2 h0 X2 D5F D5G U2 J0 k2 C0 D5H D5I C4C B2X f2 K0 t1 u0 B4G P2 A37 G1 B6H K1 o0 z0 P3 D43 D44 I1 A1 A82 L0 D45 D46 S2 B0 I0 B19 D47
D48 R1 l0 O1 Q0 D49 D42 c1 B0 B4H B6N A2w A6Y g1 B1 A6a B8K A6f N0 N4 C0 A5Q A2Y v2 Z0 A3P R3 A6J m5 A1m i3 A1t C7S C7T A6u A3N C7U C7V L2 E0 A9m S3
A6K n5 B2f T3 E4 U5 A6Z U3 A3O A6w J0 Q2 C0 A0P A6g M0 F0 A3 B2n A1n H2 K0 A0I M2 C1 d8 u3 C7g C7h m2 B0 G0 C7i C7j t0 c0 h5 r3 A0h W0 H0 d9 d0 w6 x6
A1o B4A S0 A0g a0 A3S B2M C7m C7n s0 B5 U6 C7o C7p a3 A3T C7W C7X A6x j0 C7Y C7Z v0 y0 A5S C7 V7 b0 A4l W7 e8 q0 A2x f8 C7q m0 r0 C7r C7s Y0 T0 A9s
C7t X7 i0 X0 Y7 D7 A6y M0 A3V A3P i1 g0 A1m s2 B0 A3Q A0h z1 B02 d9 E2 k0 A0g Z1 c0 A3W C7a C7b w1 Q0 B8d C7c C7d a1 T0 A4h A9m y7 b1 D9G Z7 A6Z R2 R0
D9O q2 A4i h1 N0 W1 R0 D5L D5M G1 B4o B6W A3R z0 R0 f0 I1 M0 D5J D5K H1 R0 A5U S2 C0 A5T U1 A0q P0 F9 A72 O1 B0n c1 J0 B6V A5V j3 L0 B1 g2 A4p A7e I3
C7k f3 L0 A8k k4 o3 A05 m3 A8l V6 X3 C7l X5 g3 w0 O4 B09 e9 t9 F2 B0P r2 C7u t2 A7f C3 L0 I0 t0 u9 S4 A2Z F0 A0i C5p U0 B3N a0 B6J B4 B8X C7w B4W C0
A7g E3x q0 A4B m0 K0 Y0 u0 B2p i0 X0 B08 s8 B8O o0 x8 A87 A2J t8 B3n "s_cbranch_vccnz Lc3_17\n" "Lr3_17:\n" g1 O2 A6M D3 A1D F3 a2 A4E N2 G0 X1 A4F I2
A6Q B8P A20 Y1 P0 A2 L0 C5w A6R C47 A4G e1 A34 B33 A6S z0 D2g I1 Z4 H1 S2 A6T A3L x0 A1E u2 A4H y6 v9 c1 "Lp3_13:\n" E4Z A6N b7 A8J J4 C8P x2 O0 y2 A5
M4 l2 A6O d2 D0 y4 z4 C38 G3 D8 H4 H3 A6P V1 "s_add_u32 m0, s88, 97152\n" A2I b2 S0 n1 A6U o1 l0 B3v B60 t0 Q0 B4Z B0 B8s d0 N0 A1O A10 H0 s0 B8Y A5X
B6S q0 A21 P5 A1X A0B c2 e7 q4 B57 B3A B3z A4I B30 l1 A4J k1 I0 x1 A22 s1 F0 M3 C5y G0 f1 A23 A08 B2b G1 B55 Y3 z0 B2D B01 C8 W8 y6 x9 B53 A0T j4 v3
I4 E3 A0U Z3 E0 W3 C0 y9 q6 d3 H0 T4 B9p F4 S0 t3 K0 m1 C1 S9 R5 z2 B99 p2 A7j y1 e0 A4A A0x W5 t0 j0 W0 y0 A0y i5 d0 B0 U0 C8K a0 r0 s0 T0 j5 Z2 x4
g4 A25 b0 g0 q0 K7 f7 j7 m0 A8K A4r p1 D5Y D5Z k0 A0C "s_cbranch_vccnz Lc3_18\n" "Lr3_18:\n" R0 T2 h0 D2 D5S D5T h2 N0 i2 C0 D5U D5V X2 f0 U2 D5W D5X
k2 J0 j1 C0 B6m K3 X0 f2 A4t B0F t1 K0 P2 F5U D5a D5b G1 u0 P0 K1 D5c D5d z0 A1 I0 A1B D5e D5f H1 A1 B0G m5 c4 A5Y U1 o0 u2 B0 D5g D5h R1 P3 A7B E0B
l0 b7 A0V N4 M0 Z0 v2 Q0 A1Y R3 B4z i3 A9C C8h N0 C8i C0 A5Z C8j E0 C8k L0 A1r n5 L2 R0 E4 U5 S3 A1s A2X T3 J0 U3 C0 B6L Q2 A1 F0 A3 R0 z9 u3 H2 K0 M2
C1 k5 l5 m2 A1 G0 A8A W6 r3 W0 A2b B7o B8i U0 S0 I9 V0 E39 B0u b0 j0 q0 y0 B6M m0 A02 Y0 B2e i0 r0 B0N B8C i1 T0 A5E A9E z1 g0 E2 L0 A4K Z1 P7 f0 w1
A2K A76 a1 A6j B2O A77 v8 R2 A1D X0 q2 w8 B35 h1 A6o W1 D0 B8S G1 A8R K1 B2g A26 z0 A6p I1 D0 A27 A4L H1 g1 B72 A4M k4 U1 S7 F0 F9 A4k V6 O1 A6q A6l
C6C B3T j3 M0 A6d A78 g2 A6r I3 A1E A79 S4 B8l E8 C7v A6k X3 B07 g3 F8 B09 c0 G0 F2 G8 B4 r2 A6m C8l H8 A8E C3 A3Y A8F A4N W0 A6n d0 E94 A4O A1u E38
L0 B2S k0 B4W Q0 A1v A1w b0 B2I q0 B2h A1x m0 B70 B2i A9K A0B B08 A9L B2j J0 A8v o4 A2J p4 A0j F0 "s_cbranch_vccnz Lc3_19\n" "Lr3_19:\n" O2 c0 G0 D3
Z4 a2 B8V N2 X1 B8U I2 G2 B5H Y1 A2 A6i B3 d1 B7E e1 G1 B6Q K1 z0 L0 B2D B4I I4 R1 A1G E0 B0v A4Q H4 K2 A0 "Lp3_14:\n" E4Z B1 A5R A4R A9O x2 Z0 y2 A9P
M4 l2 A3h d2 C0 A9o e2 S0 G3 I8 a4 H3 K0 V1 u0 J8 y8 b2 V0 n1 z8 A2N o1 o0 B3v A2O A4S C8t A5C A4T b4 d0 A1c U0 l0 a0 A1d A03 s0 v4 c2 A04 v0 B39 B86
A28 A2P m0 B70 B0g i0 L0 h0 Q3 p1 D5k D5l b3 J0 B2 C0 D5m D5n C2 f0 l1 D5o D5p k1 K0 x1 C1 D5q D5r s1 f9 A1V A4U B5P o2 j0 f1 y0 B5Q A9Q W2 B4U C8n L0
A9R D4N C8o r0 C8p F5U D5u D5v C8q T0 A34 D5w D5x K1 g0 z0 B0 D5y D5z I1 F0 B4g D50 D51 S2 k0 U1 A1I D4O B3E A1y A0C R1 B3F A9S O1 N0 c1 C0 A9U B2P
B3T A1Z A0n B2Q E3 Z0 Z3 A0o q4 W3 B28 d3 R0 A5O d5 B31 A30 t3 B7n m1 M0 G0 z2 T8 K4 p2 J0 y1 C0 A2a Y3 V3 A3Y M0 Z8 A9 W0 A6v M0 A4W R5 U0 A2e M0 A1C
A3z W5 v0 K0 b0 u0 q8 m9 T6 n9 H7 Y0 o0 i0 B0 J7 a8 Z2 A0z b8 B2s l0 Q0 g5 A2A A5P A1z "s_cbranch_vccnz Lc3_20\n" "Lr3_20:\n" u6 T2 N0 D2 C0 E2h B3p
X0 X2 B3q J0 k2 O7 B3r B4R K3 A7c A60 t1 A1D B3t I0 A81 A64 z0 D0 B0M B77 A65 U1 A1G T5 A1y D0 R1 A1P O1 A66 A6l o9 B8r A0W A31 v2 A67 R3 A1E B6k i3
Z0 L2 e4 E4 S3 A61 T3 B07 U3 f4 B8g H0 H2 A4n Q2 A62 M2 D0 A1Q m2 R0 c5 B9 B5 W0 A63 d0 E1y C9 A1f C6n A1g D83 K0 b0 C1 A7d B2B D88 A84 Y0 j0 i0 y0
A32 a3 B6d i1 B0O s2 r0 z1 A33 E2 X0 Z1 A2r w1 P0 a1 q9 b1 I0 B2q B7v F0 h1 A2s W1 B0Z B7w V4 z0 E9 B2D B0x C8 B5x A7B B5y Z0 b7 A6s O4 j3 I4 g2 A6t
I3 E0 f3 A3Z h4 o3 H0 m3 A3a O9 X3 S0 g3 P9 S5 r2 V0 e3 A0r F2 e0 t2 A2u w4 C3 L0 T0 A2v t0 A0s W0 A0t d0 i8 U0 g0 a0 B0 E3X j8 s0 B08 E3Y E0m v0 k0
b0 E0n A0 B8 Q0 D88 T0 E0o A2d B2v E0p E0q S8 "s_cbranch_vccnz Lc3_21\n" "Lr3_21:\n" M0 O2 f0 D3 E0r E0s a2 J0 N2 C0 E0t E0u X1 X0 I2 p1 E0v D52 G2 K0
Y1 u0 D53 D54 A2 P0 B3 D55 D56 d1 o0 e1 B0 D57 D58 P6 I0 l4 D59 E33 r4 A4o s4 l0 e5 x0 v4 g9 M0 E34 E0w C8u A0 G0 C8v F0b D6A D6B C8w B4O C8x A0
"Lp3_15:\n" E4Z D6C D6D A11 N0 F5V D6E D6F g8 Z0 f5 D6G D6H k7 J0 h8 C0 E0x E35 B1F I4 B1G A6J E36 B1H E0 B1I A6K o4 g7 B1J A1 p4 M4 B1K B2Y H4 B1L K0
B1M B2Z r9 V1 L0 H0 B1N M0 s9 s3 B1O A0X B1P C1 d8 n3 B1Q B0L A0F h5 a4 W0 V0 l9 w6 x6 A3J M0 A3G b4 s0 j0 c2 y0 A3H B0l A75 b0 B0m A2F q0 r0 m0 T0
A2H A1S B0U L0 D8Y A1T Q3 g0 b3 B2r D8Z D8T B2 h0 C2 L0 D8U E0O l1 k0 k1 Q0 E0P D8a x1 B2k B8j D8b D8c M3 N0 o2 D8d D8e f1 X0 W2 D8f D8g P6 A8R l4 D8h
D8i r4 I0 s4 D8j D8k e5 A4o B4r D8l q4 A11 S7 G0 B9R A0f A8k d5 f5 J0 k7 A9l A8l A3y h8 Q4 A8L M0 A3v A2k A8M K0 A7D u0 A2l K4 A7E Z0 A8N A0c j4 D7E
O0 D22 E0 A7F q6 Y3 A8O w0 A0d u9 A8P o0 m1 B0 C5p A7G H0 A7H D2h A2y A0e R5 D4v e0 I9 A1l A86 l0 Z2 Q0 C8A A75 A0 b0 r8 A0R D89 V8 B70 D9A D9B i0
"s_cbranch_vccnz Lc3_22\n" "Lr3_22:\n" B2c D9C D9D D2 J0 h2 C0 D9E C82 B2y C83 C84 U2 K0 k2 C1 C85 C86 j1 B2d K3 B0 C87 C88 f2 j0 t1 C89 C9A P2 f0 P6
C9B C9C l4 X0 r4 L0 C9D m5 s4 P0 e5 y4 n5 B4r I0 A11 A1G F0 B9R y0 A4H f5 Q9 k7 v9 h8 r0 D7F T0 A8J A4v Z0 A4w z4 E4 B6n B28 A4x A5 u3 B87 D8 B88 g0
Q2 K7 A2I A4y G0 B6o A0w r3 A4z k0 A40 M0 B8h t0 Q0 A2q T0 A88 B7u D6U R9 C55 D6V D6W A89 Y0 D6X D6Y i0 J0 a3 D6Z B9E i1 A37 s2 B9F B9G z1 p0 E2 B9H
B9I Z1 h0 w1 B9J B9K a1 G7 "Lb3_4:\n" M7 P0 b1 B9L B9M R2 g1 I0 q2 B9N B9O h1 F0 W1 B9P k4 P6 B9C V6 C9H r4 C9I C9J s4 C9K S4 e5 B4 g9 D8X S7 B6p W8
B4u x9 C9F B6z B4d A41 o0 B6r B0 B4N B6s Z0 B6t u8 O4 B6u O0 B6v E0 B6w S9 h4 A42 B0o C1 A7j A43 D8y H0 A44 B0s A45 j0 A46 R0 i5 X8 c3 y0 c7 B0 Y8 B29
j5 B4b r0 w2 T0 E3h E3i v0 C9L C9M b0 E3j B8 g0 C8Q E3k A4r E0y E0z "s_cbranch_vccnz Lc3_23\n" "Lr3_23:\n" B89 B4w E00 C9N a2 k0 N2 E01 C9O X1 f0 I2
E02 E03 G2 X0 Y1 E04 E05 A2 P0 B3 F49 F5A d1 I0 C6Q E06 E07 A96 F0 l4 F5B F5C r4 G0 s4 F3 e5 C9P C9Q B4r C9R C9S B4s Z4 B9R T0 E4N E4O "Lp3_16:\n" F5W
f5 o0 k7 B0 D9H D9I h8 Q4 B1F M0 B6c B1G l0 B1H k5 l5 B1I Z0 B1J F5D M4 B1K O0 B1L E0 B1M F5E H4 B1N H0 B1O B0Y V1 S0 B1P F5F a4 B1Q M0 B9S F5G B1C
C9T B4n b4 U0 B2F C9U a0 A8W C9V B9T E08 C9W c2 E09 C9X v0 R4 b0 E4P E4Q T6 F5H F5I A0Q A0 D8m D8n Q3 A1 B3A D8o D8p B2 G0 C2 D8q D8r l1 F0 k1 D8s D8t
C80 D8u D8v M3 I0 o2 D8w B9B D8x d5 W2 Y3 C9Y A96 L0 C9Z C9a l4 C9b C9c r4 C9d K4 s4 C9e C9f B27 P3 C9E B37 g8 Z0 f5 B2G B6q A78 j4 A8L I4 A8M A79 E3c
A7D V0 A7E E3b E3a A8N A1 e0 B9D q6 W5 A7F E3Z E9q C5S A8O E9p E8 A8P S0 A7G F8 R5 A7H G8 C9g A9b H8 C9h A9a A1 A8E C9i A9Z A8F C9j A9Y A4N C9k A9X
A4O C9l C9m A1u C9n B6i A1v C9o C9p R4 C9q A1w j7 C9r L0 A1x C9s p0 C9t E0Q E0R C9u "s_cbranch_vccnz Lc3_24\n" "Lr3_24:\n" A0 T2 A0 E0S E0T D2 C9v h2
E0U A6X i2 A9i X2 E0V F5J U2 A0 C9w A0 F5K C9x j1 h0 A9z E0W B9U f2 A1H C9y A47 E0X B9V P2 x0 C9z A0 F5M C90 B4q A0j A4v A0 C91 C92 A4w A8S C93 F5N
C94 C95 A0 A4Q C96 C97 c0 C98 C99 A4x D0A A0 A8T D0B A0 D0C D0D B9W A4R A9O B9X A0 D0E A4y M0 A8U A9P D0F A9o A4z D0G D0H A40 E3g E3f D0I g1 D0J D0K
A9g A1 E3e E3d A9f g1 D0L D0M A9e L0 A8V A9d L0 D6a D6b A9c A1 D8W D8V D0N g1 D0O D0P D0Q A1 I8 J8 A2h g1 y8 z8 g8 u6 E0 f5 R0 B8k D0R k7 L0 B97 h8
F0B D0S D0T D0U D0V L0 A9p B6e D0W s2 A0 D0X D0Y B9Y D0Z Q4 z1 A0 B0y E2 R0 D0a D0b Z1 B9Z D0c D0d a1 D6I D6J B2O D6K D6L R2 A05 D0e D6M D0f D0g h1 x0
W1 O0 D6N D0h M0 D6O B9a A41 c0 D6P B9b H0 D0i S0 B9c B2M D0j B0w D0k D0l D0m D6Q D6R D0n D6S D0o M0 D6T D0p B9d D6c D6d D0q F5X F5Y A42 F5Z F5a D0r
u6 D0s A43 A1 F5b F5c A44 A1 F5d D0t D0u D0v A8S F5e D0w D0x A45 A0 F5f F5g A46 "s_cbranch_vccnz Lc3_25\n" "Lr3_25:\n" F5h F5i D0y A1 D0z D00 D01 F5j
F5k A8T D02 A1 F5l F5m D03 D04 D05 F5n F5o F5p F5q D06 F5r F5s F5t F5u D07 B8E D08 D09 A0 F5v A6e D1A B9e F5w D1B A8U D1C F5x F5y D1D D1E D1F D1G R0
F5z F50 D1H F51 F52 D1I D1J B60 A8V D1K D1L D1M A0 A3n D1N D6e D1O D6f D1P R9 D1Q D1R D1S D1T D1U B9E B9F D1V A8X C2 R0 B9G D1W B9H B2 A0 B9I D1X
"Lp3_17:\n" F5W D1Y B9J A8Y s1 A8Z D1Z B9K B9L D1a B9M D1b B9N A08 A8a D1c B9O B9P D1d B9f D1e D6g D1f D6h D1g B9g D1h B9h D1i B9i D6i D1j A8X D1k D6j
A8Y D1l A8Z D1m A8a D6k D6l D1n "Lend3:\n" ".fill (Lv3+73728-.)/4, 4, 0xBF800000\n" "Lp3_18:\n" F53 ".fill (Lv3+77824-.)/4, 4, 0xBF800000\n"
"Lp3_19:\n" F53 "Lc3_0:\n" B61 B1T D1o B3D B1S D6m "s_branch Lr3_0\n" "Lc3_1:\n" B62 D15 L4 "s_branch Lr3_1\n" "Lc3_2:\n" B9j A8c N3 A98 d4
"s_branch Lr3_2\n" "Lc3_3:\n" B9k D16 L4 "s_branch Lr3_3\n" "Lc3_4:\n" v1 D1q D1x N3 D6n r6 B3Z B9l D9P B5X E1z E4a B3B "s_branch Lr3_4\n" "Lc3_5:\n"
r1 D1r B9m W4 D1x p5 B1k A8q B3Z A3o "s_branch Lr3_5\n" "Lc3_6:\n" v1 D1s B63 N3 B9m r6 B0H A8p B1k A3j "s_branch Lr3_6\n" "Lc3_7:\n" r1 D1t B4y W4
B63 p5 A8o A8q B0H A3o "s_branch Lr3_7\n" "Lc3_8:\n" v1 D1u B3C N3 B4y r6 A7O A8p A8o A3j "s_branch Lr3_8\n" "Lc3_9:\n" r1 D1v B1R W4 B3C p5 A49 A8q
A7O A3o "s_branch Lr3_9\n" "Lc3_10:\n" v1 D1w A97 N3 B1R r6 A3i A8p A49 A3j "s_branch Lr3_10\n" "Lc3_11:\n" r1 D1y A8b W4 A97 p5 A3i A3o
"s_branch Lr3_11\n" "Lc3_12:\n" v1 D1z A7I N3 A8b d4 "s_branch Lr3_12\n" "Lc3_13:\n" r1 D10 A48 W4 A7I L4 "s_branch Lr3_13\n" "Lc3_14:\n" v1 D11 B4x
N3 A48 d4 "s_branch Lr3_14\n" "Lc3_15:\n" r1 D12 D14 L4 "s_branch Lr3_15\n" "Lc3_16:\n" D1p A5A N3 A7J d4 "s_branch Lr3_16\n" "Lc3_17:\n" D13 D17 L4
"s_branch Lr3_17\n" "Lc3_18:\n" v1 D6o D18 d4 "s_branch Lr3_18\n" "Lc3_19:\n" r1 D6p D19 L4 "s_branch Lr3_19\n" "Lc3_20:\n" v1 D80 D2A
"s_branch Lr3_20\n" "Lc3_21:\n" r1 D81 D2B "s_branch Lr3_21\n" "Lc3_22:\n" v1 E1C D2C "s_branch Lr3_22\n" "Lc3_23:\n" r1 E1D D2D "s_branch Lr3_23\n"
"Lc3_24:\n" v1 E3r D2E "s_branch Lr3_24\n" "Lc3_25:\n" r1 E3s D2F "s_branch Lr3_25\n" ".fill (Lv0+393216-.)/4, 4, 0xBF800000\n" "Lv4:\n" "Lb4_0:\n"
C0s C0m C0t C0u "s_mov_b32 s54, Lend4-Lv4\n" C0v D6q D6r C1e C0p C0x D6t D6u D6v D2G D6s E4b C0n X4 "s_mov_b32 s92, 327680\n" C0o C0q X4 C0r F0c A0N
C0w C0y g7 G7 "Lb4_1:\n" M7 "s_sleep 52\n" B7b C0z C01 B7c C02 C03 C00 C04 C05 C06 C07 C08 B5Z C1A C1B p8 C09 C1D C1E C1F B7d C1G C1H C1I C1K p8 C1J
C1c C1L C1M C1C C1N C1O C1P C1Q C1i p8 C1T C1b C1U C1l B7e C1m C1R C1S C1V C1W C1X C1Y C1Z C1a C1d C1g A5I "s_mov_b32 s92, 11264\n" C1f C1h C1j C1k
C1n C1o C1p C1r C1s A5J "s_addk_i32 s92, -7168\n" C1q C1t C1u C1v C1w C1x C1y C1z A5J C10 C11 C12 C13 C14 C16 C17 C18 C15 C19 C2A B7f C2B C2C A0 C2D
p1 B7g F0c B7h B7i B7j B5a B7k B5b B1r B5c B1s C2E C2F C2G E1 B5d A5K D35 C2H D36 p1 C2I C2J B3f S8 A1N C2K A8t C2L R6 C2M E1 C2N D37 C2O D38 B1t N0
C2P A2p C2Q C2R C2S C2T C2U B1u C2V T1 A7V B1t A2p C2W C2X C2Y C2Z E1 A8t A3s D39 C2a C2b C2c C2d B1u B9n B3g B9o C2f C2g C2e B3h C2h C2i A5L A3t A3u
R6 A3v A3w A3x A3y C2j C2k C2m C2l K0 C2n A7W j0 A7X D4A D4B C2o B0 D4C D4D B3i A0 g0 B1r B1s C2p E1 A7V D4E D7N B0 A3s B3j U0 Q0 C2v B5e D4F W0 B3k
B5g B1w C2r B5f A1N C2s C2t C2q S8 z7 D4G C2u B7l A8u B5h B9n q0 B5i E1V J3 E1W C2y m0 K6 B3g h3 B9o C2x C20 C2w B3l B3h E10 E4R B3k C2z D6y E11 B1f
B7l Y0 J0 J3 B5k A0O B1v C22 C21 h3 C23 B7m C24 C25 C26 C27 C28 B1x C29 B5l B1y B5n B5m B1z K0 J3 B10 u0 K6 C3A h3 B11 J3 C3B B12 F1 B3m C3E C3C C3D
C3F A5M B13 P3 B5r C3G B14 x0 C3I B5o B15 B5p "Lp4_1:\n" F0d C3H B16 B5q C3J B17 B5s B18 l0 A8v B3n v4 F3 c0 t0 C3K C3L A8w l9 C3M C3N A1O P0 A8x I0
S6 B1U C0 Z4 v0 C3O C3P A5N C1 B1 m0 A8 Y0 X5 B5t A7Y w0 B1x y0 H0 B1y B0L B1z r0 B10 E1 V0 B11 p1 e0 B12 x0 T0 A0 A19 V1 C3Q B0 A3X B13 A5O B14 D2H
B15 A29 B16 T8 B17 k0 B18 Q0 X0 B3n f0 T0 l3 c0 t0 A8y W0 Z0 l9 A7Z U0 O0 A8x E0 B1 s0 D9Q B3o A30 t6 v0 A0p b0 A8C q0 C3R m0 m9 C3S Y0 n9 i0 N0 C3T
m1 C0 H7 C3U Z2 I7 J7 R6 A8z A80 A2A A7a A5P C3V "s_cbranch_vccnz Lc4_0\n" "Lr4_0:\n" u6 C3W A7b D2 K0 C3X u0 B3p h0 C3Y B3q o0 C3Z B0 B3r B3s C3a A7c
l0 C3b Q0 B3t g5 X0 A81 B3u C0 B0M A82 M0 B5u A0q F0 B19 A1P R1 J0 B7q o9 C3c A31 C3d K0 v2 C1 Z0 C3e B0 a7 B7r C3f e4 u3 B2A C3g f4 B7s j0 C3h B7t
C3i y0 C3j B0 p9 r3 C3k r0 C3l A1Q t0 A2q A83 B7u A2B b0 g0 B2B A7d P5 A84 i0 k0 B0N A32 C3m Q0 A5E B2k C3n A33 E2 N0 C3o C0 A2r w1 X0 C3p q9 b1 J0
C3q L0 B7v P0 C3r A2s W1 A34 B7w C3s B5w G0 B7x A1R C3t B5x C3u B5y B1 b7 A2D C3v o0 g2 A8 C3w O9 C3x C3y A2t B7y C3z P9 B7z B0o F54 A0r C30 A0N H0 F2
A7C C31 C4F A24 A8D t0 j2 c7 i8 S5 d0 C4G U0 j2 D9R j8 w4 v0 B78 b0 j2 A0u v6 T6 B6G Y0 D4J i0 D4K B2X w2 A0z f0 o4 X0 B7d p4 A2J A0j F0
"s_cbranch_vccnz Lc4_1\n" "Lr4_1:\n" O2 w0 G0 D3 B79 a2 j2 C33 Z4 X1 C34 C4J G2 j2 A2w Y1 B71 A5I d1 C4A A5Q e1 F55 G1 B5z B3u C0 A1B H1 S2 J0 U1 A90
A5L u2 A38 R1 Z0 O1 A0P c1 K0 K2 A0 B2n J4 I4 x2 E0 y2 w6 H4 l2 B4O C37 M4 e2 H0 G3 h5 x6 H3 S0 V1 A3G a4 b2 V0 n1 A3H B0l o1 e0 n2 B0m b4 t0 A2F A5b
A2H d0 A1S U0 A1T i7 c2 B50 b0 A1U q0 A4h P5 B2a A0B Q3 A4i q4 A9k B2 A5T C2 A3U l1 A5U k1 P0 A0 "Lp4_2:\n" F0d x1 C39 C1 I0 M3 B0 B51 F0 f1 A7e W2 j0
G1 y0 A5V B8A B6g B0 B2b I1 r0 H1 T0 Y3 A1R B0x C8 g0 B8q A39 B2C t9 K2 k0 v3 P3 D7R D7S E3 Q0 Z0 Z3 T0 D7T D7U W3 g1 O0 d3 A2k A9q E0G A2l B32 N0 m1
C0 A7f z2 B75 A92 y1 J0 A4A A1W A95 A74 R5 D4v e0 I9 A0e A86 K0 Z2 u0 A4B A8Q A2z A0R r8 V8 o0 B8u s8 A4C i0 "s_cbranch_vccnz Lc4_2\n" "Lr4_2:\n" B2c
A4D B3M D2 l0 h2 A5d B3L t8 B2y A4F U2 D61 k2 j2 A4E j1 B2d K3 C4D D62 t1 j2 A20 P2 f0 G1 B33 A37 z0 A4G I1 D63 H1 j2 A4 S2 q5 B19 A4H R1 A5K O1 Q0 v9
c1 F0e B1 K2 A0 A8J N4 U8 v2 N0 R3 C0 A5 B7r L2 y4 z4 B2A T3 D8 B7s J0 Q2 C0 A2I A3 I0 H2 A0w u3 M2 K0 m2 M0 B8h t0 C4M F0 B7F A88 d0 G0 U0 A1F A3b r3
B03 C7M b0 j0 q0 C4N A21 m0 A02 Y0 A1X i0 r0 a3 T0 e7 i1 A1 A5E P0 z1 A4I E2 g0 Z1 K7 A4J w1 I0 a1 A22 b1 k0 B2q B54 F0 h1 A23 W1 B0Z B55 f0 z0 X0 I1
V4 B7x S2 E31 A0 D7V A85 x0 D7W u2 B1 R1 W8 B8N B7B O4 K2 A1t A9h B7C A0T g2 E0 I3 A0U h4 f3 H0 o3 B4N m3 S0 X3 u8 S5 g3 V0 r2 D7X S9 e3 e0 F2 D7Y w4
t2 B6Z A24 A2c t0 A0x W0 A0C A0y d0 i5 U0 X8 a0 Y8 D2I w2 C0 A0 j5 B59 B8 V8 J0 Y0 "s_cbranch_vccnz Lc4_3\n" "Lr4_3:\n" C0 F56 O2 T1 B4w K0 a2 u0 B1i
o4 N2 G0 X1 A7k I2 A8v p1 D7Z A5M D7a D7b D7c p4 A0j F0 Z4 l0 v4 B1j M0 E1E c0 A8h N0 q1 x8 S1 w3 x4 D7d A0 B5N A5e D2l f7 D7e J1 w0 A8K D7f J2 D9J J3
V2 A5f B8P D20 K6 Y1 A4t h3 A2 D60 J3 B3 A5g C47 D6z A0O e1 R0 B9q h3 u1 C4R C4S a0 B9r C4T B9s C4U C4b d0 A5h W0 c0 A8d D7j c3 L7 D7k D7i p3 A8e J3
Y0 A5i m0 D0 B1l K6 q0 h3 D7h b0 B1m J3 i4 A5j J4 D0 A0O D7g x2 h3 k3 y2 C4W C4X l2 w0 C4Y C4Z d2 C4a D7l e2 D7m D7n Y2 B2N X5 B38 A5G A5F t4 C5 S1 u4
D2j N1 p0 P1 A4u U4 A6L B0A A5k D0 V1 D21 A0m x3 A1r B2L C4g A1s A7n C4h C4i A2X i9 G3 A5l H3 E1X B6b k3 c2 A9D Y2 b2 "Lp4_3:\n" F0d c0 B1 n1 Z0 o1 X5
n2 D24 z9 t0 C5 W0 w0 k5 q3 d0 l5 B9v c0 A1F D21 w0 B9T A3k A0J W6 A0A c8 J0 B5F B0Y A7K q0 A5a m0 K0 Y0 C1 B56 i0 B06 B57 B85 b3 f9 B2 v8 d5 C2 A1 P0
l1 R0 B34 k1 j0 O3 w8 D9S P1 I0 N1 F0 q1 G0 S1 p0 w3 A0 q4 K4 z3 A2f Y3 F0f Y5 D1 B1 J1 y0 D68 B5G J2 B0 X6 B9t B7D x1 Y2 D9T B8j A8G M3 r0 o2 T0 A9F
f1 Z0 W2 B4L u1 A26 Q1 a0 g0 U0 B4m l9 y3 W0 M1 A3p z7 A27 B5M p3 k0 Y0 Q0 B8z m0 B2I B2o b0 p1 E7 D3E i4 N0 v3 B84 E3 X0 Z3 H0 W3 F7 D3F d3 w0 S0 T4
J0 t4 C0 V0 S1 e0 D2j N1 A2i P1 E8 D9U U4 K0 m1 u0 P0 x3 A4k F8 J2 I0 J1 B7O D7I D1 F0 L1 G0 Q6 D7J Y5 F4 C0I N7 t3 A7 Z2 A6d A78 z2 B1 p2 Z0 y1 X5 V3
c0 l8 r5 A79 t0 C5 W0 c0 G8 q3 d0 w0 H8 B9v A1F D21 w0 s0 B0j s5 B0a L8 o0 A75 B4f A7l E4c B1X m0 A1u B0D Y0 A1v L3 i0 l0 v4 "s_cbranch_vccnz Lc4_4\n"
"Lr4_4:\n" C4B A1w C4V D3L c0 A1x D9V i2 N0 E1Y f0 t5 X0 E1Z A4 L5 E4d X2 P0 U2 I0 k2 F0 j1 G0 O3 T5 P1 A9H N1 A9I q1 A9G S1 A9J A13 B6A D1 A9K J1 A9L
h9 C0 B2j E1a X6 u5 Y2 Q1 E1b B0b P4 M5 A05 K3 A6i f2 J0 t1 C0 B0c A01 P2 O0 E1c E0 v5 K0 E1d C1 B0f q3 Q7 B0 B0d M1 C4p H0 C4q A7x u1 j0 a0 c0 A7A
A0k A5C A0D s6 B0e Y4 A9M E4e w0 A1a C4r A2g m4 C4s q5 N4 n4 v2 I0 R3 F0 i3 G0 E1e A1b A16 A9N E1f y8 N5 I8 Q2 J8 L2 z8 S3 R0 C4t C4 C4u D4 E4S B67
A12 A5m B3R A4Q B1W A4R A7n C0F A9O k3 G4 c0 O0 C4w A0 A9P Y2 C4x E0 E1g A2L Y6 w5 A2M A0E E1h C5 O5 u4 A3 H0 H2 S0 M2 V0 m2 A2N A7R z6 e0 E1i A2O A7S
x5 A4S E1j A4T B1h A1c t0 A1d W0 A03 d0 A04 U0 i7 B1n F0g i0 R4 i1 p0 s2 h0 F57 E1k D2b G0 E1l f0 B3I V4 Z1 O7 B5P D25 B5Q B4 a1 A9Q b1 A5p O3 D0 A9R
A8h q1 A5q S1 D0 A13 D2K A5r D2l A28 A2P B3Q B0g V2 A5s E1m A9W A4U k3 N9 B1 E1n B3E Y2 R7 A5t R2 D0 A1q q2 Z0 h1 B3F A06 W1 A5u E1o B90 y5 E0 E1p A9T
"Lp4_4:\n" E4f Q1 B5B D2n A5v a0 D0 A9S B9v D2o A5w c3 y0 V0 p3 F42 e0 Y0 A1K A5D q0 A9U b0 r0 i4 T0 h0 j3 A1H g2 X0 I3 B2P D4P f3 B2Q D4Q E1q g0 A0K
B4U E1r I0 c9 F0 r2 w0 G0 o3 k0 m3 B2R X3 A68 D9W B5O A12 E4g w2 B98 x3 B6h A3q A0n B3U A0o B5W E1s A0 A5O D3M O0 E1t B7n Q5 D2O T8 D2P C5 t2 C4v C3
H0 Z6 Z8 M1 E1u S0 z5 A9 O8 E1v V0 C0J B8m A35 Q0 q8 P8 B5e m9 U0 n9 a0 H7 D2I B3G J7 g4 a8 b0 b8 D3N E4h i0 "s_cbranch_vccnz Lc4_5\n" "Lr4_5:\n" c0
O2 p0 D3 h0 a2 f0 N2 X0 A1L P0 a6 I0 A6 F0 D5 G0 X1 F3 I2 A7p s3 G2 A7q n3 Y1 A7r O3 A7s P1 O6 E7 N1 F7 D7G D2Q A3A J1 B4C h9 A1z s7 X6 V9 B5J n8 A3c
E5 U8 C7x O0 d1 E0 e1 A99 t7 y3 b6 M1 B6 H0 F5 D7H E1 e0 A1O A0m d0 A5x W0 A5y s6 A7t p3 J0 B8H A7u A5D B2o F58 i4 u0 f0 J4 R4 x2 B0A y2 o0 l2 P0 A2m
I0 W9 F0 C6 G0 G5 i9 V1 e4 d2 f4 e2 a7 G3 A1e H3 E12 D9L A3e B1W A3f B5L D2u o9 A17 b5 c6 A09 k8 D6 O0 H5 M0 A1A Q1 b2 E0 n1 D2S o1 u4 n2 H0 A0J S0 u7
V0 d6 e0 E6 A7v C0P h7 p9 A7Q t0 w0 A3B W0 A3C d0 B9 U0 C9 a0 A1f s0 A1g c8 C5M Z5 C5N a5 E1N A7K B6j B8R l3 i0 Q3 b3 B2 C2 A1M A4Y E1 C5O A0M m4 C5P
P0 A0L n4 A1h I0 l1 F0 k1 G0 x1 A54 K4 s1 A55 Y3 O3 A4Z X9 P1 A4a Y9 N1 B0r A93 q1 D9 A7y S1 B2V B2W L6 M6 w3 w0 B0i C5Q D4 A4d A3K z3 B4D Y5 A3M N6
L1 Q9 C5T C5U D1 B2U A2R J1 X6 J2 B6T V2 l0 C5V Q0 C4 A4X v7 g5 Z0 F59 A3p F6 B4E Q1 K8 N0 M3 P7 C7y M0 O0 C5a B4F P4 C5b A6A f1 D0 E0 W2 B5M q3 w7 M1
e6 A6C G6 D0 H0 A0a D7H A56 B3O U0 A6D B0k A5C T1 A57 B3P s6 A58 p3 A6E Y0 L7 A59 A5D B2o b0 A6F i4 D0 f0 v3 R4 E3 B0E Z3 A6G W3 B4e A2n I0 f6 F0 H6
A6H I5 D0 G0 m1 j9 E1S A6I F4 B4M A69 t3 B1g J0 q1 C0 N1 A12 K0 Z2 E1 B4J x3 A2g A4f B2L x0 B7 J1 B4h A7n j0 L1 B4i k3 Q6 B82 "Lp4_5:\n" E4f Y2 B1e Q4
Z9 B83 A6s I6 Z0 J5 A6t l8 z2 O0 p2 E0 y1 r5 Q1 V3 A3Z P4 s5 H0 x7 A3a q3 g6 O9 M1 J6 S0 K5 c0 P9 K9 B4a A0r B8o A2u L9 a0 A2v A4g s0 A0s B7M A0t g4
i8 b0 j8 B7N "s_cbranch_vccnz Lc4_6\n" "Lr4_6:\n" M0 T2 D2y P0 l7 I0 t5 F0 h6 y0 G0 L5 B0 A4 X2 B1Y C0H B1Z D2z r0 j1 T0 B2t O3 B2u A8h g0 q1 B0 S1
A13 B68 k0 D1 v6 A0v B3Q A36 V2 B1 m7 A2E k3 u5 Z0 i6 B9y Y2 M5 B4L K3 C72 E0 t1 B9z A01 P2 B0f Q1 n7 y3 v5 M1 j6 H0 Q7 D7B T1 A2e D9X U0 C0L A0D W0
B42 s6 B43 Y4 B44 C0R A0 B45 B7W w6 N5 d8 Q2 h5 L2 c0 x6 S3 P3 E7 T3 F7 U3 B67 A12 A0C A0 B3R A6J B1W A6K A7n x0 L1 B2Y k3 G4 N0 A1J C0 B2Z Y2 w5 Z0
k6 r9 Y6 O5 O0 A3 s9 Q1 H2 E0 M2 B91 m2 A3G q3 z6 J0 o7 C0 A3H M1 x5 H0 l6 A2x B1h c5 D2T A5n U0 A2F a0 K0 s0 A2H A0Y A1S v0 A1T b0 q0 I7 A2C D3O i1
V4 s2 z1 E2 q5 D2q C0C B46 S4 Z1 B47 B7Z B48 a1 B49 b1 J9 B5A L1 A1U D1 A4h J1 D9G h9 B3x p7 X6 N9 B5J A0H B0n R7 D2L A72 D2M E0 W1 B5K q7 y3 y5 M1
D2r C0D D26 A1K d0 B5C W0 B5D s6 B3J Y4 B3K A3l C0U F1 B5S F6A C5n Z5 B93 C5o G0 A3m a5 B94 A0K u0 A07 E13 w0 o3 o0 m3 B0 X3 E14 l0 S1 v4 B7Y N0 U4
B0R w2 E1 A0 C5q A0q m4 Y5 C5r J0 x3 C0 n4 N7 D3P A0M A7 D1 x0 L1 A39 G4 K0 A2j C1 t9 m8 A05 C5s M0 A2k T9 C5t B4z m6 A2l Q1 Q5 D2O C4 D2P j0 t2 y0 D4
P4 C3 A2b Z6 A0c q3 r7 r0 z5 T0 A0d M1 n6 S0 C0J u9 O8 B4a C5p B8o B6J P8 a0 g0 s0 B0 A7g C0X C5u R0 A1l C5v k0 v0 B0p B0h b0 A2z A9V q0 r8 V8 X4 B0U
"s_cbranch_vccnz Lc4_7\n" "Lr4_7:\n" X9 O2 c0 C0T X0 D27 I0 a6 F0 A6 G0 D5 F3 X1 B2z B7A B20 C0K B21 Y9 Y1 B22 A1i O3 A1j A2S P1 A2T N1 D9 q1 A7y S1
B2V B2W "Lp4_6:\n" E4f w3 L6 z3 s8 L1 M6 D1 Q0 B8O J1 T0 A8r A87 V2 B1 s7 t8 k3 V9 N0 n8 C0 B4k Y2 E5 U8 A2 C5w J0 d1 C0 B4l A5F e1 O0 t7 E0 b6 K0 B6
A5G Q1 F5 D2n T1 B23 M1 a0 B8F A6v B4Z e0 z7 A0m p3 B24 A14 Y0 A1H m0 X0 q0 p0 A7K i4 A0 E4V A0 B25 B7S B26 D3H W9 G0 A15 C6 A5 D3I A4d G5 y4 A7z N6
V1 z4 C5z C50 d2 M0 D8 e2 B7T B7J P3 A3q A2V B3U A2W B5W C0V B0q k8 c6 O0 D6 B8Q H5 E0 b2 M0 w9 A2o n1 C5 o1 u4 n2 A2I A0J H0 u7 A6U d6 S0 E6 V0 h7 e0
A0F A3k W0 A0S d0 A4s U0 A3n a0 R9 s0 B5V B3X B4j b3 u0 f0 A9t P0 D3J o0 A0L B0 F0 E3w l3 k1 A9v K4 x1 l0 s1 x8 A9w Y3 O3 A9x P1 A9y N1 A5e q1 D0 S1
A13 D2w A5f D2l A21 B3Q A1X V2 A5g v7 D0 e7 k3 A0G B1 F6 B04 Y2 K8 A5h M3 L7 C5y Z0 f1 B05 A3p W2 A5i w7 B90 e6 E0 G6 B5M Q1 A8f D2n A5j a0 D0 A8m B9v
D2o A5k c3 D0 V0 Y4 e0 Y0 A2i m0 A5l D2i p0 b0 F0e D2t A1Z A8n D3E E3 N0 Z3 C0 X0 W3 B1c D3F A2n P0 f6 B1d H6 J0 I5 B8M m1 w0 F0 d3 G0 T4 K0 F4 j9 t3
B7K A1G A3q B8w B3U B8x B5W A7U B7B D3K O0 I6 B7C A0T J5 E0 z2 M0 A0U r5 p2 C5 y1 y9 q3 V3 H0 s5 A7i M1 x7 S0 g6 u8 K9 J6 V0 K5 B7L A8A A5C A2c L9 d0
A0x U0 A0y a0 B7 i5 s0 X8 B7M Y8 g4 j5 A8u C0Q D3B A2d j0 i0 "s_cbranch_vccnz Lc4_8\n" "Lr4_8:\n" y0 w0 T2 A02 D3L r0 i2 B2k l7 X0 t5 A4 h6 B1i L5 g0
X2 P0 U2 I0 k2 F0 j1 G0 O3 T5 P1 B0F N1 B0G q1 B1j S1 K7 A13 B5N f7 D1 k0 J1 P3 C4k J2 F0h B2N V2 A9B m7 X4 u5 B0V i6 B1 M5 R0 D60 k3 C7z A05 C70 A7x
K3 O0 f2 D6z Y2 t1 E0 P2 A1Y n7 B64 v5 M0 C5 C73 "s_waitcnt vmcnt(8) lgkmcnt(3)\n" m4 q3 C74 H0 j6 n4 M1 Q7 D26 A0D d0 A8d W0 A8e c3 c0 A0 B1l C75 c0
C4 C76 I7 p3 D4 Y0 h0 A89 B7P b0 A1a i4 T1 A0 B1m C78 T1 A0 C79 Q0 P0 N4 T0 I0 v2 F0 R3 G0 i3 N0 B1a C0 A1b A16 A2U A0 "Lp4_7:\n" E4f W6 N5 J0 Q2 C0
k5 L2 l5 S3 A0A T3 K0 U3 B67 x0 A8g B3R A1r B1W A1s B5L B0X B5R D2p A9D Y6 w5 O0 k6 z9 Q1 O5 E0 A3 M0 B91 H2 u4 M2 H0 m2 B0Y z6 S0 o7 E4H A7R x5 V0 l6
D5Q a9 B1D B1C A7S A5b B4n d0 B2F U0 A8W a0 g7 s0 B1n F0g i0 A1H i1 C8y p0 D2a D2b h0 C0C V4 Z1 A76 D25 A77 B4 a1 v8 b1 w8 J9 A2f D3G B5G h9 B7D p7 X6
N9 B5J A0H A9F R7 D2L A8G D2M E0 W1 B5K q7 y3 y5 A0X b9 u0 A4L M1 B5B H0 D2W o0 U0 B0 A0k A5C A1K c3 l0 p3 O7 A0 A26 Y0 A27 A5D q0 A5p A4P D2t j3 R4
g2 A5q I3 D0 A7L f3 P0 A3m A4M A0K A5r A07 A94 c9 F0 r2 T1 G0 o3 A5s m3 A9W B2R X3 B8J E14 A5t S1 D0 B7Y A5u U4 E4g w2 D0 B98 x3 A0q A3q A4k C0S A5v
D2Z B7O B7R Q4 B7X B2G m8 A5w m6 Q0 B58 Q5 E9l Z0 e3 E8 T9 F2 O0 t2 E0 C3 U9 Q1 r7 F8 P4 z5 N0 Z6 C0 G8 q3 n6 H8 M1 C0J A3Y B0a W0 A6v A4N O8 C6n A4O
P8 v0 J0 C60 A1u C7L A1v Y0 B7G C4P A4V A0 A8v p0 p4 A0j F0 B0h A9V L3 G0 o4 Z4 "s_cbranch_vccnz Lc4_9\n" "Lr4_9:\n" w0 O2 D3 a2 N2 A1L a6 A6 A9G D5
A9H X1 A9I I2 A9J G2 Y1 C0M A00 B7 z3 B6A D1 A9K A5B j0 J2 y0 A9L B2J V2 Q9 s7 B2K N7 V9 r0 n8 T0 B0b k3 E5 Z0 A2 M0 D4L Y2 B3 g0 d1 D4M B0c e1 G7
"Lb4_2:\n" M7 O0 t7 E0 b6 C5 B6 A99 F5 u4 u1 A1F A7A E1 A0k B0 A5C A0m s6 B0d Y4 B0e Y0 k0 m0 Q0 p0 q0 T0 h0 b0 P3 E7 D7C i4 N0 J4
"s_mov_b32 s92, 434176\n" A0 F7 x2 f0 y2 X0 l2 X4 A2m A9M D7D W9 P0 C6 A9N G5 I0 V1 F0 d2 G0 e2 i9 G3 I8 H3 J8 t4 y8 S1 z8 B7J A0 A8r A7 A7P C0F x0 G4
E1 A4Q k8 C8C I4 C8D A7x A17 E0 c6 A4R A2o D6 A9O Q1 H5 H0 b2 A9P A2L n1 y3 o1 A2M M1 n2 C0 S0 A0J R0 V0 C8E O6 m4 C0P C8F e0 u7 n4 A7Q d6 J0 E6 B8k
h7 C0 B8y A0F B97 W0 B0r d0 A1c A2S U0 K0 A2T a0 u0 A1d D9 A7y s0 L6 M6 c8 M0 A03 B0i C8H o0 A0 "Lp4_8:\n" E4i v0 D4 A4d A3K D87 A3M N6 q0 A04 C8I C8J
A2C D3O C8L C4 q4 Q3 d5 b3 q5 B2 I0 C2 F0 A1M G0 A4Y E1 t6 C8N C8M A0L A9Q A1h B5P l1 B5Q k1 A9R x1 s1 J9 D2K A28 D1 A2P J1 D64 h9 B6Y v7 D2V C5R F6
A7o B65 A1q o2 O0 f1 E0 W2 B0C w7 y3 e6 M1 G6 H0 A0a w0 D26 A2i d0 A9S W0 A9U s6 B2P Y4 B2Q A3l B66 F1 B7V G0 f6 j9 H6 I5 D2x d3 B0 T4 A4e t4 l0 S1 v4
B7Y N0 U4 B0R B41 D2d A3q A0n C0S J0 L1 C0 A0o B7R Q4 A7U A0Z Z9 K0 I6 C1 A29 Q1 J5 A8 z2 T8 l8 p2 O0 y1 E0 V3 r5 P4 x7 C4v g6 j0 s5 y0 Z8 M1 J6 D23
H0 K5 w0 A7Z A2y A3z K9 A2G q8 L9 s0 r0 L8 T0 A4m A8Q H7 C0Q J7 D3B a8 A2d g0 i0 B0 b8 "s_cbranch_vccnz Lc4_10\n" "Lr4_10:\n" L0 T2 p0 D3L k0 i2 P7 f0
l7 X0 t5 P0 h6 A6A L5 A94 X2 E1 F0 U2 C9w A6C j1 D0 A4 O3 A7p u3 P1 A7q r3 N1 A6D q1 D0 A7r S1 A7s A13 F6B A6E D1 L7 A7a A3A B3Q C6e V2 A6F m7 D0 A7b
k3 u5 B1 i6 D4R Y2 M5 A6G K3 D0 A7c Z0 t1 D4S A01 P2 A6H n7 B90 v5 E0 j6 B0f Q1 D2m D2n A6I a0 Q0 A5x B9v F6C D2o A5y D3Q c3 N0 Y4 V0 Y0 e0 C0Y D3R
A0W A1a E4j A7t C92 A16 G0 A2U A7u C9x N5 e4 D3S C3h f4 L2 a7 S3 A1e T3 B8L U3 B67 A12 E1 B4J a3 "s_mov_b32 s92, 679936\n" B8n x3 X4 J2 A3e A5B B1b
A3f D2Y A1G A7 G4 A5m A1J o9 w5 Z0 k6 A09 Y6 O5 O0 A3 A1A Q1 H2 E0 M2 B91 m2 A7v q3 z6 H0 o7 p9 M1 x5 S0 l6 A3B A7R a9 c0 C0G A3C C9T B9 A7S U0 C9 a0
A1f s0 A1g B1n C0W B3S A0B i1 B5v D2d E7 k4 C81 F7 V6 B0I P0 A7T I0 A0b F0 A4j w0 G0 Z1 A54 D25 A55 B4 a1 A4Z b1 C0 A4a C0M q1 J0 S1 C0 A13 F6D K0 D1
u0 B2U A2R B3Q B6T V2 o0 p7 A4X k3 N9 B1 A0H B4E Y2 R7 U8 C8S Z5 D8G C8T O0 R2 a5 D8I q2 E0 h1 B4F A06 W1 A9T Q1 q7 y3 y5 M1 D2r A3I D7B T1 A2e A5c
A1K d0 A56 "Lp4_9:\n" E4i W0 A57 s6 A58 C8U F0i m4 C8V I7 p3 n4 Y0 h0 A89 B7P b0 A7L i4 F1 A59 j3 F1 B5S F6E T1 F0 C8X C4 B93 C8Y G0 A3m D4 B94 A0K
A07 E13 E15 B7U w2 E1 A0 C8Z A0 B2N C8a A7 x3 A2Q A6B A7P A3F D3T Q6 A6s T9 A2j I4 m8 A6t Q1 m6 E0 Q5 A3Z U9 e3 H0 F2 A3a P4 t2 S0 C3 F6F q3 Z6 V0 r7
F6G M1 z5 e0 n6 A70 O8 A1k M0 A71 P8 C8t F6H E1G W0 F6I E1H d0 E1I E1J U0 l0 a0 Q0 A0s X9 s0 B0 A0t Y9 A4V A0 F6J A1i v0 N0 C60 F6K B5T A9V A1j m0 L3
A2S Y0 J0 i0 "s_cbranch_vccnz Lc4_11\n" "Lr4_11:\n" A2T w0 O2 C0 p0 D3 h0 a2 K0 N2 C1 f0 A1L f9 a6 P0 A6 F6L o4 D5 j0 X1 y0 I0 I2 B0 B1Y p4 G2 F0 Y1
B1Z s3 U4 r0 P1 T0 G0 N1 F6M n3 q1 D9 S1 g0 w3 B0 A7y z3 A0 B68 L6 D1 v6 A0v J1 k0 J2 "s_mov_b32 s92, 581632\n" A36 V2 M6 s7 A2E N7 V9 X4 n8 A7 E5 F6N
Y5 B0i A2 M0 Z0 A4d B3 F6O F6P A7z d1 O0 N6 C8b e1 F6Q Q1 C8c t7 E0 b6 B1 B6 A99 F5 C4c C4d u1 C4e C4f a0 y3 U0 T1 F6R M1 d0 A2q S0 z7 V0 Y4 e0 Y0 A0m
A5D D3R A5R B0A F6S F6T D3H A2m Q0 F0 W9 T0 F6U F6V C6 G0 G5 F6W D3I V1 N0 d2 M0 F6X C8d e2 F6Y C8e G3 F6Z C8f H3 C8g w3 S1 B7J O6 A3q C0 E9h C0S J0
L1 C0 E9i B7R Q4 C0V B5o B5p c6 K0 D6 B5q B5s H5 U8 b2 O0 n1 E0 o1 D24 n2 F6a Q1 A0J H0 u7 F6b P4 d6 u4 E6 F6c F6d C0B c5 D2T A3k U0 D7o D7p a0 D7q
D7r s0 F6e c8 F6f v0 F0j b0 F0k q0 p0 A2C D3O Q3 l3 b3 B2 C2 q5 D3J F0 A0L G0 D7A F6g K4 l1 F6h Y3 k1 F6i x1 F6j s1 U4 P1 A5H F6k L1 F6l D1 F6m J1 F6n
h9 F6o F6p v7 D2V C5R F6 F6q F6r B65 F6s F6t o2 O0 f1 E0 W2 B0C w7 C4l C4m e6 C4n C4o G6 y3 A0a w0 F6u M1 u1 u0 A1F A7A A0k o0 B7F e0 z7 A2i Y4 F6v Y0
l0 m0 Q0 p0 q0 A4l D2R A0 F6w D3E v3 N0 E3 C0 X0 Z3 F6x D3F W3 P0 A2n J0 f6 D23 I0 H6 F0 I5 G0 m1 T1 j9 d3 K0 T4 E4k A4e w3 A6M S1 D0 B7Y A6N U4 D0 F1
B41 P3 A3q F6y C0S A6O D2Z F6z B7R Q4 A7U F60 F61 Z9 A6P "Lp4_10:\n" E4i I6 B4K F62 F63 J5 Z0 z2 R0 F64 l8 p2 O0 y1 E0 V3 r5 Q1 x7 F65 P4 g6 A6Q s5 D0
F66 q3 J6 M1 D7s K5 F67 D7t t0 c0 A2q F68 D7u d0 A7A D9R B0j v0 F69 D7v b0 A6R A91 F7A F7B P5 F7C F7D i0 A6S L8 D0 F7E F7F C4y A0z C4z F7G B1X C40 F7H
B0D C41 F7I L3 C42 A6T C43 C1 F7J F7K "s_add_u32 m0, s88, 4480\n" "s_cbranch_vccnz Lc4_12\n" "Lr4_12:\n" A1 T2 P0 D2 F7L m5 h2 j0 i2 y0 I0 l7 B0 F0 t5
G0 h6 r0 L5 B2k X2 X0 U2 F7M u3 k2 g0 j1 F7N r3 m7 n5 C44 u5 F7O C45 i6 C46 C48 M5 F7P Y5 A9z B1 f2 X6 t1 F0l Y2 P2 Z0 n7 F0m F0n v5 O0 j6 F0o F0I Q7
E0 D7w F0p F0J D7x F0q C5W D7y F0r C5X D7z F0s C5Y D70 C5Z C5c D71 A0 C5d C5e D72 A0 C5f A9X A0 S0 A9Y V0 A9Z e0 A9a H0 A9b A0 F0K F0L A5z F1 A50 I7
A51 h0 A52 f0 A53 X0 C49 E1 F0M F0N N4 D77 D78 v2 B0 D73 D74 Q2 D75 D76 R3 P0 i3 I0 B1a k0 A16 Q0 F0 A2U d7 G0 N5 x0 T0 a3 F0t D9M L2 F0u D9N S3 N0 T3
C0 F0v F0w U3 D79 D8A A0Y E4l E4m A1J J0 w5 C0 E4n k6 E4o O5 F0x A3 K0 H2 u0 F0y M2 E4p m2 E4q o7 o0 x5 B0 F0z l6 F00 a9 R0 E4r D8B l0 H1 v4 E4s I1
E4t z0 E4u K1 N0 C5A S7 C0 E4v D8C E4w c1 E4x E8a R1 J0 u2 B8I D8D I4 C5B A0 F01 E4X C5C K0 C5D P7 F02 F03 C5F V0 C5G F04 F05 C5H A6j i1 R0 D0 F06 s2
e0 z1 E0 E2 A6k B0I D0 F0P E4Y A7T C5I C5J A0b F07 D8E A4j A6m Z1 D0 F08 F09 w1 H0 a1 F1A D8F b1 A6n p7 B4K D8H D8J N9 S0 A0H F1B F0O R7 p0 R2 F1C C5K
q2 A6o h1 D0 F1D C5L W1 h0 q7 F1E F0Q y5 A6p b9 D0 f0 B5B X0 D8K F1F F0R D8L F1G D8M A6q D8N D0 F1H D8O F1I D8P F1 E4y D8Q A6r A9c E4z A9d I0 A9e G0
A9f P0 A9g A0 F0 B9t F0S F0T J2 b5 J1 O0 D1 E0 L1 B1 Q6 F0U F0V A9h E40 g2 A0 C5 r2 u4 I3 H0 f3 S0 A3m V0 A0K e0 A07 B4B c9 A2g x0 E41 w2 T1 C1 E4N o3
"s_add_u32 m0, s88, 55168\n" E4O m3 j0 X3 y0 F7Q F7R g3 B0 A4V A3u A3t A2j r0 m8 T0 E42 E43 m6 F0W Q5 E44 E45 e3 g0 F2 E46 L3 t2 E47 C3 E48 r7 E49 z5
E5A n6 E5B A1k L0 E5C t0 "s_cbranch_vccnz Lc4_13\n" "Lr4_13:\n" E5D W0 E1w d0 E5E A6X U0 F0X a0 E5F C51 s0 E5G C52 v0 D4w C53 b0 D4x C54 C7L F7S F7T
Y0 B2x F7U F7V O2 X0 D3 p0 a2 F3 E3t D5 E5H E5I A6 M0 I0 X1 E5J E5K I2 F0 G2 B0 G0 g7 Y1 Z4 E5 E5L E5M n8 k0 A2 E5N E5O B3 A6e d1 E5P e1 E5Q F5 A0X B6
Q0 E5R E5S G1 T0 K1 E5T E5U z0 N0 A0 "Lp4_11:\n" E4i A1B E5V H1 E5W S2 F1 E5X B36 E5Y B1A C5k C5l A7Y J4 p1 D4d x2 A6L y2 S0 l2 x0 G5 C0 E5Z a4 C6 J0
d2 C0 E5a E5b e2 V0 V1 E5c E5d G3 K0 H3 u0 E5e E5f H5 e0 D6 L0 E5g b4 b2 o0 n1 B0 E5h E5i c2 o1 E5j E5k n2 l0 h7 F0Y E5l E5m E6 g5 B8W E5n E5o A8w B2E
F7W F7X U0 a0 D4e S6 B5F E5p b0 E5q q0 I7 m0 E5r E5s Y0 B7G E5t E5u B57 h0 A9k B2 P0 C2 I0 A1h E5v E5w E4C l1 F7Y F7Z k1 G0 x1 X0 s1 l3 A6V t6 K8 E5x
E5y F6 M3 F7a F7b o2 A2Y f1 D4f o6 W2 D4g p6 A0a E5z T7 G6 E50 U7 A6W L0 F7c C56 G1 F7d C57 K1 D4b C58 z0 D4c C59 I1 D4h V5 H1 D4i o8 S2 A0q D4j y7 U1
D4k Z7 B1A E51 B8N O0 K2 A7m A6Y A0 A30 A1Z H0 E3 S0 Z3 V0 W3 O6 e0 I5 x0 H6 C1 F7e R5 d3 T1 F7f W5 A6Z T4 B0 F7g C6A A6a j0 m1 y0 E52 F4 B0 C6B C6D
t3 F7h E97 J5 r0 I6 T0 E98 F7i z2 C6E C7 p2 F7j E53 Z2 g0 y1 B0 E54 F7k V3 D7 A0 j7 K5 F7l L3 J6 k0 A6b R0 E55 E56 t0 "s_cbranch_vccnz Lc4_14\n"
"Lr4_14:\n" o6 W0 Q0 F7m F7n B5e p6 T7 U0 E57 E58 D2J D4l U7 A6c D4m C6F v0 F7o C6G E0I C6H C6I q0 E59 A9m A5D B2f Y0 F7p F7q A6Z i0 M0 h0 C5g C5h T2
E6A C5i C5j D2 f0 A1n h2 E6B E6C A0I i2 P0 A1o A6f E6D X2 F54 I0 U2 F7r F7s k2 M0 F0 j1 G7 "Lb4_3:\n" M7 F7t F7u C6J C6K K3 C6O C6L C6M f2 F7v E6E A0h
t1 L0 G0 d9 P2 E6F E6G A0g A6g C6P G1 E6H K1 A4 z0 c0 E6I E6J I1 T5 H1 E6K E6L c4 C6N C6R A6u E6M E6N U1 b5 u2 E6O C6S R1 p1 O0 B0v C0 E6P E6Q K2 J0
N4 R0 D9H D9I v2 C0 E0 A6w E6R U5 R3 K0 C6T i3 u0 E6S E6T A1n L2 B1 A0I S3 o0 A1o T3 B0 E6U C6U C6V U3 c0 E4 C6W C6X M8 Q2 l0 N8 G9 A3 Q0 E6V E6W H9
Q8 H2 A2b R8 C6f M2 E6X C6g C6h m2 N0 C6i C7 t0 E6Y E6Z D7 o6 W0 S0 p6 T7 d0 E6a B5 U7 U0 e9 A2e c0 A3N A1C c0 U6 C6o A6V E6b E6c a3 C6Z v0 E6d E6e
C6p C6q b0 R0 h0 C6r V5 q0 D9Y D9Z B6O m0 C6a B8T Y0 D9a D9b o5 i0 X0 A6W A3O A6x p0 i1 F7w F7x s2 f0 z1 F7y F7z E2 A1 I0 Z1 F70 F71 w1 F0 a1 D4n D4o
b1 G0 B2q D4p D4q q2 P0 A6y F1J F1K h1 V4 C6s C6t W1 F1L F1M C6u C6v G1 E9 A2Y A9n F1N F1O A3P z0 A1m I1 A3Q C6w C6x H1 "Lp4_12:\n" E6f C6y C6z A9m c4
F1P F1Q B2f U1 A6Z C6j C6k u2 p1 A3R C6l C6m B80 A1n O1 E16 D4r D4s A0I c1 F1R F1S A1o K2 J0 A6b R0 A3S j3 C0 B1 g2 F1T F1U C61 C62 I3 K0 C63 C64 f3
c0 A39 A0h o3 F0Z Z0 d9 m3 B4z A0g A6c j0 X3 F0a E0 g3 B0 A3T O4 B09 T1 t9 F2 B0P r2 r0 t2 F1V A7f C3 A3Y A92 W0 A1 A6v L0 B69 S5 B6K V0 I9 A1C g0 w4
A75 D2h b0 A0z q0 A0e A1l C67 C68 m0 k0 C69 C7A Y0 A4p C8A A1n i0 f0 A0I B08 s8 r8 A1o C65 A3V A4C A2J A4D B3M C66 A0C "s_cbranch_vccnz Lc4_15\n"
"Lr4_15:\n" B4V X0 C7B C7C D3 B3L t8 C7D C7E a2 N0 A0h N2 C0 A4F d9 X1 p0 A0g I2 A3W G2 J0 Y1 C0 D4t D4u A2 R0 P0 B3 A4E d1 K0 e1 u0 C7K G1 B6a A20 z0
o0 I1 e9 D4y C4I L0 A4G U1 d7 C35 A2V o4 O1 C7H c1 A2W p4 B3T A5R v9 x2 C7I y2 B0 A8J l2 A05 d2 l0 e2 Q0 O0 G3 A9r E0 H3 A7Y b2 y4 s3 n1 N0 V1 C0 z4
n3 o1 H0 n2 A5 D8 A0F c7 B95 a4 A2G A3g b4 C8z C0 A6U v0 C7F C7G b0 B60 q0 K0 m0 C1 B8s B8u E4I L0 A5X B1B Q3 j0 b3 y0 B4c B8t B2 F72 h0 C2 B8v e7 l1
B06 k1 A4J x1 r0 s1 T0 A4I M3 A37 o2 C8r f1 g0 W2 O7 A22 G1 P0 A9n D41 z0 A60 I1 D0 A23 H1 B72 l3 A85 F0 F9 W8 O1 A61 A6l A0P B3T v3 M0 B2n E3 A62 C6b
F1W K4 C6c F1X A9q A63 F4 B4K X5 t3 F1Y B4S G0 p2 F1Z Y3 m1 A64 y1 D0 F1a F1b V3 M0 A3Y F1c F1d W0 A65 B0k F1e F1f C6d F1g A0p v0 A66 A4P B5A F1h T6
A1U A4h Y0 A67 i0 K7 D9G D20 Z2 A0z A4t f0 k0 A0C X0 A2A A4 A0j "s_cbranch_vccnz Lc4_16\n" "Lr4_16:\n" c0 T2 N0 D2 C0 F0 h2 F73 F74 i2 C0A F75 F76 U2
J0 k2 F0n F0o j1 T5 K3 F0l F0m f2 C7N C7O t1 F0r F0s P2 C7P C7Q G1 F0p F0q K1 C7e C7f z0 L0 A69 I1 A68 H1 A1R B4I D4T A1G Z0 B0v F0t E4 B8D A0W F0u
F0v v2 E0 R3 F0w U5 i3 A3h L2 E4l E4m S3 S0 T3 E4n B5 U3 V0 Q2 E4o F0x A3 e0 H2 C0 F0y U6 M2 E4p A8i m2 R0 E4q A8j t0 F0z A4f W0 K0 d0 F00 E4r U0 A4g
A3D a0 u0 E4s E4t s0 A3E A4b a3 E4u A0 "Lp4_13:\n" E6f A4c v0 F06 b0 o0 B2B F05 F04 A89 Y0 F03 F02 i0 A3U i1 l0 s2 Q0 F01 V6 z1 A02 E2 p1 D43 D44 Z1
N0 w1 C0 D45 D46 a1 h0 b1 D47 D48 R2 A1 P0 q2 M0 F08 F09 A6Y J0 h1 C0 F07 F1A W1 A34 F1C F1D K1 K0 z0 C1 F1B F1E I1 B0 F0 H1 D49 D42 S2 g1 G0 A6a A0
F1F F1G U1 j0 C6Y F1H F1I B8q A7B p1 D5B k4 B6R K2 T0 D5C S4 A2Y j3 F4C Z0 A3P g2 x0 A1m A6u g0 I3 B0 F1i C3x o3 F1j F1k m3 k0 X3 Q0 E0 g3 c0 F1l B09
E6g E0M B4 C7S C7T F2 E0N C7U C7V V5 r2 N0 B6O t2 C0 A3N B8T C3 A3h o5 A6w A3O A2y D5D S5 l9 B7H A10 D5E w4 S6 w2 C0 F1m F1n A6f C7W C7X v0 E17 A73 b0
E18 L3 q0 K0 m0 u0 F1o F1p Y0 "s_cbranch_vccnz Lc4_17\n" "Lr4_17:\n" E19 i0 A1 E2A C7Y A6g A6h O2 M0 D5F D5G C7Z D3 F1q F1r A2Y a2 E2B E2C A3P N2 A37
A1m X1 L0 A3Q C7a C7b I2 E9R p4 C7c C7d G2 E9S A9m y7 Y1 M0 F77 Z7 A6Z A2 M0 F78 F79 A1n B3 F8A A0I X7 d1 o0 Y7 e1 F8B A3R A6x R0 p0 G1 D5J D5K A9n h0
z0 M0 D5H D5I I1 l0 H1 R0 A3S S2 A1I F8C F8D C7g C7h A1p v4 E6h E6i C7i C7j u2 N0 A0h R1 A4p E6j E6k d9 O1 L0 B5H A0g c1 C7k K2 A0 A3T A6y B4O J4 E6l
E6m x2 C7l y2 C0 B7E l2 A05 d2 A4Q o4 e2 O0 G3 E0 H3 c0 A7Y A4R b2 w0 A4q n1 A9o V1 J0 o1 C0 E2D E2E n2 A1 P0 C7m C7n B8W y8 z8 C7o C7p W0 A1 I0 A1n
A0i E2F s3 A0I U0 F0 A1o a0 c0 A3V G0 B3o C7q C7r s0 K0 n3 A19 C7s C7t c2 C1 E2G A4S A0h v0 A02 d9 b0 A4T A1c A0g q0 j0 m0 y0 A3W B8u B2v D5L D5M Q3
R0 A1d q4 b3 B0y B2 r0 C2 e9 B8Z l1 B3s k1 L0 C7J x1 C7u B8j B6Y A1V X0 o2 A5o f1 C7w W2 L0 B0z G1 T0 P0 A9n A1q z0 g0 A1B A7o H1 A9r B72 B00 U1 d7 F0
F9 k0 O1 Q0 A8B c1 T0 B1 b7 A0n d5 v3 N0 E3 C0 A0o A5O Z3 Z0 W3 D2H j4 d3 O0 B31 A29 B32 J0 m1 M0 T8 K4 z2 D2k p2 A2a Y3 y1 K0 V3
"s_addk_i32 m0, -21376\n" Z8 A9 A8A A2q B8m d0 A7A R0 D9R q8 A0p v0 o0 b0 B0 A4m B7P m0 B4T Y0 l0 i0 Q0 A8z Z2 g5 p0 B2s A3A S8 B4C A2A A1z h0
"s_cbranch_vccnz Lc4_18\n" "Lr4_18:\n" g1 T2 J0 D2 A0N E2h B3p P0 X2 B3q A6Q k2 D0 B3r I0 "Lp4_14:\n" E6f C71 A7c A6R t1 D0 B3t F0 A81 A6S z0 D0 B0M
G0 H1 T5 S2 A6T U1 A90 x0 E1y u2 D5O D5P y6 E6n E6o c1 K0 b7 E6p E6q N4 x8 Z0 v2 O0 R3 a7 E0H E0 L2 e4 U5 S3 A6M T3 D0 f4 A1e U3 H0 Q2 A4n A3 A6N H2
D0 A1Q M2 C77 L0 F8E B5 t0 A6O W0 D0 F8F E2H A2G E2I U6 s0 A6P a3 C1 E26 v0 F1s b0 A0z q0 F1t F1u m0 j0 Y0 y0 F1v E0J A9A X0 i1 B0 E0K s2 r0 z1 T0 E6r
E6s E2 p0 Z1 "ds_read_b64_tr_b16 a[216:217], v217 offset:59168\n" "ds_read_b64_tr_b16 a[218:219], v217 offset:59680\n" w1 g0 a1 B0 E6t E6u b1 f0 R2
"ds_read_b64_tr_b16 a[224:225], v217 offset:59200\n" "ds_read_b64_tr_b16 a[226:227], v217 offset:59712\n" q2 k0 h1 Q0 E6v E6w W1 B52 T0
"ds_read_b64_tr_b16 a[232:233], v217 offset:59232\n" "ds_read_b64_tr_b16 a[234:235], v217 offset:59744\n" K1 B3u C0 B5w I0 C4I J0 U1 A90 C35 C0 E1T
E1U O1 K0 c1 u0 F1w F1x B8r A9h F1y F1z g2 o0 I3 B0 F10 S4 B8l F11 C7v O0 X3 E0 g3 h4 B4 e3 F12 F13 F2 B0o S7 D5S D5T t2 B9A C3 M0 D5U D5V C8t c7 D5W
B29 D5X B4b N0 w2 E16 D5Y D5Z A8Q F14 B8 F15 V8 F16 F17 Y0 "s_cbranch_vccnz Lc4_19\n" "Lr4_19:\n" i0 L0 F18 F19 O2 J0 D3 D9c F2A F2B a2 h0 N2 L0 F2C
F2D X1 A3U I2 F2E F2F G2 A6o Y1 "s_waitcnt vmcnt(12) lgkmcnt(6)\n" D5a D5b A2 B4o B3 D0 D5c D5d C8h A6p C8i D0 D5e D5f C8j f0 C8k D5g D5h d1 E4L E4M
e1 B52 F2G F2H K1 A6q z0 D0 F2I F2J B6I I0 "ds_read_b64_tr_b16 a[228:229], v217 offset:29568\n" H1 R0
"ds_read_b64_tr_b16 a[230:231], v217 offset:30080\n" F2K S2 A5m U1 F2L F2M u2 A6r R1 C4K F2N "ds_read_b64_tr_b16 a[236:237], v217 offset:29600\n" B2C
"ds_read_b128 a[212:215], v215 offset:33856\n" "ds_read_b64_tr_b16 a[238:239], v217 offset:30112\n" B6N Z0 A5R
"ds_read_b128 a[208:211], v215 offset:33792\n" o4 x2 K0 y2 P7 E22 p4 l2 B4L d2 E23 M4 e2 A6j G3 D0 E0 H3 c0 F4d C5x F0 n1 F4e s3 V1 A6k o1 D0 F2O F2P
n2 G0 A0F "ds_read_b64_tr_b16 a[220:221], v217 offset:29632\n" n3 W0 A6m B0k "ds_read_b64_tr_b16 a[222:223], v217 offset:30144\n" F2Q B6K H0 I9 A3b
F2R B8Y A6n b0 C1 "ds_read_b64_tr_b16 a[228:229], v217 offset:29664\n" "ds_read_b64_tr_b16 a[230:231], v217 offset:30176\n" q0 E2e h0 m0 D9Y D9Z Y0 j0
i0 y0 D9a D9b c2 B3s Q3 B0 "ds_read_b128 a[192:195], v215 offset:42752\n" "ds_read_b128 a[196:199], v215 offset:42816\n" b3 r0 B2 F1V E9M E9N C2 A37
l1 "ds_read_b128 a[208:211], v215 offset:34304\n" "ds_read_b128 a[212:215], v215 offset:34368\n" k1 g0 x1 B2r F1J F1K s1 g1 p0 M3 F1L F1M o2 k0 f1 Q0
F1N F1O A08 F0 G1 T0 F1P F1Q K1 B3u F1R F1S I1 g1 G0 H1 F1T F1U S2 q5 U1 p1 D5k q4 F9 A0f A8k d5 O1 J0 c1 A9l A8l A3y B3T v3 M0 A3v A2k E3 K0 Z3 u0
A2l D5l C6c D5m A9q O0 F4 E0 t3 D5n B4S o0 p2 B0 D5o D5p C5S y1 D5q D5r A4A c5 A0c Y3 W0 l0 D2e A0d R5 U0 A9r A2e e0 I0 s0 K4 W5 v0 A92 C55 A1W A2C
A7g i0 J0 Z2 A5d A4B B2p P3 D50 D51 D5u A2A D5v E2V A64 D0 "Lp4_15:\n" E6f "s_cbranch_vccnz Lc4_20\n" "Lr4_20:\n" T2 C8O D5w D5x h2 A65 i2 D0 D5y D5z
C8n p0 C8o s8 A4C C8p A66 C8q L0 A4D B3M X2 D0 f0 U2 B3L t8 k2 A67 j1 F8G A4F K3 P0 f2 A4E t1 K0 B8f C7K G1 O7 B6a A20 z0 A60 A1B D4y H1 x0 D0 c4 A4G
U1 A61 u2 D0 A2V m5 y6 A2W n5 c1 A62 K2 D0 v9 N4 U8 v2 A7h E4 R3 O0 i3 w9 u3 L2 E0 S3 y4 U5 T3 A63 U3 C1 z4 A5 Q2 "s_add_u32 m0, s88, 105856\n" F0 A3
M0 B6l H2 j0 M2 y0 A3g A0w m2 F72 G0 t0 B0T r3 W0 E1O B60 U0 S0 I9 B2S A3n B03 r0 b0 T0 C7M T6 A21 Y0 g0 i0 B4M A1X a3 h0 i1 e7 k4 s2 k0 z1 Q0 A4I E2
A1 I0 Z1 T0 A4J w1 N0 a1 A22 b1 F0 R2 B54 G0 h1 A23 W1 f0 G1 L0 B55 X0 z0 V6 C9H I1 C9I C9J H1 C9K B4 S2 p1 D52 D53 A1p B8p A0f W8 R1 J0 O1 A9l A0P
C3c B2n j3 K0 g2 u0 D54 D55 I3 Z0 f3 L0 D56 O4 o3 O0 B7y X3 D57 B7z o0 r2 B0 D58 D59 e3 H0 F2 F1X F1Y t2 l0 C3 C1 F1W F1Z c3 D8y c7
"s_waitcnt vmcnt(9) lgkmcnt(6)\n" D6A B29 D6B w4 P0 s0 j0 S4 v0 y0 D6C D6D b0 A02 q0 D6E D6F m0 r0 Y0 T0 D6G D6H A0B B08 F1a F1b C8u g0 C8v B0 F1c A73
C8w A1 F1d L3 C8x f0 F1e k0 "s_cbranch_vccnz Lc4_21\n" "Lr4_21:\n" O2 F1f F1g D3 F1h C9L a2 X0 N2 R0 D8Y D8Z X1 B4R I2 D8T D8U G2 I0 Y1 L0 E0y E0z A2
L0 C9M C9N B3 "ds_read_b64_tr_b16 a[168:169], v217 offset:55008\n" C9O d1 F0 e1 R0 "ds_read_b64_tr_b16 a[170:171], v217 offset:55520\n"
"ds_read_b64_tr_b16 a[172:173], v217 offset:59232\n" A96 B9C R0 "ds_read_b64_tr_b16 a[174:175], v217 offset:59744\n"
"ds_read_b64_tr_b16 a[176:177], v216 offset:54912\n" r4 F3 s4 R0 "ds_read_b64_tr_b16 a[178:179], v216 offset:55424\n"
"ds_read_b64_tr_b16 a[180:181], v217 offset:0\n" e5 x0 g9 T0 "ds_read_b64_tr_b16 a[182:183], v217 offset:512\n"
"ds_read_b64_tr_b16 a[184:185], v216 offset:54944\n" A11 o0 A2h B0 "ds_read_b64_tr_b16 a[186:187], v216 offset:55456\n"
"ds_read_b64_tr_b16 a[188:189], v217 offset:32\n" g8 B1 f5 F2S "ds_read_b64_tr_b16 a[190:191], v217 offset:544\n" k7 l0 h8 L0 F2T A8H B1F b5 B1G A8I
M4 B1H O0 B1I E4l E4m B1J A1 E0 B1K E4n H4 B1L C9P C9Q B1M E4o C9R V1 C9S s3 B1N M0 F8H n3 B1O H0 B1P F8I E4p B1Q S0 c3 E4q a4 A9j F8J F8K B6K A10 E4r
b4 B9T E4s C9U c2 E4t C9V v0 R4 b0 "ds_read_b128 a[144:147], v215 offset:8960\n" E4u T6 "ds_read_b128 a[148:151], v215 offset:9024\n" E0Y A0Q A0 E0Z
"ds_read_b128 a[160:163], v215 offset:4736\n" Q3 A1 B3A "ds_read_b128 a[164:167], v215 offset:4800\n" C9W B2 G0 C2
"ds_read_b64_tr_b16 a[168:169], v217 offset:12704\n" C9X l1 F0 k1 "ds_read_b64_tr_b16 a[170:171], v217 offset:13216\n"
"ds_read_b64_tr_b16 a[172:173], v217 offset:16928\n" C80 "ds_read_b64_tr_b16 a[174:175], v217 offset:17440\n"
"ds_read_b64_tr_b16 a[176:177], v217 offset:12736\n" A1V I0 o2 "ds_read_b64_tr_b16 a[178:179], v217 offset:13248\n"
"ds_read_b64_tr_b16 a[180:181], v217 offset:16960\n" f1 l3 W2 "ds_read_b64_tr_b16 a[182:183], v217 offset:17472\n"
"ds_read_b64_tr_b16 a[184:185], v217 offset:12768\n" A96 Y3 C9Y l4 "ds_read_b64_tr_b16 a[186:187], v217 offset:13280\n"
"ds_read_b64_tr_b16 a[188:189], v217 offset:16992\n" r4 C9Z C9a s4 "ds_read_b64_tr_b16 a[190:191], v217 offset:17504\n" C9b e5 A1 C9c C9d g9 F1 K4 A11
P3 C9e C9f B9R F1i F1j f5 Z0 k7 F1k F1l h8 Q4 A8L A0 E0M j4 A8M A0 V0 A7D E0N "ds_read_b128 a[164:167], v215 offset:30144\n" A7E A1 e0 A8N
"ds_read_b128 a[160:163], v215 offset:30080\n" W5 D7E O0 D22 E0 A7F "ds_read_b128 a[156:159], v215 offset:34368\n" q6 A8O H0 A8P
"ds_read_b128 a[152:155], v215 offset:34304\n" F4S m1 S0 A7G F4R R5 A7H M0 F1m C9g A9b F1n C9h A9a E17 C9i A0 A9Z E18 C9j A9Y F1o C9k A9X F1p C9l C9m
E19 C9n B6i E2A C9o C9p R4 C9q L0 F1q j7 C9r F1r C9s p0 C9t E2B E2C C9u "s_cbranch_vccnz Lc4_22\n" "Lr4_22:\n" A0 C4B
"ds_read_b64_tr_b16 a[168:169], v217 offset:42336\n" "ds_read_b64_tr_b16 a[170:171], v217 offset:42848\n" D2 C9v h2
"ds_read_b64_tr_b16 a[172:173], v217 offset:21216\n" A6X i2 F0 X2 R0 "ds_read_b64_tr_b16 a[174:175], v217 offset:21728\n"
"ds_read_b64_tr_b16 a[176:177], v217 offset:25344\n" U2 A0 C9w A0 "ds_read_b64_tr_b16 a[178:179], v217 offset:25856\n" E27 j1 h0 K3 R0 E28 B9U f2 A1H
C9y A47 "ds_read_b64_tr_b16 a[184:185], v217 offset:25376\n" B9V P2 x0 C9z A0 "ds_read_b64_tr_b16 a[186:187], v217 offset:25888\n" C9x D7F c0 A0j A4v
A0 C91 C92 A4w A8S C90 C93 E29 E3A C95 A0 C94 C96 C97 A3x C98 A4x M0 D0A A0 A8T B9W A0 A3w C99 B9X B4P D0C D0B A0 B4Q D0D A4y R0 A2L D0G D0F A2M D0E
A4z A8U A40 L0 E3E D0H A9g L0 E3D D0J A9f L0 E3C D0K A9e L0 E3B D0O A9d L0 D8W D0P A9c L0 D8V D0S D0N A0 E2D D0T D0I L0 E2E D0W D0Q L0 D4U D4V A2h A1
D0L D0M g8 L0 A8V D6a D6b f5 g1 E2F E2G D0R k7 u6 D4W D4X h8 A7m D0V F0c A9p D0Z u6 B1 i1 A1 D0X D0U B9Y s2 A0 D0a D0b z1 c0 B9Z D0Y E2 B0y Z1 D0d D0c
D6I D6J a1 D6K D6L b1 A1 D2L M0 D6M D0f O0 D0e D0g B9a h1 x0 A0 W1 H0 D6N D0h M0 D6O B9b A41 c0 D6P D0k S0 D0i V0 B9c e0 D0j B0w D0l D0m D6Q D6R D0n
D6S D0o M0 D6T D0p B9d D6c D6d D0q F5X F5Y A42 F5Z F5a D0r u6 D0s A43 A1 F5b F5c A44 A1 F5d D0t D0u D0v A8S F5e D0w D0x A45 A0 F5f F5g A46
"s_cbranch_vccnz Lc4_23\n" "Lr4_23:\n" F5h F5i D0y A1 D0z D00 D01 F5j F5k A8T D02 A1 F5l F5m D03 D04 D05 F5n F5o F5p F5q D06 F5r F5s F5t F5u D07 B8E
D08 D09 A0 F5v A6e D1A B9e F5w D1B A8U D1C F5x F5y D1D D1E D1F D1G R0 F5z F50 D1H F51 F52 D1I D1J B60 A8V D1K D1L D1M A0 A3n D1N D6e D1O D6f D1P R9
D1Q D1R D1S D1T D1U B9E B9F D1V A8X C2 R0 B9G D1W B9H B2 A0 B9I D1X D1Y B9J A8Y s1 A8Z D1Z B9K B9L D1a B9M D1b B9N A08 A8a D1c B9O B9P D1d B9f D1e D6g
D1f D6h D1g B9g D1h B9h D1i B9i D6i D1j A8X D1k D6j A8Y D1l A8Z D1m A8a D6k D6l D1n "Lend4:\n" "Lc4_0:\n" B61 A8c D1o A98 B1S D6m "s_branch Lr4_0\n"
"Lc4_1:\n" B62 D16 L4 "s_branch Lr4_1\n" "Lc4_2:\n" B9j A5A N3 A7J d4 "s_branch Lr4_2\n" "Lc4_3:\n" B9k D17 L4 "s_branch Lr4_3\n" "Lc4_4:\n" v1 D1q
B63 N3 B9m r6 A5A B9l D9P A7J E1z E4a B3B "s_branch Lr4_4\n" "Lc4_5:\n" r1 D1r B4y W4 B63 p5 B5X A8q A5A A3o "s_branch Lr4_5\n" "Lc4_6:\n" v1 D1s B3C
N3 B4y r6 B3Z A8p B5X A3j "s_branch Lr4_6\n" "Lc4_7:\n" r1 D1t B1R W4 B3C p5 B1k A8q B3Z A3o "s_branch Lr4_7\n" "Lc4_8:\n" v1 D1u A97 N3 B1R r6 B0H
A8p B1k A3j "s_branch Lr4_8\n" "Lc4_9:\n" r1 D1v A8b W4 A97 p5 A8o A8q B0H A3o "s_branch Lr4_9\n" "Lc4_10:\n" v1 D1w A7I N3 A8b r6 A7O A8p A8o A3j
"s_branch Lr4_10\n" "Lc4_11:\n" r1 D1y A48 W4 A7I p5 A49 A8q A7O A3o "s_branch Lr4_11\n" "Lc4_12:\n" v1 D1z B4x N3 A48 r6 A3i A8p A49 A3j
"s_branch Lr4_12\n" "Lc4_13:\n" r1 D10 D14 p5 A3i A3o "s_branch Lr4_13\n" "Lc4_14:\n" v1 D11 B1T N3 B3D d4 "s_branch Lr4_14\n" "Lc4_15:\n" r1 D12 D15
L4 "s_branch Lr4_15\n" "Lc4_16:\n" D1p D18 d4 "s_branch Lr4_16\n" "Lc4_17:\n" D13 D19 L4 "s_branch Lr4_17\n" "Lc4_18:\n" v1 D6o D2A
"s_branch Lr4_18\n" "Lc4_19:\n" r1 D6p D2B "s_branch Lr4_19\n" "Lc4_20:\n" v1 D80 D2C "s_branch Lr4_20\n" "Lc4_21:\n" r1 D81 D2D "s_branch Lr4_21\n"
"Lc4_22:\n" v1 E1C D2E "s_branch Lr4_22\n" "Lc4_23:\n" r1 E1D D2F "s_branch Lr4_23\n" ".fill (Lv0+491520-.)/4, 4, 0xBF800000\n" "Lv5:\n" "Lb5_0:\n"
C0s C0m C0t C0u "s_mov_b32 s54, Lend5-Lv5\n" C0v D6q D6r C1e C0p C0x D6t D6u D6v D2G D6s "s_mov_b32 s92, 311296\n" C0n X4 F2U C0o C0q X4 C0r F2V A0N
C0w C0y g7 G7 "Lb5_1:\n" M7 "s_sleep 42\n" B7b C0z C01 B7c C02 C03 C00 C04 C05 C06 C07 C08 B5Z C1A C1B C09 C1D p8 C1H C1I B7d C1E C1F C1G C1J p8 C1O
C1T C1K C1P C1C C1Q C1S C1L C1Z C1c C1R C1U C1V C1M C1N B7e p8 C1a C1b C1d C1g C1h C1i C1j C1k C1l C1m A5I "s_mov_b32 s92, 10240\n" C1f C1n C1o C1p
C1r C1s C1t C1u A5J "s_addk_i32 s92, -5120\n" C1q C1v C1w C1x C1y C1z C10 A5J C11 C12 C13 C14 C16 C17 C18 C19 C2A C15 C1W C1X C1Y B7f C2B C2C A0 C2D
p1 B7g F2V B7h B7i B7j B5a B7k B5b B1r B5c B1s C2E C2F C2G E1 B5d A5K D35 C2H D36 p1 C2I C2J B3f S8 A1N C2K A8t C2L R6 C2M E1 C2N D37 C2O D38 B1t N0
C2P A2p C2Q C2R C2S C2T C2U B1u C2V T1 A7V B1t A2p C2W C2X C2Y C2Z E1 A8t A3s D39 C2a C2b C2c C2d B1u B5Y C2h C2g C2e C2f C2i A5L A3t A3u R6 A3v A3y
A3w A3x C2j C2k C2m C2l K0 C2n A7W j0 A7X D4A D4B C2o B0 D4C D4D B3i A0 g0 B1r B1s C2p E1 A7V D4E D7N B0 D4H B3j U0 Q0 C2v B5e D4F W0 A3s B5f A1N B1w
B5g C2q C2r C2s C2t S8 z7 C2y C2u D4G A8u B5h D6w q0 B5i D6x J3 B3k m0 K6 B3h h3 B3g B1v C2z C20 C2w C2x E6x B3l B1f B5j Y0 J3 B5k A0O B1v C22 C21 J0
h3 B7m C23 C24 C25 C26 C29 C27 B1x B5l C28 B1y B5n B5m B1z K0 J3 B10 u0 K6 C3A h3 B11 J3 C3B B12 F1 B3m C3C C3D C3F C3E A5M B13 P3 B5r C3G B14 x0 C3I
B5o B15 B5p "Lp5_1:\n" F2W C3H B16 B5q C3J B17 B5s B18 l0 p0 A4u v4 F3 c0 t0 C3K C3L A8w l9 C3M C3N A1O P0 A8x I0 S6 B1U C0 Z4 v0 C3O C3P A5N C1 B1 m0
A8 Y0 X5 B5t A7Y w0 B1x y0 H0 B1y B0L B1z r0 B10 D7Q V0 B11 x0 T0 e0 B12 A0 A19 V1 C3Q B0 A3X B13 A5O B14 D2H B15 A29 B16 T8 B17 k0 B18 Q0 X0 B3n f0
T0 l3 c0 t0 A8y A8w l9 A7Z A1O Z0 A8x X5 B1 s0 D9Q B3o A30 t6 v0 A0p b0 A8C q0 C3R m0 m9 C3S Y0 n9 i0 C3T m1 H7 C3U Z2 R6 J7 A8z B2s A3A A2A C6e C4Q
"s_cbranch_vccnz Lc5_0\n" "Lr5_0:\n" u6 C3W A7b D2 p0 C3X B3p o0 C3Y B0 B3q h0 C3Z B3r B3s C3a A7c l0 C3b Q0 B3t g5 X0 A81 B3u C0 B0M A82 M0 B5u A0q
F0 B19 A1P R1 J0 B7q o9 C3c A31 C3d K0 v2 C1 Z0 C3e B0 a7 B7r C3f e4 u3 B2A C3g f4 B7s j0 C3h B7t C3i y0 C3j B0 p9 r3 C3k r0 C3l A1Q t0 A2q A83 B7u
A2B b0 g0 B2B A7d P5 A84 i0 k0 B0N A32 C3m Q0 A5E B2k C3n A33 E2 N0 C3o C0 A2r w1 X0 C3p q9 b1 J0 C3q L0 B7v P0 C3r A2s W1 A34 B7w C3s B5w G0 B7x A1R
C3t B5x C3u B5y B1 b7 A2D C3v o0 g2 A8 C3w O9 C3x C3y A2t B7y C3z P9 B7z B0o C4A A0r C30 A2b F2 A7C C31 N0 A24 A8D t0 F0h c7 A0N i8 S5 d0 C4F U0 j2
D9R j8 w4 v0 C4G b0 j2 A0u v6 B7P m0 B6G Y0 B78 i0 j2 B2X w2 I7 h0 f0 o4 B5Z p4 A2J A0j F0 "s_cbranch_vccnz Lc5_1\n" "Lr5_1:\n" O2 w0 G0 D3 C4H a2 j2
C33 Z4 X1 C34 B79 G2 j2 A2w Y1 B71 C4J d1 j2 A5Q e1 G1 B5z A5I z0 F1V A1B H1 S2 J0 U1 A90 A5L u2 A38 y6 A0P c1 K0 K2 C1 B2n J4 A8 x2 O0 y2 w6 M4 l2
B2m C37 H4 e2 j0 G3 h5 x6 H3 H0 V1 A5S b2 S0 n1 B0l a4 o1 V0 n2 B0m A2F A35 R0 A2H b4 d0 A1S U0 A1T i7 c2 B50 b0 A1U q0 A4h P5 B2a A0B Q3 A4i q4 A9k
B2 A5T C2 A3U l1 A5U k1 P0 A0 "Lp5_2:\n" F2W x1 C39 I0 M3 B51 F0 f1 A7e W2 y0 B0Z B0 A5V B8A B2b z0 Y3 I1 r0 H1 T0 A1R B0x C8 g0 B8q A39 B2C t9 K2 k0
v3 Q0 B0P E3 B6X Z3 u9 j4 W3 B28 d3 p1 D7R D7S B31 D7T D7U t3 B4S A7f p2 N0 m1 C0 C5p y1 H0 V3 L0 D2h t0 J0 B8G A0e A1l d0 A7A D9R A0p v0 C8A A5N u0
s8 r8 P5 B8O i0 o0 Z2 B0 A87 t8 A4u m5 A2A l0 A5d n5 P0 "s_cbranch_vccnz Lc5_2\n" "Lr5_2:\n" R0 T2 I0 D2 D61 h2 j2 A4E i2 F0 X2 A4F U2 D62 k2 j2 A20
j1 G0 K3 C4D D63 t1 j2 A4G P2 T5 G1 L0 B33 A5K z0 d7 Q0 D7W I1 F0e D7V H1 D7X S2 N0 A1p C0 B8p A4H y6 v9 c1 J0 K2 C0 A8J N4 b5 v2 O0 R3 A5 E0H B2m L2
y4 U5 S3 K0 T3 C4M z4 D8 U3 A2b Q2 A2I A3 j0 H2 C4N A6U M2 B0 C77 A0S B5 t0 r0 A5b A88 B5e B7H A10 R9 U6 s0 g0 a3 K7 D7Y v0 A5X b0 A0z q0 A21 m0 k0 Y0
A1X i0 p0 i1 e7 k4 s2 f0 z1 A4I E2 A1 X0 Z1 A4J w1 P0 a1 A22 b1 I0 R2 B54 F0 h1 A23 W1 B0Z L0 B55 C8s z0 B4 B2D A2g C8 W8 y6 x9 C4L A0 A0T O4 j3 I4 g2
A0U I3 E0 f3 M0 y9 h4 o3 H0 m3 B9p X3 S0 g3 S9 S5 r2 V0 e3 A7j F2 Q0 e0 t2 T0 A0x w4 C3 A0y t0 i5 A2Z X8 d0 N0 A1O Y8 a0 j5 s0 x4 w2 J0 B5F F2X h0 q0
A73 m0 A2d B7G "s_cbranch_vccnz Lc5_3\n" "Lr5_3:\n" u0 O2 E1 X0 D3 p0 a2 p1 D7Z D7a N2 o0 X1 B0 D7b D7c I2 D9Q l0 Q0 c0 E1E w0 B0F o4 P1 B0O N1 B0G p4
q1 N0 S1 x8 B1i s3 w3 A0 B1j n3 D7d A0 B5N A5e D2l f7 D7e J1 R0 A8K D7f J2 D9J J3 V2 A5f B8P D20 K6 Y1 A4t h3 A2 D60 J3 B3 A5g C47 D6z A0O e1 R0 B9q
h3 u1 C4R C4S a0 B9r C4T B9s C4U D7h d0 A8d D7j B1V D7k D7i z7 A8e J3 p3 A5h Y0 L7 B1l K6 m0 h3 Y2 q0 B1m J3 b0 A5i i4 D0 A0O D7g J4 h3 k3 x2 C4X C4Y
y2 C4Z C4W l2 T1 C4a D7l d2 D7m D7n C4b e2 C5 E0 B2N B38 O0 A99 t4 k5 q3 S1 A5j D2j D0 N1 l5 M1 P1 A4u p0 U4 A6L B0A B9u V1 e0 A0m x3 A1r B2L A5k J1
D0 A1s A7n P0 L1 I0 C4i i9 G3 A2X k3 H3 A5l c2 E1X B6b Y2 b2 w0 B1 n1 Z0 "Lp5_3:\n" F2W o1 X5 A9D n2 z9 D24 t0 J0 W0 c0 W6 Q1 l9 y3 A1O A0A M1 A0J c0
A1F w0 A3b A0 A3k v0 B0Y A5N C1 A5a m0 A4l Y0 B56 B5t c8 A0 B85 Q3 g1 B3A B3z v8 d5 C2 R0 P0 l1 B34 k1 I0 O3 w8 K4 P1 F0 N1 G0 q1 p0 S1 q4 Y3 w3 A0
D9S C4V z3 A2f D9T F0f Y5 D1 B1 J1 D68 B5G J2 y0 X6 V2 B0 B7D x1 Y2 D9U B8j A8G M3 r0 o2 T0 A9F f1 Z0 W2 B4L u1 A26 Q1 a0 g0 U0 B4m l9 y3 W0 M1 A3p z7
A27 B5M p3 k0 Y0 Q0 B8z m0 B2I B2o b0 F8L D3E i4 N0 v3 B84 E3 X0 Z3 H0 W3 P3 E7 D3F d3 w0 S0 T4 F7 B3O t4 J0 S1 C0 V0 D2j N1 e0 P1 E8 B3P U4 K0 m1 u0
P0 x3 A4k F8 J2 I0 J1 B7O D7I D1 F0 L1 G0 Q6 D7J Y5 F4 C0I N7 t3 o0 Z2 B0 A6d k3 z2 A78 Y2 p2 B1 y1 Z0 X5 V3 c0 l8 r5 A79 t0 l0 W0 w0 G8 Q1 D2e y3 U0
w0 H8 M1 a0 B0 B3o w0 s0 B0j s5 B0a L8 N0 B5F B4f A7l A1u B1X m0 A1v B0D Y0 A1w L3 i0 R6 A1x "s_cbranch_vccnz Lc5_4\n" "Lr5_4:\n" B2c D9V D3L K0 i2 f0
E1Y X0 t5 A4 E1Z D9X L5 P0 X2 E1 I0 U2 F0 k2 G0 j1 A9H u3 O3 A9I r3 P1 A9G N1 A9J D7G B6A D1 A9K J1 A9L h9 B2j E1a X6 u5 C1 Y2 Q1 E1b B0 B0b P4 M5 Z0
K3 A6i f2 j0 t1 B0c A01 P2 O0 E1c E0 v5 B0f q3 E1d A9M M1 Q7 A3h D2W D3C A0D W0 B0d s6 B0e Y4 A9N C0R F1 F0F F8M E1e F2Y A1b E1f y8 N5 I8 Q2 J8 C4p Z5
C4q M0 a5 L2 z8 B3W E12 a3 E1 A0 C4r A2g m4 Y5 C4s B1 x3 A2Q y0 n4 N7 J1 A8 D1 A4Q k3 L1 r0 G4 T0 A4R Y2 E1g I4 w5 A9O Y6 E1h g0 O5 A9P A2L A3 E0 H2
M0 A2M A0E C4t C4 Q1 C4u H0 M2 D4 P4 m2 S0 z6 u4 E1i A2N A7R x5 V0 E1j A2O A4S a9 e0 A0F A4T A7S W0 A1c d0 A1d U0 A03 a0 A04 s0 A0Y C4w R0 C4x A0l q0
I7 A2C D3O i1 V4 s2 z1 E2 q5 E1k F0G E1l O7 G0 A4j B5P S4 Z1 T1 B5Q B7Z A9Q a1 A5p b1 D0 A9R C0M A5q q1 D0 S1 D9S w3 D9T z3 B39 D9U L1 A5r D2l A28 A2P
B3Q B0g V2 A5s E1m A9W A4U k3 N9 B1 E1n B3E Y2 R7 A5t R2 D0 A1q q2 Z0 h1 B3F A06 W1 A5u E1o B90 "Lp5_4:\n" E6y y5 E0 E1p A9T Q1 B5B D2n A9S M1 a0 A5v
U0 D0 D2o B9S A0 A9U B6B p3 A5w Y0 F4G e0 A5D B2o b0 B2P D4P i4 k0 j3 Q0 f0 g2 T0 X0 I3 D4Q B6D f3 B2Q E1q N0 A0K C0 P0 E1r I0 c9 F0 r2 T1 G0 o3 E4c
B93 m3 J0 X3 A68 B94 D9W B5O T1 D9V A12 E4g w2 B98 x3 E31 A3q A0n B3U A0o B5W E1s A0 A5O D3M O0 E1t B7n Q5 D2O T8 D2P C5 t2 C4v C3 H0 Z6 Z8 M1 E1u S0
z5 A9 O8 E1v V0 C0J B8m A35 q8 P8 d0 m9 U0 n9 a0 E4d s0 H7 B3G J7 g4 a8 b0 b8 D3N B2x "s_cbranch_vccnz Lc5_5\n" "Lr5_5:\n" M0 O2 X0 D3 p0 a2 q5 N2 I0
A1L F0 a6 G0 A6 h0 D5 F3 X1 A7p B7A A7q C0K A7r Y1 A7s O3 A0M E7 P1 F7 A5H D2Q A3A J1 B4C h9 A1z s7 X6 V9 B5J n8 A3c E5 U8 C7x O0 d1 E0 e1 A99 t7 y3
b6 C0 M1 B6 H0 F5 D7H E1 A5c A5x C0Z d0 K0 W0 u0 A5y C0a s6 A7t Y4 A7u Y0 o0 m0 A02 D7K l0 J4 R4 x2 B0A y2 P0 l2 I0 A2m F0 W9 G0 C6 i9 G5 e4 V1 f4 d2
a7 e2 A1e B7T B7J A0 A8r A7 J1 x0 D1 A1P L1 B1 Q6 o9 A17 b5 c6 A09 k8 D6 O0 H5 R0 A1A Q1 b2 E0 n1 D2S o1 u4 n2 A4n A0J H0 u7 A1Q d6 S0 E6 V0 C0B B1D
A3k W0 B9 d0 C9 U0 A1f a0 A1g s0 B5V B3X B4j A9k A9t l3 A1M D2v D3D A54 B3H A55 B9x A4Z s1 A4a J9 D2U A2R J1 B6E h9 Q0 A32 v7 g5 D2V Q1 F6 q9 K8 N0 M3
C0 C7y Z0 f1 O0 W2 J0 w7 C0 E0 e6 B0C G6 y3 A0a w0 A56 M1 u1 K0 a0 C1 B8F B0 A6v B4Z A57 B3O c3 j0 p3 y0 e0 B8u A58 B3P A5D q0 A59 b0 r0 i4 T0 h0 v3
A1H E3 X0 Z3 g0 W3 B0 B0E A2n P0 f6 I0 H6 k0 I5 Q0 F0 D2x G0 d3 T0 j9 T4 N0 F4 C0 t3 B1g J0 q1 C0 N1 A12 K0 Z2 u0 F1 C5M Z5 A0 Y5 C5N B1 x3 a5 N7 J2
o0 J1 A1G A7 D1 x0 L1 A6B G4 E4h A7U A3F Z9 Z0 I6 A6s l8 J5 O0 z2 A6t Q1 p2 L0 E0 C5O m4 r5 C5P H0 y1 n4 P4 V3 S0 s5 A3Z q3 x7 V0 g6 "Lp5_5:\n" E6y
A3a M1 J6 e0 K5 c0 O9 K9 t0 P9 L9 W0 A70 X9 d0 A71 Y9 U0 A2u B0r a0 A2v A93 s0 A0s C8G L8 L0 A0t A3d B2V C5Q p0 B2W v0 D4 B1X L6 b0 i8 B0D M6 B2B j8
L3 A14 B96 A15 Y0 "s_cbranch_vccnz Lc5_6\n" "Lr5_6:\n" m5 A7M i0 l0 A7N T2 w0 f0 A3K D2 Q0 X0 A3M h2 B0 n5 N6 C5T i2 N0 C5U C5V C0 C4 C5b P0 l7 I0 t5
J0 h6 F0 L5 C0A B1Y C0H T1 B1Z r3 C5a B2t k2 B2u j1 J9 B68 v6 D1 A0v J1 E2J h9 B2X m7 X6 u5 B5J i6 A2w M5 Z0 K3 C72 O0 t1 E0 P2 B64 n7 y3 v5 M1 j6 H0
Q7 w0 D26 A0D d0 B42 W0 B43 s6 B44 Y4 B45 C0R F1 B7W w6 N5 d8 B3Y h5 L2 x6 B3W B67 A12 d7 x0 A0 B3R A6J B1W A6K B5L A0P B5R D2p r9 Y6 w5 O0 k6 s9 Q1
O5 E0 A3 B91 H2 u4 M2 P7 H0 m2 A5S z6 S0 o7 B0l A7R x5 A6A l6 D0 B0m B1h V0 A35 A6C B0k A2F A7S U0 A2H a0 A1S s0 A6D A0Y D0 A1T A0l q0 A6E m0 L7 h0 Y0
D3O i1 A6F s2 D0 p0 D2a A6G A7T D2g C0C V4 Z1 B46 D25 A6H a1 D0 B47 B4 b1 B48 O3 B49 P1 A6I N1 E4U A69 D7G B5A L1 K0 D1 C1 A1U A4h J1 B0 A8r B2a V2 j0
p7 y0 A4i k3 N9 Q9 A0H D66 Y2 R7 r0 R2 A72 q2 Z0 h1 D67 A06 W1 O0 q7 E0 y5 A9T Q1 b9 y3 B5B B5C M1 u1 A1F A7A A0k A5C A1K z7 B5D A8i p3 B3J A8j A3l
C0U B3K A4f B5S B7I A4g C0E E15 B7Y D9X U4 x4 E2K B6h A3q A8k B3U A8l B5W B7X A3y D3M O0 m6 A3v A2k Q5 D2O A2l D2P C5 t2 A0c q3 C3 H0 Z6 A0d M1 r7 S0
z5 u9 O8 n6 V0 C0J C5p A35 B6J P8 d0 A74 U0 A0e a0 A1l s0 B0p B3G A2z g4 r8 b0 A0M E7 B5T F7 C0O A4r "s_cbranch_vccnz Lc5_7\n" "Lr5_7:\n" B4V C0T X0
D27 I0 a6 F0 A6 G0 D5 T0 F3 X1 B2z B7A B20 C0K B21 Y1 g0 O3 B0 B22 A8h q1 k0 S1 A0C A13 s8 L1 N0 D1 B8O B3Q A87 V2 B1 s7 t8 k3 V9 Z0 n8 B4k "Lp5_6:\n"
E6y Y2 E5 B4L A2 C5w E0 d1 B4l A5F e1 A5G Q1 t7 y3 b6 M1 B6 H0 F5 D7B T1 A2e A5c A0m d0 B23 W0 B24 s6 B25 Y4 B26 A3l D28 B7S D29 i9 C6 A5 G5 y4 V1 z4
d2 c0 D8 e2 B7T B7J A0 A8r A7 A7P C0F D2u A2V k8 A17 I4 c6 A2W B0q D6 E0 H5 E1x A2o b2 A7h Q1 n1 H0 o1 w9 P4 n2 S0 A0J u4 C5n Z5 C0P C5o V0 u7 a5 d6
e0 E6 B95 A7Q h7 c0 A3g t0 A0w W0 B0T d0 A0S U0 A4s a0 A3n s0 R9 c8 A1 C5q A7X m4 C5r C0 p0 v0 n4 A7K B6j J0 B8H E21 l3 Q3 b3 K0 B2 u0 B30 D3J E1 F0
C5s C4 K4 C5t o0 A0L B0 D4 A1h E93 A9v Y3 k1 l0 x1 v4 A9w s1 A9x O3 A9y P1 N0 N1 x8 A00 c0 C5u A5e C5v D0 z3 A5X A0 Y5 L1 B1b A21 J1 A5f J2 D0 A1X V2
X6 v7 e7 Y2 A0G A5g F6 D0 A22 B65 C5y A5h f1 L7 O0 W2 E0 w7 B0C e6 A5i G6 D0 C5 A0a w0 u4 u1 A1F A8m X9 U0 A5j B0k c7 B9S A0 e0 Y4 A8n B3O Y0 A5k m0
D0 p0 D7K A0 B1c D3E v3 A5l E3 F5R X0 Z3 D3F B3P W3 P0 A2n B1d Y9 f6 J0 H6 B8M I5 F0 m1 T1 G0 d3 K0 T4 C1 j9 F4 B0 B0r t3 A93 t4 j0 S1 D9 q1 A7y N1
B2V B2W P1 L6 B6C M6 Z2 F1 A14 x3 A1G A3q B8w B3U B8x B5W A7U B7B D3K O0 I6 B7C A0T J5 E0 A15 z2 M0 A0U r5 A7M p2 C5 A7N y1 y9 q3 A3K V3 H0 A3M s5 A7i
M1 N6 x7 S0 C5z g6 u8 K9 C50 J6 V0 K5 B7L A0F A5C A2c L9 d0 A0x U0 A0y a0 i5 s0 X8 B7M Y8 g4 j5 A8u C0Q D3B A2d B2x "s_cbranch_vccnz Lc5_8\n"
"Lr5_8:\n" c0 T2 C8O p0 h2 y0 P0 i2 B0 I0 l7 F0 t5 h0 h6 r0 L5 T0 C0A A4 U2 B0F u3 k2 g0 j1 K7 B0G r3 O3 B1i P1 B1j N1 k0 q1 "s_mov_b32 s92, 761856\n"
S1 A13 X4 L1 A25 f7 D1 J1 A9B J2 B2N V2 B0V m7 B1 u5 A7 i6 A1Y M5 U8 K3 A0V f2 O0 t1 E0 P2 B64 n7 C5 v5 u4 j6 H0 Q7 D7H E1 A5c A0D d0 A8d W0 A8e s6
B1l Y4 B1m C0R P3 Q0 E7 v2 T0 P0 R3 E6z N0 A16 D2k A2U A1b N5 k5 Q2 J0 L2 C0 l5 S3 W6 T3 A0A U3 K0 t4 F7 S1 A0 "Lp5_7:\n" E6y B7U B3R A2Q A1r A5B B1b
A1s D2Y A7 Q6 B0X A1J b5 w5 A9D Y6 k6 O0 O5 M0 z9 Q1 A3 E0 H2 B91 M2 u4 m2 B0Y z6 H0 o7 A5a x5 S0 l6 V0 a9 e0 A0F A5n W0 B1C d0 B4n U0 B2F a0 A8W s0
B1n B3X i1 A5E f0 C81 P0 D2q C0C V4 Z1 A76 D25 A77 B4 a1 v8 b1 w8 O3 g7 P1 A5H A2f D3G B5G h9 B7D p7 X6 N9 B5J A0H A9F R7 D2L A8G D2M E0 W1 B5K q7 y3
y5 M1 D2r C0D D7B A0X a0 u0 D3C A1K W0 o0 c3 A7W A26 Y4 A27 B40 l0 q0 "s_addk_i32 s92, -32768\n" h0 D2R R4 j3 X4 g2 A7L I3 P0 f3 I0 A3m A4L B93 A0K F0
A07 A4M c9 G0 r2 w0 B94 D2s A0M A3q A4k B3U B7O B7R Q0 Z0 A2j B0 C0I D3M O0 m6 A6d A78 Q5 N0 e3 M0 A79 Q1 F2 E0 t2 U9 P4 C3 C0 E8 q3 Z6 H0 r7 F8 M1 z5
S0 n6 G8 O8 A1k J0 t0 c0 H8 A8E B8G A0k A8F A4N A3J A4O P8 s0 K0 B3G A1u C7z A0 Z5 C70 p0 v0 a5 B0h b0 A1v A9V q0 A1w L3 A2C A1x o4 i0
"s_cbranch_vccnz Lc5_9\n" "Lr5_9:\n" B4V f0 B9w p4 D27 I0 a6 T1 F0 C73 m4 s3 C74 G0 A6 n4 n3 D5 A9H X1 A9I I2 A9G G2 A9J Y1 C0M A00 T1 C75 C4 C76 D4
z3 B2h A0 Y5 L1 B1b B2i A9K J1 X6 J2 A9L B2J k9 s7 B2K Y2 D3U B5H E5 E0 A2 A6i B3 A1 A99 C78 C5 C79 u4 d1 H0 e1 S0 t7 V0 b6 e0 B6 A0m F5 B0d u1 B0e a0
A9M U0 c0 A9N C0b D9F B40 G7 "Lb5_2:\n" M7 h0 E2L E4B A0 D28 B7S C1 D29 B0 i9 C6 y8 G5 I8 V1 j0 d2 A7w J8 e2 z8 D9K r0 t4 T0 S1 B5O D2d x0 P1 g0 U4 B0
F1 D9L A4Q B2L k0 J1 Q0 A4R A7n T0 F8N A4q G4 N0 A17 C0 A9o c6 B1 D6 Z0 H5 A1t b2 E0 n1 D24 o1 A2N Q1 n2 J0 A0J C0 A2O P4 u7 u4 d6 B97 E6 K0 h7 u0 A3Y
M0 c7 A0k A5c A3k a0 A1c s0 o0 c8 B0 A1d v0 A03 b0 A0z q0 A04 m0 l0 Y0 O7 E21 p0 B3y A5p B2 D0 l3 B30 D3J A5q A0L D2g E3w B5P B3H A5r x1 D0 B5Q Y3 s1
A9Q O3 "Lp5_8:\n" E60 A9R P1 A5s N1 A9W B8J A00 A5t z3 D0 D2K A28 D1 A2P A5B A5u J2 D0 B0g V2 B1 v7 A4U N7 A0G A5v F6 D0 B3E k3 B65 D4N Y2 o2 A5w f1
Q0 D4O B3F W2 D84 O0 w7 E0 e6 N0 G6 C0 B0C A8f C5 u1 u4 a0 A9S A3D U0 J0 B2E A2q S0 z7 V0 Y4 A9U B3O Y0 K0 E1R I7 A1Z B0E B7V B2P D7I f6 G0 H6 B2Q D7J
I5 e0 m1 w0 B3P A3E d3 B98 T4 A4e B7K d7 A3q A0n B3U A0o B5W A7U A5O D3K O0 I6 B7n J5 E0 z2 M0 T8 r5 p2 C5 y1 C4v V3 H0 s5 Z8 M1 x7 S0 g6
"s_mov_b32 s92, 450560\n" A9 K9 J6 V0 K5 B8m A8A A5C X4 d0 q8 L9 U0 m9 a0 n9 s0 H7 B7M J7 g4 a8 b0 b8 B7N "s_cbranch_vccnz Lc5_10\n" "Lr5_10:\n" M0 T2
D2y P0 l7 I0 t5 F0 h6 G0 L5 A4 X2 A7p C0H A7q D2z A7r j1 A7s O3 A0M E7 P1 F7 A5H B7 D2Q A3A A5B B3V C6e V2 j0 m7 y0 A7b N7 u5 A8 i6 D4R k3 M5 A1t K3
A7c r0 t1 T0 D4S Y2 P2 E0 n7 B64 v5 g0 j6 C5 Q7 u4 u1 A1F T1 A7A A0k A5C A0D s6 A5x Y4 A5y C0R A0 A7t E4j A7u C92 A16 G0 A2U a7 C9x N5 e4 Q2 f4 L2 M0
A1e B3W B5U A3e B1W A3f B5L D2u o9 A1J b5 w5 A09 Y6 k6 O0 O5 A1A Q1 A3 M0 E0 H2 B91 M2 u4 m2 H0 z6 S0 o7 A7v A7R x5 V0 l6 p9 A3B a9 B1D A3C A7S B1V B9
d0 C9 U0 A1f a0 A1g s0 B1n B3X i1 A5E B0O C81 V4 B0I P0 A7T k0 A0b Q0 I0 A4j T0 F0 C8C Z5 S4 C8D T1 G0 Z1 a5 B7Z N0 a1 C0 A54 b1 A55 O3 A4Z P1 J0 N1
C0 A4a A00 K0 z3 u0 D2U A2R A5B o0 J2 B0 B6T V2 B4O C8E A1I m4 A4X C8F l0 p7 "s_mov_b32 s92, 499712\n" n4 N7 N9 Z0 A0H A7 R7 B1E R2 X4 X9 q2 q9 Y9 h1
O0 A1i W1 E0 A1j q7 B5K A2S y5 C5 A2T w0 C8H D4 q3 D9 D2r B5E A3I A56 M1 A3d u1 S0 L6 a0 V0 M6 U0 e0 A14 d0 A1K A15 W0 A57 A4d z7 A58 A7z p3 A59 N6
C8I Y0 p0 C8J E1R A0 A7L C8L T1 C4 C8M Q0 P0 j3 g5 I0 g2 "Lp5_9:\n" E60 F0 I3 G0 f3 N0 A3m C0 B2R A0K E1 C8N A07 J0 E13 C0 E1K g3 K0 E12 E2K O6 A3q
B4h B3U B4i B5W B7X B82 D3M O0 m6 B83 A6s Q5 D2O A6t D2P C5 t2 A3Z q3 C3 H0 Z6 A3a M1 r7 S0 z5 O9 O8 n6 V0 C0J P9 A70 A35 A71 P8 d0 A2u U0 A2v a0 A0s
s0 A0t B3G i8 g4 j8 A8u D3N B2v "s_cbranch_vccnz Lc5_11\n" "Lr5_11:\n" c0 O2 f0 B9w q5 N2 I0 A1L F0 a6 G0 A6 p0 D5 F3 X1 B1Y B7A B1Z C0K B2t Y1 B2u J9
B68 v6 D1 A0v J1 E2J h9 B2X s7 X6 V9 B5J n8 A2w E5 U8 B71 O0 d1 E0 e1 C1 A99 t7 B0 y3 b6 M1 B6 H0 F5 j0 u1 y0 C4j E1 B7H B0 C0L r0 W0 T0 A0m s6 B42 Y4
B43 Y0 g0 m0 A02 D7K k0 J4 P7 X0 x2 B0A y2 P0 l2 A5Q A2m A6A W9 A94 C6 F0 G5 G0 V1 A6C d2 D0 i9 e2 d8 G3 h5 H3 A6D t4 D0 w6 S1 x6 B5O A6E A12 L7 F1
D9L A1I A3q A6J C0S A6F D2Z A6K B7R Q4 C0V A0P c6 A6G D6 D0 B2n H5 U8 b2 O0 n1 E0 o1 D24 n2 A3G Q1 A0J A6H u7 D0 A3H P4 d6 u4 E6 A2x C0B A3Y A6I W0 Q0
A6v D65 B7H A10 A3k s0 A2F c8 N0 B5F A2H B86 A1S m0 J0 B8H A1T A0B B3y K0 A9t P0 D3J F0 A0L G0 D7A l3 l1 B46 B3H B47 B9x B48 s1 B49 O3 B8L P1 A5H E1
B5A A8i L1 A1U A8j D1 A4h A5B B3V B2a V2 X6 v7 A4i Y2 A0G Z0 F6 B0n K8 O0 M3 B51 u0 E0 f1 B0C W2 C5 w7 u4 e6 o0 G6 H0 A0a w0 D26 A2i d0 B5C A4f W0 B5D
A4g s6 B3J Y4 B3K E4e B0 B66 F1 F8O W3 l0 A2n "s_mov_b32 s92, 548864\n" F0 f6 G0 H6 j9 I5 X4 D2X F4 p1 E7 t3 F7 B7K A2Q A8k A5B B1b A8l D2Y A7 Q6 t9
B1e b5 Z9 A2k l8 I6 O0 J5 A2l Q1 z2 E0 p2 A0c r5 y1 H0 V3 A0d P4 s5 S0 x7 u9 q3 g6 M1 K9 J6 Q0 C5p K5 R0 C0G B0 D2h C9T A0e L9 U0 N0 B8c A1l s0 B0p
B7M A2z v0 J0 C60 r8 C0Q D3B A2d B7G "s_cbranch_vccnz Lc5_12\n" "Lr5_12:\n" c0 T2 p0 A0 "Lp5_10:\n" E60 D3L f0 i2 X0 l7 A4 t5 P0 h6 I0 L5 F0 X2 G0 U2
T5 k2 B2z j1 B20 O3 B21 P1 B22 A5H s8 L1 A4C D1 A4D J1 B3M h9 B3L t8 C8S X6 C8T A7x m7 Z0 u5 Y2 A01 i6 B4k Q1 M5 A1t K3 D5s P4 f2 E0 t1 D5t C0N c0 H0
C8U O6 m4 q3 C8V S0 n7 n4 B4l v5 V0 j6 M1 D3Q Q7 B7Q B23 D3S a0 B24 U0 B25 d0 B26 W0 E4D E1 A1a C8X A0 C4 C8Y q5 N4 D4 v2 I0 R3 F0 i3 G0 B1a A1b A16
A2U A5 N5 y4 B3Y z4 C8Z D8 C8a L2 B3W B7 t4 B8y S1 B0r q1 A93 N1 E61 x0 D9 P1 j0 U4 y0 A7y a3 B0 L6 x3 A2V B2L r0 J1 T0 A2W B5L v9 G4 g0 A1J B0 A8J w5
B1 k6 Z0 O5 k0 A3 "s_mov_b32 s92, 598016\n" O0 H2 E0 M2 Y6 A0E m2 B95 Q1 z6 X4 o7 A3g P4 x5 u4 l6 A6U B1h A3Y c7 A0k A5c A5n a0 A0S s0 M6 A0Y A4s A14
v0 A15 b0 A3n A7M T6 R9 A7N A0Q f0 i1 C8y V4 D2a D2b G0 A0b E9 B3I A9v A3K Z1 A9w A3M w1 A9x N6 a1 A9y C8b b1 Q0 C8c C8d O3 T0 C8e C8f P1 C8g N1 q1 N0
S1 C0 A13 D2w J0 D1 C0 A21 B3Q A1X V2 K0 p7 e7 k3 N9 B1 A0H B04 Y2 R7 D2L B54 F8P B05 A06 W1 E0 q7 A9T Q1 y5 y3 b9 B1c M1 A3I A3h D2W D3C A1K W0 A8m
s6 A8n Y4 B1d A3l C0U F1 B5S B7I C0E D2s E61 A3q B8w B3U B8x B5W B7X B7B D3M O0 m6 B7C A0T Q5 D2O A0U D2P C5 t2 y9 q3 C3 H0 Z6 A7i M1 r7 S0 z5 u8 O8
n6 V0 C0J B7L A35 A2c P8 d0 A0x E1G U0 A0y E1H a0 E1I E1J s0 i5 B3G X8 v0 g5 Y8 b0 j5 D3N o0 i0 "s_cbranch_vccnz Lc5_13\n" "Lr5_13:\n" B0 c0 O2 p0 D3
h0 a2 l0 N2 Q0 f0 A1L f9 a6 P0 A6 N0 D5 B8M X1 F3 I2 F0 G2 J0 Y1 D2k U4 B0F s3 P1 B0G n3 N1 K0 q1 B7 B1i S1 B1j A13 F1 B5N j0 D1 y0 f7 A8K J1 B0 A8r
B6m V2 r0 s7 T0 A4t k3 V9 B1 n8 B9q Y2 E5 U8 A2 A0V B3 g0 d1 B0 B9r A5F e1 O0 t7 E0 b6 k0 B6 Q0 A5G C4c F5 T0 C4d C4e u1 T1 C4f Q1 a0 A8d P4 U0 N0 d0
"Lp5_11:\n" E60 E4k u4 W0 H0 z7 S0 Y4 A8e C0Z Y0 A6M m0 A1D p0 D7K A0 B1l D7C J4 A6Q x2 D0 X0 y2 B1m D7D l2 P0 A2m A6R W9 A94 C6 F0 G5 G0 V1 k5 D3H d2
w0 V0 e2 l5 D3I G3 A6S H3 D0 e0 w3 W6 C0a S1 A0A q1 A6T N1 A1E g7 A8g c2 A6N x3 S7 x0 D0 J2 A1r A5B B1b A1s D2Y A6O G4 D0 A2X k3 A17 B6b Y2 c6 Z0 D6
A9D k8 H5 A6P b2 M0 z9 Q1 n1 O0 o1 E0 n2 D2S u7 A0X d6 "s_add_u32 m0, s88, 42368\n" B0t q3 A0J B9Q M1 E6 H0 h7 A5a A0F c7 F8Q C0P A2G A7Q D7o s0 F8R
D7p c8 J0 B5F D7q D7r b0 F8S T6 F8T Y0 B7G u0 h0 B3y B3z o0 C2 l3 A1M D2v D3D F8U B3H F8V B9x F8W s1 F8X U4 P1 A5H A2f D3G B5G h9 B7D v7 D2V C5R F6
F8Y F8Z B65 F8a F8b o2 B4z f1 E0 W2 B0C w7 C4l C4m e6 l0 G6 "s_mov_b32 s92, 647168\n" C4n C4o A0a w0 D2n F8c M1 a0 X4 U0 D2o B9S A0 e0 Y4 A2i A3l B66
A0 F8d B7V F8e D7I f6 G0 H6 F8f D7J I5 m1 w0 E1S A4e w3 S1 B7U B41 d7 A3q A3x C0S F0Y D86 B0 A3w B5W A7U F8g D3K N0 I6 C0 F8h F8i J5 O0 z2 M0 F8j Q1
p2 E0 y1 r5 P4 V3 F8k q3 s5 J0 x7 C0 F8l M1 g6 H0 J6 F8m D7s K5 K0 t0 c0 F8n D7t W0 A6v F8o K9 C6n L9 D7u v0 F8p D7v b0 F8q T6 F8r F8s A0Q A0 F8t F8u
L8 A1H C4y A0 E0E F8v C4z A0 B1X m5 C40 E0D B0D C41 G7 "Lb5_3:\n" M7 F8w L3 C42 I0 C43 F8x F8y "s_cbranch_vccnz Lc5_14\n" "Lr5_14:\n" L0 T2 F0 D2 F8z
N7 h2 G0 i2 X0 l7 P0 t5 T5 h6 n5 C45 L5 F80 C46 X2 Z0 U2 F81 C48 k2 F82 k3 j1 O0 m7 F83 Y2 u5 E0 i6 F2Z C44 M5 F2a Y5 K3 B4O f2 F2b F0I t1 F2c F0J P2
O6 E4v C5c n7 E4w C5d v5 E4x C5e j6 F2d C5f Q7 F2e C5W D7w F2f C5X D7x F2g C5Y D7y E4y C5Z D7z E4z D70 E40 D71 F1 E41 D72 A0 E5A A9X A0 S0 A9Y e0 A9Z
H0 A9a V0 A9b A0 F0K F0L A5z F1 A50 A1H A51 X0 A52 F0N A53 p0 C49 E1 h0 A0W F0M D79 v2 A0 D73 D74 Q2 D75 D76 R3 P0 i3 I0 B1a C1 F0 A16 B0 G0 A2U E61
D9M D9N N5 x0 a3 j0 L2 y0 F2h F2i S3 B0 D77 D78 T3 F2j F2k U3 r0 A0Y T0 F2l F2m A1J D8A w5 F2n k6 g0 O5 B0 F2o F2p A3 H2 F2q M2 k0 m2 A0C F2r o7 F2s
"Lp5_12:\n" E62 x5 F2t l6 N0 B1h K7 F2u D8B E63 H1 E64 I1 J0 z0 C0 E2H K1 E2I C5A D8C K0 c1 u0 O1 R1 u2 o0 D8D Q4 C5B F1 F2v C5C B99 C5D F2w F2x C5F
e0 C5G F2y F2z C5H l0 i1 R0 F20 F0P s2 Q0 Z0 z1 B4z E2 N0 B0I C0 E0 A7T H0 A0b S0 A4j F21 F0O Z1 J0 w1 C0 F22 E4X a1 E4Y C5K b1 E6r C5L p7 K0 N9 P7
E6s C5I A0H C5J D8E R7 D8F D8H R2 F23 D8J q2 A6j h1 A1D F24 E6t W1 p0 q7 E6u y5 A6o b9 D0 h0 A3I M0 f0 D8K X0 D8L A6p D8M D0 F25 F0Q D8N F26 F0R D8O
E6v D8P A6q D8Q D0 A0 E6w A9c A9d A0 F0 A9e A6r A9f A1E G0 A9g q5 B9t I0 J2 A6k J1 C8P D1 O0 L1 E0 G4 A6m j3 D0 F0S F0T g2 B1 r2 C5 I3 A6n f3 u4 A3m
F0U F0V A0K H0 A07 S0 c9 d7 V0 w2 E1 e0 o3 x0 m3 F0Z E1T O8 X3 F84 E1U P8 g3 j0 A4V F0a F1w F1x A2j B0 m8 F1y F1z m6 r0 Q5 T0 F11 F12 e3 F0W F2 F10
F13 t2 g0 C3 F14 L3 r7 F15 z5 F16 n6 F17 A1k L0 F18 t0 "s_cbranch_vccnz Lc5_15\n" "Lr5_15:\n" F19 W0 F0X d0 F2A F2B U0 E1w a0 E5X A6e s0 E5Y A6X v0
F2C C51 b0 F2D C52 T6 F2E F2F A0Q E4L E4M O2 B3s B9w P0 N2 I0 D5 F2G F2H A6 F0 X1 F2I F2J I2 G0 G2 F3 Y1 E5F s3 E5 F2K n3 n8 F2L C53 A2 F85 C54 B3 F86
d1 E5G e1 E5P F5 F2M B6 A1 F87 G1 F88 K1 F2N z0 E5Q I1 E5V H1 E5W S2 B0 F1 B36 F89 B1A F9A O1 k0 C4L Q0 O0 J4 T0 E0 x2 A7Y y2 H0 l2 N0 G5 F2O F2P C6
"s_waitcnt vmcnt(16) lgkmcnt(2)\n" S0 d2 x0 e2 C0 B4P a4 V1 J0 G3 C0 B4Q E65 H3 V0 H5 E66 F9B D6 K0 b2 u0 e0 n1 F9C b4 o1 p1 D4j n2 o0 c2 B0 D4k D4l
h7 E6 D4m F2Q t0 l0 A2Z F2R F9D d0 v4 U0 F9E E67 D2J C0 E68 E69 A0l E7A E7B q0 J0 m0 C0 E7C D4Y B0U D4Z F9F Q3 B2d b3 F9G q4 B2 K0 C2 I0 A1h E7D E7E
E4C l1 E7F E7G k1 G0 x1 f0 s1 X0 A6V A1I D4d d5 K8 P0 F6 c0 E7H E7I M3 t6 o2 E7J E7K f1 A2Y W2 D4e o6 A6W D4c p6 A0a E7L T7 G6 E7M U7 G1 E7N C56 K1
E7O C57 z0 D4f C58 I1 D4g C59 H1 D4h V5 c4 D4i o8 A6Y D4b y7 U1 Q4 u2 E51 R1 C5k C5l A30 Z7 v3 w0 A6Z E3 E52 A6a H0 Z3 S0 W3 V0 I5 e0 H6 A0M A0p d3 x0
T4 C1 E7P E7Q m1 B0 C6A C6B F4 j0 t3 y0 E7R E7S J5 B0 C6D C6E I6 E7T E7U z2 r0 p2 T0 E7V E7W Z2 A2Y y1 E7X E7Y V3 g0 K5 K7 E7Z j7 J6 E7a L3 A6b D4n
C5g A6c "Lp5_13:\n" E62 A1 D4o C5h t0 "s_cbranch_vccnz Lc5_16\n" "Lr5_16:\n" C5i C5j W0 k0 V7 D2e D4p D4q W7 D2f A0I X7 a0 E7b E7c Y7 D2I C6J A6f E6D
C6K v0 C6L C6M M8 b0 E7d E7e N8 B7P G9 m0 E7f E7g H9 Y0 p0 Q8 i0 M0 E7h E7i R8 A6g h0 o6 T2 E6H p6 D2 F0 T7 h2 E7j E7k U7 i2 G0 C6F A6u E59 C6G X2 f0
C6H U2 E7l E7m C6I k2 P0 V5 j1 M0 E7n E7o o8 K3 I0 C6N f2 E7p E7q C6R t1 A4 C6S P2 F9H F9I C6T G1 T5 V7 K1 F9J F9K W7 z0 L0 A0I X7 I1 F9L F9M Y7 H1
C6U C6V C6W c4 E6U C6X A6x A0h G9 U1 A0M F9N F9O H9 A1y Q8 R1 C0 F9P F9Q R8 O1 J0 y7 c1 C0 E6X Z7 B3T M9 A6y K0 o5 A6w u0 E6A C6f N4 w0 Z0 C6g v2 o0
C6j R3 B0 E6B E6C C6k i3 O0 C6l L2 l0 C6m S3 Q0 E6Y E6Z V7 T3 B4m W7 U3 E4J E4 U5 e8 A3 E4K F9R f8 H2 F9S A1o A6b N0 C61 Q2 F0h A3S C62 M2 H0 C63 m2
M0 F9T F9U C64 t0 C6O M8 W0 F9V F9W N8 A0i S0 G9 U0 F9X B5 H9 a0 C6P B2M Q8 s0 e9 F9Y U6 R8 A6c C6Z C6h a3 A3T C6i v0 f0 C7 b0 F9Z F9a D7 q0 C6a o6 m0
F9b F9c p6 Y0 X0 T7 i0 M0 F9d F9e U7 A6V p0 C6o i1 A3N C6p s2 F0 C6q z1 F9f F9g C6r E2 h0 C67 Z1 M0 E95 E96 C68 w1 G0 C69 a1 F9h F9i C7A b1 P0 V7 B2q
F9j F9k W7 q2 I0 e8 h1 F9l F9m f8 W1 L0 V6 k4 X7 G1 F9n F9o Y7 K1 E9 C7B z0 A3V C7C B6I C7D C7E M8 H1 F9p F9q N8 c4 d9 Q8 U1 p1 F9r F9s R8 A1y V5 R1
E16 A3W o8 O1 J0 y7 c1 C0 D4t D4u Z7 B3T C6s j3 R0 F9t F9u C6t g2 K0 C6u I3 C1 F2S F2T C6v f3 A8 C7 o3 j0 D7 m3 y0 O0 o6 X3 B4m p6 g3 w0 O4 h4 M9 e3
r0 F9v F9w o5 F2 A5d E99 F0A T7 A6W H0 U7 r2 A3O C6w t2 A64 C6x A24 A3Q C6y t0 D0 S0 C6z W0 w0 F9x S5 V5 d0 A65 o8 U0 D0 V0 y7 a0 e0 Z7 s0 F9y w4 M9
v0 E17 E18 o5 b0 A66 A91 A3R m0 F9z F90 Y0 E19 E2A i0 A67 w2 E1X D4r D4s A69 F91 F92 E2B E2C F93 A2J F94 g0 B0 "s_cbranch_vccnz Lc5_17\n" "Lr5_17:\n"
B9e O2 p0 D3 F95 F96 a2 k0 N2 O7 F97 F98 X1 R0 h0 I2 F99 G0A G2 A60 Y1 D0 E6h E6i A2 f0 B3 E6j E6k d1 A61 e1 D0 B5H G1 A37 K1 E6l E6m z0 A62 I1 D0 B7E
H1 P0 c4 F3 C7F U1 A63 C7G u2 B01 A68 C7P C7N B80 Q0 G0B C7O O1 N0 c1 C0 A9u K2 c0 B1 A5R A4q x2 J0 y2 C0 A9o l2 Z0 d2 E2D M4 e2 K0 G3 A4p X5 H3 E2E
C5x I0 n1 y8 s3 V1 C65 o1 p1 D5B D5C A0 "Lp5_14:\n" E62 n2 M0 F0 C7Q t0 D5D D5E C7e W0 C66 C7f d0 u0 D5F D5G U0 G0 a0 o0 B8X w0 D5H D5I n3 A19 A6f B0
B4J A6g l0 c2 Q0 z8 E2F V7 v0 "s_add_u32 m0, s88, 92928\n" f0 W7 b0 E2G A4S e8 q0 N0 f8 m0 C0 A4T A1c X7 Y0 X0 Y7 i0 L0 A3S C7g A6x J0 C7h Q3 M0 A1d
A03 C7i b3 e9 A04 d5 C7j B2 p0 M8 C2 B8Z N8 l1 R0 h0 G9 k1 C7J H9 x1 C7H Q8 s1 F0b D43 D44 R8 A6y P0 C7m M3 A3T C7n o2 B6Y C7o f1 C7I C7p W2 c0 D45
D46 V7 A6Y B8M W7 G1 D47 D48 e8 K1 K0 f8 z0 C1 D49 D42 X7 A1B F0 Y7 H1 B0 A3V C7q S2 R0 A5o C7r A6a B0z C7s A1p C7t u2 A1q M8 R1 j0 N8 O1 y0 B00 G9 c1
E4U A7o H9 B6N B1 Q8 v3 L0 A8B R8 E3 r0 C7 Z3 T0 A3W D7 W3 U8 o6 d3 c0 D5L D5M p6 T4 A0n q4 C7S F4 A0o j4 X5 C7T t3 B2Y q6 C7U z2 R0 B2Z r9 C7V p2 s9
K4 V5 m1 g0 o8 y1 A0N F1X F1Y y7 V3 L0 G0 Z7 t0 F1W Y3 T7 A2Z H0 U7 B7o A3N M9 U0 A6Q o5 a0 c0 A3O D21 C7W s0 D0 A0p C7X A6u F1Z F1a C7Y A6w A6R C7Z
Z2 "s_add_u32 m0, s88, 67712\n" F1b F1c C7 v0 h0 D7 b0 k0 o6 D2i F1d j7 p6 m0 T0 F1e L3 T7 Y0 N0 U7 i0 "s_cbranch_vccnz Lc5_18\n" "Lr5_18:\n" F1f A4f
c0 T2 B84 C7a D2 A3Q C7b h2 F1g F1h C7c i2 J0 C7d X2 M0 B8d V5 U2 "s_add_u32 m0, s89, 129088\n" X0 o8 k2 B3w y7 j1 B4o Z7 A9z A1z M9 f2 A6S o5 t1
"s_add_u32 m0, s88, 80512\n" A3R A4g P2 R0 F0 A3D G1 D5J D5K A3E K1 B3N A4b z0 F75 F76 A4c B6I A82 F73 F74 S2 K0 U1 "s_add_u32 m0, s89, 130144\n" F2Z
F2c u2 I0 R1 F2e F2f O1 A6T c1 A2g F2a F2b B6N B1 N4 L0 F2d F2g v2 x0 "s_addk_i32 m0, 13728\n" R3 F2h m5 i3 A6M C8P c0 L2 F2i n5 S3 O0 T3 F2j E4 E0 U3
A6N B8g w0 F2k F2l H2 D0 F2m u3 Q2 H0 M2 F2n r3 m2 A6O A0F F2o F2p W0 D0 S0 A0i F2q B5 B9s A2e M0 A1C F2r D83 A6P b0 A4p F2s F2t B6j F2u E63 Y0 C7k i0
E64 E2H a3 A3U i1 F20 E2I s2 C7l z1 G0C F2z F2y E2 B4o Z1 o0 w1 e9 F2x F2w a1 h0 b1 R0 F2v F21 B2q P0 q2 F22 E6r h1 C7u W1 A0M D5S D5T G1 A1 B6a D5U
D5V z0 C7w A1B D5W D5X H1 B0 D5A M0 D5Y D5Z C8h M0 E6s k4 C8i l0 U1 Q0 F23 F24 u2 g5 B1 R1 E6t E6u O1 N0 c1 x0 D9c K2 R0 F25 F26 C8j b5 C8k E6v E6w j3
B4L g2 F1i V6 I3 A6o f3 D0 F1j O4 o3 L0 E0 m3 A0M F1k D5a X3 A6p g3 M0 D5b D5c B09 D0 C9H C9I F2 D5d D5e r2 A6q C8l D5f D5g C3 F1l C9J C9K D5h E0M u6
c3 "Lp5_15:\n" E62 E0N S4 W0 A6r d0 B4M F1m F1n G0 H0 R0 U0 c0 S0 J0 a0 w0 B2S P7 B4 B4W E53 E54 b0 A6j A91 F1o F1p m0 E55 E56 B0U F1q F1r w2 A6k D0
E57 E58 E9R E9S F77 F78 A2J F79 F8A A6m D0 "s_cbranch_vccnz Lc5_19\n" "Lr5_19:\n" B2H O2 C9L C9M D3 C9N C9O a2 E6E E6F N2 A6n X1 E1y F8C F8D I2 h0 G2
E6J E6K Y1 A6h A2 K0 B3 C1 E6G E6I d1 f9 e1 E6L E6M C5A j0 K1 y0 E6N E6O z0 B4U I1 F3 H1 r0 S2 T0 C9P C9Q U1 C9R C9S B1A A0q Z0 B0v g0 K2 E1 E6P B8B
B4z x2 O6 E6Q D5k y2 k0 l2 Q0 D5l D5m d2 E6g E0 e2 D5n D5o G3 N0 H3 C0 D5p D5q C5x D9H D9I n1 A1 E6R E6S C8n A1 I0 C8o J0 V1 R0 D5r s3 C8p C0 F0 o1
E6T E6V n2 K0 c3 u0 E6W E6a A5b G0 d0 E6b n3 U0 o0 a0 A5d B8X c0 A19 C8q E6c E6d c2 A64 v0 E6e D9Y E0I q0 D9Z D9a m0 D0 p0 Y0 G0D D9b A0B B57 G0E E4P
b3 A65 B2 P3 D50 D51 C2 D0 C9U C9V l1 L0 D5u D5v k1 A66 x1 D0 D5w D5x s1 E4Q C9W M3 A1 G0F C9X o2 G0G B9B A67 W2 D65 D5y D5z A96 B9C F27 F28 r4 A6h s4
R0 F29 F3A e5 L0 d5 C9Y g9 l0 A11 O7 F3B F3C A2h F0 g8 L0 F3D F3E f5 O6 Z0 k7 x0 B6z F3F F3G A8L A60 A8M D0 F3H F3I A7D V0 A7E L0 A39 A8N L0 e0 D7E
A3y Y3 D22 A61 B1 X5 A7F R0 A30 A8O c0 A3v A2k A8P D0 A2l W5 m1 A62 A7G D0 D9E C9Z A7H H0 C9a C9b S0 D9D R0 A9b D9C R5 A9a A63 A9Z c0 D9B D9A A9Y C1
C9c C9d A9X F4D D89 A0c A0j C9m A0d K4 j0 y0 Z2 B0 C9e C9f A92 L0 C9p B69 j7 C9q r0 C9r T0 B6J C9s R0 A7g C9t "s_cbranch_vccnz Lc5_20\n" "Lr5_20:\n"
C9g C9u L0 A4B B2c C9v D2 g0 h2 B0 B2p i2 A3U X2 M0 C82 A6X U2 k0 k2 T0 C83 C84 j1 R0 C85 C90 A9z F0 f2 o0 C9y p1 D52 D53 P2 B0 C86 C87 C9z B3N D7F M0
D54 D55 B3n C91 f0 C88 C89 A0j A4v L0 B9U B9V A1b C8u w0 D56 D57 C8v l0 C97 D58 D59 A4w C9A C93 C9B C9C C95 R0 C9D C94 C8w C96 C98 A4x A0 D0A M0 y4
C99 C8x C9h C9i D0B A0 z4 C9j B9W C9k C9l B9X c0 D0E A4y w0 A5 D0C D0F D0D D0G A4z D8 D0H A40 D2d D6A D0J D0I A0 D6B D0K D6C D6D D6E D6F D6G D6H A8S
D0L D0M A8T A8U A8V D6a D6b D0O A2I A0w C9n C9o B0T D0P D0S D0T D0W F2V D0R R0 D0U E1 D0Y "s_waitcnt vmcnt(2)\n" x0 D5O D5P E6n E6o E6p E6q F4g F4f
"ds_read_b128 a[156:159], v215 offset:51264\n" "ds_read_b128 a[152:155], v215 offset:51200\n" "ds_read_b128 a[148:151], v215 offset:55488\n"
"ds_read_b128 a[144:147], v215 offset:55424\n" F8E F8F F0j F0k "ds_read_b64_tr_b16 a[168:169], v217 offset:59168\n"
"ds_read_b64_tr_b16 a[170:171], v217 offset:59680\n" "ds_read_b64_tr_b16 a[172:173], v216 offset:29600\n"
"ds_read_b64_tr_b16 a[174:175], v216 offset:30112\n" "ds_read_b64_tr_b16 a[176:177], v217 offset:59200\n"
"ds_read_b64_tr_b16 a[178:179], v217 offset:59712\n" "ds_read_b64_tr_b16 a[180:181], v216 offset:29632\n"
"ds_read_b64_tr_b16 a[182:183], v216 offset:30144\n" E7r A9g "ds_read_b64_tr_b16 a[184:185], v217 offset:59232\n" A0 A0 A9f
"ds_read_b64_tr_b16 a[186:187], v217 offset:59744\n" A9e "ds_read_b64_tr_b16 a[188:189], v216 offset:29664\n" A9d
"ds_read_b64_tr_b16 a[190:191], v216 offset:30176\n" A9c "ds_read_b64_tr_b16 a[48:49], v216 offset:33792\n" D0N F1
"ds_read_b64_tr_b16 a[50:51], v216 offset:34304\n" D0V F1 "ds_read_b64_tr_b16 a[52:53], v216 offset:38016\n" D0Q A7m A2h A0 D0X g8 R0 B9Y f5 B1 k7
"ds_read_b64_tr_b16 a[54:55], v216 offset:38528\n" "ds_read_b64_tr_b16 a[60:61], v216 offset:38048\n" B4t D0Z A0
"ds_read_b64_tr_b16 a[62:63], v216 offset:38560\n" B9a i1 I4 s2 A0 "ds_read_b64_tr_b16 a[64:65], v216 offset:33856\n" D0a z1 D0b B9b E2 B9Z Z1 D0d D0c
"ds_read_b64_tr_b16 a[66:67], v216 offset:34368\n" D0f a1 "ds_read_b64_tr_b16 a[68:69], v216 offset:38080\n" D0g b1 H0 D0h A0
"ds_read_b64_tr_b16 a[70:71], v216 offset:38592\n" D0k R2 A0 S0 D0e D0i h1 x0 A0 B9c W1 V0 A41 e0 D0j B0w D0l D0m
"ds_read_b64_tr_b16 a[72:73], v216 offset:33888\n" "ds_read_b64_tr_b16 a[74:75], v216 offset:34400\n" D0n
"ds_read_b64_tr_b16 a[76:77], v216 offset:38112\n" D0o A0 D0p B9d D6c D6d D0q "ds_read_b64_tr_b16 a[78:79], v216 offset:38624\n"
"ds_read_b64_tr_b16 a[80:81], v216 offset:42240\n" A42 D0s D0r A0 "ds_read_b64_tr_b16 a[82:83], v216 offset:42752\n"
"ds_read_b64_tr_b16 a[92:93], v216 offset:46496\n" A43 A1 "ds_read_b64_tr_b16 a[56:57], v216 offset:33824\n" D0u A44 L0
"ds_read_b64_tr_b16 a[58:59], v216 offset:34336\n" D0t D0v A45 A8S D0x A46 "s_cbranch_vccnz Lc5_21\n" "Lr5_21:\n" D03 D0w A0
"ds_read_b64_tr_b16 a[94:95], v216 offset:47008\n" "ds_read_b64_tr_b16 a[220:221], v216 offset:46528\n" D01 R0 D08 D0z D02 c0
"ds_read_b64_tr_b16 a[222:223], v216 offset:47040\n" A6e D0y D00 D04 A8T D05 "ds_read_b64_tr_b16 a[84:85], v216 offset:46464\n" D1E
"ds_read_b64_tr_b16 a[86:87], v216 offset:46976\n" D06 A0 "ds_read_b64_tr_b16 a[88:89], v216 offset:42272\n" D1F
"ds_read_b64_tr_b16 a[90:91], v216 offset:42784\n" D07 A0 D1K D1L D09 "ds_read_b64_tr_b16 a[224:225], v216 offset:42336\n"
"ds_read_b64_tr_b16 a[226:227], v216 offset:42848\n" D1A A0 "ds_read_b64_tr_b16 a[216:217], v216 offset:42304\n" D1B A8U D1C
"ds_read_b64_tr_b16 a[218:219], v216 offset:42816\n" D1G D1R D1S D1H "ds_read_b64_tr_b16 a[228:229], v216 offset:46560\n"
"ds_read_b64_tr_b16 a[230:231], v216 offset:47072\n" D1D D1T D1J R0 E63 D1N D1M A0 E64 D1Q D1I D1O A8V D6e D6f D1P E2H E2I D1U
"ds_read_b64_tr_b16 a[168:169], v216 offset:50720\n" "ds_read_b64_tr_b16 a[170:171], v216 offset:51232\n" D1V A8X C2
"ds_read_b64_tr_b16 a[172:173], v216 offset:54944\n" D1W "ds_read_b64_tr_b16 a[174:175], v216 offset:55456\n" B2 g1
"ds_read_b64_tr_b16 a[176:177], v216 offset:50752\n" D1X D1Y "ds_read_b64_tr_b16 a[178:179], v216 offset:51264\n" A8Y s1 A8Z D1Z
"ds_read_b64_tr_b16 a[180:181], v216 offset:54976\n" "ds_read_b64_tr_b16 a[182:183], v216 offset:55488\n" D1a
"ds_read_b64_tr_b16 a[184:185], v216 offset:50784\n" D1b "ds_read_b64_tr_b16 a[186:187], v216 offset:51296\n" A08 A8a D1c
"ds_read_b64_tr_b16 a[188:189], v216 offset:55008\n" "ds_read_b64_tr_b16 a[190:191], v216 offset:55520\n" D1d B9f D1e D6g D1f D6h D1g B9g D1h B9h D1i
B9i D6i D1j A8X D1k D6j A8Y D1l A8Z D1m A8a D6k D6l D1n "Lend5:\n" "Lc5_0:\n" B61 A5A D1o A7J B1S D6m "s_branch Lr5_0\n" "Lc5_1:\n" B62 D17 L4
"s_branch Lr5_1\n" "Lc5_2:\n" B9j D18 d4 "s_branch Lr5_2\n" "Lc5_3:\n" B9k D19 L4 "s_branch Lr5_3\n" "Lc5_4:\n" v1 D1q B3C N3 B4y r6 A8c B9l D9P A98
E1z E4a B3B "s_branch Lr5_4\n" "Lc5_5:\n" r1 D1r B1R W4 B3C p5 A7J A8q A8c A3o "s_branch Lr5_5\n" "Lc5_6:\n" v1 D1s A97 N3 B1R r6 A5A A8p A7J A3j
"s_branch Lr5_6\n" "Lc5_7:\n" r1 D1t A8b W4 A97 p5 B5X A8q A5A A3o "s_branch Lr5_7\n" "Lc5_8:\n" v1 D1u A7I N3 A8b r6 B3Z A8p B5X A3j
"s_branch Lr5_8\n" "Lc5_9:\n" r1 D1v A48 W4 A7I p5 B1k A8q B3Z A3o "s_branch Lr5_9\n" "Lc5_10:\n" v1 D1w B4x N3 A48 r6 B0H A8p B1k A3j
"s_branch Lr5_10\n" "Lc5_11:\n" r1 D1y D14 p5 A8o A8q B0H A3o "s_branch Lr5_11\n" "Lc5_12:\n" v1 D1z B1T N3 B3D r6 A7O A8p A8o A3j "s_branch Lr5_12\n"
"Lc5_13:\n" r1 D10 D15 p5 A49 A8q A7O A3o "s_branch Lr5_13\n" "Lc5_14:\n" v1 D11 A8c N3 A98 r6 A3i A8p A49 A3j "s_branch Lr5_14\n" "Lc5_15:\n" r1 D12
D16 p5 A3i A3o "s_branch Lr5_15\n" "Lc5_16:\n" D1p D2A "s_branch Lr5_16\n" "Lc5_17:\n" D13 D2B "s_branch Lr5_17\n" "Lc5_18:\n" v1 D6o D2C
"s_branch Lr5_18\n" "Lc5_19:\n" r1 D6p D2D "s_branch Lr5_19\n" "Lc5_20:\n" v1 D80 D2E "s_branch Lr5_20\n" "Lc5_21:\n" r1 D81 D2F "s_branch Lr5_21\n"
".fill (Lv0+589824-.)/4, 4, 0xBF800000\n" "Lv6:\n" "Lb6_0:\n" C0m C0n C0o C0p C0q C0r E4b A0N C0s C0t g7 G7 "Lb6_1:\n" M7 F4k B7b C0u
"s_mov_b32 s54, Lend6-Lv6\n" C0v C0w C0x B7c C0y C0z C00 C01 C02 B5Z C03 C04 B7d C05 C09 C1D C1H C1I C1K C1J C1O C1T C1C C1R C1S C1P C1Q C06 C1Z C1V
C1U D6q D6r C08 C07 B7e C1A C1a C1b C1c C1B p8 C1E A5I C1d C1g C1h C1i C1w C1x C1y C17 C1F C1G C1e "s_mov_b32 s92, 9216\n" C1f p8 C1L C1j C1k C1l C1m
C1M C1N p8 A5J "s_addk_i32 s92, -3072\n" C1q C1n "v_cndmask_b32_e64 v160, v209, 0, s[56:57]\n" "v_cndmask_b32_e64 v161, v209, 0, s[58:59]\n"
"v_cndmask_b32_e64 v162, v209, 0, s[60:61]\n" "v_cndmask_b32_e64 v163, v209, 0, s[62:63]\n" C1o C1p C1r A5J C1s C1t C1u C1v C1z C10 C11 C12 C15 C13
C14 C16 C18 C19 C2A C1W C2C C1X C1Y B7f C2B D6t D6u D6v D2G D6s C2D P3 G0H E4b B7h G0I G0J B5a G0K B5b c0 D9d D9e D9f D9g D9h D9i D9j D9k F3J B5c F3K
C0c B3a G0L G0M G0N G0O E1 D9l D9m A5K D9n D9o D9p D9q D9r D9s B7a C0c P3 G0P C2J B3a G0Q S8 G0R G0S G0T D9t D9u D9v D9w D9x D9y D9z D90 D3V R6 E1 D9d
D9e D9f D9g D9h D9i D9j D9k D3W D3X C2O B7a B3b C0d B3c C0c N0 G0U B3a G0V G0W G0X G0Y G0Z G0a G0b G0c D91 D92 D93 D94 D95 D96 D97 D98 D3V D3W D3X B3b
G0d T1 D9l D9m D9n D9o D9p D9q D9r D9s B7a C0d B3c C0c B3a G0e G0f G0g G0h G0i G0j G0k G0l E1 D9t D9u D9v D9w D9x D9y D9z D90 D3V D3W D3X B3b G0m C0d
B3c G0n G0o G0p G0q C2d D91 D92 D93 D94 D95 D96 D97 D98 D3V D3W D3X B3c C0d G0r B3b G0s G0t G0u C2i x0 G0v A0N A3t A3u B7b A3v A3y A3w A3x C2j C2k C2m
C2l B7c A0 B78 A7X G0w G0x j2 G0y G0z B5Z A0 C4H c0 D9d D9e D9f D9g D9h D9i D9j D9k B7a C0c B3a G00 G01 G02 G03 E1 D9l D9m D9n D9o D9p D9q D9r D9s B7a
D7L j2 D9z D90 D3Y B79 D3Z E1X D9v D9w D3a D9x D9y D3b E7s E7t B3a G04 C0c G05 G06 G07 R6 E2M A0 D9t D9u G08 E7u E7v A53 A0 B5h E7w A52 K0 B5i J3 E7x
E7y E7z A51 K6 B3c h3 J3 C0d G09 B3b G1A D94 G1B D96 D97 D98 D92 D93 E7v D95 E7x E7s E7t G1C D91 E7y E7z E7u E7w A50 A0O B3b h3 C0d B3c G1D G1E G1F
G1G C4R C4S A5z C2n C4T C4U C4c C4d C4e C4f B0 G1H G1I B5l E70 E71 j0 E72 C2o B5n G1J J3 E73 B0 K6 G1K h3 E74 J3 C3A E75 p1 A0O C3B h3 C4X C4Y C4Z C4W
B3i E76 x0 C3I A0 "Lp6_1:\n" F3L E77 B5o G1L E78 B5p G1M E79 B5q G1N E8A B5s E8B X0 B3n f0 E8C g0 E8D E8E G1O G1P G1Q G1R E0 I0 w0 E2M C3K C3L D3b F0
D3a C3M C3N D3Z M0 G0 D3Y B0 C3O F3M D7L I4 P0 A2o F3N D99 C3P C4n A53 k0 A52 Q0 h0 A51 T0 X0 A50 e0 B38 A5z N0 k8 C0 C4o F3O C4l C4m C3R p0 E1 G1I J0
E71 B84 E72 F3P F3Q E73 F0 E74 K0 E8F E75 P3 B7g A3k E76 u0 G0 E77 o0 E78 B7i C3S E79 Z0 E8A B7j G1S E8C A7m G1T G1U G1V c8 B7k F3R e0 A0j B1 F3S O0
l8 r5 C3T E8F B0j C44 C45 C46 C48 C3U G1W G1X G1Y G1Z C5K C5L C5I C5J E1 C2N D3c C0e C0f A2p C2E C2F C2G A7V A8t A3s B1f B5j B5Y C2Q C2R C2S C2T E6x
B3l D3c E8G "s_add_u32 m0, s89, 144320\n" E8H C0f A2p C2K B1f B7e B5j B5Y C2W A5I B7h A0 B5a A0 B5b A0 B5c A0 A5K F4y p1 x0 A3X l0 C4A B0 A0Z B2l A8y
A7Z A8C m9 N0 n9 B4T E7r C5B D7M C0 A7b C5C D7M C3V C5D D7M B4C C5F D7M A3A C5G E2V A7a C5H F1 A80 s5 J0 L8 R0 A8z A5z B4o A50 R0 C0 F3T A51 M0 L3 A52
c0 h0 A53 w0 f0 C49 "s_cbranch_vccnz Lc6_0\n" "Lr6_0:\n" F3U E1Y K0 t5 C1 X0 E1Z B0 B4X L5 P0 E1a j0 u5 B4Y E1b I0 M5 B81 E1c A9i v5 A3c E1d G0 Q7 M0
A5x F3V E1e D9M D9N A16 A5y A7t G1a E1f A7u A1e N5 A1 E1g R0 e4 f4 w5 E1h a7 A3B O5 A1 E1i R0 A4n x5 E1j A3C a9 A7w A0 B3i p1 C2I g0 B0 C2P C2V C2a k0
A0C c0 E8I E8J N0 K7 E2N D3c C0e C0f B3f J0 A1N C2U C0 E1 A7V A8t C4Q P3 D4A D4B A3s D4C B1f o0 P3 x0 B0 A1P o9 A31 A83 A2B l0 B5j C2b u6 C4y g1 A4X
C2c C4z g1 C5E D6y C40 A1 B6E E11 C41 A2R E10 C42 M0 B2U D4I C43 A0 B4D E37 F3W R0 V2 M0 F3X E8K J2 c0 D4D F3Y J1 c0 B1b w0 C0F T1 O0 Q6 E0 E1k A0 F3Z
F3a A7T A6L E1l A4a A4Z A4j S0 E1m A2r N9 V0 E1n q9 R7 e0 E1o B1E y5 B4B E1p A2s A3I L0 F3b E2O E1q B6U A0K C0e A4V G1b E1r A0 B1w B5g c9 M0 G1c B5f
E1s A1N C2X m8 C2Y E1t C2Z F3c Q5 E1 G1d E1u G1e z5 G1f E1v D3c A1k "s_cbranch_vccnz Lc6_1\n" "Lr6_1:\n" C0e F3d v4 F3e B1w A2p S8 C2p A7V R6 G1g
"Lp6_2:\n" F3L E2O C0e C0f A2p K0 C4M B0 C2q C2r C2s C2t B1f B7l E2P j0 C4N B0 E2Q B5Y C2h C2g C2e C2f D4H r0 F3f A3s B1f B7l E2P p1 x0 E16 A6B g0 A3F
B0 A2D A2t B6F A0r A7C A8D C32 E2Q k0 E7r G1h Q0 B5k W0 T0 B1v d0 D7M C2z C20 U0 B9e C2w C2x D2J A0 D6y E11 C2u E10 D4I A8u B5h E2R q0 B5i E2S m0 J3
B3l E1P Y0 K6 E1Q D4G B5j B5Y C22 C21 h3 J3 i0 E1 A0O D5 B1Y h3 A6 C23 C24 E5 B1Z C25 n8 C26 B5m O3 B2t B5l P1 B5n C29 N1 B2u J3 q1 A0 K6 C27 S1 G1i
h3 w3 A0 J3 C28 z3 A0u A0O L1 v6 h3 D1 A0v C3E J1 E2J C3C J2 F3g C3D B9t A2E C3F F5 B5r C3G B6 G1j C3H p0 A4u F3 M0 u1 B9y C3J a0 P0 U0 w0 B9z B42 d0
I0 W0 F0 z7 G0 s3 C0 n3 p3 B43 B44 Y0 E0 m0 B45 C4V q0 J0 b0 D5i D2t G5 R4 C6 c0 H0 H5 d8 D7D D6 S0 B38 O0 A99 t4 O6 h5 C0Z S1 x0 q1 w6 x6 N1 V0 P1
A5S p0 B6C e0 D7C C0a x3 A6J B0l J2 C4g A6K B4P D1 C4h C4i i9 B4Q V1 E65 c2 M0 B1 h7 E66 B0m E6 K0 A0F E67 E68 W0 u0 Z0 A0i E69 k8 U0 A1t A8x E0 B9T
E7B A2o g4 H0 b0 S0 q0 V0 m0 e0 Y0 A3k i0 o0 A0J A4l c8 f0 A1h E7D E7E A0L l0 K8 w0 E7F E7G F6 Q0 X0 O3 B0 E7H d5 P1 N0 N1 C0 E7I E7J q1 P0 S1 E7K w3
J0 z3 C0 A0 E7A L1 E7C D1 D4Y J1 D4Z J2 D9O V2 F1 A4i A0a A0 I0 G6 E7L K4 p0 K0 B7 q4 M0 u1 F0 a0 E7O E7M B4A G0 d0 E7N Y3 B1V B1 c3 j0 p3 b5 Y0 O0 m0
E0 q0 B0C b0 y0 p0 i4 A4l I5 A1H H6 X0 J5 r0 I6 A6z t4 S0 S1 V0 q1 p1 e0 N1 x0 P1 B0E B6C E7V B3O m1 E7W B3P x3 P0 J2 E7P E7Q J1 I0 D1 F0 L1 G0 E7R G4
M0 E7S j9 K5 E7T E7U J6 g0 Z2 M0 E7X E7Y t0 Q9 A5b E7Z E7a d0 U8 U0 A1t A8x E0 s0 l8 r5 B3o s5 B0j v0 E7b E7c b0 k0 D2i E7d D9S m0 B2I Y0 E7e D9T i0
N0 L8 C0 E7f D9U E7g h0 E4c f0 R6 B1X B0D L3 X0 m5 A0j "s_cbranch_vccnz Lc6_2\n" "Lr6_2:\n" C3W c0 F0 D2 K0 C3X u0 G0 i2 n5 u3 l7 E1 r3 D9V t5 o0 h6
B0 E7p L5 E7n C3Y E7o U2 l0 C3Z v4 E7q j1 D3A N0 N1 A00 E4d z3 E7h L1 E7i D1 E7j J1 B3V E7k E7l k9 m7 E7m A01 u5 O0 i6 G1k G1l M5 E0 C3a G1m G1n f2 H0
C3b C0N S0 n7 V0 v5 e0 j6 A0D Q7 w0 G1o u1 G1p A0 "Lp6_3:\n" F3L a0 G1q U0 G1r B3d A1a C3d F1 F3h C3e E6z F3i E9r N5 E9t B3Y E9u C3f E9s S3 C3g U3 B5U
D3d S7 Z0 J1 x0 D1 G1s Y6 L1 O0 G4 G1t G1u A1J A7m w5 G1v A0E k6 H0 O5 A8J C3i S0 H2 V0 C3k e0 m2 A5n z6 E9v o7 C0 E9w x5 E9x l6 E9y B1h A0S t0 J0 W0
A4s d0 A3n U0 R9 i7 B1n B3X C3m A5E f0 C3n F3j V4 B0I P0 A7T I0 A0b F0 B3I G0 C3o A9v D25 A9w B4 C3p A9x b1 A9y O3 D23 A8h B0B K0 w3 B7 z3 D2w B1B D1
j0 J1 G1w G1x C0g V2 G1y G1z p7 Z0 B92 B04 A06 D3e C3q B54 H0 C3r B05 D2c S0 q7 V0 y5 e0 b9 A1K B5B A8m u1 A8n a0 B1c U0 B1d B3d A7L C3v F1 F3k C3w
B7I C0E C3y m3 C3z D9W E3y p1 A18 W8 B0J B7B T9 B3e B7X B7C A0T m8 E0 m6 A0U U9 Q5 H0 C30 B4N F2 S0 C31 u8 O8 C3 V0 Z6 B7L r7 e0 z5 A2c P8 n6 A0x C0h
A0y t0 i5 W0 X8 d0 Y8 U0 j5 i7 C0X B59 B5T C0O A0N A4r "s_cbranch_vccnz Lc6_3\n" "Lr6_3:\n" O2 E1 B4w A5e a2 D0 f0 N2 X0 A1L F3 a6 A5f A6 B4e D5 I0 X1
F0 I2 A5g B8P G0 Y1 B0F s3 O3 B0G n3 P1 A5h N1 E8L B1i q1 B1j S1 w3 A5i z3 D0 B5N f7 D1 Q4 J1 A9B J2 A5j V2 D0 B0V s7 Z0 D3U A5k E5 D0 A0V A2 E0 B3
A1Y d1 A5l e1 A99 t7 y0 H0 b6 E1y S0 B6 V0 F5 w0 B7Q r0 a0 T0 A0m U0 A8d d0 A8e W0 g0 c3 A7W B1l Y4 B1m B40 k0 D3R A5R B0A B7S D29 i9 C6 W6 G5 k5 V1
l5 d2 c0 A0A e2 B7T B7J A2g A18 B0W B0J A2X k8 B3e C0V B6L c6 E0 D6 z9 A2o H5 H0 b2 S0 n1 Q0 V0 o1 T0 e0 n2 B0t D3f B9Q A7Q u7 N0 d6 C0 A5a E6 C0B B1C
t0 J0 B8G B4n d0 B2F U0 A8W a0 K0 s0 u0 B5V p1 B2w q0 o0 m0 E6g p0 A0Q f0 Q3 l0 b3 T1 X0 C4p E1 C4 q4 C4q P0 B2 D4 d5 C2 I0 "Lp6_4:\n" E8M A1M F0 A4Y
G0 A0L t6 A1h T1 A76 C4r T1 A77 C4s v8 l1 w8 k1 x1 s1 D3A E4h A5H A2f D3G D3d O6 C4k k9 v7 A4K A0G O0 F6 A2K A3p K8 E0 M3 A8G o2 L0 H0 C4t R0 Z5 E4E
C4u S0 f1 a5 B2g W2 V0 w7 B5M B3O e6 e0 G6 A4L B3P A0a M0 A26 C4w c0 A27 C4x A4M u1 a0 U0 d0 v4 W0 D9F Y0 N0 m0 C0 p0 D7K J0 v3 C0 X0 E3 B0E Z3 P0 W3
K0 A2n I0 f6 F0 H6 G0 I5 j9 m1 d3 E1 T4 A4e B7K O6 A18 B37 B0J C0I l8 B3e A7U A6d A3u Z9 E0 I6 A3t r5 J5 H0 z2 E42 E43 p2 S0 y1 E44 K9 V3 V0 s5 E45 x7
e0 g6 E46 L9 J6 E47 K5 M0 E48 t0 E49 W0 E5C d0 E5D U0 E5B a0 E5E s0 L8 x4 B59 B7N "s_cbranch_vccnz Lc6_4\n" "Lr6_4:\n" T2 E1 D2y A4 l7 P0 t5 I0 h6 F0
L5 C0A E5H C0H E5I D2z E5J j1 E5K J9 D4w L1 D4x D1 A9K A3r A9L B2J k9 m7 B2K A01 u5 O0 i6 E5N M5 E0 K3 E5L E5M f2 C1 H0 t1 D5i E5O C0N S0 n7 V0 v5 j0
j6 y0 e0 D2m A0D u1 B0 E5R a0 r0 D2f E5S d0 E5T W0 E5U c3 g0 p3 B0 F1 Y0 C0Y q0 k0 b0 A5d D2t N4 R4 v2 A5p R3 D0 A1a i3 P0 B1a I0 A16 A5q A2U D2g N5
G0 Q2 E1 A1b L2 A5r S3 D0 E5f T3 E5g U3 E5h t4 A5s S1 E5i q1 G10 A68 N1 A1I x0 A8g a3 A5t x3 D0 E5Z D3P E5a E5b D1 A5u D2Z E5c E5d B5R D2p E5e Y6 w5
A5v k6 B90 O5 E0 A3 E5j A0E H2 A5w M2 B9A m2 F8B E5k E5l z6 S0 o7 N0 x5 C0 E5m A7R l6 V0 a9 c0 B1D E5n A7S W0 J0 B2E E5o A3D U0 G11 A3E a0 G12 A4b s0
K0 A0Y A4c A0l C0W I7 A0Q f0 i1 C8y V4 D2a D2b G0 A0b E9 B3I E5v Z1 E5w w1 G13 a1 G14 b1 J9 E5p L1 E5q D1 E5r A3r E5s E5t k9 p7 E5u A06 B92 u0 G15 G16
D3e R2 E5x E5y q2 H0 h1 o0 W1 A9T q7 S0 y5 V0 b9 e0 C0D A1K u1 E5z a0 E50 U0 G17 d0 G18 W0 D9F B40 A4l E2L E4B A0 C0U l0 g2 I3 q5 f3 I0 A3m D9X A0K F0
A07 G0 c9 B2R r2 D7Q B2w E15 q1 A0 "Lp6_5:\n" E8M N1 A12 E1 F1 C5M A0 C4 C5N Q4 w2 D4 x3 Z0 J2 O0 J1 E0 D1 T9 U9 L1 G4 T1 F1 C5O
"s_waitcnt vmcnt(6) lgkmcnt(0)\n" E7 C5P F0i H0 A2j x0 m8 F7 F3l B8y m6 S0 B0r Q5 F3m F3n A93 e3 Q0 V0 C8G F2 c0 F3o F3p A3d L6 C5Q B0 e0 M6 t2 a5 F3q
B0i C3 N0 A4d Z6 C0 A2a O8 A7z r7 Z8 P8 N6 C5T z5 c0 A9 C5U C5V J0 C5b C0 Z5 A4W n6 A1k A8C t0 K0 W0 A4m B5I H7 a0 A1 J7 s0 L0 a8 C5a b8 C0X C8B A0
D3N B2v "s_cbranch_vccnz Lc6_5\n" "Lr6_5:\n" o4 O2 f0 B9w p4 D27 I0 a6 F0 A6 G0 D5 E1 A7p s3 X1 A7q n3 I2 A7r G2 A7s Y1 J9 G19 L1 G2A D1 G2B A3r G2C
G2D k9 s7 G2E A5F D3U A3c E5 E0 C7x H0 d1 A5G e1 S0 t7 V0 b6 e0 B6 A0m F5 w0 A5x u1 A5y a0 A7t U0 A7u B3d D28 B7S D29 i9 C6 a7 G5 e4 V1 w0 f4 d2 A1e
e2 B7 B7T j0 q1 A7w N1 A12 r0 c2 T0 D2N A3e D3P g0 D1 B0 A3f L1 A7X Z0 G4 A5m A17 k0 c6 D9c A8H k8 D6 O0 H5 A8I A09 b2 A6A n1 D0 E0 o1 A1A A2o n2 H0
A0J A4n u7 A6C d6 D0 A1Q E6 S0 h7 R0 C0G A6D W0 D0 C0L A3k U0 B9 a0 c0 C9 s0 A6E c8 E8L A1f v0 A1g F3r C7L A6F Y0 D0 D3O Q3 l3 b3 A6G B2 B4e C2 I0 A1M
F0 A4Y A6H A0L D0 G0 A1h t6 l1 E1 A54 k1 A6I x1 Q0 E3v A55 s1 A4Z O3 A4a P1 N0 N1 K7 A69 A00 J0 z3 C0 D2U Q4 J1 A84 J2 K0 V2 A32 v7 Z0 A0G O0 F6 B4E
A3p K8 E0 M3 C7y H0 f1 B4F D3g S0 w7 V0 e6 e0 G6 A2i A8f A56 A8i u1 A57 A8j a0 A58 A4f U0 A59 A4g B3d B66 F1 B7V G0 f6 j9 H6 I5 u0 D2X F4 o0 t3 B7K
A1I A18 A6B B0J B82 l8 B3e B1e B0 B83 A6s Z9 E0 I6 A6t r5 J5 H0 z2 l0 p2 Q0 A2t y1 B0L V3 O9 K9 s5 N0 x7 C0 P9 A70 g6 V0 J6 A71 A2u K5 A1 B1D p1 B2w
W0 L9 d0 A2v U0 J0 B8c A0s s0 A0t L8 M0 A0 i8 C5n M0 C4 C5o C7R C1 D4 B1X b0 B0 j8 B0D q0 V8 j0 Y0 "s_cbranch_vccnz Lc6_6\n" "Lr6_6:\n" y0 i0 w0 p0
C5q "Lp6_6:\n" E8M "s_waitcnt vmcnt(9) lgkmcnt(1)\n" B0 E7 C5r r0 T2 E16 F7 D3L f0 i2 g0 l7 f9 t5 T1 A4 C5s E1 Z5 C5t k0 h6 "s_mov_b32 s92, 180224\n"
a5 L5 P0 X2 I0 U2 X4 k2 F0 j1 T1 G0 C5u E1 B1Y u3 C5v B1Z r3 O3 B2t X9 A1i P1 B2u Y9 A1j A2S N1 A2T C8G q1 A3d L6 M6 S1 B0i w3 A4d A3K z3 A0u A3M N6
C5z L1 B1 C50 D1 v6 A0v J1 Z0 J2 A36 V2 O0 m7 A2E A01 u5 E0 i6 B9y B0f M5 H0 K3 C72 S0 t1 B9z D3Q P2 V0 n7 Q0 e0 v5 T0 D3S j6 B44 Q7 M0 B42 u1 N0 B8c
B43 U0 B45 C0b J0 c3 D5i F1 C0i B40 K0 D3R A0W A1a B7W w6 N5 d8 B3Y h5 L2 x6 B3W B5U P3 A18 A38 B0J B4P Y6 B3e D2p B4Q E65 w5 E0 k6 E66 A0E O5 H0 A3
S0 H2 V0 M2 e0 m2 A3G A7R z6 A3H A7S o7 B0l x5 B0m l6 E69 a9 R0 E67 t0 E68 W0 E7B B5I i7 B1n B3X i1 A5E f0 C81 V4 B0I P0 A7T I0 A0b F0 B3I G0 Z1 E7D
D25 E7E B4 a1 E7F b1 E7G C0M q1 u0 S1 A13 E7A L1 o0 D1 B0 E7C D4Y J1 B3V D4Z D9O V2 l0 p7 A0N A4i N9 Z0 A0H E7J A06 R7 A5e R2 D0 E7H E7I D2M E7K W1
A5f q7 D0 E0 y5 A9T D2r B5B D7B A5g a0 D0 D3C A1K W0 A5h c3 E8L A0 E7L Y4 E7O B40 A5i A91 h0 D2R R4 j3 A5j g2 D0 A7L I3 P0 f3 I0 A3m E7M B93 A0K A5k
A07 D0 E7N c9 F0 r2 w0 G0 o3 A5l m3 Q0 F55 B94 X3 E14 N0 S1 C0 B7Y J0 U4 B0R E2K A2g A18 E7P E7Q D1 K0 L1 E7R E7S B5R B7X E7T D3M O0 m6 E7U E7V Q5 D2O
R0 E7W D2P H0 t2 E7X E7Y C3 S0 Z6 E7Z O8 r7 V0 z5 E7a E7b n6 e0 A1k E7c P8 t0 E7d A5b E7e d0 E7f U0 E7g i7 C0X B59 B5T C0O A4r
"s_cbranch_vccnz Lc6_7\n" "Lr6_7:\n" O2 C0T X0 N2 F3 A1L C1 P0 a6 B0 I0 A6 F0 D5 E1 G0 X1 E7n B7A j0 G2 y0 E7o n3 Y1 B0 E7p O3 E7q P1 r0 N1 T0 A00 g0
z3 E7h L1 E7i D1 E7j J1 B3V E7k E7l k9 s7 E7m A5F D3U G1k E5 E0 A2 G1m A5G B3 H0 d1 B0 G1n e1 S0 t7 G1l C0Z b6 V0 B6 p1 B2w F5 R0 B7Q k0 a0 Q0 G1o C0a
D2f G1p d0 G1q A8w c3 M0 "s_mov_b32 s92, 229376\n" G1r C7z M0 C4 C70 I7 p3 X4 Y0 "Lp6_7:\n" E8M D4 E1R T1 B0A C73 O6 E7 J4 q5 x2 T1 I0 C74 F7 D3H y2
F0 l2 G0 A2m D3I W9 C6 E9r G5 E9t V1 E9u d2 E9s e2 D9K M0 C75 Z5 C76 a5 B67 A12 A5m c2 C0 A0 x3 G1s D3P G1t G1u D1 J0 L1 C0 G1v A7h G4 A05 C78 w9 k8
C79 K0 A17 u0 O0 c6 E0 D6 E9x A2o H5 o0 b2 H0 n1 S0 o1 V0 n2 E9v D3f e0 u7 E9w A7Q d6 E9y E6 A3n C0B A0S t0 A4s W0 R9 B5I i7 B5V B3X B4j A9k A9t l3
A1M D2v D3D A9v B3H A9w B9x g7 A9x s1 A9y J9 D2w B1B D1 G1w A3r G1x G1y k9 v7 G1z A3p A0G O0 F6 B04 K8 E0 M3 C5y H0 f1 B05 D3g S0 w7 V0 e6 e0 G6 A2i
A8f A8m u1 A8n a0 B1c U0 B1d B3d B66 F1 B7V A0X f6 B0 G0 H6 j9 I5 D2x d3 l0 T4 "s_mov_b32 s92, 278528\n" A4e t4 X4 S1 B7U B41 A7X A18 W8 B0J B7B l8
B3e A7U B7C A0T Z9 E0 I6 A0U r5 J5 H0 z2 B4N p2 S0 y1 u8 K9 V3 V0 s5 B7L x7 e0 g6 A2c L9 J6 A0x K5 R0 Q0 A0y C8t i5 W0 X8 d0 Y8 U0 N0 B8c j5 s0 L8 x4
v0 J0 B86 B1X m0 B0D Y0 D23 L3 A0B f0 "s_cbranch_vccnz Lc6_8\n" "Lr6_8:\n" T2 E1 C8O K0 h2 A4 i2 P0 l7 I0 t5 F0 h6 G0 L5 B0F u3 X2 B0G r3 U2 B1i k2
B1j j1 J9 B5N f7 D1 A8K A3r B6m k9 m7 A4t A01 u5 O0 i6 A1Y M5 E0 K3 A0V f2 H0 t1 C0N S0 n7 V0 v5 e0 j6 A0D Q7 w0 A8d u1 A8e a0 B1l U0 B1m C0b c3 G7
"Lb6_2:\n" M7 F1 C0i C0R F1 B7W W6 N5 k5 B3Y l5 L2 A0A B3W B5U A1r D3P A1s D1 E61 C0F x0 B7 Q6 A2X Y6 A1J I4 w5 B6L k6 j0 O5 A7w z9 A3 E0 H2 B0t A0E
M2 r0 C3l B0u z6 H0 o7 D5Q x5 g0 l6 B0L B1h V0 A35 k0 D2e A5n D2f B1C a0 B4n D2I A0Y C0 B2F v0 A8W B86 J0 m0 A5d h0 Y0 D3O i1 A5p s2 D0 V4 z1 P0 E2 I0
B0I A5q G2F C0C A76 S4 A0 "Lp6_8:\n" E8N Z1 D0 A77 B7Z v8 a1 w8 b1 A5r O3 D0 A8h q1 A5s S1 G10 B8J A13 A2f L1 A5t D2l A9E J1 B3V A4K V2 A5u p7 R0 A2K
A3D N9 C8P A0H A9F R7 A5v R2 D0 A8G D2M E0 W1 A5w q7 F84 B5K y5 H0 b9 S0 C0D V0 u1 K0 a0 u0 A5c A1K d0 A26 A3E W0 o0 c3 E6g A27 A4b p3 A4L A4c B3S B96
q0 l0 D2R R4 A9h A7L B5S B7I A4M E13 T1 D2s D3d Z0 J1 O0 D1 E0 L1 A0q T9 U9 G4 x0 B7X B37 m8 m6 B2G Q5 H0 e3 M0 A3u A3t F2 S0 t2 E42 O8 C3 V0 Z6 E43
E44 r7 e0 z5 E45 P8 n6 E46 C0h E47 t0 E48 W0 E49 d0 E5C U0 E5D a0 E5B s0 v4 E5E C0X C8B A0 D3N N0 i0 "s_cbranch_vccnz Lc6_9\n" "Lr6_9:\n" C0 T1 O2 C0T
J0 N2 C0 X0 A1L F3 a6 P0 A6 E5J D5 K0 X1 p1 B2w I2 C1 I0 G2 D5i F0 Y1 E5H s3 O3 j0 P1 y0 G0 N1 B0 E5I n3 q1 E5K S1 r0 w3 c0 T0 C8C C4 C8D D4 z3 g0 L1
B0 D4w D4x D1 B1 J1 B6P J2 k0 V2 R0 B2j C8E U8 C8F B2T s7 "s_mov_b32 s92, 483328\n" O0 X9 A1i V9 X4 Y9 A1j n8 E5N A5F A93 E5 E0 C8G T1 C8H a5 E5L A3d
L6 A2 H0 M6 B3 E5M E5O B0i d1 S0 A4d e1 c0 A5G C0Z A7z C8M V0 N6 C8I t7 e0 C8J b6 C0a C8L Z5 B6 E5T F5 E5R u1 E5S a0 E5U U0 C0b c3 M0 F1 C8N F1 p3 Q0
p0 Y0 T0 h0 A89 B7P D69 i4 C0 D28 F3s J0 l2 B8M A2m F0 W9 G0 C6 E5h D3H G5 K0 V1 E5f D3I d2 E5g e2 E5i B7T B7J P3 A18 E5Z E5a B0J E5b k8 B3e C0V E5c
E5d c6 E0 D6 E5e A2o H5 L0 H0 b2 S0 n1 V0 o1 e0 n2 E5j D3f E5k A7Q u7 E5l d6 E5m E6 G11 h7 R0 E5n t0 E5o W0 G12 B5I i7 B5V B3X B4j A9k A9t l3 A1M D2v
D3D E5v B3H E5w B9x G13 s1 G14 J9 E5p L1 E5q D1 E5r A3r E5s E5t k9 v7 E5u A3p A0G u0 O0 F6 G15 K8 E0 M3 E5x E5y o2 o0 f1 B0 G16 D3g H0 w7 S0 e6 l0 G6
D9c V0 A8f B7Q A2i a0 A6A U0 D0 E5z d0 E50 W0 G17 c3 A6C p3 D0 A0 G18 Y0 C0Y q0 A6D A4P D2t v3 R4 E3 A6E Z3 "Lp6_9:\n" E8N E8L B0E W3 P0 A2n I0 f6 A6F
H6 D2g I5 G0 D2x j9 d3 A6G T4 A4e D0 B67 A6H P1 D0 U4 x4 B41 A0q A18 F3l F3m D1 A6I L1 Q0 F3n F3o G4 D84 Z0 A7U F3p D3K N0 I6 C0 F3q A2a J5 O0 z2 Z8
A9 p2 E0 y1 A4W r5 V3 J0 s5 C0 A8C x7 H0 g6 A4m J6 K0 K5 R0 c5 D2T B0j U0 H7 a0 J7 s0 a8 B7M b8 g4 B8L A8u B7N "s_cbranch_vccnz Lc6_10\n" "Lr6_10:\n"
T2 w0 D2y A4 l7 P0 t5 I0 h6 F0 L5 C0A A7p C0H A7q D2z w0 A7r A8i j1 A7s A8j O3 B8n A8h B7 D7G G19 L1 j0 D1 y0 G2A G2B J1 D23 B3V G2C G2D V2 r0 m7 T0
G2E u5 Z0 i6 A3c M5 g0 K3 A7c O0 t1 E0 P2 B64 n7 H0 v5 S0 j6 V0 Q7 w0 B7Q A0D a0 A5x U0 A5y d0 A7t W0 A7u c3 B0 F1 C0i B3S B96 q0 k0 b0
"s_mov_b32 s92, 532480\n" f0 i4 R4 A0W A1a v2 X4 R3 P0 i3 I0 B1a F0 A16 G0 A2U A1b N5 A1I m4 Q2 T1 n4 L2 e4 S3 f4 T3 a7 U3 A1e B5U D3d Z0 J1 O0 D1 E0
L1 x0 G4 A3e Y6 D2p A3f A0E w5 A6L k6 o9 O5 S0 A3 A31 H2 Q0 V0 M2 T0 e0 m2 A5n z6 A7v o7 N0 x5 c0 C0 p9 C8S C4 C8T D4 l6 J0 a9 C0 A1Q t0 W0 B9 d0 K0
B9s C9 a0 A1f s0 A1g B1n C0W B3S i0 T1 h0 C8U P3 E7 k4 C8V f0 i1 F7 s2 X0 z1 V6 E2 P0 D2q G2G A4j A54 S4 Z1 A55 B7Z w0 A4Z C8X Z5 C8Y a5 a1 A4a b1 J9
D2U A2R A3r B6T V2 A05 C8Z A4X A06 C8a O0 p7 E0 N9 A9T X9 A0H B4E Y9 R7 H0 R2 B7v S0 h1 B4F B6B W1 V0 q7 e0 y5 B6D A1i b9 A58 A1j A3I M0 A56 A2S u1
A57 A2T a0 A59 D9 U0 A7y d0 L6 W0 M6 s6 A14 Y4 A15 A3l C0U A0 A7M B5S B7I u0 A7N A3K c9 A3M N6 r2 E1 C8b C8c o3 C8d C8e m3 o0 X3 B0 C8f C8g D9W l0 q1
v4 N1 A12 N0 w2 C0 D2N A7X A2Q A6B J1 J0 D1 C0 A3F L1 B1 Q6 A2D A2j K0 m8 C1 A2t m6 A8 Q5 F6F T9 e3 O0 F2 E0 t2 F6G U9 C3 j0 Z6 y0 A0r r7 A2b z5 F6H
F6I n6 r0 C0h c5 T0 D2T B4B U0 A0s a0 g0 s0 "Lp6_10:\n" E8N B0 A0t B3G F6J C8B F6K D3N k0 i0 "s_cbranch_vccnz Lc6_11\n" "Lr6_11:\n" E4k O2 w0 C0T A6M
N2 A1D X0 A1L F3 a6 P0 A6 F6L D5 A6Q X1 A94 I2 F0 G2 G0 Y1 B1Y s3 O3 A6R P1 D0 B1Z n3 N1 F6M B0B A6S w3 D0 z3 B68 v6 D1 A6T J1 A1E B6G C0g V2 B2X s7
A6N V9 C8P n8 F6P A5F E5 O0 A2 R0 F6N F6O B3 A6O C47 F6Q e1 E0 t7 A5G b6 A6P B6 B9A F5 "s_add_u32 m0, s88, 63360\n" D7H T1 A5c F6R C0Z d0 N0 W0 F6T
C0a s6 F6U Y4 F6V A3l D28 B7S D29 i9 C6 F6Y G5 F6W V1 F6X d2 c0 F6Z e2 B7T "s_mov_b32 s92, 565248\n" q1 X4 N1 A8g D9L A1G A18 C3I B0J B5o k8 B3e C0V
B5p B5q c6 E0 D6 B5s A2o H5 H0 b2 S0 n1 V0 o1 e0 n2 F6a D3f F6b A7Q u7 F6c d6 F6d E6 F0j h7 R0 F6e t0 F6f W0 F0k B5I i7 B5V B3X B4j A9k A9t l3 A1M D2v
D3D F6g B3H F6h B9x F6i s1 F6j O3 C0 A8h B0B J0 w3 C0 z3 F6k L1 F6l D1 K0 J1 u0 F6m F6n C0g V2 F6o F6p v7 o0 A0G Z0 F6 F6q A3p K8 O0 M3 F6s F6t o2 E0
f1 F6r D3g H0 w7 S0 e6 V0 G6 e0 A8f A2i u1 F6u a0 F6v U0 F6w d0 F6x W0 E4D A0 B66 F1 B7V G0 f6 j9 H6 I5 D2X A4e B7K A0q A18 F6y F6z B0J F60 l8 B3e A7U
F61 F62 Z9 E0 I6 F63 r5 J5 H0 z2 F64 F65 p2 S0 y1 F66 K9 V3 V0 s5 F67 F68 x7 e0 g6 F69 L9 J6 F7A K5 R0 F7B t0 F7C W0 F7D d0 F7E U0 F7F i7 L8 x4 A8Q B0
B7N "s_cbranch_vccnz Lc6_12\n" "Lr6_12:\n" T2 E1 h0 D2 l0 h2 Q0 f0 i2 f9 l7 A4 t5 N0 h6 C0 P0 L5 I0 X2 F0 U2 J0 k2 D23 G0 j1 F7M u3 O3 F7N r3 P1 K0 N1
B7 F7O q1 F7P S1 w3 j0 z3 A7w F7L L1 F7K D1 B1 J1 F7J F7I J2 r0 V2 T0 F7H F7G m7 Z0 u5 O0 i6 F0n A01 M5 g0 K3 B0 F0l F0m f2 E0 t1 F0o C0N k0 n7 B9A v5
T0 S0 j6 V0 D2m B7Q F0p D3Q E0A U0 "s_mov_b32 s92, 614400\n" F0q D3S d0 F0r W0 F0s c3 X4 p3 x4 Y0 C0Y D3R A0W A1a B7W "Lp6_11:\n" E8N F0x N5 E4n B3Y
E4o L2 F0y B3W B5U O6 A18 C0 F0t F0u B0J F0v Y6 G4 J0 A1J C0 F0w E4l w5 O0 k6 E4m O5 E0 A3 E4p A0E H2 K0 M2 u0 H0 m2 E4q F0z z6 S0 o7 F00 A7R x5 o0 l6
V0 a9 c0 B1D E4r A7S W0 E4s d0 E4t U0 E4u i7 B1n g7 C0W I7 A0Q f0 i1 C8y V4 D2a D2b G0 A0b E9 B3I F08 Z1 F09 w1 F07 a1 F1A b1 J9 F06 L1 F05 D1 F04 A3r
F03 F02 k9 p7 F01 A06 B92 F1B F1E D3e R2 F1C F1D q2 H0 h1 D2c S0 q7 V0 y5 e0 b9 A1K C0D F1F u1 F1G a0 F1H U0 F1I B3d C0U F1 B5S B7I c9 A0X r2 E1 B0
E1K l0 g3 v4 B1g q1 N0 N1 C0 A8g w2 J0 x3 O6 A5L J2 E4N E4O A7P F7Q F7R L1 K0 G4 C1 A3u A3t A2j A8 m8 E42 T9 m6 O0 Q5 E43 E44 e3 E0 F2 E45 U9 t2 j0 C3
y0 E46 E47 Z6 A2b r7 E48 E49 z5 r0 n6 T0 S0 C0h V0 A35 E5C O8 d0 E5D P8 U0 g0 a0 B0 E5B E5E s0 C0X v0 k0 b0 Q0 D4w B5T T0 D4x C0O F7S A2d N0 i0 D9c
F7T "s_cbranch_vccnz Lc6_13\n" "Lr6_13:\n" O2 M0 p0 D3 F7U C5W a2 A6j N2 G2H F7V C5X A1L h0 a6 f0 A6 E5J E5K D5 A6o X1 D0 E5H E5I I2 X0 G2 F3 Y1 A6p
s7 B4e V9 I0 n8 E5N s3 E5 A6q A2 D0 E5L E5M B3 F0 d1 E5O C5Y e1 A6r t7 G2I G0 b6 E5F n3 B6 E1w F5 M0 E5R E5S G1 A6k K1 D0 E5T E5U z0 A6X C5Z I1 E5G
C51 H1 A6m S2 D0 E5P C52 U1 E5Q C53 B1A A6n B8N O0 K2 G7 "Lb6_3:\n" M7 E0 A5R A7Y x2 A6L y2 S0 l2 V0 A2m e0 W9 A19 C6 E5h C54 G5 A2g x0 E5f V1 w0
"s_add_u32 m0, s88, 76160\n" E5Z d2 J0 e2 C0 E5a E5b D9K E5c E5d A17 K0 c6 u0 E5e E5g c2 D6 E5i H5 o0 b2 E5j n1 E5k o1 E5l n2 E5m u7 E5V d6 E5W E6 F7W
h7 A1 E5n t0 E5o W0 F7X d0 E5X U0 E5Y a0 E4v s0 E4w v0 E5p b0 E5q q0 E5r P5 E5s A0B Q3 E5t q4 A9k B2 E5u C2 X0 A1M E4x d5 A4Y P0 A0L F7Y A1h M0 I0 l1
E5v E5w k1 F0 x1 F7Z K4 s1 G0 v7 B0 E4y Y3 A0G E4z F6 F7a K8 E5x M3 l0 o2 Q0 E5y F7b f1 g5 W2 E40 w7 N0 e6 C0 E41 E5A G6 A0a E5z E50 G1 J0 K1 C0 F7c
F7d z0 A1B A0 "Lp6_12:\n" E8O H1 K0 A1R B4I D4T p1 "ds_read_b64_tr_b16 a[184:185], v219 offset:50688\n"
"ds_read_b64_tr_b16 a[186:187], v219 offset:51200\n" B8N "ds_read_b64_tr_b16 a[188:189], v219 offset:50720\n" j4 B8D v3 F1
"ds_read_b64_tr_b16 a[190:191], v219 offset:51232\n" E3 E0 Z3 q6 W3 H0 A2n S0 f6 V0 H6 e0 I5 A0p m1 d3 x0 T4 C1 F7e F7f F4 B0 t3 F7g F7h B1e j0 Z9 y0
E97 E98 Z2 B0 I6 F7i F7j J5 g1 z2 r0 p2 T0 E53 E54 y1 j7 V3 F7k F7l x7 g0 g6 B0 E55 E56 A6V L3 C5c J6 p1
"ds_read_b64_tr_b16 a[160:161], v219 offset:33792\n" "ds_read_b64_tr_b16 a[162:163], v219 offset:34304\n" K5 g1 C5g C5h t0 "s_cbranch_vccnz Lc6_14\n"
"Lr6_14:\n" F7m W0 k0 D2e F7n "ds_read_b64_tr_b16 a[164:165], v219 offset:33824\n" D2f E0C a0 "ds_read_b64_tr_b16 a[166:167], v219 offset:34336\n"
"ds_read_b64_tr_b16 a[144:145], v219 offset:33856\n" D2I A6W "ds_read_b64_tr_b16 a[146:147], v219 offset:34368\n"
"ds_read_b64_tr_b16 a[148:149], v219 offset:33888\n" v0 A6e C5i b0 "ds_read_b64_tr_b16 a[150:151], v219 offset:34400\n" E57 C7L F7o E58 C9G F7p F7q
C5d C5e T2 g1 p0 C5f C7 D2 F7r F7s D7 o6 h2 f0 p6 T7 i2 F7t F7u U7 C56 A6Y I0 C57 C58 X2 E51 C59 V5 U2 F0 B6O k2 F7v E6E B8T j1 G0 o5 A6a E52 A9z P0
f2 E6F E6I A6b A4 t1 "ds_read_b64_tr_b16 a[168:169], v219 offset:50752\n" "ds_read_b64_tr_b16 a[170:171], v219 offset:51264\n" P2 T5 G1 E6J E6G K1 C5j
V7 z0 E6K E6L I1 W7 e8 H1 E6M E6N c4 f8 X7 A6c "ds_read_b64_tr_b16 a[172:173], v219 offset:50784\n"
"ds_read_b64_tr_b16 a[174:175], v219 offset:51296\n" U1 Q4 u2 E6O Y7 R1 Z0 O1 A0M E6D c1 x0 K2 C0 E6P E4 A6f A1t N4 J0 C6A C6B v2 C0 E6Q D9H C6J C6K
R3 E0 C6L C6M i3 K0 A0h L2 u0 D9I U5 d9 S3 H0 A0g A6g E6H C6D C6E T3 o0 A2Y U3 B0 E6R E6S A3P Q2 S0 A1m A6u E59 C6F C6G A3 c0 V0 C6H C6I H2 l0 A9m M2
Q0 E6T B5 B2f m2 B0 e0 A6Z t0 E6A A8w d0 E6B E6C B4A U6 C6N a0 E6V E6W s0 C6R C6S C6f C6g a3 E4b E6a E6b C6h C6i A6w h0 A2Y v0 R0 E6c E6d A3P b0 C6O
A1m q0 G2J C6T V7 m0 c0 f0 W7 e8 Y0 E6e D9Y f8 X7 i0 C6P Y7 C6U A6V E6U C6V C6W i1 X0 C6X M8 s2 D9Z D9a N8 G9 z1 D9b V6 H9 Q8 E2 M0 p0 R8 C6o Z1 E6X
C6p C6q w1 P0 C6r V5 A6x E6Y E6Z B6O a1 F7w F7x B8T b1 R0 F7y k4 o5 C6s A6W e9 I0 C6t C6u R2 G2K C6v C7 q2 R0 F7z F70 D7 o6 h1 F71 S4 C6j C6k W1 C6Z
C6l C6m A6y M0 F1J F1K A1n G1 C6a A0I K1 F1L F1M A1o z0 M0 F0 C61 C62 I1 G2L C63 C64 H1 G0 A0h c4 F1R F1S d9 A6b B4 A0g C67 U1 G2M C68 C69 u2 p1 B1
C7A V7 B80 W7 e8 B7q F1N F1O f8 X7 c1 J0 Y7 C7B K2 C0 G2N C7C C7D A6c c0 Z0 C7E p6 j3 K0 A1m g2 C1 G2O A0h I3 B0 F1P F1Q d9 f3 F1T F1U A0g o3 A1t C6w
C6x m3 G2P C6y C6z X3 A8k O4 E0 A9m g3 M0 A8l h4 B2f r2 A3y A6Z e3 M0 A3v A2k F2 A2l C7P t2 j0 A24 "ds_read_b128 a[64:67], v221 offset:50688\n"
"ds_read_b128 a[68:71], v221 offset:51840\n" B8W A2q R0 G2Q A0i A7A M0 "ds_read_b128 a[16:19], v221 offset:33792\n"
"ds_read_b128 a[20:23], v221 offset:34944\n" I9 B2S y0 B4W B0 A7f A92 b0 r0 q0 T0 A1W m0 A7g B0U g0 A0 "Lp6_13:\n" E8O w2 B0 A4B B2p s8 B8O B3M A2J
B3L k0 A4p "s_cbranch_vccnz Lc6_15\n" "Lr6_15:\n" u6 O2 B4w t8 o4 a2 C65 N2 A5d A4F X1 f0 I2 A4E G2 A60 Y1 R0 C7K A2 G2H X0 B3 A20 d1 A64 e1 D0 D4y G1
P0 K1 A4G z0 A65 I1 D0 p4 C7Q B4g B72 F0 U1 d7 C35 A4H O1 A66 A6l v9 B3T A5R A8J x2 A67 y2 G2I A5 s3 l2 Z0 d2 R0 y4 M4 e2 A61 G3 B07 H3 H4 n3 b2 z4 D8
n1 A62 V1 D0 A2I o1 H0 n2 A6U t0 A63 A2Z B60 d0 A7A A2e A3n a4 A1C C66 b4 v0 Q0 C7M b0 "s_add_u32 m0, s88, 80256\n" p0 q0 N0 m0 A9l A21 A0Q A1X c2 A6h
Q3 J0 b3 e7 q4 B2 P0 C2 A4J l1 I0 k1 A4I x1 F0 s1 C8r M3 A1 G0 o2 A22 f1 X0 W2 D41 G1 B2b K1 A23 z0 Y3 C7e I1 A69 C7f H1 A1 A68 C7F c4 C7G C7N U1 A1I
x0 C7O u2 W8 R1 O0 O1 B7C B7B C5l A0T q6 v3 Q4 E3 R0 A0U A8i Z3 H0 W3 B4N d3 S0 T4 Z0 F4 u8 j4 t3 C0 V0 m1 S9 R5 z2 e0 p2 B6Z W5 y1 K0 V3 c0 A2c A0x
t0 e9 A8j A4f W0 A0y i5 d0 C7H U0 C8K a0 A4g A3D s0 j5 A3E Z2 C7I g4 A25 A4b b0 u0 X0 q0 f7 j7 m0 A8K A4r A5M "s_cbranch_vccnz Lc6_16\n" "Lr6_16:\n"
T2 c0 B6m D3L l0 i2 P3 "ds_read_b64_tr_b16 a[176:177], v219 offset:50688\n" "ds_read_b64_tr_b16 a[178:179], v219 offset:51200\n" X2 Q0 f0 U2 B0
"ds_read_b64_tr_b16 a[180:181], v219 offset:50720\n" "ds_read_b64_tr_b16 a[182:183], v219 offset:51232\n" k2 N0 j1 C0
"ds_read_b64_tr_b16 a[184:185], v219 offset:50752\n" "ds_read_b64_tr_b16 a[186:187], v219 offset:51264\n" K3 P0 f2
"ds_read_b64_tr_b16 a[188:189], v219 offset:50784\n" "ds_read_b64_tr_b16 a[190:191], v219 offset:51296\n" t1 A4t m5 P2 A1 A34 J0 K1 C0 A7k z0 F0 I1 p1
"ds_read_b64_tr_b16 a[152:153], v219 offset:33792\n" "ds_read_b64_tr_b16 a[154:155], v219 offset:34304\n" H1 M0 D5j K0 A6f C1
"ds_read_b64_tr_b16 a[156:157], v219 offset:33824\n" "ds_read_b64_tr_b16 a[158:159], v219 offset:34336\n" U1 B0 A5Y u2 B1 R1 j0 O1 y0
"ds_read_b64_tr_b16 a[160:161], v219 offset:33856\n" "ds_read_b64_tr_b16 a[162:163], v219 offset:34368\n" c1 F4E A0V B6N A1Y A6Y g1 Z0 A6a r0 A6g T0
"ds_read_b64_tr_b16 a[164:165], v219 offset:33888\n" "ds_read_b64_tr_b16 a[166:167], v219 offset:34400\n" A2Y N4 g1 O0 A3P v2 g0 A1m M0 A6u K7 G2J C7S
C7T R3 A9C C7U C7V i3 A5Z A9m L2 B2m B2f S3 k0 A1n T3 Q0 n5 E4 A0I U3 w0 U5 u3 A1o A6x T0 G2L A6Z A6w N0 C7W C7X Q2 C0 G2K C7Y C7Z A3 w0 H0 A2Y H2 A0M
k5 r3 A3P M2 x0 B0W G2R G2S G2T A1m m2 G2U C7g C7h C7i R6 G2O B9u L0 A8A e0 C7j M8 B1V l5 B5 N8 G9 l9 W6 U6 H9 Q8 U0 K0 R8 C7a a0 w0 G2M C7b C7c s0 u0
C7m C7n C7d V5 A6y o0 B6O a3 B0 B8i B8T v0 f0 o5 b0 l0 D2i G2Q C8Q B0u Y0 D5Q C7o i0 A3U i1 M0 "ds_read_b128 a[32:35], v221 offset:33792\n"
"ds_read_b128 a[36:39], v221 offset:34944\n" s2 F3t F3u z1 F3v F3w E2 g1 p0 Z1 L0 G2V G2W w1 N0 a1 C0 G2X G2Y B2O h0 R2 M0 B7D C7p q2 J0 A1n h1 C0 G2Z
G2a A0I W1 B4R A1o G1 G2N C7q A0 "Lp6_14:\n" E8O C7r B6W I0 C7s C7t z0 G2b G2c A0h I1 M0 F0 d9 H1 K0 A0g S2 C1 G2P U1 A0M G0 A1y B0 R1
"ds_read_b128 a[80:83], v221 offset:50688\n" "ds_read_b128 a[84:87], v221 offset:51840\n" O1 G2d G2e c1 G2f G2g K2 A1 B1 j3 j0 g2 y0 G2h G2i I3 B0 G2j
G2k f3 U8 o3 M0 D5O k4 m3 r0 X3 x8 X5 g3 c0 V6 O4 h4 D5P e3 w0 E6n E6o F2 E6p E6q r2 A6Q C8l G2l S4 C3 L0 A3Y R0 G2m B4 A5b A6v M0 E17 S5 B4A A2e c0
A1C E18 w4 v0 A6R A4P G2n G2o B6j E19 E2A Y0 A6S C8m G2p G2q w2 R4 E2B E2C F1s A6T A4p F1t A2J F1u F1v E0J E0K "s_cbranch_vccnz Lc6_17\n" "Lr6_17:\n"
B2H O2 C7k D3 p0 a2 E6h E6i N2 C7l X1 "s_add_u32 m0, s88, 46592\n" G2r G2s I2 h0 G2 g0 Y1 M0 E6j E6k A4c A2 A0N P0 B3 B5H d1 A6M e1 D0 E6l E6m G1 B6a
B7E z0 A6N I1 D0 F3 C4I A0 G0 U1 A6O u2 D0 Z4 R1 d7 A7B E0B A9u K2 w0 Z0 A5R A9O M4 x2 A6P y2 E94 A9P A2L l2 O0 d2 A2M E2D e2 k0 G3 e9 E0 H3 E2E C5x
H0 n1 y8 z8 V1 C7u o1 E2F E2G B3v c5 O6 "ds_read_b64_tr_b16 a[48:49], v219 offset:50688\n" "ds_read_b64_tr_b16 a[50:51], v219 offset:51200\n" W0 C7w
D2e "ds_read_b64_tr_b16 a[52:53], v219 offset:50720\n" "ds_read_b64_tr_b16 a[54:55], v219 offset:51232\n" D2f A2e N0 A1C C0
"ds_read_b64_tr_b16 a[56:57], v219 offset:50752\n" "ds_read_b64_tr_b16 a[58:59], v219 offset:51264\n" A19 A0l J0 D82
"ds_read_b64_tr_b16 a[60:61], v219 offset:50784\n" "ds_read_b64_tr_b16 a[62:63], v219 offset:51296\n" m0 B97 A0Q K0 c2 R0 A9p Q3 u0 B3A B0y B2 o0 C2
B0 B8Z l1 A3U k1 C7J x1 l0 B8j B6Y A1V p0 o2 Q0 A5o f1 B0 F0 W2 L0 B0z G1 N0 A9n A7o B8e A1B B00 H1 P0 S2 C0 A1q B36 I0 u2 A8B y6 J0 c1 A9l Z0 B8D C8h
A7m C8i K0 C8j P3 "ds_read_b64_tr_b16 a[0:1], v219 offset:33792\n" "ds_read_b64_tr_b16 a[2:3], v219 offset:34304\n" C8k w0 H0 v3 P7
"ds_read_b64_tr_b16 a[4:5], v219 offset:33824\n" "ds_read_b64_tr_b16 a[6:7], v219 offset:34336\n" E3 p1 l3 Z3 x0 W3
"ds_read_b64_tr_b16 a[8:9], v219 offset:33856\n" "ds_read_b64_tr_b16 a[10:11], v219 offset:34368\n" d3 A6o T4 D0
"ds_read_b64_tr_b16 a[12:13], v219 offset:33888\n" "ds_read_b64_tr_b16 a[14:15], v219 offset:34400\n" F4 A0n j4 t3 A0o B4S A0Z p2 B2l m1 A6p y1 D0 A2a
K4 V3 L0 c5 R0 Z8 Y3 A5b A0k M0 A9 R5 B4A A10 c0 A4W A86 A6q Z2 D0 A8C A8Q m9 A0R A6r m0 C1 n9 A2d "s_add_u32 m0, s88, 97408\n" B2v B4T
"s_cbranch_vccnz Lc6_18\n" "Lr6_18:\n" L0 T2 j0 D2 D9c A8z h2 f0 i2 R0 B2s X2 A3U U2 A5P k2 A6j j1 L0 A1z K3 B4o f2 D0 B4Y t1 A6k B8f B4X G1 B4e A9n
B81 z0 A6m A1B A3c H1 D0 B72 D5R U1 A6n C6Y B6f R1 A1I A7B x0 F8G B6R b7 A3e m5 N4 B6X v2 A3f n5 R3 g0 i3 B0 A8H E4 L2 O0 B2A T3 k0 U3 w0 A8I B8g Q0
F0 H2 T0 A31 Q2 N0 M2 C0 e4 u3 m2 G0 A0F f4 r3 W0 J0 B7o B6k B9s A1F M0 A3b C0 B03 K0 b0 P3 "ds_read_b64_tr_b16 a[64:65], v219 offset:50688\n"
"ds_read_b64_tr_b16 a[66:67], v219 offset:51200\n" q0 G0C A4n m0 A3B "Lp6_15:\n" E8O A3C Y0 o0 i0 B0
"ds_read_b64_tr_b16 a[68:69], v219 offset:50720\n" "ds_read_b64_tr_b16 a[70:71], v219 offset:51232\n" a3 g1 p0 i1
"ds_read_b64_tr_b16 a[72:73], v219 offset:50752\n" "ds_read_b64_tr_b16 a[74:75], v219 offset:51264\n" s2 g1 A83 C8n A2B z1 l0 E2 L0
"ds_read_b64_tr_b16 a[76:77], v219 offset:50784\n" "ds_read_b64_tr_b16 a[78:79], v219 offset:51296\n" C8o Q0 I0 Z1 g5 A7d w1 A4o C8p A84 a1 N0 b1 M0
A32 C8q D2k R2 A2r q2 J0 h1 A33 W1 B2d G1 B1E B8A f0 z0 O7 q9 I1 A37 H1 B6U S2 A64 U1 P3 "ds_read_b64_tr_b16 a[16:17], v219 offset:33792\n"
"ds_read_b64_tr_b16 a[18:19], v219 offset:34304\n" u2 B4e R1 "ds_read_b64_tr_b16 a[20:21], v219 offset:33824\n"
"ds_read_b64_tr_b16 a[22:23], v219 offset:34336\n" O1 A65 A6l "ds_read_b64_tr_b16 a[24:25], v219 offset:33856\n"
"ds_read_b64_tr_b16 a[26:27], v219 offset:34368\n" B6N B1 A9h "ds_read_b64_tr_b16 a[28:29], v219 offset:33888\n"
"ds_read_b64_tr_b16 a[30:31], v219 offset:34400\n" g2 A66 I3 D0 A2s B8l O0 m3 p1 E0 X3 x0 B4h g3 R0 B4i O4 h4 V4 e3 w0 B82
"ds_read_b128 a[196:199], v215 offset:29632\n" B83 F2 "ds_read_b128 a[192:195], v215 offset:29568\n" C9H r2 A67 t2 E1y
"ds_read_b64_tr_b16 a[216:217], v217 offset:33792\n" C9I C3 H0 C9J C9K B9u R0 c3 K0 W0 w0 "ds_read_b64_tr_b16 a[218:219], v217 offset:34304\n" S4 d0
A5d A5c "ds_read_b64_tr_b16 a[220:221], v217 offset:38016\n" B4 a0 w0 B0w s0 "ds_read_b64_tr_b16 a[222:223], v217 offset:38528\n"
"ds_read_b64_tr_b16 a[224:225], v217 offset:33824\n" w2 A60 v0 D0 "ds_read_b64_tr_b16 a[226:227], v217 offset:34336\n"
"ds_read_b64_tr_b16 a[228:229], v217 offset:38048\n" A7l "ds_read_b64_tr_b16 a[230:231], v217 offset:38560\n" A73 m0 A61 Y0 D0
"ds_read_b64_tr_b16 a[232:233], v217 offset:33856\n" L3 A0B "ds_read_b64_tr_b16 a[234:235], v217 offset:34368\n" "s_cbranch_vccnz Lc6_19\n"
"Lr6_19:\n" B4V "ds_read_b64_tr_b16 a[236:237], v217 offset:38080\n" "ds_read_b64_tr_b16 a[238:239], v217 offset:38592\n" D3 A62 a2 D0
"ds_read_b128 a[144:147], v215 offset:30080\n" "ds_read_b128 a[148:151], v215 offset:30144\n" N2 A6h X1 E0m E0n I2 A63 G2 C1 E0o E0p Y1 g1 C9L C9M A2
L0 "ds_read_b64_tr_b16 a[168:169], v217 offset:33888\n" C9N B3 E26 "ds_read_b64_tr_b16 a[170:171], v217 offset:34400\n" C9O d1 j0 C6Q
"ds_read_b64_tr_b16 a[172:173], v217 offset:38112\n" "ds_read_b64_tr_b16 a[174:175], v217 offset:38624\n" A96 y0 X0 l4 L0
"ds_read_b64_tr_b16 a[176:177], v217 offset:42240\n" "ds_read_b64_tr_b16 a[178:179], v217 offset:42752\n" r4 B4U s4 L0 F4T F4U e5 A1I I0 g9 A5m A11 r0
A2h T0 "ds_read_b64_tr_b16 a[184:185], v217 offset:42272\n" "ds_read_b64_tr_b16 a[186:187], v217 offset:42784\n" g8 B1 f5 F4V F4W k7 g0 B6z F3l o4 B1F
A8 B1G F3m p4 B1H k0 B1I Q0 F3n M4 B1J T0 O0 B1K E0 B1L N0 B1M "s_waitcnt vmcnt(10) lgkmcnt(2)\n" "ds_read_b64_tr_b16 a[80:81], v219 offset:50688\n"
"ds_read_b64_tr_b16 a[82:83], v219 offset:51200\n" H4 B1N C0 F3o F3p B1O F3q C9P V1 J0 B1P C0 "ds_read_b64_tr_b16 a[84:85], v219 offset:50720\n"
"ds_read_b64_tr_b16 a[86:87], v219 offset:51232\n" B1Q R0 C9Q C9R C9S C8u "ds_read_b64_tr_b16 a[88:89], v219 offset:50752\n"
"ds_read_b64_tr_b16 a[90:91], v219 offset:51264\n" c3 K0 W0 u0 "ds_read_b64_tr_b16 a[92:93], v219 offset:50784\n"
"ds_read_b64_tr_b16 a[94:95], v219 offset:51296\n" d0 A1 F0 U0 G0 a0 L0 B8X M0 Z4 A19 C8v w0 "ds_read_b64_tr_b16 a[216:217], v217 offset:42304\n"
"ds_read_b64_tr_b16 a[218:219], v217 offset:42816\n" C8w w0 X0 C8x o0 c2 B0 F4X F4Y v0 p0 b0 "ds_read_b64_tr_b16 a[224:225], v217 offset:42336\n"
"ds_read_b64_tr_b16 a[226:227], v217 offset:42848\n" q0 l0 m0 C1 F4Z F4a Y0 D8y B2v L0 D9Y D9Z B57 B3A j0 B2 y0 D9a D9b C2 L0 C9U C9V l1 M0
"ds_read_b128 a[144:147], v215 offset:59648\n" "ds_read_b128 a[148:151], v215 offset:59712\n" k1 B0 C9W C9X x1 r0 s1
"s_waitcnt vmcnt(11) lgkmcnt(4)\n" "ds_read_b64_tr_b16 a[32:33], v219 offset:33792\n" "ds_read_b64_tr_b16 a[34:35], v219 offset:34304\n"
"ds_read_b128 a[152:155], v215 offset:55424\n" M3 R0 "ds_read_b128 a[156:159], v215 offset:55488\n" "ds_read_b128 a[160:163], v215 offset:51200\n" o2
T0 "ds_read_b128 a[164:167], v215 offset:51264\n" B9B g0 W2 A1 "ds_read_b64_tr_b16 a[36:37], v219 offset:33824\n"
"ds_read_b64_tr_b16 a[38:39], v219 offset:34336\n" A96 g1 B9C R0 "ds_read_b64_tr_b16 a[40:41], v219 offset:33856\n"
"ds_read_b64_tr_b16 a[42:43], v219 offset:34368\n" r4 A9i s4 R0 "ds_read_b64_tr_b16 a[44:45], v219 offset:33888\n"
"ds_read_b64_tr_b16 a[46:47], v219 offset:34400\n" e5 L0 F27 F28 g9 B0 F29 F3A A11 k0 A2h T0 F3B F3C g8 O6 Z0 f5 x0 k7 o0 F3D F3E h8 L0 d5 Y3 A8L M0
F3F F3G A8M B99 A7D l0 A7E M0 F3H F3I A8N M0 e0 D7E B1 D22 A8k j4 X5 A7F c0 A8l q6 A8O w0 t9 A8P B0P C5S A7G D9E W5 A7H S0 C9Y C9Z C9a C9b R5 D9D L0
A9b D9C D9B A9a L0 C9c C9d A9Z D9A D89 A9Y B4R A9X A7f I0 C9m K4 C9g C9e Z2 C9f A92 L0 C9p A1W C9q j7 C9h C9r A74 C9s L0 A0e C9i C9t X0 C9u
"s_cbranch_vccnz Lc6_20\n" "Lr6_20:\n" A1l A1 C4B C9v D2 C8A h2 r8 A6X i2 A9i X2 M0 C82 C83 U2 A0 C9w A0 C84 C85 j1 B4o A9z C86 C87 f2 A0z C9y A47 C88
C89 P2 x0 C9z M0 C9A B9U D7F A1H A0j C91 A4v A0 A0 A0 B9Y B9V A1b A4w C9B C90 C93 C9C C9D C95 A0 C94 C96 C97 M0 A2V C98 A4x D0A A0 B9Z B9W A0 A2W C99
B9X B0q C9j D0B R0 E1x C9k A4y R0 A7h C9l D0F w9 D0E A4z B9c A40 L0 D6Z D0C D0D D0G D0H A9g L0 D6Y D0J A9f L0 D6X D0K A9e L0 D6W D0O A9d L0 D6V C9n
A9c L0 D6U C9o D0N A0 y4 D0P D0I L0 z4 D0S D0Q L0 A5 D8 A2h A1 D0L D0M g8 L0 B9d "ds_read_b128 v[0:3], v221 offset:33792\n"
"ds_read_b128 v[4:7], v221 offset:34944\n" f5 g1 A2I D0R k7 u6 A6U h8 A7m D0V u6 B60 i1 u6 D0T D0W s2 A1 D0X D0Z G0v A8S D0U z1 B8s E2 R0 D0a D0b Z1
A8T D0Y D0c D0d a1 B9E B9F b1 R0 B9G B9H R2 B4O D0e B9I D0f D0g h1 x0 W1 Z0 B9J D0h M0 B9K B9a A41 c0 B9L D0k O0 D0i H0 A8U D21 D0j B9b B0w D0l D0m
B9M B9N D0n B9O D0o c0 B9P D0p A8V "ds_read_b128 v[16:19], v221 offset:50688\n" "ds_read_b128 v[20:23], v221 offset:51840\n" D0q G2t G2u A42 G2v G2w
D0r u6 D0s A43 A1 G2x G2y A44 A1 G2z D0t D0u D0v B9Y G20 D0w D0x A45 A0 G21 G22 A46 "s_cbranch_vccnz Lc6_21\n" "Lr6_21:\n" G23 G24 D0y A1 D0z D00 D01
G25 G26 B9Z D02 A1 G27 G28 D03 D04 D05 "ds_read_b64_tr_b16 a[80:81], v216 offset:59136\n" "ds_read_b64_tr_b16 a[82:83], v216 offset:59648\n"
"ds_read_b64_tr_b16 a[84:85], v217 offset:0\n" "ds_read_b64_tr_b16 a[86:87], v217 offset:512\n" D06 "ds_read_b64_tr_b16 a[92:93], v217 offset:32\n"
"ds_read_b64_tr_b16 a[94:95], v217 offset:544\n" "ds_read_b64_tr_b16 a[88:89], v216 offset:59168\n" "ds_read_b64_tr_b16 a[90:91], v216 offset:59680\n"
D07 B8E D08 D09 A0 F8H A6e D1A B9e F8I D1B B9c D1C "ds_read_b64_tr_b16 a[216:217], v216 offset:59200\n"
"ds_read_b64_tr_b16 a[218:219], v216 offset:59712\n" D1D D1E D1F D1G R0 "ds_read_b64_tr_b16 a[224:225], v216 offset:59232\n"
"ds_read_b64_tr_b16 a[226:227], v216 offset:59744\n" D1H F8J F8K D1I D1J A83 B9d D1K D1L D1M A0 A1f D1N "ds_read_b128 v[32:35], v221 offset:33792\n"
D1O "ds_read_b128 v[36:39], v221 offset:34944\n" D1P A1g D1Q D1R D1S D1T D1U E0a E0b D1V B9f C2 R0 E0c D1W E0d B2 A0 E0e D1X D1Y E0f B9g s1 B9h D1Z
E0g E0h D1a E0i D1b E0j A08 B9i D1c E0k E0l D1d A8X D1e "ds_read_b128 v[52:55], v221 offset:51840\n" D1f "ds_read_b128 v[48:51], v221 offset:50688\n"
D1g A8Y D1h A8Z D1i A8a "ds_read_b128 v[64:67], v221 offset:33792\n" D1j B9f D1k "ds_read_b128 v[68:71], v221 offset:34944\n" B9g D1l B9h D1m B9i
"ds_read_b128 v[80:83], v221 offset:50688\n" "ds_read_b128 v[84:87], v221 offset:51840\n" D1n "Lend6:\n" "Lc6_0:\n" B61 A3i B1S D9P A49 B1S F3x
"s_branch Lr6_0\n" "Lc6_1:\n" B62 A3i A8s "s_branch Lr6_1\n" "Lc6_2:\n" v1 D1q A97 D1o B1R B1S F3y B1T B1o B3D B1p "s_branch Lr6_2\n" "Lc6_3:\n" r1
D1r A8b W4 A97 p5 A98 B1q B1T A8s "s_branch Lr6_3\n" "Lc6_4:\n" v1 D1s A7I N3 A8b r6 A8c B1o A98 B1p "s_branch Lr6_4\n" "Lc6_5:\n" r1 D1t A48 W4 A7I
p5 A7J B1q A8c A8s "s_branch Lr6_5\n" "Lc6_6:\n" v1 D1u B4x N3 A48 r6 A5A B1o A7J B1p "s_branch Lr6_6\n" "Lc6_7:\n" r1 D1v D14 p5 B5X B1q A5A A8s
"s_branch Lr6_7\n" "Lc6_8:\n" v1 D1w B1T N3 B3D r6 B3Z B1o B5X B1p "s_branch Lr6_8\n" "Lc6_9:\n" r1 D1y D15 p5 B1k B1q B3Z A8s "s_branch Lr6_9\n"
"Lc6_10:\n" v1 D1z A8c N3 A98 r6 B0H B1o B1k B1p "s_branch Lr6_10\n" "Lc6_11:\n" r1 D10 D16 p5 A8o B1q B0H A8s "s_branch Lr6_11\n" "Lc6_12:\n" v1 D11
A5A N3 A7J r6 A7O B1o A8o B1p "s_branch Lr6_12\n" "Lc6_13:\n" r1 D12 D17 p5 A49 B1q A7O A8s "s_branch Lr6_13\n" "Lc6_14:\n" B61 D18 d4
"s_branch Lr6_14\n" "Lc6_15:\n" B62 D19 L4 "s_branch Lr6_15\n" "Lc6_16:\n" B9j D2A "s_branch Lr6_16\n" "Lc6_17:\n" B9k D2B "s_branch Lr6_17\n"
"Lc6_18:\n" D1p D2C "s_branch Lr6_18\n" "Lc6_19:\n" D13 D2D "s_branch Lr6_19\n" "Lc6_20:\n" v1 D6o D2E "s_branch Lr6_20\n" "Lc6_21:\n" r1 D6p D2F
"s_branch Lr6_21\n" ".fill (Lv0+688128-.)/4, 4, 0xBF800000\n" "Lv7:\n" "Lb7_0:\n" C0m C0n C0o C0p C0q C0r F2U A0N C0s C0t g7 G7 "Lb7_1:\n" M7
"s_sleep 4\n" B7b C0u "s_mov_b32 s54, Lend7-Lv7\n" C0v C0w C0x B7c C0y C0z C00 C01 C02 B5Z C03 C04 B7d C05 C06 C07 C08 C1D C1A C1B C1C p8 C1K C1E C1F
C1G C09 C1H C1I C1c B7e p8 C1J C1i C1L C1M C1N C1O C1T C1P A5I C1Q C1l C1R C1S p8 C1Z C1U C1m C1V C2A C1W D6q C1e "s_mov_b32 s92, 8192\n" C1f C1X C1Y
D6r D6s C1a C1b C1d C1g C1h C1j A5J "s_addk_i32 s92, -1024\n" C1q C1k C1n C1o C1p C1r C1s C1t A5J C1u C1v C1w C1x C1y C1z C10 C11 C15 C12 C13 C14 C16
C17 C18 C19 B7f C2C C2B D6t D6u D6v D2G C2D P3 G0H F2U B7h G0I G0J B5a G0K B5b c0 D9d D9e D9f D9g D9h D9i D9j D9k F3J B5c F3K C0c B3a G0L G0M G0N G0O
E1 D9l D9m A5K D9n D9o D9p D9q D9r D9s B7a E8P P3 G0P C2J F3z S8 B3a G0Q G0R G0S G0T D9t D9u D9v D9w D9x D9y D9z R6 D90 E1 D9d D9e D9f D9g D9h D9i D9j
D9k D3V D3W C2O D3X B7a B3b C0d B3c E8P N0 F3z G0U B3a G0V G0W G0X G0Y G0Z G0a G0b G0c D91 D92 D93 D94 D95 D96 D97 D98 D3V D3W D3X G0d T1 D9l D9m D9n
D9o D9p D9q D9r D9s B7a B3b C0d B3c C0c B3a G0e G0f G0g G0h G0i G0j G0k G0l E1 D9t D9u D9v D9w D9x D9y D9z D90 D3V D3W D3X G0m B3b C0d B3c G0n G0o G0p
G0q C2d D91 D92 D93 D94 D95 D96 D97 D98 D3V D3W D3X B3b E8Q G0u E8R G0t G0s B3c G0r C2i A5L A3t A3u R6 A3v A3y A3x A3w C2j C2k C2m C2l K0 C2n A7W j0
A7X G0w G0x C2o B0 G0y G0z B3i A0 g0 c0 D9d D9e D9f D9g D9h D9i D9j D9k B7a C0c B3a G00 G01 G02 G03 E1 D9l D9m D9n D9o D9p D9q D9r D9s B7a
"v_mfma_f32_16x16x32_bf16 v[160:163], a[192:195], a[144:147], 0\n" B0 D9t D9u D3Y k0 D3Z Q0 D9x D9y D3a T0 E7y E7z D3b D9v D9w E8P G29 G05 G06 G3A B3a
G07 G04 S8 E2M A0 E7w E7x G08 B3b E7u A53 A0 B5h E7v A52 J0 B5i J3 D9z D90 E7s E7t A51 K6 E8Q h3 G1C E8R G1B D96 D91 D92 D94 D95 E7x B3c G1A E7y E7z
D93 E7u E7v E7w G09 D97 D98 E7s E7t A50 J3 B3b E8R A0O E8Q B3c G1D G1E G1F G1G h3 C4R A5z C0 C4S C4T C4U E70 G1J G3B G1K B5l E71 B5n G1H E72 K0 J3 E73
u0 K6 C3A h3 E74 J3 C3B E75 F1 B3m C4Z C4W C4X C4Y A5M E76 P3 p0 E77 x0 C3I B5o E78 B5p B5q E79 B5s G1M E8A A0 "Lp7_1:\n" F30 l0 E8C Q0 A4u B0 E8D E8E
R0 F31 M0 C3K G1N D3b M0 B1 D3a w0 C3L E8B D3Z N0 D3Y w0 C3M G1L D7L B8I D9Q R6 F3N F3M D99 C3N C3O A53 O0 A52 K0 A51 C1 E0 A50 B0 C3P k8 A5z A6L A2o
c0 G3B j0 E71 y0 S0 E72 T1 V0 E73 O6 e0 E74 E1 A3k E75 x0 A7W F3O r0 c8 T0 A3X E76 A5O E77 D2H E78 A29 E79 T8 E8A g0 E8C f9 B3n f0 F3P F3Q c0 F31 k0
D3b Q0 A8y D3a c0 I0 D3Z T0 A7Z D3Y A3h B38 X5 D7L A3z l8 D21 P0 B1U r5 S8 s5 F3S F3R D99 B0j A53 q8 m9 A52 J0 A51 C0 C3R A50 n9 C3S A5z C3T L8 A0 H7
C3U C4Q J7 A8z F3T L3 B2s A5P "s_cbranch_vccnz Lc7_0\n" "Lr7_0:\n" u6 E1Y o0 t5 B0 A1z E1Z p0 L5 B4Y E1a u6 h0 u5 B4X E1b l0 M5 Q0 B81 E1c B06 v5 g5
A3c E1d N0 Q7 C0 D5R t4 X0 S1 B6f q1 J0 N1 L0 G3C G3D P1 C0 P0 U4 S7 I0 x3 A5m J2 A1P J1 K0 D1 C1 o9 L1 Q9 Q6 A31 E1e j0 A16 y0 Z0 E1f B0 a7 Y6 N5 O0
E1g R0 e4 "v_cvt_pk_bf16_f32 v152, v164, v165\n" w5 E0 E1h f4 A0E O5 r0 "v_mfma_f32_16x16x32_bf16 v[196:199], a[248:251], v[144:147], v[192:195]\n" T0
B7t E1i F0 x5 p9 A3B E1j g0 a9 B0 A3C B9 F31 G0 D3b L0 C9 "v_cvt_pk_bf16_f32 v153, v166, v167\n" D3a H0 D3Z S0 D3Y B2M D7L A5n D99 A2B A53 k0 A52 Q0
A7d A51 B2k A50 A84 A5z N0 A0Y C0 A32 E1k A37 A7T p0 E1l A33 A4j J0 E1m A2r N9 h0 E1n q9 R7 P0 E1o L0 B1E y5 I0 E1p A2s A3I F0 t4 B6U S1 G0 q1
"v_cvt_pk_bf16_f32 v148, v160, v161\n" "v_cvt_pk_bf16_f32 v149, v162, v163\n" N1 "v_cndmask_b32_e64 v160, v209, v184, s[56:57]\n"
"v_cndmask_b32_e64 v161, v209, v185, s[58:59]\n" P1 "v_cndmask_b32_e64 v162, v209, v186, s[60:61]\n" "v_cndmask_b32_e64 v163, v209, v187, s[62:63]\n"
B6C "v_cvt_pk_bf16_f32 v156, v164, v165\n" "v_cvt_pk_bf16_f32 v157, v166, v167\n" x3 S7 A2Q C0 A6B J1 K0 D1 u0 A3F L1 B1 G4 M0 A2D E1q o0 A0K A8 E1r
O9 T9 c9 O0 E1s A2t m8 E0 E1t P9 U9 Q5 l0 Z6 C4A A0r E1u A2b z5 A7C E1v N0 C0h A8D E2M C0 S0 D3b i8 O8 D3a V0 D3Z e0 D3Y j8 P8 D7L J0 A4V C0 C4c C4d
D99 A0u C4e A53 v6 "v_max_f32 v208, v198, v199\n" A52 K0 A51 C1 A0v L3 A50 A4l A5z A36 "s_cbranch_vccnz Lc7_1\n" "Lr7_1:\n" A1L R0 f0 a6 A2E C4f A6 j0
D5 B6H s7 X0 V9 B4G n8 G3E p0 E5 A0N B4H t7 C4F b6 j2 A2w B6 P0 F5 L0 B8K w3 C4G S1 j2 B44 E8D q1 I0 N1 B45 E8E P1 B78 U4 j2 G1O G1P x3 B01 G1Q G1R J2
E8S A38 D1 D4J L1 D4K A0P G4 M0 B1 A2m A0 B2n W9 C4H C6 j2 w6 F3N G5 Z0 A17 d8 k8 c6 B79 D6 j2 X5 H5 h5 A2o u7 F0 d6 x6 A3G F3O C4J E6 j2 A3H B0l h7
B3N E2M B0m F3M D3b A5I D3a y0 A9s D3Z F5Q H0 D3Y D21 D7L r0 A3k D99 T0 B02 A53 p0 A52 B8d A51 g0 A50 B0 B3w A5z h0 A0 "Lp7_2:\n" F30 c8 L0 A4i D9O
A1M k0 A4Y C4l C4m A0L B48 C4n A1h P0 v7 B46 C4o A0G I0 F6 B47 B49 K8 F0 w7 M0 A72 e6 G0 G6 D67 B3J A0a f0 w3 B3K D66 S1 X0 q1 B5C F3P N1 B5D G1S P1
F3Q G1T B6C G1U G1V x3 B6h F3S F3R J2 E8S A39 D1 D86 t9 G4 M0 Z0 A2n A0 A2l l8 f6 A0 S0 H6 A2k C44 I5 V0 B1e A7f Z9 O0 I6 E0 J5 u9 r5 x7 e0 g6 Q0 A5W
L9 s5 A6z J6 B69 K9 K5 c0 C45 C46 C5B N0 t8 C5C C0 B3L C48 C5D B3M G1W C5F A4D G1X C5G A4C G1Y C5H A0 s8 G1Z L8 R0 B6J C5K A5z R0 p0 A50 M0 A74 F3T
A51 M0 A0e L3 A52 M0 h0 A53 c0 A4B C49 "s_cbranch_vccnz Lc7_2\n" "Lr7_2:\n" F3U l7 A0M B7g B7i t5 B2p h6 J0 L5 A1 B7j B7k m7 g1 A4F u5 A5L i6 K0 A4E
u6 F3c M5 u0 f0 G3F n7 L0 C7K G3G v5 o0 G3H j6 B0 A20 G3I Q7 A3U G3J B1a M0 D9Q G3K A16 l0 F3V D9M D9N E2N A2U A0M C2I A2V D4y A4G E8K N5 Q0 C5L C5I
F3Y G1a L0 A2W B0q F3b A1J B0 C5J E2O E8G w5 B8Q E8H k6 N0 F3d O5 L0 w9 y4 F3e o7 L0 B1w B3f x5 C0 z4 A5 A1N l6 J0 C2E a9 C0 B6l C2F C4y L0 e7 C2G C4z
B8v B5d C40 B8t G3L C41 B4c G3M C42 L0 B1B F32 C43 M0 A5X D4H V2 M0 C2P C2v J2 M0 B1 F33 J1 c0 A3g A0w F34 D1 M0 B0T A3s L1 M0 Z0 E1P Q6 C2V A0S E1Q
B0I A4s F3Z D7P A7T B8s D7O A0b K0 D6w A4j L0 A4J D6x p7 B28 E2P N9 A5d A4I E2Q A0H D2d E0 B3h R7 L0 C8r B3g q7 L0 H0 B9o y5 x0 B9n b9 A22 C2Q A3I L0
S0 C2R A3m M0 B2M C2S A0K A8m F3a B4B C2T A07 A8n A23 D6y c9 D61 E11 A4V M0 W8 E10 A2j j2 E4R m8 x9 F3W m6 D62 F3X Q5 L0 C2a A0T E8K r7 L0 G1c G3N z5
j2 A0U y9 E2O n6 D63 E8G A1k "s_cbranch_vccnz Lc7_3\n" "Lr7_3:\n" A7i E8H j2 C0f A2p A5K C4M C4K C2K E8I E8J j0 C4N B0 E2N D3c C0e C0f B5g B3i B5f A1N
C2U B5d F35 g0 K7 F32 D3c C0e C0f B5g k0 A0C B5f A1N C2X C2Y C2Z u8 B7L S8 A2c B0s i5 C8K j5 B1f D6w R6 D6x B3k B5Y C2W E1V K0 E1W F3f A3s B1f B5j C2b
C2c E1 E6x B3l B1f B5j B5Y C2h C2g C2e C2f u0 A0 A5M A0 l0 v4 A0 S8 p1 B7i B7g R6 B7j B7k K0 B7 w0 E8I E8J j0 x8 E2N D3c C0e C0f B3f "Lp7_3:\n" F30
A5e D0 A1N C2p B5d F35 G3O A5f D0 G3P D3c C0e C0f B3f A5g D0 A1N C2q C2r C2s C2t E1 E1V G1h E1W G3Q W0 G3R F33 d0 A5h U0 L7 F34 C2L a0 C2M E1P B9T E1Q
D7P C2u D7O D6w b0 A5i A91 B5h D6x E2P m0 B5i E2Q B5Y A5j D0 C2z C20 C2w C2x J3 D6y Y0 K6 E9g D4I A5k D0 E37 B3l B1f B5j A5l B5Y C22 C21 h3 J3 i0 A0
A0O D5 B0F h3 A6 C23 C24 E5 B0G C25 n8 C26 B5m O3 B1i B5l P1 B5n C29 N1 B1j J3 q1 A0 K6 C27 S1 D60 h3 w3 A0 J3 C28 z3 A25 A0O L1 f7 h3 D1 A8K C3E J1
D9J C3C J2 D20 C3D V2 A4t C3F F5 I7 B6 A4u F3 M0 u1 D6z B5r a0 c0 C3G C3H U0 B9q C3J d0 w0 P0 W0 I0 z7 B1U Z4 p3 B9r A8d Y0 B1 m0 A8e B1l q0 Z0 b0 B1m
A5F i4 I4 G5 A7m C6 R0 k5 A5G H5 M0 p0 D6 h0 t4 l5 D7C S1 f0 D2j N1 y0 X0 P1 E9Q H0 B6C S0 D7D B2M V1 A0m x3 A1r W6 J2 C4g A1s A2X D1 C4h B1U G4 M0 i9
B6b h7 B6c E6 r0 c2 R0 B8i A8A B1 W0 T0 B0u d0 U8 U0 A1t A8x E0 B9T D24 v0 B6M b0 H0 q0 B2e m0 S0 Y0 B2M i0 A3k A0J A8W C4V c8 g0 A1h c0 B34 A0L A02
K8 v8 w8 F6 k0 O3 A7X C4k P1 Q0 h0 N1 T0 B35 q4 q1 N0 S1 C0 B8S w3 f0 z3 A0 A2f B2g D3G B5G J2 F36 V2 g1 A2K C4p X0 C4q A7x A0a P0 G6 I0 d5 R0 u1 A26
K4 a0 J0 B4A F0 B2E A27 A4L A5b G0 c3 c0 A4M Y3 C4r K0 C4s D2d B2w p3 u0 B1 Y0 Z0 m0 o0 q0 O0 b0 E0 i4 T1 B0C C4t E1 C4 C4u p0 I5 D4 H6 h0 J5 f0 I6 x0
t4 f9 S1 H0 q1 S0 N1 E8 D3E P1 V0 U4 T1 e0 D3F C4w F8 B3O C4x G8 B3P m1 l0 x3 O7 A4k H8 J2 C4g C6C D1 C4h C4i j9 A6d K5 B58 J6 A5p Z2 R0 B0a A8A B1 W0
D0 B4f d0 U8 U0 A1t A8x E0 s0 l8 r5 B3o s5 B0j v0 A1u D9S b0 A5q A91 A1v D9T P5 A1w D9U i0 A5r L8 D0 A0 A1x A4u m5 P0 B1X B0D L3 A5s A9W I0
"s_cbranch_vccnz Lc7_4\n" "Lr7_4:\n" C3W F0 D2 G0 C3X F8L n5 i2 A5t l7 D0 T5 t5 A68 h6 A9G L5 c0 A9H C3Y A5u U2 c0 A9I D9V C3Z D0 A9J j1 O3 A5v P1 M0
E1M N1 D0 B98 B0B A5w w3 Q0 D84 z3 B6A D1 N0 J1 C0 B6P C0g V2 B2j m7 J0 u5 B8I i6 B0b A01 M5 O0 C3a A6i f2 K0 C3b B0c P2 E0 n7 B0f v5 H0 j6 S0 D2m V0
u1 "Lp7_4:\n" E8T A10 A0D U0 B0d d0 B0e W0 A9M s6 A9N C0i A3l A1a C3d F1 F3h C3e E6z F3i y8 N5 I8 B3Y J8 C3f z8 S3 D9X C3g U3 B5U A2Q A9u A7P A4q D3T
Q6 A2L Y6 A1J I4 w5 A2M k6 E0 O5 A2N A0E C3i H0 H2 S0 C3k V0 m2 A2O A7R z6 e0 o7 A4S A7S x5 A4T l6 A03 B1h A1c t0 A1d W0 A04 d0 A0M E7 U0 F7 i7 B1n
C0W B7 B0U h0 C3m B5v j0 C3n y0 F3j B0 V4 B0I P0 A7T r0 A0b T0 I0 B3I F0 C3o G0 w1 B5P S4 C3p g0 b1 B5Q B4 O3 A9Q P1 A9R A5H D2K A28 D1 A2P A3r B0g k9
p7 A4U A06 B92 B3E D3e C3q A1q q2 H0 C3r B3F D2c S0 q7 V0 y5 e0 b9 A1K B5B A9S u1 A9U a0 B2P U0 B2Q B3d A7L C3v F1 F3k C3w B7I C0E C3y m3 C3z D9W E3y
A2Q A3X A7P A0Z D3T Q6 A29 T9 A2j I4 m8 T8 A2a m6 E0 Q5 Z8 U9 C30 H0 F2 A7Z C31 S0 C3 A3z O8 Z6 V0 r7 q8 m9 z5 e0 n6 n9 P8 A1k L0 H7 D9S C8t J7 D9T W0
a8 D9U d0 b8 U0 E4c B3j s0 A0C C0X C8B A0 B5T E1 A9V D9V m0 A2d N0 i0 "s_cbranch_vccnz Lc7_5\n" "Lr7_5:\n" C0 O2 C0T J0 N2 C0 X0 A1L F3 a6 P0 A6 A7r
D5 K0 X1 u0 I0 I2 F0 G2 G0 Y1 A7p s3 O3 o0 P1 B0 A7q n3 N1 A7s B0B l0 w3 Q0 g5 z3 D2Q N0 J1 C0 A5P C0g V2 A1z s7 J0 V9 B8I n8 A3c E5 O0 C7x K0 d1 C1
E0 e1 B0 A99 t7 H0 b6 j0 B6 S0 F5 w0 V0 u1 A10 A5x C0Z U0 E4d d0 A5y C0a W0 A7t s6 A7u C0i A3l D28 B7S D29 i9 C6 a7 G5 e4 V1 w0 f4 d2 A1e e2 B7T B7J
D3d Z0 J1 O0 D1 E0 L1 x0 G4 y0 A3e k8 A17 B0 A3f A2o c6 r0 C5M T0 o9 C5N H0 D6 A31 H5 g0 b2 B0 A7x n1 S0 o1 V0 n2 A7v D3f k0 u7 e0 d6 p9 A7Q E6 A3B h7
c0 A3C t0 B9 W0 C9 d0 A1f U0 A1g i7 c8 C5O D8R m4 C5P n4 E1N A7K E2L X9 m0 X0 A0 "Lp7_5:\n" E8T Y9 Y0 l3 A1i i0 A1j A93 E1 C5Q D4 C8G Q3 P0 A3d b3 I0
L6 B2 F0 M6 C2 G0 A14 A1M t6 A15 A4Y A4d A0L A4Z A7z A1h A54 N6 C5T l1 A55 C5U k1 A4a C5V C4 x1 s1 R0 C5b C0M A00 E1 C5a z3 D2U Q4 J1 A84 J2 Z0 V2 A32
v7 O0 A0G E0 F6 B4E A3p K8 H0 M3 C7y S0 f1 B4F D3g V0 w7 e0 e6 A2i G6 A58 A0a M0 A56 u1 A57 a0 A59 U0 C0b c3 P7 F1 C0i B3S B96 q0 A6A A4P f0 i4 R4 A1Z
B0E E3 A6C Z3 B4e W3 I0 A2n F0 f6 A6D H6 D0 G0 I5 j9 D2x d3 A6E T4 L7 A4e t4 A6F S1 D0 B7Y A6G U4 D0 F1 B41 B01 A18 A6B D1 A6H D2Z A3F B5R A7U A6s D3K
A6I I6 Q0 A6t A3Z J5 D65 O0 z2 A3a O9 p2 E0 y1 P9 r5 V3 N0 s5 C0 A0r x7 H0 g6 A7C J6 J0 K5 R0 c5 C0 D2T B0j U0 A0s a0 K0 s0 A0t B7M i8 g4 j8 b0 A69
B7N "s_cbranch_vccnz Lc7_6\n" "Lr7_6:\n" T2 c0 D2y A4 l7 P0 t5 I0 h6 F0 L5 C0A B1Y C0H B1Z D2z w0 B2t A8i j1 B2u A8j O3 B8n P1 A5H B68 v6 D1 A0v A3r
u0 A36 k9 m7 A2E A01 u5 O0 i6 B9y M5 o0 K3 B0 C72 E0 t1 B9z C0N l0 n7 B9A v5 B0L j6 V0 D2m B7Q B42 D3Q E0A U0 B43 D3S d0 B44 W0 B45 c3 E4h p3 P3 A0 E7
Y0 F7 A5D D3R A0W A1a B7W w6 N5 d8 B3Y h5 L2 x6 B3W B5U A2Q A38 A7P A0P D3T Q6 r9 Y6 A1J I4 w5 s9 k6 E0 O5 A3G A0E A3 H0 H2 S0 M2 C0 V0 m2 A3H A7R z6
e0 o7 B0l A7S x5 J0 l6 C0 B0m a9 c0 A2F t0 A2H W0 K0 d0 A1S U0 A1T a0 B7 s0 B1n j0 C0W B3S A0B i1 B5v X0 z1 V4 E2 P0 D2q C0C B46 S4 Z1 B47 B7Z B48 a1
B49 b1 J9 B5A L1 A1U D1 A4h A3r B2a k9 p7 A4i A06 B92 D66 D3e R2 A72 q2 H0 h1 D67 D2c S0 q7 V0 y5 e0 b9 A1K B5B B5C u1 B5D a0 B3J U0 B3K B3d C0U F1
B5S G3S A0K B93 "Lp7_6:\n" E8T B94 A07 C0E D2s A2Q A39 A7P t9 L1 y0 Z0 G4 B0 A2k T9 A2j I4 m8 A2l A0c m6 E0 Q5 A0d U9 e3 r0 F2 T0 A92 t2 H0 C3 A1W Z6
g0 r7 B0 A7g z5 S0 n6 V0 A1k L0 B1D A1l O8 W0 B0p P8 d0 k0 U0 A0C A2z a0 r8 s0 A4V N0 B5F F2X I7 q0 B0h m0 A9V Y0 L3 i0 R6 "s_cbranch_vccnz Lc7_7\n"
"Lr7_7:\n" O2 E1 C0T K0 N2 u0 X0 A1L F3 a6 P0 A6 B21 D5 o0 X1 B0 I0 I2 F0 G2 G0 Y1 B2z s3 O3 l0 P1 Q0 B20 n3 N1 B0 B22 B0B N0 w3 C0 z3 s8 L1 A4C D1 J0
J1 C0 A4D B3M C0g V2 B3L t8 C5n K0 C5o C1 A7x s7 A8 D3U B4k A5F E5 j0 A2 D5s B3 E0 d1 D5t A5G e1 c0 H0 C5q A90 m4 C5r S0 t7 n4 B4l b6 V0 B6 e0 F5 A0m
u1 B23 a0 B24 U0 B25 d0 B26 W0 c3 D9X p3 x4 Y0 C0Y D7K E1 X0 C5s A0 C4 D7C C5t q5 J4 D4 D7D x2 I0 y2 F0 l2 G0 A2m i9 W9 C6 A5 G5 y4 V1 w0 z4 C5u D8
C5v d2 e2 D9K t4 A0 X9 A1i S1 Y9 A1j q1 A93 N1 B0x x0 D9 P1 y0 A7y U4 B0 L6 c2 r0 x3 T0 A2V D3P A2W B0q D1 g0 L1 K7 B8Q B5R C0V w9 k8 c6 k0 D6 O0 H5
E0 b2 B95 A2o n1 H0 o1 S0 n2 A3g D3f V0 u7 A6U d6 e0 E6 A3n A7Q C0B M6 t0 A0S A14 W0 A15 d0 A4s A7M U0 R9 A7N a0 A7z s0 N6 C5z c8 C50 A0l q0 I7 A2C
D3O Q3 l3 b3 B2 C2 q5 D3J F0 A0L G0 D7A A9v K4 l1 A9w Y3 k1 A9x x1 A9y s1 J9 D2w B1B D1 B4c A3r A1X k9 v7 e7 A3p A0G O0 F6 B04 K8 E0 M3 C5y H0 f1 B05
D3g S0 w7 V0 e6 e0 G6 A2i A8f A8m u1 A8n a0 x8 B1c U0 B1d C0b c3 A5e p3 D0 F1 Y0 C0Y q0 A5f A4P D2t v3 R4 E3 A5g C6b B0E W3 P0 A2n I0 f6 A5h H6 L7 F0
I5 G0 D2x j9 d3 A5i T4 D0 A4e t4 A5j S1 D0 B7Y A5k U4 D0 F1 B41 B01 A18 W8 D1 A5l L1 Q0 x9 G4 D4a Z0 A7U A0T D3K O0 I6 A0U y9 J5 E0 z2 A7i r5 p2 N0 y1
C0 u8 S9 V3 H0 s5 A7j x7 J0 g6 C0 B0s J6 S0 K5 L0 V0 A35 B0j d0 i5 U0 K0 a0 X8 s0 Y8 B7M j5 B59 B7N "s_cbranch_vccnz Lc7_8\n" "Lr7_8:\n" T2 T1 h0 D2
f0 "Lp7_7:\n" E8T h2 X0 i2 A4 l7 P0 t5 I0 h6 F0 L5 C0A B0F C0H B0G D2z B1i j1 B1j J9 u0 B5N f7 D1 A8K J1 B3V B6m V2 o0 m7 B0 A4t u5 Z0 i6 A1Y M5 l0 K3
R0 A0V f2 Q0 O0 t1 B4m P2 N0 n7 C0 B64 v5 H0 j6 S0 Q7 A8d D3Q u1 J0 B8c B7H T1 C0L A8e D3S W0 K0 s6 B1l Y4 B1m C0R A1I E7 E4j F2Y
"s_mov_b32 s92, 262144\n" G3T g7 W6 N5 k5 Q2 l5 L2 X4 S3 A0A T3 F7 U3 B5U A1r D3P A1s D1 x0 L1 B0X B5R D2p A9D Y6 w5 O0 k6 z9 O5 B2m A3 B0t A0E H2 H0
M2 C77 B9Q A7R z6 V0 o7 A5a x5 e0 l6 B2F A7S a9 B1C A8A B4n W0 A8W B5I a0 A0X s0 B7 B1n j0 q0 y0 D88 A4l Y0 B2x r0 i1 T0 C8y V4 z1 P0 E2 g0 D2q C0C
A76 S4 Z1 A77 B7Z v8 a1 w8 b1 J9 A2f D3G D3d A4K k9 p7 A2K A06 B92 A9F D3e R2 A8G q2 H0 h1 D2c S0 q7 V0 y5 e0 b9 A1K C0D A26 u1 A27 a0 A4L U0 A4M B3d
C0U F1 B5S B7I C0E E15 B7U w2 F1 C7z Z5 C70 B1 x3 B0 a5 J2 Z0 J1 O0 D1 E0 L1 x0 G4 T1 A4k T9 C73 "s_waitcnt vmcnt(13) lgkmcnt(1)\n" m4 U9 C74 k0 A2j
Q0 n4 B7O m8 A6z m6 B2G Q5 N0 e3 C0 B58 F2 S0 t2 E8 O8 C3 J0 Z6 C0 F8 G8 r7 V0 z5 H8 A8E n6 K0 C0h A8F A4N C75 u0 e0 C76 G7 "Lb7_2:\n" M7 C4 P8 t0 o0
B7F D4 A4O B5I A1u a0 l0 s0 v4 A1v B3G A1w C8B A1x D3N N0 i0 c0 h0 C78 "s_cbranch_vccnz Lc7_9\n" "Lr7_9:\n" o4 C79 O7 f0 O2 A5p D3 D0 G3U p4 D27 A5q
a6 A94 A6 F0 D5 G0 X1 A9H B7A A5r B8P A9I n3 Y1 A9G O3 A9J P1 A5s N1 A9W B8J A00 A5t z3 D0 B6A D1 Q4 J1 B6P J2 A5u V2 D0 B2j s7 Z0 D3U B0b A5F E5 B2m
A2 A6i B3 A5v C47 B0c A5G e1 H0 t7 S0 b6 A5w B6 "s_add_u32 m0, s88, 80384\n" V0 F5 B7Q A0m a0 T1 B0d A3D U0 J0 B2E B0e A3E W0 A9M A4b z7 A9N A4c p3 K0
Y0 B7 A5D B2o b0 j0 D85 J4 R4 x2 A0 "Lp7_8:\n" E8U B0A y2 P0 l2 I0 A2m F0 W9 G0 C6 i9 G5 I8 V1 J8 d2 w0 y8 e2 z8 B7T B7J "s_waitcnt vmcnt(26)\n" A18
A9u B0J A9O k8 B3e C0V A9P A2L c6 E0 D6 A2M A2o H5 H0 b2 S0 n1 V0 o1 e0 n2 A2N D3f A2O A7Q u7 A4S d6 A4T E6 A03 h7 R0 "s_mov_b32 s92, 712704\n" A1c t0
A1d W0 A04 B5I X4 i7 B5V B3X B4j A9k A9t l3 A1M D2v D3D B5P B3H B5Q B9x A9Q s1 A9R O3 A0M E7 P1 F7 A5H D2K A28 D1 A7w A2P J1 B3V B0g k9 v7 A4U A3p A0G
r0 F6 T0 B3E K8 O0 M3 A1q o2 g0 f1 B0 B3F W2 E0 w7 B5M e6 k0 G6 H0 A8f D26 A2i d0 A9S W0 A9U s6 B2P Y4 B2Q A3l B66 F1 B7V G0 f6 j9 H6 I5 D2X A4e B7K
A2Q A3X A7P A0Z D3T Q6 A29 l8 B1e I4 Z9 T8 A2a I6 E0 J5 Z8 r5 z2 B75 A7Z y1 S0 V3 A3z K9 s5 V0 x7 q8 m9 g6 e0 J6 n9 L9 K5 L0 H7 t0 J7 W0 a8 d0 b8 U0
i7 L8 x4 B59 B7N "s_cbranch_vccnz Lc7_10\n" "Lr7_10:\n" T2 E1 D2y Q0 A4 l7 T0 P0 t5 I0 h6 F0 L5 A7p u3 X2 N0 U2 C0 C9w A7q r3 j1 A7r O3 J0 P1 C0 A7s
N1 B0B K0 w3 u0 z3 D2Q o0 J1 B0 A5P C0g V2 A1z m7 l0 u5 Q0 Z0 i6 g5 A3c M5 O0 K3 A7c N0 t1 C0 E0 P2 B64 n7 H0 v5 J0 j6 C0 S0 Q7 w0 V0 u1 A10 A5x D3Q
U0 K0 d0 C1 A5y D3S B7F A7t s6 A7u p3 j0 Y0 C0Y D3R A0W A1a B7W a7 N5 e4 B3Y f4 L2 "s_mov_b32 s92, 466944\n" A1e S3 X4 E4S B5U A3e D3P A3f B0J D2u A8H
Y6 A1J I4 w5 A8I A09 k6 y0 E0 O5 B0 A1A A0E A3 H0 H2 S0 M2 A7v A7R m2 r0 z6 T0 V0 o7 p9 A3B x5 e0 l6 A3C A7S B1h B9 t0 g0 B7F C9 d0 A1f U0 A1g B3j s0
B1n B3X i1 A5E f0 C81 V4 B0I P0 A7T I0 C8C Z5 S4 C8D F0 A0b a5 A4j w0 G0 Z1 A54 B7Z A55 a1 A4Z b1 A4a C0M A00 w0 C8E B6h m4 C8F "Lp7_9:\n" E8U n4 z3
Q4 L1 A7d D1 Z0 J1 A84 J2 O0 V2 A32 p7 E0 N9 B5K A0H B4E B8y R7 H0 A1i R2 B1E A1j q2 S0 A2S h1 B4F B6B A2T W1 M0 V0 D9 C8H D4 A7y q7 e0 L6 y5 B6D M6
b9 A58 B0i A3I A56 A4d u1 A57 A7z a0 A59 N6 C8I U0 C8J C0b F0E C8L w0 C4 C8M I7 p3 h0 Y0 f0 m0 X0 q0 A7L b0 i4 E1 F1 C8N P7 A0 j3 q5 g2 I0 I3 F0 f3
A6A A3m D0 G0 G3V c9 A6C r2 D0 E1K A6D g3 D0 B1g q1 A6E N1 L7 A8g w2 A6F x3 B0x x0 D0 J2 A6B A7P A3F L1 A6G G4 D0 A2D A2j b5 m8 A3Z T9 m6 O0 Q5 A3a O9
e3 E0 F2 P9 U9 t2 A6H C3 D0 A0r Z6 H0 r7 A7C z5 A6I n6 Q0 S0 C0h C0G D65 A5C B4B d0 A0s U0 N0 B8c A0t s0 i8 B3G j8 v0 J0 C60 p0 q0 B8L B0h m0 A9V Y0
L3 i0 K0 h0 "s_cbranch_vccnz Lc7_11\n" "Lr7_11:\n" O2 T1 f0 B9w F3 D27 I0 a6 F0 A6 G0 D5 B1Y s3 X1 B1Z n3 I2 w0 B2t A8i G2 B2u A8j Y1 B8n J9 B68 v6 D1
A0v A3r A36 k9 s7 A2E A5F D3U B9y E5 u0 E0 B71 H0 d1 B9z A5G e1 o0 t7 B0L b6 V0 B6 e0 F5 c0 A0m u1 B42 a0 l0 U0 v4 B43 d0 B44 W0 B45 c3 N0 p3 B0R Y0
p1 E7 A5D q0 F7 B6S i4 C0 h0 J4 A1H x2 X0 y2 K0 l2 B0A A2m P0 W9 I0 C6 F0 G5 G0 V1 E1 i9 C37 e2 h5 G3 w6 H3 x6 B1g "s_mov_b32 s92, 516096\n" B5O A12
X4 c2 x4 x3 A2Q A38 A7P A0P D3T Q6 r9 k8 A17 I4 c6 s9 D6 E0 H5 A3G A2o b2 H0 n1 S0 o1 V0 n2 A3H D3f e0 u7 B0l A7Q d6 B0m E6 A1S C0B A2F t0 A2H W0 A1T
B5I i7 B5V q0 B7 C0Y A0Q f0 Q3 j0 b3 y0 B3z B0 l3 B30 A1M r0 A4Y T0 D3D B46 B3H g0 x1 B47 Y3 s1 B48 O3 B49 P1 A5H B5A L1 A1U D1 A4h A3r B2a k9 v7 A4i
A3p A0G O0 F6 D66 K8 E0 M3 B51 H0 f1 D67 D3g S0 w7 V0 e6 e0 G6 A2i A8f B5C u1 B5D a0 B3J U0 B3K B3d B66 F1 B7V G0 f6 j9 H6 I5 D2X A4e B7K A2Q A39 A7P
t9 D3T A0 "Lp7_10:\n" E8U Q6 A2k l8 B1e I4 Z9 A2l A0c I6 E0 J5 A0d r5 z2 B75 B0 A92 y1 S0 V3 B69 K9 s5 V0 x7 D2h g6 k0 J6 A0C A0e K5 L0 B1D A1l L9 A8w
B2E B0p U0 A2z a0 r8 S6 L8 B0R B59 C0Q D3B A2d B7G "s_cbranch_vccnz Lc7_12\n" "Lr7_12:\n" T2 E1 p0 D3L f0 i2 X0 l7 A4 t5 P0 h6 I0 L5 F0 X2 G0 U2 T5 k2
B2z j1 B20 O3 B21 P1 B22 A5H s8 L1 A4C D1 A4D A3r A87 k9 m7 t8 A01 u5 O0 i6 B4k M5 E0 K3 C4D H0 t1 B4l C0N S0 n7 V0 v5 e0 j6 A0D D2m B23 u1 B24 a0 B25
U0 B26 B3d A1a C8S A0 Z5 C8T q5 N4 a5 v2 I0 R3 F0 i3 G0 B1a A1b A16 A2U A5 N5 y4 B3Y z4 L2 D8 B3W C8U A2g m4 C8V n4 B1g "s_mov_b32 s92, 630784\n" q1
X4 N1 A8g B3R A1I A18 g5 A4H D1 o0 L1 B0 v9 G4 M0 Z0 C8X C4 Y6 C8Y l0 A1J Q0 D4 A7h w5 B4z k6 w9 O5 E0 A3 B95 A0E H2 N0 M2 H0 m2 S0 z6 V0 o7 A3g A7R
x5 w0 e0 C8Z A0w A7S C8a B0T l6 A3n a9 A0S X9 t0 A4s Y9 W0 R9 A1i d0 A1j A2S U0 A2T a0 D9 s0 A7y A0Y L6 v0 M6 F3r B2o A89 C9G V4 i1 A14 s2 A15 D2a D2b
G0 A0b E9 B3I A9v A7M Z1 A9w A7N w1 A9x A3K a1 A9y A3M b1 N6 C8b O3 C8c C8d P1 C8e C8f N1 C8g D7G D2w B1B D1 B4c A3r A1X k9 p7 e7 A06 B92 B04 D3e R2
B54 H0 h1 P7 B05 D2c S0 q7 V0 y5 e0 b9 B1c B6B A3I M0 A8m B6D u1 A6j a0 A1D A8n U0 B1d C0b A6o c3 D0 F1 C0i B40 A6p A91 h0 D2R R4 j3 A6q g2 D0 A7L I3
P0 f3 I0 A3m A6r A0K A1E F0 A07 G0 c9 B2R r2 E1 o3 A6k m3 D0 X3 E14 A6m S1 D0 B7Y A6n U4 "s_add_u32 m0, s88, 12800\n" F1 E2K d7 A18 W8 D1 J0 L1 C0 x9
B5R B7X A0T D3M K0 m6 C1 A0U y9 Q5 B4z e3 B9p F2 E0 t2 S9 U9 C3 j0 Z6 A7j r7 H0 z5 B0s n6 S0 C0h V0 A35 B4B d0 i5 U0 X8 a0 Y8 s0 j5 C0X B59 B5T C0O
A4r "s_cbranch_vccnz Lc7_13\n" "Lr7_13:\n" O2 E1 C0T X0 N2 F3 A1L P0 a6 I0 A6 F0 D5 G0 X1 B0F B7A B0G C0K B1i Y1 B1j D3A A7w N1 A00 r0 z3 T0 B5N f7 A0
"Lp7_11:\n" E8U D1 Q4 J1 A9B J2 g0 V2 B0 B0V s7 Z0 D3U B9q A5F E5 k0 A2 E4k A0V B3 E0 d1 B9r A5G e1 A6M t7 A1D H0 b6 S0 B6 V0 F5 c0 B7Q A8d C0Z a0 A6Q
U0 A8e C0a d0 B1l W0 B1m D9F B3S F0H A4P X0 i4 A0 D28 F3s A6R l2 A94 A2m F0 W9 G0 C6 W6 D3H G5 A6S V1 T1 k5 D3I d2 D0 l5 e2 A0A G3 A6T H3 A1E B1g q1
A6N N1 D0 A8g c2 A6O x3 A90 x0 D0 J2 B0W A7P B0X L1 A6P G4 Q0 B6c A17 G0B Z0 c6 O0 D6 E0 H5 D24 b2 g7 B0t n1 N0 o1 H0 n2 B0u A0J S0 u7 D5Q C0P d6 A0X
E6 C0 V0 h7 c0 B1D F8Q A7Q W0 J0 B2E F8R U0 F8S a0 F8T s0 K0 B5V C0W B3S A0B B3y B3z l3 B30 D3J F0 A0L G0 D7A F8U K4 l1 F8V Y3 k1 F8W x1 F8X s1 J9 A2f
D3G D3d A4K k9 v7 A2K A3p A0G O0 F6 F8Y K8 E0 M3 F8a F8b o2 H0 f1 F8Z D3g u0 S0 w7 V0 e6 e0 G6 A2i A8f F8c u1 o0 a0 B0 F8d U0 F8e d0 F8f W0 l0 c3
"s_mov_b32 s92, 663552\n" F1 C0i B40 X4 D3R A1Z B0E B7V G0 f6 j9 H6 I5 D2X A4e B7K d7 A18 A3x A3w B0J F8g l8 B3e A7U F8h F8i Z9 E0 I6 F8j r5 J5 B9A z2
B0 F8k F8l p2 S0 y1 F8m K9 V3 N0 s5 C0 F8n F8o x7 V0 g6 F8p F8q J6 J0 K5 R0 C0 F8r A35 F8s L9 d0 K0 U0 F8t a0 F8u s0 F8v L8 x4 B59 B7N
"s_cbranch_vccnz Lc7_14\n" "Lr7_14:\n" T2 E1 D2y A4 l7 P0 t5 I0 h6 F0 L5 C0A F80 C0H F81 D2z F82 j1 F83 C0M B0B G7 "Lb7_3:\n" M7 A13 F8z L1 F8y D1 F8x
A3r F8w E0D k9 m7 E0E A01 u5 O0 i6 F2Z M5 E0 K3 F2a F2b f2 H0 t1 F2c C0N S0 n7 V0 v5 e0 j6 A0D D2m F2e u1 F2f a0 F2d U0 F2g B3d G3W F1 B7W F2p N5 F2n
B3Y F2o L2 F2q B3W B7 B67 j0 P1 A7w U4 x4 B3R F0i A18 r0 D1 T0 F2h F2i D3T Q6 F2j Y6 A1J g0 w5 B0 F2k F2l k6 O0 O5 F2m A3 k0 H2 Q0 E0 M2 T0 F2r A0E m2
H0 A0 "Lp7_12:\n" E8V z6 F2s F2t o7 N0 x5 K7 F2u l6 S0 B1h C0G J0 B8G C0L A5n U0 E63 a0 K0 s0 u0 E64 A0Y E2H v0 E2I b0 o0 q0 A02 A2C B2x l0 i1
"s_mov_b32 s92, 696320\n" C8y V4 z1 P0 E2 X4 D2q C0C F21 S4 Z1 F22 B7Z E6r a1 E6s b1 J9 F20 L1 F2z D1 F2y A3r F2x F2w k9 p7 F2v A06 B92 E6t D3e R2 F23
F24 q2 H0 h1 E6u D2c S0 q7 V0 y5 Q0 e0 b9 B0 A1K B5B F25 u1 F26 E0A A1O E6v d0 E6w W0 c3 J0 p3 B0R Y0 C0Y q0 K0 A7K D85 j3 R4 g2 A7L I3 P0 f3 I0 A3m
F0 A0K G0 A07 B2R C0E D2s O6 A18 E1T E1U B0J F1w T9 B3e B7X F1x F1y m8 E0 m6 F1z U9 Q5 H0 e3 F11 F12 F2 S0 t2 F10 O8 C3 V0 Z6 F13 F14 r7 e0 z5 F15 P8
n6 F16 C0h F17 t0 F18 W0 F19 d0 F2A U0 F2B i7 C0X g4 F2C b0 F2D B5T F2E C0O F2F A4r E4L "s_cbranch_vccnz Lc7_15\n" "Lr7_15:\n" O2 R0 B4w E4M o4 a2 f0
N2 X0 A1L E5F p4 a6 P0 A6 F2I F2J D5 I0 X1 F2G F2H I2 F0 G2 G0 Y1 Z4 s7 E5G C5W V9 E1w n8 F85 F86 E5 A6X C5X A2 C1 F2K F2L B3 B0 C5Y C5Z d1 E5P C51 e1
E5Q C52 t7 j0 b6 y0 E5V C53 B6 B0 F87 C54 F5 L0 F2M G1 r0 K1 T0 F2N F88 z0 I1 E5W H1 g0 S2 A7W E5X B36 E5Y B1A k0 O1 Q0 E9O T0 O0 K2 A7m J4 N0 x2 A7Y
y2 H0 l2 S0 A2m V0 W9 e0 C6 A19 G5 O6 F2O F2P V1 w0 x0 d2 C0 F89 F9A e2 J0 G3 C0 B4P B4Q H3 A17 E65 E66 c6 K0 c2 u0 F9B F9C D6 H5 F2Q F2R b2 o0 n1 B0
F9D F9E o1 n2 D4j D4k u7 l0 d6 Q0 D4l D4m E6 B0 h7 L0 E67 E68 t0 N0 B8G E69 E7B B5I E4v a0 J0 s0 C0 E4w v0 E7A b0 E7C q0 K0 m0 D4Y B0U D4Z F9F B4j b3
F9G E4x B2 f0 C2 E4y E4z A1M X0 A4Y E40 E41 A0L P0 A1h E5A E7D l1 g1 I0 k1 E7E E7F x1 F0 s1 E7G q4 v7 G0 A0G D4d d5 F6 E7J K4 K8 E7H Y3 M3 E7I o2 E7K
f1 D4e W2 D4f w7 D4g e6 G6 E7M A0a E7L G1 E7O K1 E7N z0 B73 M0 A1R B4I D4T C5k C5l A30 v3 F1 E3 A6L Z3 S0 W3 V0 A2n e0 f6 A0p H6 I5 m1 d3 A0M x0 T4 C1
E7P E7Q F4 B0 t3 E7R E7S B1e j0 Z9 y0 E7T E7U Z2 B0 I6 E7V E7W J5 g1 "Lp7_13:\n" E8V z2 r0 p2 T0 E7X E7Y y1 j7 V3 E7Z L3 x7 g0 g6 K7 E7a E7b A6V
"s_cbranch_vccnz Lc7_16\n" "Lr7_16:\n" C5c J6 D4h D4i K5 A1 E0C t0 k0 W0 Q0 E7c E7d B5e A6e C5g U0 E7e E7f D2J E7g C5h A6W D4b C5i v0 D4c C5j A7K q0
E7h E7i A89 Y0 E7j E7k i0 X0 C5d B2c E7l E7m C5e D2 P0 C5f h2 E7p E7q C7 i2 I0 D7 X2 E7n E7o o6 U2 p0 p6 A6b D4n D4o T7 k2 F0 U7 j1 E51 C56 A6Y G0 C57
K3 F9H F9I C58 f2 n5 m5 C59 t1 F9J F9K V5 P2 T5 o8 A6c M0 D4p D4q y7 G1 A1n Z7 K1 F9L F9M M9 z0 A0I o5 I1 E52 H1 A1o c4 E6D A6a A0 C6J C6K A6f F9N F9O
U1 Z0 u2 O0 R1 A0M E0 B0v C0 F9P U5 C6A K2 J0 C6L N4 C0 F9Q E4J C6M v2 B1 M8 R3 K0 N8 i3 c0 E4K E4 G9 L2 u0 H0 H9 S3 o0 Q8 T3 B0 F9R F9S R8 U3 S0 C6B
A6g E6H C6D Q2 l0 C6E A3 Q0 F9T B5 C7 H2 B99 D7 M2 F9U F9V o6 m2 M0 e0 p6 t0 F9W U6 T7 A8w U7 d0 E59 C6F U0 C6N C6R C6G a0 F9X F9Y C6H s0 C6S C6T C6I
A6u F9Z F9a V5 a3 p0 o8 v0 F9b F9c V7 A7K W7 q0 F9d F9e e8 A89 f8 Y0 F9f F9g X7 i0 A3U Y7 A6x E6U C6U i1 q5 C6V s2 E95 E96 C6W z1 I0 C6X E2 F9h F9i M8
Z1 F0 N8 w1 F9j F9k G9 a1 G0 H9 B2O F9l F9m Q8 R2 V4 R8 q2 E6X C6j A6y E9 C6k h1 E6Y E6Z C6l W1 C6m A1n G1 F9o F9p e8 B6W f8 A1o z0
"s_mov_b32 s92, 368640\n" A3S C61 I1 M0 C62 C63 C64 H1 F9q F9r M8 S2 C6Z N8 A6b A0 F9s F9t G9 U1 "s_waitcnt vmcnt(7) lgkmcnt(5)\n" Z0 H9 A1y Q8 R1 F9u
F9n R8 O1 C6a y7 c1 C0 A3T Z7 B8D M9 A6c J0 o5 A6w C0 E6A C6f j3 E0 C6g g2 K0 C6h I3 C1 E6B E6C C6i f3 c0 B1 C67 o3 B0 F2S O4 C68 m3 j0 C69 X3 y0 F2T
h4 C7A g3 B0 F9v A1n r2 r0 e8 e3 T0 F9w E99 f8 F2 F0A A1o t2 g0 C7B A24 A3V C7C A0F H0 C7D B7F F9x F9y C7E A0i S0 M8 U0 c0 V0 N8 B3j e0 G9 s0 Q0 B0w
H9 v0 T0 E17 E18 Q8 D69 R8 q0 A4p A3W C7 m0 F9z F90 D7 Y0 E19 E2A o6 i0 C6O p6 B08 D4t D4u T7 F91 F92 C6P E2B A2J E2C A5d F93 F94
"s_cbranch_vccnz Lc7_17\n" "Lr7_17:\n" g1 O2 B4o U7 D3 A3N C6o A6V A64 C6p a2 D0 F95 F96 C6q N2 h0 C6r X1 F97 F98 V5 I2 A65 o8 B8P F99 G0A y7 Y1 A6h
Z7 A2 E6h E6i M9 B3 A66 o5 A6W D0 A3O C6s d1 X0 C6t e1 E6j E6k C6u G1 A67 C6v K1 M0 A69 E6l C7 z0 e9 P0 D7 I1 E6m B0b o6 H1 C7H p6 c4 B0c A9M T7 U1 I0
U7 u2 A3Q C6w R1 C7I C6x O1 A9N o4 C6y c1 B0x B1 C6z K2 x0 "Lp7_14:\n" E8V F1 V5 J4 w0 A4Q p4 o8 x2 G3X Z0 y7 y2 J0 Z7 l2 C0 A4R M4 M9 d2 O0 o5 e2 A3R
G3 K0 E0 H3 u0 D4r D4s C5x M0 A4q n1 A2L s3 V1 o0 o1 O7 A2M E2D n2 F0 C7F A8A E2E y8 C7G A2Z G0 C7N A0i z8 n3 C7O B9s A1F M0 A3b E2F B8Y A60 A4P E2G
A4S B7P m0 D2d D5I D5B C7P Y0 A61 C7Q C8m D5C D5D C7e c2 L0 A4T C7f Q3 A62 b3 D0 D5E D5F B2 A9p C2 L0 B0y A8i A6f A63 A8j l1 L0 D5G D5H A4f k1 A1 p0
V7 A6g E3v A68 B39 W7 x1 A28 A0I s1 A1 A2P C7g C7h A1V D64 C7i C7j o2 E1F A0h f1 A4U d9 W2 R0 h0 X7 G1 M0 A5o Y7 B6W f0 Q8 z0 M0 A3S R8 I1 M0 P0 C7m
H1 c0 A3T C7n c4 B0z C7o A6x l0 C7p A6y A4p A1q A4g A1p V7 u2 A7o W7 R1 C65 e8 O1 B00 f8 B74 X7 b7 A8B Y7 v3 C66 C7q E3 Q0 A3V C7r Z3 A8 C7s W3 N0 C7t
d3 c0 A0n q4 M8 T4 C0 O0 N8 F4 J0 E0 G9 t3 C0 d5 A30 A0o H9 m1 A5O A0g z2 w0 B7n A3D p2 T8 A3E A4b y1 K0 A4c V3 C1 A3W A8A I0 B1V D5L D5M A0i F0 U0
F4l D43 D44 I9 G0 B8X B0 D45 D46 t6 A0p v0 A8y A7Z b0 j0 q0 A0N D47 D48 m0 A8C Y0 B2H A4m i0 A6Q A6Y B2H D49 D42 A6a D0 h0 B6i B4T C7 A6R D0 A8z A2A
B2s A5P "s_cbranch_vccnz Lc7_18\n" "Lr7_18:\n" B2H T2 A6S D7 D2 D0 A1z o6 h2 f0 p6 i2 B4Y T7 X2 A6T U7 U2 y0 A3N C7S A6u E4U X0 C7T k2 r0 C7U j1 c0
B4X C7V K3 B2I V5 f2 g0 o8 t1 B0 A3c y7 P2 P0 Z7 G1 k0 M9 K1 Q0 D5R o5 z0 T0 I0 C7W I1 A3O C7X H1 N0 C7Y S2 C0 B81 C7Z A6w F0 C7 U1 B6f D7 u2 J0 o6 R1
d7 B1 p6 O1 A5L C7a c1 A3e m5 C7b K2 c0 Z0 C7c N4 K0 C7d v2 u0 A3f n5 V5 R3 O0 o8 i3 o0 y7 L2 e9 G2R E4 Z7 B2A T7 T3 G2S U5 U7 U3 C7u M9 Q2 A3Q o5 A3
c0 G2T u3 H2 G2U M2 C7w m2 B0 A3R t0 G0 W0 c0 D5J D5K d0 l0 U0 F0b D5S D5T I9 B8X Q0 D5U D5V r3 B03 g5 e4 f4 B6k D69 q0 x8 D5W D5X m0 A4n Y0 B2H A1Q
i0 A6M a3 D0 D5Y D5Z C8h B2H p0 C8i L0 F3t F3u C8j A6N i1 D0 F3v F3w s2 h0 z1 G2V G2W E2 A6h C8k G2X G2Y Z1 A6O w1 D0 A32 a1 X0 B2O G2Z G2a R2 A6P q2
A4p G2b G2c h1 P0 W1 L0 G2d G2e G1 C7k K1 G2f G2g z0 I0 A1B G2h G2i H1 C7l S2 L7 G2j G2k U1 S7 "Lp7_15:\n" E8V V4 F9 D5O D5P O1 A6o A6l E6n E6o B3T j3
L0 E6p E6q g2 A6p I3 C8P f3 G2l O4 o3 O0 m3 G2m C9H X3 A6q E0 g3 D0 h4 C9I e3 w0 E53 C9J F2 C9K S4 r2 E54 G2n t2 A6r C3 B4M G2o E55 c3 F0 W0 E56 G2p
d0 J0 A1O G0 a0 B8X K0 B4 B4W C1 G2q E57 b0 A02 q0 j0 m0 y0 F1s E58 B8u B2v O6 D5e D5f w2 B3i D5g D5h D5a D5b D5c D5d A2J F1t F1u g0 B0
"s_cbranch_vccnz Lc7_19\n" "Lr7_19:\n" B2H O2 F1v E0J D3 E2d k0 N2 P7 E0K C9L X1 X0 I2 M0 G2r C9M G2 A6j Y1 D0 G2s C9N A2 E6E C9O B3 P0 d1 E6F E6G e1
A6k C5A D0 E6I E6J A9n B0S E6K E6L I1 A6m H1 E6M E6N c4 F0 B19 D0 E6O o4 y6 E6P p4 B53 E6Q B8B A6n x2 Q0 D9H D9I y2 D65 O0 l2 p1 E6R D5k d2 N0 e2 C0
D5l D5m G3 E0 H3 L0 D5n D5o C5x J0 n1 C0 D5p D5q V1 E6S C9P L0 C8n D5r C9Q o1 K0 B3v C9R C9S C8o c0 E6T s3 C8p u0 G0 c3 o0 W0 A5d E6V n3 d0 B8F w0 C4j
E6W a4 B2S A64 b4 C8q D0 E6a E6b c2 R4 v0 E6c E6d b0 A65 A91 E6e D9Y P5 D9Z D9a i0 B2d B57 G0D D9b b3 A66 B2 G0E E4P C2 g1 C9U C9V l1 L0 E4Q G0F k1
C9W C9X x1 D0 G0G F27 s1 g1 G0 A1V F28 F29 o2 f0 f1 F3A F3B W2 A67 A96 L0 F3C F3D l4 F4F F0 r4 F3E F3F s4 l0 e5 L0 F3G q4 g9 C1 F3H d5 A11 A0q P0 G3Y
D8y g8 j0 f5 y0 A8k F3I k7 A8 h8 p1 D50 D51 A8L B4O A8M D5u D5v A7D r0 A7E G3X D5w D5x A8N V0 D7E D5y D5z D22 A8l j4 X5 A7F A3y q6 A8O L0 A3v Y3 A8P
B0P m1 g0 A7G O7 D9E C9Y A7H e0 C9Z C9a C9b H0 R0 A60 A9b D0 D9D W5 A9a M0 D9C C9c A9Z D9B C9d A9Y M0 D9A C9g A9X D89 C9h S0 I0 C9m A0c R5 K4 A61 D0
Z2 C9e C9f A0d u9 R0 C9p A5W A0 j7 C9q A62 C9r D0 B69 C9s M0 D2h C9t "s_cbranch_vccnz Lc7_20\n" "Lr7_20:\n" A0e C9u L0 C9v T2 M0 A4B D2 A63 h2
"s_add_u32 m0, s88, 97280\n" B2p i2 A3U X2 M0 C82 A6X U2 k0 k2 T0 C83 C84 j1 R0 C85 C90 A9z F0 f2 o0 C9y p1 D52 D53 P2 B0 C86 C87 C9z B3N D7F M0 D54
D55 B3n C91 f0 C88 C89 A0j A4v L0 B9U B9V A1b C8u w0 D56 D57 C8v l0 C97 D58 D59 A4w C9A C93 C9B C9C C95 R0 C9D C94 C8w C96 C98 A4x A0 D0A M0 y4 C99
C8x C9i C9j D0B A0 z4 C9k B9W C9l D0C B9X c0 D0E A4y w0 A5 D0D D0F D0G D0H A4z D8 D0J A40 D0K D0O D0I A0 A2I A0w C9n C9o B0T D0P D0L A0 A0 D0M D0S D0T
D0W D0R "s_waitcnt vmcnt(3)\n" D6A D6B D6C D6D D6E D6F D6G D6H A8S A8T A8U A8V D6a D6b G3E A47 x0 T1 D0U A4H v9 A8J D6Z D6Y D6X D6W B2H D0Y D6V D6U
A0S A88 R9 B9E E7r A9g A9f D7M B9F B9G A9e D7M A9d B9H B9I A9c B9e D0N A0 B9J B9K D0V x4 D0Q B9L B9M A2h A0 D0X g8 B9Y f5 A1 E0 k7 B9N B9O h8 Q4 D0Z
A0 B9P G2t i1 b5 s2 A0 G2u D0a z1 D0b B9a E2 B9Z Z1 D0d D0c G2v D0f a1 I4 b1 G2w D0g R2 H0 D0e G2x B9b D0h A0 S0 h1 A5m W1 A0 D0i A41 w0 B9c B2M D0j
B0w D0k D0l D0m G2y G21 D0n G22 D0o A0 D0p B9d D6c D6d D0q G23 G24 A42 D0s D0r A0 G25 G26 A43 G2z D0u A44 L0 G20 D0t D0v A45 A8S D0x A46
"s_cbranch_vccnz Lc7_21\n" "Lr7_21:\n" D03 D0w A0 G27 G28 D01 R0 D08 D0z D02 c0 "ds_read_b64_tr_b16 a[80:81], v217 offset:29568\n" A6e D0y D00 D04 A8T
D05 "ds_read_b64_tr_b16 a[82:83], v217 offset:30080\n" D1E "ds_read_b64_tr_b16 a[84:85], v217 offset:33792\n"
"ds_read_b64_tr_b16 a[86:87], v217 offset:34304\n" D06 A0 "ds_read_b64_tr_b16 a[92:93], v217 offset:33824\n"
"ds_read_b64_tr_b16 a[94:95], v217 offset:34336\n" "ds_read_b64_tr_b16 a[88:89], v217 offset:29600\n"
"ds_read_b64_tr_b16 a[90:91], v217 offset:30112\n" D07 A0 D1F D1K D09 "ds_read_b64_tr_b16 a[220:221], v217 offset:33856\n"
"ds_read_b64_tr_b16 a[222:223], v217 offset:34368\n" D1A A0 "ds_read_b64_tr_b16 a[216:217], v217 offset:29632\n" D1B A8U D1C
"ds_read_b64_tr_b16 a[218:219], v217 offset:30144\n" D1G g1 "ds_read_b64_tr_b16 a[224:225], v217 offset:29664\n"
"ds_read_b64_tr_b16 a[226:227], v217 offset:30176\n" D1H "ds_read_b64_tr_b16 a[228:229], v217 offset:33888\n"
"ds_read_b64_tr_b16 a[230:231], v217 offset:34400\n" D1D D1L D1R D1J F3t D1N D1M A1 F3u D1Q D1I D1O A8V D6e D6f D1P F3v F3w D1S D1T D1U
"ds_read_b64_tr_b16 a[168:169], v217 offset:38048\n" "ds_read_b64_tr_b16 a[170:171], v217 offset:38560\n" D1V A8X C2
"ds_read_b64_tr_b16 a[172:173], v217 offset:42272\n" D1W "ds_read_b64_tr_b16 a[174:175], v217 offset:42784\n" B2 A1
"ds_read_b64_tr_b16 a[176:177], v217 offset:38080\n" D1X D1Y "ds_read_b64_tr_b16 a[178:179], v217 offset:38592\n" A8Y s1 A8Z D1Z
"ds_read_b64_tr_b16 a[180:181], v217 offset:42304\n" "ds_read_b64_tr_b16 a[182:183], v217 offset:42816\n" D1a
"ds_read_b64_tr_b16 a[184:185], v217 offset:38112\n" D1b "ds_read_b64_tr_b16 a[186:187], v217 offset:38624\n" A08 A8a D1c
"ds_read_b64_tr_b16 a[188:189], v217 offset:42336\n" "ds_read_b64_tr_b16 a[190:191], v217 offset:42848\n" D1d B9f D1e D6g D1f D6h D1g B9g D1h B9h D1i
B9i D6i D1j A8X D1k D6j A8Y D1l A8Z D1m A8a D6k D6l D1n "Lend7:\n" "Lc7_0:\n" B61 A7O B1S D9P A8o B1S F3x "s_branch Lr7_0\n" "Lc7_1:\n" B62 A49 B1q
A7O A8s "s_branch Lr7_1\n" "Lc7_2:\n" B9j A3i B1o A49 B1p "s_branch Lr7_2\n" "Lc7_3:\n" B9k A3i A8s "s_branch Lr7_3\n" "Lc7_4:\n" v1 D1q A7I D1o A8b
B1S F3y B4x B1o A48 B1p "s_branch Lr7_4\n" "Lc7_5:\n" r1 D1r A48 W4 A7I p5 B3D B1q B4x A8s "s_branch Lr7_5\n" "Lc7_6:\n" v1 D1s B4x N3 A48 r6 B1T B1o
B3D B1p "s_branch Lr7_6\n" "Lc7_7:\n" r1 D1t D14 p5 A98 B1q B1T A8s "s_branch Lr7_7\n" "Lc7_8:\n" v1 D1u B1T N3 B3D r6 A8c B1o A98 B1p
"s_branch Lr7_8\n" "Lc7_9:\n" r1 D1v D15 p5 A7J B1q A8c A8s "s_branch Lr7_9\n" "Lc7_10:\n" v1 D1w A8c N3 A98 r6 A5A B1o A7J B1p "s_branch Lr7_10\n"
"Lc7_11:\n" r1 D1y D16 p5 B5X B1q A5A A8s "s_branch Lr7_11\n" "Lc7_12:\n" v1 D1z A5A N3 A7J r6 B3Z B1o B5X B1p "s_branch Lr7_12\n" "Lc7_13:\n" r1 D10
D17 p5 B1k B1q B3Z A8s "s_branch Lr7_13\n" "Lc7_14:\n" v1 D11 D18 r6 B0H B1o B1k B1p "s_branch Lr7_14\n" "Lc7_15:\n" r1 D12 D19 p5 A8o B1q B0H A8s
"s_branch Lr7_15\n" "Lc7_16:\n" B61 D2A "s_branch Lr7_16\n" "Lc7_17:\n" B62 D2B "s_branch Lr7_17\n" "Lc7_18:\n" B9j D2C "s_branch Lr7_18\n"
"Lc7_19:\n" B9k D2D "s_branch Lr7_19\n" "Lc7_20:\n" D1p D2E "s_branch Lr7_20\n" "Lc7_21:\n" D13 D2F "s_branch Lr7_21\n" "Lrare0_0:\n" D3h
"v_mbcnt_lo_u32_b32 v96, -1, 0\n" "v_mbcnt_hi_u32_b32 v96, -1, v96\n" "v_lshlrev_b32 v97, 4, v96\n" C0j
"buffer_store_dwordx4 v[160:163], v97, s[16:19], s15 offen\n" D3i "buffer_store_dwordx4 v[164:167], v97, s[16:19], s15 offen\n" D3j
"buffer_store_dwordx4 v[168:171], v97, s[16:19], s15 offen\n" D3k "buffer_store_dwordx4 v[172:175], v97, s[16:19], s15 offen\n" D3l
"buffer_store_dwordx4 v[200:203], v97, s[16:19], s15 offen\n" D3m "buffer_store_dwordx4 v[204:207], v97, s[16:19], s15 offen\n" D3n
"buffer_store_dwordx4 v[208:211], v97, s[16:19], s15 offen\n" D3o "buffer_store_dwordx4 v[232:235], v97, s[16:19], s15 offen\n" D3p
"buffer_store_dwordx4 v[236:239], v97, s[16:19], s15 offen\n" D3q "buffer_store_dwordx4 v[240:243], v97, s[16:19], s15 offen\n" D3r
"buffer_store_dwordx4 v[244:247], v97, s[16:19], s15 offen\n" D3s "buffer_store_dwordx4 v[248:251], v97, s[16:19], s15 offen\n" D3t
"buffer_store_dwordx4 v[252:255], v97, s[16:19], s15 offen\n" A47 "v_and_b32 v200, 15, v96\n" "v_lshrrev_b32 v201, 4, v96\n" D3u G3Z G3a G3b G3c G3d
G3e D3v G3f G3g G3h G3i G3j G3k D3w G3l G3m G3n G3o G3p G3q D3x G3r G3s G3t G3u G3v G3w D3y G3x D3z "s_cbranch_vccz Lrk0_0\n" D30 G3y G3z G30 G31 D31
"v_mul_f32 v192, v192, v205\n" D32 "s_cbranch_scc1 Lrk0_0\n" G32 G33 G34 G35 G36 G37 G38 G39 G4A G4B G4C G4D G4E G4F G4G G4H G4I G4J G4K G4L G4M G4N
G4O G4P G4Q G4R G4S G4T G4U G4V G4W G4X G4Y G4Z G4a G4b G4c G4d G4e G4f G4g G4h G4i G4j G4k G4l G4m G4n "Lrk0_0:\n" B5m G4o G4p G4q G4r G4s G4t G4u
G4v G4w G4x G4y G4z G40 G41 G42 D33 "v_mov_b32 v207, v97\n" F3 "v_cvt_pk_bf16_f32 v98, v164, v165\n" "v_cvt_pk_bf16_f32 v99, v166, v167\n" E2b E2c
"v_cvt_pk_bf16_f32 v102, v172, v173\n" "v_cvt_pk_bf16_f32 v103, v174, v175\n" F1 "v_mfma_f32_16x16x32_bf16 v[200:203], a[240:243], v[96:99], 0\n"
"v_mfma_f32_16x16x32_bf16 v[200:203], a[240:243], v[100:103], v[200:203]\n" B0K "v_add_f32 v192, v192, v200\n" D34 "Lrare0_1:\n" D3h
"v_mbcnt_lo_u32_b32 v128, -1, 0\n" "v_mbcnt_hi_u32_b32 v128, -1, v128\n" "v_lshlrev_b32 v129, 4, v128\n" C0j
"buffer_store_dwordx4 v[160:163], v129, s[16:19], s15 offen\n" D3i "buffer_store_dwordx4 v[164:167], v129, s[16:19], s15 offen\n" D3j
"buffer_store_dwordx4 v[168:171], v129, s[16:19], s15 offen\n" D3k "buffer_store_dwordx4 v[172:175], v129, s[16:19], s15 offen\n" D3l
"buffer_store_dwordx4 v[200:203], v129, s[16:19], s15 offen\n" D3m "buffer_store_dwordx4 v[204:207], v129, s[16:19], s15 offen\n" D3n
"buffer_store_dwordx4 v[208:211], v129, s[16:19], s15 offen\n" D3o "buffer_store_dwordx4 v[232:235], v129, s[16:19], s15 offen\n" D3p
"buffer_store_dwordx4 v[236:239], v129, s[16:19], s15 offen\n" D3q "buffer_store_dwordx4 v[240:243], v129, s[16:19], s15 offen\n" D3r
"buffer_store_dwordx4 v[244:247], v129, s[16:19], s15 offen\n" D3s "buffer_store_dwordx4 v[248:251], v129, s[16:19], s15 offen\n" D3t
"buffer_store_dwordx4 v[252:255], v129, s[16:19], s15 offen\n" A47 "v_and_b32 v200, 15, v128\n" "v_lshrrev_b32 v201, 4, v128\n" D3u G3Z G3a G3b G3c
G3d G3e D3v G3f G3g G3h G3i G3j G3k D3w G3l G3m G3n G3o G3p G3q D3x G3r G3s G3t G3u G3v G3w D3y G3x D3z "s_cbranch_vccz Lrk0_1\n" D30 G3y G3z G30 G31
D31 "v_mul_f32 v196, v196, v205\n" D32 "s_cbranch_scc1 Lrk0_1\n" G32 G33 G34 G35 G36 G37 G38 G39 G4A G4B G4C G4D G4E G4F G4G G4H G4I G4J G4K G4L G4M
G4N G4O G4P G4Q G4R G4S G4T G4U G4V G4W G4X G4Y G4Z G4a G4b G4c G4d G4e G4f G4g G4h G4i G4j G4k G4l G4m G4n "Lrk0_1:\n" B5m G4o G4p G4q G4r G4s G4t
G4u G4v G4w G4x G4y G4z G40 G41 G42 D33 "v_mov_b32 v207, v129\n" A4 "v_cvt_pk_bf16_f32 v130, v164, v165\n" "v_cvt_pk_bf16_f32 v131, v166, v167\n" E4X
E4Y "v_cvt_pk_bf16_f32 v134, v172, v173\n" "v_cvt_pk_bf16_f32 v135, v174, v175\n" F1
"v_mfma_f32_16x16x32_bf16 v[200:203], a[240:243], v[128:131], 0\n" "v_mfma_f32_16x16x32_bf16 v[200:203], a[240:243], v[132:135], v[200:203]\n" B0K
"v_add_f32 v196, v196, v200\n" D34 "Lrare1_0:\n" D3h "v_mbcnt_lo_u32_b32 v104, -1, 0\n" "v_mbcnt_hi_u32_b32 v104, -1, v104\n"
"v_lshlrev_b32 v105, 4, v104\n" C0j "buffer_store_dwordx4 v[160:163], v105, s[16:19], s15 offen\n" D3i
"buffer_store_dwordx4 v[164:167], v105, s[16:19], s15 offen\n" D3j "buffer_store_dwordx4 v[168:171], v105, s[16:19], s15 offen\n" D3k
"buffer_store_dwordx4 v[172:175], v105, s[16:19], s15 offen\n" D3l "buffer_store_dwordx4 v[200:203], v105, s[16:19], s15 offen\n" D3m
"buffer_store_dwordx4 v[204:207], v105, s[16:19], s15 offen\n" D3n "buffer_store_dwordx4 v[208:211], v105, s[16:19], s15 offen\n" D3o
"buffer_store_dwordx4 v[232:235], v105, s[16:19], s15 offen\n" D3p "buffer_store_dwordx4 v[236:239], v105, s[16:19], s15 offen\n" D3q
"buffer_store_dwordx4 v[240:243], v105, s[16:19], s15 offen\n" D3r "buffer_store_dwordx4 v[244:247], v105, s[16:19], s15 offen\n" D3s
"buffer_store_dwordx4 v[248:251], v105, s[16:19], s15 offen\n" D3t "buffer_store_dwordx4 v[252:255], v105, s[16:19], s15 offen\n" A47
"v_and_b32 v200, 15, v104\n" "v_lshrrev_b32 v201, 4, v104\n" D3u G43 G44 G45 G46 G47 G48 D3v G49 G5A G5B G5C G5D G5E D3w G5F G5G G5H G5I G5J G5K D3x
G5L G5M G5N G5O G5P G5Q D3y G5R D3z "s_cbranch_vccz Lrk1_0\n" D30 G5S G5T G5U G5V D31 "v_mul_f32 v193, v193, v205\n" D32 "s_cbranch_scc1 Lrk1_0\n" G5W
G5X G5Y G5Z G5a G5b G5c G5d G5e G5f G5g G5h G5i G5j G5k G5l G5m G5n G5o G5p G5q G5r G5s G5t G5u G5v G5w G5x G5y G5z G50 G51 G52 G53 G54 G55 G56 G57
G58 G59 G6A G6B G6C G6D G6E G6F G6G G6H "Lrk1_0:\n" G6I G6J G6K G6L B5r G6M G6N G6O G6P G6Q G6R G6S G6T G6U G6V G6W D33 "v_mov_b32 v207, v105\n"
"v_cvt_pk_bf16_f32 v104, v160, v161\n" "v_cvt_pk_bf16_f32 v105, v162, v163\n" i9 "v_cvt_pk_bf16_f32 v108, v168, v169\n"
"v_cvt_pk_bf16_f32 v109, v170, v171\n" A0p F1 "v_mfma_f32_16x16x32_bf16 v[200:203], a[244:247], v[104:107], 0\n"
"v_mfma_f32_16x16x32_bf16 v[200:203], a[244:247], v[108:111], v[200:203]\n" B0K "v_add_f32 v193, v193, v201\n" D34 "Lrare1_1:\n" D3h
"v_mbcnt_lo_u32_b32 v136, -1, 0\n" "v_mbcnt_hi_u32_b32 v136, -1, v136\n" "v_lshlrev_b32 v137, 4, v136\n" C0j
"buffer_store_dwordx4 v[160:163], v137, s[16:19], s15 offen\n" D3i "buffer_store_dwordx4 v[164:167], v137, s[16:19], s15 offen\n" D3j
"buffer_store_dwordx4 v[168:171], v137, s[16:19], s15 offen\n" D3k "buffer_store_dwordx4 v[172:175], v137, s[16:19], s15 offen\n" D3l
"buffer_store_dwordx4 v[200:203], v137, s[16:19], s15 offen\n" D3m "buffer_store_dwordx4 v[204:207], v137, s[16:19], s15 offen\n" D3n
"buffer_store_dwordx4 v[208:211], v137, s[16:19], s15 offen\n" D3o "buffer_store_dwordx4 v[232:235], v137, s[16:19], s15 offen\n" D3p
"buffer_store_dwordx4 v[236:239], v137, s[16:19], s15 offen\n" D3q "buffer_store_dwordx4 v[240:243], v137, s[16:19], s15 offen\n" D3r
"buffer_store_dwordx4 v[244:247], v137, s[16:19], s15 offen\n" D3s "buffer_store_dwordx4 v[248:251], v137, s[16:19], s15 offen\n" D3t
"buffer_store_dwordx4 v[252:255], v137, s[16:19], s15 offen\n" A47 "v_and_b32 v200, 15, v136\n" "v_lshrrev_b32 v201, 4, v136\n" D3u G43 G44 G45 G46
G47 G48 D3v G49 G5A G5B G5C G5D G5E D3w G5F G5G G5H G5I G5J G5K D3x G5L G5M G5N G5O G5P G5Q D3y G5R D3z "s_cbranch_vccz Lrk1_1\n" D30 G5S G5T G5U G5V
D31 "v_mul_f32 v197, v197, v205\n" D32 "s_cbranch_scc1 Lrk1_1\n" G5W G5X G5Y G5Z G5a G5b G5c G5d G5e G5f G5g G5h G5i G5j G5k G5l G5m G5n G5o G5p G5q
G5r G5s G5t G5u G5v G5w G5x G5y G5z G50 G51 G52 G53 G54 G55 G56 G57 G58 G59 G6A G6B G6C G6D G6E G6F G6G G6H "Lrk1_1:\n" G6I G6J G6K G6L B5r G6M G6N
G6O G6P G6Q G6R G6S G6T G6U G6V G6W D33 "v_mov_b32 v207, v137\n" B9U B9V A1b B9a B9b B0w F1
"v_mfma_f32_16x16x32_bf16 v[200:203], a[244:247], v[136:139], 0\n" "v_mfma_f32_16x16x32_bf16 v[200:203], a[244:247], v[140:143], v[200:203]\n" B0K
"v_add_f32 v197, v197, v201\n" D34 "Lrare2_0:\n" D3h "v_mbcnt_lo_u32_b32 v112, -1, 0\n" "v_mbcnt_hi_u32_b32 v112, -1, v112\n"
"v_lshlrev_b32 v113, 4, v112\n" C0j "buffer_store_dwordx4 v[160:163], v113, s[16:19], s15 offen\n" D3i
"buffer_store_dwordx4 v[164:167], v113, s[16:19], s15 offen\n" D3j "buffer_store_dwordx4 v[168:171], v113, s[16:19], s15 offen\n" D3k
"buffer_store_dwordx4 v[172:175], v113, s[16:19], s15 offen\n" D3l "buffer_store_dwordx4 v[200:203], v113, s[16:19], s15 offen\n" D3m
"buffer_store_dwordx4 v[204:207], v113, s[16:19], s15 offen\n" D3n "buffer_store_dwordx4 v[208:211], v113, s[16:19], s15 offen\n" D3o
"buffer_store_dwordx4 v[232:235], v113, s[16:19], s15 offen\n" D3p "buffer_store_dwordx4 v[236:239], v113, s[16:19], s15 offen\n" D3q
"buffer_store_dwordx4 v[240:243], v113, s[16:19], s15 offen\n" D3r "buffer_store_dwordx4 v[244:247], v113, s[16:19], s15 offen\n" D3s
"buffer_store_dwordx4 v[248:251], v113, s[16:19], s15 offen\n" D3t "buffer_store_dwordx4 v[252:255], v113, s[16:19], s15 offen\n" A47
"v_and_b32 v200, 15, v112\n" "v_lshrrev_b32 v201, 4, v112\n" D3u G6X G6Y G6Z G6a G6b G6c D3v G6d G6e G6f G6g G6h G6i D3w G6j G6k G6l G6m G6n G6o D3x
G6p G6q G6r G6s G6t G6u D3y G6v D3z "s_cbranch_vccz Lrk2_0\n" D30 G6w G6x G6y G6z D31 "v_mul_f32 v194, v194, v205\n" D32 "s_cbranch_scc1 Lrk2_0\n" G60
n0 G61 G62 n0 G63 G64 n0 G65 G66 n0 G67 G68 n0 G69 G7A n0 G7B G7C n0 G7D G7E n0 G7F G7G n0 G7H G7I n0 G7J G7K n0 G7L G7M n0 G7N G7O n0 G7P G7Q n0 G7R
G7S n0 G7T G7U n0 G7V G7W n0 G7X G7Y n0 G7Z G7a n0 G7b G7c n0 G7d G7e n0 G7f G7g n0 G7h G7i n0 G7j G7k n0 G7l G7m n0 G7n G7o n0 G7p G7q n0 G7r G7s n0
G7t G7u n0 G7v G7w n0 G7x G7y n0 G7z G70 n0 G71 G72 n0 G73 G74 n0 G75 G76 n0 G77 G78 n0 G79 G8A n0 G8B G8C n0 G8D G8E n0 G8F G8G n0 G8H G8I n0 G8J G8K
n0 G8L G8M n0 G8N G8O n0 G8P G8Q n0 G8R G8S n0 G8T G8U n0 G8V G8W n0 G8X "Lrk2_0:\n" E70 G8Y G8Z G8a G8b G8c G8d G8e C4b G8f G8g G8h G8i G8j G8k G8l
D33 "v_mov_b32 v207, v113\n" E8D E8E "v_cvt_pk_bf16_f32 v114, v164, v165\n" "v_cvt_pk_bf16_f32 v115, v166, v167\n" B0C
"v_cvt_pk_bf16_f32 v118, v172, v173\n" "v_cvt_pk_bf16_f32 v119, v174, v175\n" F1 "v_mfma_f32_16x16x32_bf16 v[200:203], a[248:251], v[112:115], 0\n"
"v_mfma_f32_16x16x32_bf16 v[200:203], a[248:251], v[116:119], v[200:203]\n" B0K "v_add_f32 v194, v194, v202\n" D34 "Lrare2_1:\n" D3h
"v_mbcnt_lo_u32_b32 v144, -1, 0\n" "v_mbcnt_hi_u32_b32 v144, -1, v144\n" "v_lshlrev_b32 v145, 4, v144\n" C0j
"buffer_store_dwordx4 v[160:163], v145, s[16:19], s15 offen\n" D3i "buffer_store_dwordx4 v[164:167], v145, s[16:19], s15 offen\n" D3j
"buffer_store_dwordx4 v[168:171], v145, s[16:19], s15 offen\n" D3k "buffer_store_dwordx4 v[172:175], v145, s[16:19], s15 offen\n" D3l
"buffer_store_dwordx4 v[200:203], v145, s[16:19], s15 offen\n" D3m "buffer_store_dwordx4 v[204:207], v145, s[16:19], s15 offen\n" D3n
"buffer_store_dwordx4 v[208:211], v145, s[16:19], s15 offen\n" D3o "buffer_store_dwordx4 v[232:235], v145, s[16:19], s15 offen\n" D3p
"buffer_store_dwordx4 v[236:239], v145, s[16:19], s15 offen\n" D3q "buffer_store_dwordx4 v[240:243], v145, s[16:19], s15 offen\n" D3r
"buffer_store_dwordx4 v[244:247], v145, s[16:19], s15 offen\n" D3s "buffer_store_dwordx4 v[248:251], v145, s[16:19], s15 offen\n" D3t
"buffer_store_dwordx4 v[252:255], v145, s[16:19], s15 offen\n" A47 "v_and_b32 v200, 15, v144\n" "v_lshrrev_b32 v201, 4, v144\n" D3u G6X G6Y G6Z G6a
G6b G6c D3v G6d G6e G6f G6g G6h G6i D3w G6j G6k G6l G6m G6n G6o D3x G6p G6q G6r G6s G6t G6u D3y G6v D3z "s_cbranch_vccz Lrk2_1\n" D30 G6w G6x G6y G6z
D31 "v_mul_f32 v198, v198, v205\n" D32 "s_cbranch_scc1 Lrk2_1\n" G60 n0 G61 G62 n0 G63 G64 n0 G65 G66 n0 G67 G68 n0 G69 G7A n0 G7B G7C n0 G7D G7E n0
G7F G7G n0 G7H G7I n0 G7J G7K n0 G7L G7M n0 G7N G7O n0 G7P G7Q n0 G7R G7S n0 G7T G7U n0 G7V G7W n0 G7X G7Y n0 G7Z G7a n0 G7b G7c n0 G7d G7e n0 G7f G7g
n0 G7h G7i n0 G7j G7k n0 G7l G7m n0 G7n G7o n0 G7p G7q n0 G7r G7s n0 G7t G7u n0 G7v G7w n0 G7x G7y n0 G7z G70 n0 G71 G72 n0 G73 G74 n0 G75 G76 n0 G77
G78 n0 G79 G8A n0 G8B G8C n0 G8D G8E n0 G8F G8G n0 G8H G8I n0 G8J G8K n0 G8L G8M n0 G8N G8O n0 G8P G8Q n0 G8R G8S n0 G8T G8U n0 G8V G8W n0 G8X
"Lrk2_1:\n" E70 G8Y G8Z G8a G8b G8c G8d G8e C4b G8f G8g G8h G8i G8j G8k G8l D33 "v_mov_b32 v207, v145\n" G3C G3D
"v_cvt_pk_bf16_f32 v146, v164, v165\n" "v_cvt_pk_bf16_f32 v147, v166, v167\n" B5K "v_cvt_pk_bf16_f32 v150, v172, v173\n"
"v_cvt_pk_bf16_f32 v151, v174, v175\n" F1 "v_mfma_f32_16x16x32_bf16 v[200:203], a[248:251], v[144:147], 0\n"
"v_mfma_f32_16x16x32_bf16 v[200:203], a[248:251], v[148:151], v[200:203]\n" B0K "v_add_f32 v198, v198, v202\n" D34 "Lrare3_0:\n" D3h
"v_mbcnt_lo_u32_b32 v120, -1, 0\n" "v_mbcnt_hi_u32_b32 v120, -1, v120\n" "v_lshlrev_b32 v121, 4, v120\n" C0j
"buffer_store_dwordx4 v[160:163], v121, s[16:19], s15 offen\n" D3i "buffer_store_dwordx4 v[164:167], v121, s[16:19], s15 offen\n" D3j
"buffer_store_dwordx4 v[168:171], v121, s[16:19], s15 offen\n" D3k "buffer_store_dwordx4 v[172:175], v121, s[16:19], s15 offen\n" D3l
"buffer_store_dwordx4 v[200:203], v121, s[16:19], s15 offen\n" D3m "buffer_store_dwordx4 v[204:207], v121, s[16:19], s15 offen\n" D3n
"buffer_store_dwordx4 v[208:211], v121, s[16:19], s15 offen\n" D3o "buffer_store_dwordx4 v[232:235], v121, s[16:19], s15 offen\n" D3p
"buffer_store_dwordx4 v[236:239], v121, s[16:19], s15 offen\n" D3q "buffer_store_dwordx4 v[240:243], v121, s[16:19], s15 offen\n" D3r
"buffer_store_dwordx4 v[244:247], v121, s[16:19], s15 offen\n" D3s "buffer_store_dwordx4 v[248:251], v121, s[16:19], s15 offen\n" D3t
"buffer_store_dwordx4 v[252:255], v121, s[16:19], s15 offen\n" A47 "v_and_b32 v200, 15, v120\n" "v_lshrrev_b32 v201, 4, v120\n" D3u G8m G8n G8o G8p
G8q G8r D3v G8s G8t G8u G8v G8w G8x D3w G8y G8z G80 G81 G82 G83 D3x G84 G85 G86 G87 G88 G89 D3y G9A D3z "s_cbranch_vccz Lrk3_0\n" D30 G9B G9C G9D G9E
D31 "v_mul_f32 v195, v195, v205\n" D32 "s_cbranch_scc1 Lrk3_0\n" G9F n0 G9G G9H n0 G9I G9J n0 G9K G9L n0 G9M G9N n0 G9O G9P n0 G9Q G9R n0 G9S G9T n0
G9U G9V n0 G9W G9X n0 G9Y G9Z n0 G9a G9b n0 G9c G9d n0 G9e G9f n0 G9g G9h n0 G9i G9j n0 G9k G9l n0 G9m G9n n0 G9o G9p n0 G9q G9r n0 G9s G9t n0 G9u G9v
n0 G9w G9x n0 G9y G9z n0 G90 G91 n0 G92 G93 n0 G94 G95 n0 G96 G97 n0 G98 G99 n0 H0A H0B n0 H0C H0D n0 H0E H0F n0 H0G H0H n0 H0I H0J n0 H0K H0L n0 H0M
H0N n0 H0O H0P n0 H0Q H0R n0 H0S H0T n0 H0U H0V n0 H0W H0X n0 H0Y H0Z n0 H0a H0b n0 H0c H0d n0 H0e H0f n0 H0g H0h n0 H0i H0j n0 H0k H0l n0 H0m
"Lrk3_0:\n" H0n H0o H0p H0q E8B H0r H0s H0t H0u H0v H0w H0x C4a H0y H0z H00 D33 "v_mov_b32 v207, v121\n" "v_cvt_pk_bf16_f32 v120, v160, v161\n"
"v_cvt_pk_bf16_f32 v121, v162, v163\n" E2Z E2a "v_cvt_pk_bf16_f32 v124, v168, v169\n" "v_cvt_pk_bf16_f32 v125, v170, v171\n" B0j F1
"v_mfma_f32_16x16x32_bf16 v[200:203], a[252:255], v[120:123], 0\n" "v_mfma_f32_16x16x32_bf16 v[200:203], a[252:255], v[124:127], v[200:203]\n" B0K
"v_add_f32 v195, v195, v203\n" D34 "Lrare3_1:\n" D3h "v_mbcnt_lo_u32_b32 v152, -1, 0\n" "v_mbcnt_hi_u32_b32 v152, -1, v152\n"
"v_lshlrev_b32 v153, 4, v152\n" C0j "buffer_store_dwordx4 v[160:163], v153, s[16:19], s15 offen\n" D3i
"buffer_store_dwordx4 v[164:167], v153, s[16:19], s15 offen\n" D3j "buffer_store_dwordx4 v[168:171], v153, s[16:19], s15 offen\n" D3k
"buffer_store_dwordx4 v[172:175], v153, s[16:19], s15 offen\n" D3l "buffer_store_dwordx4 v[200:203], v153, s[16:19], s15 offen\n" D3m
"buffer_store_dwordx4 v[204:207], v153, s[16:19], s15 offen\n" D3n "buffer_store_dwordx4 v[208:211], v153, s[16:19], s15 offen\n" D3o
"buffer_store_dwordx4 v[232:235], v153, s[16:19], s15 offen\n" D3p "buffer_store_dwordx4 v[236:239], v153, s[16:19], s15 offen\n" D3q
"buffer_store_dwordx4 v[240:243], v153, s[16:19], s15 offen\n" D3r "buffer_store_dwordx4 v[244:247], v153, s[16:19], s15 offen\n" D3s
"buffer_store_dwordx4 v[248:251], v153, s[16:19], s15 offen\n" D3t "buffer_store_dwordx4 v[252:255], v153, s[16:19], s15 offen\n" A47
"v_and_b32 v200, 15, v152\n" "v_lshrrev_b32 v201, 4, v152\n" D3u G8m G8n G8o G8p G8q G8r D3v G8s G8t G8u G8v G8w G8x D3w G8y G8z G80 G81 G82 G83 D3x
G84 G85 G86 G87 G88 G89 D3y G9A D3z "s_cbranch_vccz Lrk3_1\n" D30 G9B G9C G9D G9E D31 "v_mul_f32 v199, v199, v205\n" D32 "s_cbranch_scc1 Lrk3_1\n" G9F
n0 G9G G9H n0 G9I G9J n0 G9K G9L n0 G9M G9N n0 G9O G9P n0 G9Q G9R n0 G9S G9T n0 G9U G9V n0 G9W G9X n0 G9Y G9Z n0 G9a G9b n0 G9c G9d n0 G9e G9f n0 G9g
G9h n0 G9i G9j n0 G9k G9l n0 G9m G9n n0 G9o G9p n0 G9q G9r n0 G9s G9t n0 G9u G9v n0 G9w G9x n0 G9y G9z n0 G90 G91 n0 G92 G93 n0 G94 G95 n0 G96 G97 n0
G98 G99 n0 H0A H0B n0 H0C H0D n0 H0E H0F n0 H0G H0H n0 H0I H0J n0 H0K H0L n0 H0M H0N n0 H0O H0P n0 H0Q H0R n0 H0S H0T n0 H0U H0V n0 H0W H0X n0 H0Y H0Z
n0 H0a H0b n0 H0c H0d n0 H0e H0f n0 H0g H0h n0 H0i H0j n0 H0k H0l n0 H0m "Lrk3_1:\n" H0n H0o H0p H0q E8B H0r H0s H0t H0u H0v H0w H0x C4a H0y H0z H00
D33 "v_mov_b32 v207, v153\n" F3U F3V D9M D9N F3Z F3a B4B F1 "v_mfma_f32_16x16x32_bf16 v[200:203], a[252:255], v[152:155], 0\n"
"v_mfma_f32_16x16x32_bf16 v[200:203], a[252:255], v[156:159], v[200:203]\n" B0K "v_add_f32 v199, v199, v203\n" D34
        : "+v"(tid), "+s"(karg), "+s"(bid)
        :
        :
        "v1","v2","v3","v4","v5","v6","v7","v8","v9","v10","v11","v12","v13","v14","v15","v16","v17","v18","v19","v20",
        "v21","v22","v23","v24","v25","v26","v27","v28","v29","v30","v31","v32","v33","v34","v35","v36","v37","v38","v39","v40",
        "v41","v42","v43","v44","v45","v46","v47","v48","v49","v50","v51","v52","v53","v54","v55","v56","v57","v58","v59","v60",
        "v61","v62","v63","v64","v65","v66","v67","v68","v69","v70","v71","v72","v73","v74","v75","v76","v77","v78","v79","v80",
        "v81","v82","v83","v84","v85","v86","v87","v88","v89","v90","v91","v92","v93","v94","v95","v96","v97","v98","v99","v100",
        "v101","v102","v103","v104","v105","v106","v107","v108","v109","v110","v111","v112","v113","v114","v115","v116","v117","v118","v119","v120",
        "v121","v122","v123","v124","v125","v126","v127","v128","v129","v130","v131","v132","v133","v134","v135","v136","v137","v138","v139","v140",
        "v141","v142","v143","v144","v145","v146","v147","v148","v149","v150","v151","v152","v153","v154","v155","v156","v157","v158","v159","v160",
        "v161","v162","v163","v164","v165","v166","v167","v168","v169","v170","v171","v172","v173","v174","v175","v176","v177","v178","v179","v180",
        "v181","v182","v183","v184","v185","v186","v187","v188","v189","v190","v191","v192","v193","v194","v195","v196","v197","v198","v199","v200",
        "v201","v202","v203","v204","v205","v206","v207","v208","v209","v210","v211","v212","v213","v214","v215","v216","v217","v218","v219","v220",
        "v221","v222","v223","v224","v225","v226","v227","v228","v229","v230","v231","v232","v233","v234","v235","v236","v237","v238","v239","v240",
        "v241","v242","v243","v244","v245","v246","v247","v248","v249","v250","v251","v252","v253","v254","v255","a0","a1","a2","a3","a4",
        "a5","a6","a7","a8","a9","a10","a11","a12","a13","a14","a15","a16","a17","a18","a19","a20","a21","a22","a23","a24",
        "a25","a26","a27","a28","a29","a30","a31","a32","a33","a34","a35","a36","a37","a38","a39","a40","a41","a42","a43","a44",
        "a45","a46","a47","a48","a49","a50","a51","a52","a53","a54","a55","a56","a57","a58","a59","a60","a61","a62","a63","a64",
        "a65","a66","a67","a68","a69","a70","a71","a72","a73","a74","a75","a76","a77","a78","a79","a80","a81","a82","a83","a84",
        "a85","a86","a87","a88","a89","a90","a91","a92","a93","a94","a95","a96","a97","a98","a99","a100","a101","a102","a103","a104",
        "a105","a106","a107","a108","a109","a110","a111","a112","a113","a114","a115","a116","a117","a118","a119","a120","a121","a122","a123","a124",
        "a125","a126","a127","a128","a129","a130","a131","a132","a133","a134","a135","a136","a137","a138","a139","a140","a141","a142","a143","a144",
        "a145","a146","a147","a148","a149","a150","a151","a152","a153","a154","a155","a156","a157","a158","a159","a160","a161","a162","a163","a164",
        "a165","a166","a167","a168","a169","a170","a171","a172","a173","a174","a175","a176","a177","a178","a179","a180","a181","a182","a183","a184",
        "a185","a186","a187","a188","a189","a190","a191","a192","a193","a194","a195","a196","a197","a198","a199","a200","a201","a202","a203","a204",
        "a205","a206","a207","a208","a209","a210","a211","a212","a213","a214","a215","a216","a217","a218","a219","a220","a221","a222","a223","a224",
        "a225","a226","a227","a228","a229","a230","a231","a232","a233","a234","a235","a236","a237","a238","a239","a240","a241","a242","a243","a244",
        "a245","a246","a247","a248","a249","a250","a251","a252","a253","a254","a255","s3","s4","s5","s6","s7","s8","s9","s10","s11",
        "s12","s13","s14","s15","s16","s17","s18","s19","s20","s21","s22","s23","s24","s25","s26","s27","s28","s29","s30","s31",
        "s32","s33","s34","s35","s36","s37","s38","s39","s40","s41","s42","s43","s44","s45","s46","s47","s48","s49","s50","s51",
        "s52","s53","s54","s55","s56","s57","s58","s59","s60","s61","s62","s63","s64","s65","s66","s67","s68","s69","s70","s71",
        "s72","s73","s74","s75","s76","s77","s78","s79","s80","s81","s82","s83","s84","s85","s86","s87","s88","s89","s90","s91",
        "s92","s93","s94","s95","s96","s97","s98","s99","s100","s101","vcc","scc","memory");
#endif
}
```
